```python
import torch

def _softmax(x, axis=-1):
    return torch.softmax(x, dim=axis)
def _gelu(x):
    return torch.nn.functional.gelu(x, approximate="tanh")
def _silu(x):
    return torch.nn.functional.silu(x)
def _sigmoid(x):
    return torch.sigmoid(x)
def _layernorm(x, axis=-1, eps=1e-5):
    m = x.mean(dim=axis, keepdim=True)
    v = x.var(dim=axis, keepdim=True, unbiased=False)
    return (x - m) / torch.sqrt(v + eps)
def _scatter_add(tgt, idx, src, axis=0):
    return tgt.clone().index_add_(axis, idx, src)
def _segment_sum(src, seg, n):
    out = torch.zeros((n,) + src.shape[1:], dtype=src.dtype)
    return out.index_add_(0, seg, src)
def _pad(x, pw):
    flat = tuple(v for pair in reversed(pw) for v in pair)
    return torch.nn.functional.pad(x, flat)

kernel_name = 'gated_mix__compressed_kv_cla__geglu_ffn__prompt'

def setup_inputs():

    _w = (lambda a: (lambda t: t.to(torch.bfloat16) if t.is_floating_point() else t)(torch.as_tensor(a)))
    def _A_setup():
        B, H, Sq, Sk, D, M = 1, 32, 2048, 2048, 128, 1024
        Dc = 4 * D
        return {
            "x": _w((lambda *s: torch.randn(*s, dtype=torch.bfloat16) * 0.05)(B, Sq, M)),
            "c_kv": _w((lambda *s: torch.randn(*s, dtype=torch.bfloat16) * 0.4)(B, Sk, Dc)),
            "w_q": _w((lambda *s: torch.randn(*s, dtype=torch.bfloat16) * 6.4)(M, H, D) * (M ** -0.5)),
            "w_uk": _w((lambda *s: torch.randn(*s, dtype=torch.bfloat16) * 6.4)(Dc, H, D) * (Dc ** -0.5)),
            "w_uv": _w((lambda *s: torch.randn(*s, dtype=torch.bfloat16) * 0.4)(Dc, H, D) * (Dc ** -0.5)),
            "w_o": _w((lambda *s: torch.randn(*s, dtype=torch.bfloat16) * 0.4)(H, D, M) * ((H * D) ** -0.5)),
        }
    _A_in = _A_setup()

    def _B_setup():
        B, S, D, F = 1, 2048, 1024, 3584
        return {
            "x": _w((lambda *s: torch.randn(*s, dtype=torch.bfloat16) * 0.05)(B, S, D)),
            "w_gate": _w((lambda *s: torch.randn(*s, dtype=torch.bfloat16) * 0.05)(D, F)),
            "w_up": _w((lambda *s: torch.randn(*s, dtype=torch.bfloat16) * 0.05)(D, F)),
            "w_down": _w((lambda *s: torch.randn(*s, dtype=torch.bfloat16) * 0.05)(F, D)),
        }
    _B_in = _B_setup()

    return {
        "A_x": _A_in["x"],
        "A_c_kv": _A_in["c_kv"],
        "A_w_q": _A_in["w_q"],
        "A_w_uk": _A_in["w_uk"],
        "A_w_uv": _A_in["w_uv"],
        "A_w_o": _A_in["w_o"],
        "B_x": _B_in["x"],
        "B_w_gate": _B_in["w_gate"],
        "B_w_up": _B_in["w_up"],
        "B_w_down": _B_in["w_down"],
        "gate_w": _w((lambda *s: torch.randn(*s, dtype=torch.bfloat16) * 0.05)(1024) * 16.0),
    }

def reference(A_x, A_c_kv, A_w_q, A_w_uk, A_w_uv, A_w_o, B_x, B_w_gate, B_w_up, B_w_down, gate_w):
    def _A_ref(x, c_kv, w_q, w_uk, w_uv, w_o):
        B, Sq, M = x.shape[0], x.shape[1], x.shape[2]
        Sk = c_kv.shape[1]
        H, D = w_q.shape[1], w_q.shape[2]
        q = torch.einsum("bqm,mhd->bhqd", x, w_q)
        k = torch.einsum("bkc,chd->bhkd", c_kv, w_uk)
        v = torch.einsum("bkc,chd->bhkd", c_kv, w_uv)
        scores = torch.einsum("bhqd,bhkd->bhqk", q, k) * (D ** -0.5)
        off = Sk - Sq
        qi = torch.arange(Sq)[:, None]
        kj = torch.arange(Sk)[None, :]
        cmask = kj <= (qi + off)
        scores = torch.where(cmask[None, None], scores, -1e9)
        p = _softmax(scores, axis=-1)
        ctx = torch.einsum("bhqk,bhkd->bhqd", p, v)
        return torch.einsum("bhqd,hdm->bqm", ctx, w_o)

    def _B_ref(x, w_gate, w_up, w_down):
        return (_gelu(x @ w_gate) * (x @ w_up)) @ w_down

    x = A_x
    g = _sigmoid((x * gate_w).sum(-1, keepdims=True))
    a = _A_ref(x, A_c_kv, A_w_q, A_w_uk, A_w_uv, A_w_o)
    b = _B_ref(x, B_w_gate, B_w_up, B_w_down)
    return g * a + (1 - g) * b
```

```cpp
#include <hip/hip_runtime.h>
#include <hip/hip_bf16.h>
typedef unsigned short u16;
__device__ __forceinline__ float bf2f(u16 b){ return __uint_as_float(((unsigned)b)<<16); }
__device__ __forceinline__ u16 f2bf(float f){ unsigned u=__float_as_uint(f); u += 0x7FFFu + ((u>>16)&1u); return (u16)(u>>16); }

__global__ void n_gemm(const u16* A, const u16* B, u16* C, int M, int N, int K, int lda){
  __shared__ float As[32][33], Bs[32][33];
  int tx=threadIdx.x, ty=threadIdx.y; int row=blockIdx.y*32+ty, col=blockIdx.x*32+tx; float acc=0;
  for(int k0=0;k0<K;k0+=32){ As[ty][tx]=bf2f(A[(size_t)row*lda+k0+tx]); Bs[ty][tx]=bf2f(B[(size_t)(k0+ty)*N+col]); __syncthreads();
    for(int k=0;k<32;++k) acc+=As[ty][k]*Bs[k][tx]; __syncthreads(); }
  C[(size_t)row*N+col]=f2bf(acc);
}
__global__ void n_gate(const u16* x, const u16* gw, float* g){ int r=blockIdx.x; float s=0; for(int i=threadIdx.x;i<1024;i+=64) s+=bf2f(x[r*1024+i])*bf2f(gw[i]);
  for(int o=32;o;o>>=1) s+=__shfl_xor(s,o); if(threadIdx.x==0) g[r]=1.f/(1.f+expf(-s)); }
__global__ void n_geglu(const u16* ga, const u16* up, u16* h, int n){ int i=blockIdx.x*blockDim.x+threadIdx.x; if(i<n){ float v=bf2f(ga[i]); float t=tanhf(0.7978845608f*(v+0.044715f*v*v*v)); float ge=bf2f(f2bf(0.5f*v*(1.f+t))); h[i]=f2bf(ge*bf2f(up[i])); } }
__global__ void n_attn(const u16* Q, const u16* K, const u16* V, u16* ctx){
  int qi=blockIdx.x, h=blockIdx.y, t=threadIdx.x; __shared__ float s[2048]; __shared__ float q[128]; __shared__ float red[128];
  q[t]=bf2f(Q[(size_t)qi*4096+h*128+t]); __syncthreads();
  float mx=-1e30f; for(int k=t;k<=qi;k+=128){ float a=0; for(int d=0;d<128;++d) a+=q[d]*bf2f(K[(size_t)k*4096+h*128+d]); a*=0.08838834764831845f; s[k]=a; mx=fmaxf(mx,a);}
  red[t]=mx; __syncthreads(); for(int o=64;o;o>>=1){ if(t<o) red[t]=fmaxf(red[t],red[t+o]); __syncthreads(); } mx=red[0]; __syncthreads();
  float sum=0; for(int k=t;k<=qi;k+=128){ float e=expf(s[k]-mx); s[k]=e; sum+=e; } red[t]=sum; __syncthreads(); for(int o=64;o;o>>=1){ if(t<o) red[t]+=red[t+o]; __syncthreads(); } sum=red[0];
  float o=0; for(int k=0;k<=qi;++k) o+=s[k]*bf2f(V[(size_t)k*4096+h*128+t]); ctx[(size_t)qi*4096+h*128+t]=f2bf(o/sum);
}
__global__ void n_mix(const u16* a, const u16* b, const float* g, u16* out){ int i=blockIdx.x*blockDim.x+threadIdx.x; float gg=g[i>>10]; out[i]=f2bf(gg*bf2f(a[i])+(1.f-gg)*bf2f(b[i])); }

extern "C" void kernel_launch(void* const* d_in, const int* in_sizes, int n_in, void* d_out, int out_size, void* d_ws, size_t ws_size, hipStream_t stream){
  const u16* x=(const u16*)d_in[0]; const u16* c=(const u16*)d_in[1]; const u16* wq=(const u16*)d_in[2]; const u16* wuk=(const u16*)d_in[3]; const u16* wuv=(const u16*)d_in[4]; const u16* wo=(const u16*)d_in[5];
  const u16* wg=(const u16*)d_in[7]; const u16* wu=(const u16*)d_in[8]; const u16* wd=(const u16*)d_in[9]; const u16* gw=(const u16*)d_in[10];
  char* ws=(char*)d_ws; size_t MB=1<<20;
  u16* Q=(u16*)(ws); u16* K=(u16*)(ws+16*MB); u16* V=(u16*)(ws+32*MB); u16* ctx=(u16*)(ws+48*MB); u16* ga=(u16*)(ws+64*MB); u16* up=(u16*)(ws+80*MB); u16* h=(u16*)(ws+96*MB); u16* a=(u16*)(ws+112*MB); u16* b=(u16*)(ws+120*MB); float* g=(float*)(ws+128*MB);
  dim3 tb(32,32);
  n_gate<<<2048,64,0,stream>>>(x,gw,g);
  n_gemm<<<dim3(4096/32,64),tb,0,stream>>>(x,wq,Q,2048,4096,1024,1024);
  n_gemm<<<dim3(4096/32,64),tb,0,stream>>>(c,wuk,K,2048,4096,512,512);
  n_gemm<<<dim3(4096/32,64),tb,0,stream>>>(c,wuv,V,2048,4096,512,512);
  n_gemm<<<dim3(3584/32,64),tb,0,stream>>>(x,wg,ga,2048,3584,1024,1024);
  n_gemm<<<dim3(3584/32,64),tb,0,stream>>>(x,wu,up,2048,3584,1024,1024);
  n_geglu<<<2048*3584/256,256,0,stream>>>(ga,up,h,2048*3584);
  n_attn<<<dim3(2048,32),128,0,stream>>>(Q,K,V,ctx);
  n_gemm<<<dim3(1024/32,64),tb,0,stream>>>(ctx,wo,a,2048,1024,4096,4096);
  n_gemm<<<dim3(1024/32,64),tb,0,stream>>>(h,wd,b,2048,1024,3584,3584);
  n_mix<<<2048*1024/256,256,0,stream>>>(a,b,g,(u16*)d_out);
}
```

```cpp
#include <hip/hip_runtime.h>
#include <hip/hip_bf16.h>
struct MArgs { void* ws; const void* x; const void* ckv; const void* wq; const void* wuk; const void* wuv; const void* wo; const void* wg; const void* wu; const void* wd; const void* gw; void* out; };
__device__ __attribute__((aligned(256))) const unsigned mtab[1*256*64 + 4*64*8] = {
1,0,0,0,0,0,0,94437376,4,3670016,0,65011712,14336,0,7,94531584,6,65472,0,0,0,0,0,0,0,0,0,0,0,0,0,0,0,0,0,0,0,0,0,0,0,0,0,0,0,0,0,0,0,0,0,0,0,0,0,0,0,0,0,0,0,0,0,0,
1,0,1024,4325376,0,0,0,94441472,4,3670016,1024,65273856,14336,4325376,7,94532096,6,65216,0,0,0,0,0,0,0,0,0,0,0,0,0,0,0,0,0,0,0,0,0,0,0,0,0,0,0,0,0,0,0,0,0,0,0,0,0,0,0,0,0,0,0,0,0,0,
1,0,2048,8650752,0,0,0,94445568,4,3670016,2048,65536000,14336,8650752,7,94532608,6,67008,128,0,0,0,0,0,0,0,0,0,0,0,0,0,0,0,0,0,0,0,0,0,0,0,0,0,0,0,0,0,0,0,0,0,0,0,0,0,0,0,0,0,0,0,0,0,
1,0,3072,12976128,0,0,0,94449664,4,3670016,3072,65798144,14336,12976128,7,94533120,6,66752,128,0,0,0,0,0,0,0,0,0,0,0,0,0,0,0,0,0,0,0,0,0,0,0,0,0,0,0,0,0,0,0,0,0,0,0,0,0,0,0,0,0,0,0,0,0,
1,0,4096,17301504,0,0,0,94453760,4,3670016,4096,66060288,14336,17301504,7,94533632,6,68544,256,0,0,0,0,0,0,0,0,0,0,0,0,0,0,0,0,0,0,0,0,0,0,0,0,0,0,0,0,0,0,0,0,0,0,0,0,0,0,0,0,0,0,0,0,0,
1,0,5120,21626880,0,0,0,94457856,4,3670016,5120,66322432,14336,21626880,7,94534144,6,68288,256,0,0,0,0,0,0,0,0,0,0,0,0,0,0,0,0,0,0,0,0,0,0,0,0,0,0,0,0,0,0,0,0,0,0,0,0,0,0,0,0,0,0,0,0,0,
1,0,6144,25952256,0,0,0,94461952,4,3670016,6144,66584576,14336,25952256,7,94534656,6,70080,384,0,0,0,0,0,0,0,0,0,0,0,0,0,0,0,0,0,0,0,0,0,0,0,0,0,0,0,0,0,0,0,0,0,0,0,0,0,0,0,0,0,0,0,0,0,
1,0,7168,30277632,0,0,0,94466048,4,3670016,7168,66846720,14336,30277632,7,94535168,6,69824,384,0,0,0,0,0,0,0,0,0,0,0,0,0,0,0,0,0,0,0,0,0,0,0,0,0,0,0,0,0,0,0,0,0,0,0,0,0,0,0,0,0,0,0,0,0,
1,0,512,2162688,0,0,0,94439424,4,3670016,256,65077248,14336,1081344,7,94531712,6,63424,0,0,0,0,0,0,0,0,0,0,0,0,0,0,0,0,0,0,0,0,0,0,0,0,0,0,0,0,0,0,0,0,0,0,0,0,0,0,0,0,0,0,0,0,0,0,
1,0,1536,6488064,0,0,0,94443520,4,3670016,1280,65339392,14336,5406720,7,94532224,6,63168,0,0,0,0,0,0,0,0,0,0,0,0,0,0,0,0,0,0,0,0,0,0,0,0,0,0,0,0,0,0,0,0,0,0,0,0,0,0,0,0,0,0,0,0,0,0,
1,0,2560,10813440,0,0,0,94447616,4,3670016,2304,65601536,14336,9732096,7,94532736,6,64960,128,0,0,0,0,0,0,0,0,0,0,0,0,0,0,0,0,0,0,0,0,0,0,0,0,0,0,0,0,0,0,0,0,0,0,0,0,0,0,0,0,0,0,0,0,0,
1,0,3584,15138816,0,0,0,94451712,4,3670016,3328,65863680,14336,14057472,7,94533248,6,64704,128,0,0,0,0,0,0,0,0,0,0,0,0,0,0,0,0,0,0,0,0,0,0,0,0,0,0,0,0,0,0,0,0,0,0,0,0,0,0,0,0,0,0,0,0,0,
1,0,4608,19464192,0,0,0,94455808,4,3670016,4352,66125824,14336,18382848,7,94533760,6,66496,256,0,0,0,0,0,0,0,0,0,0,0,0,0,0,0,0,0,0,0,0,0,0,0,0,0,0,0,0,0,0,0,0,0,0,0,0,0,0,0,0,0,0,0,0,0,
1,0,5632,23789568,0,0,0,94459904,4,3670016,5376,66387968,14336,22708224,7,94534272,6,66240,256,0,0,0,0,0,0,0,0,0,0,0,0,0,0,0,0,0,0,0,0,0,0,0,0,0,0,0,0,0,0,0,0,0,0,0,0,0,0,0,0,0,0,0,0,0,
1,0,6656,28114944,0,0,0,94464000,4,3670016,6400,66650112,14336,27033600,7,94534784,6,68032,384,0,0,0,0,0,0,0,0,0,0,0,0,0,0,0,0,0,0,0,0,0,0,0,0,0,0,0,0,0,0,0,0,0,0,0,0,0,0,0,0,0,0,0,0,0,
1,0,7680,32440320,0,0,0,94468096,4,3670016,7424,66912256,14336,31358976,7,94535296,6,67776,384,0,0,0,0,0,0,0,0,0,0,0,0,0,0,0,0,0,0,0,0,0,0,0,0,0,0,0,0,0,0,0,0,0,0,0,0,0,0,0,0,0,0,0,0,0,
2,0,0,0,0,0,0,94438400,4,3670016,512,65142784,14336,2162688,7,94531840,6,61376,0,0,0,0,0,0,0,0,0,0,0,0,0,0,0,0,0,0,0,0,0,0,0,0,0,0,0,0,0,0,0,0,0,0,0,0,0,0,0,0,0,0,0,0,0,0,
2,0,1024,4325376,0,0,0,94442496,4,3670016,1536,65404928,14336,6488064,7,94532352,6,61120,0,0,0,0,0,0,0,0,0,0,0,0,0,0,0,0,0,0,0,0,0,0,0,0,0,0,0,0,0,0,0,0,0,0,0,0,0,0,0,0,0,0,0,0,0,0,
2,0,2048,8650752,0,0,0,94446592,4,3670016,2560,65667072,14336,10813440,7,94532864,6,62912,128,0,0,0,0,0,0,0,0,0,0,0,0,0,0,0,0,0,0,0,0,0,0,0,0,0,0,0,0,0,0,0,0,0,0,0,0,0,0,0,0,0,0,0,0,0,
2,0,3072,12976128,0,0,0,94450688,4,3670016,3584,65929216,14336,15138816,7,94533376,6,62656,128,0,0,0,0,0,0,0,0,0,0,0,0,0,0,0,0,0,0,0,0,0,0,0,0,0,0,0,0,0,0,0,0,0,0,0,0,0,0,0,0,0,0,0,0,0,
2,0,4096,17301504,0,0,0,94454784,4,3670016,4608,66191360,14336,19464192,7,94533888,6,64448,256,0,0,0,0,0,0,0,0,0,0,0,0,0,0,0,0,0,0,0,0,0,0,0,0,0,0,0,0,0,0,0,0,0,0,0,0,0,0,0,0,0,0,0,0,0,
2,0,5120,21626880,0,0,0,94458880,4,3670016,5632,66453504,14336,23789568,7,94534400,6,64192,256,0,0,0,0,0,0,0,0,0,0,0,0,0,0,0,0,0,0,0,0,0,0,0,0,0,0,0,0,0,0,0,0,0,0,0,0,0,0,0,0,0,0,0,0,0,
2,0,6144,25952256,0,0,0,94462976,4,3670016,6656,66715648,14336,28114944,7,94534912,6,65984,384,0,0,0,0,0,0,0,0,0,0,0,0,0,0,0,0,0,0,0,0,0,0,0,0,0,0,0,0,0,0,0,0,0,0,0,0,0,0,0,0,0,0,0,0,0,
2,0,7168,30277632,0,0,0,94467072,4,3670016,7680,66977792,14336,32440320,7,94535424,6,65728,384,0,0,0,0,0,0,0,0,0,0,0,0,0,0,0,0,0,0,0,0,0,0,0,0,0,0,0,0,0,0,0,0,0,0,0,0,0,0,0,0,0,0,0,0,0,
2,0,512,2162688,0,0,0,94440448,4,3670016,768,65208320,14336,3244032,7,94531968,6,59328,0,0,0,0,0,0,0,0,0,0,0,0,0,0,0,0,0,0,0,0,0,0,0,0,0,0,0,0,0,0,0,0,0,0,0,0,0,0,0,0,0,0,0,0,0,0,
2,0,1536,6488064,0,0,0,94444544,4,3670016,1792,65470464,14336,7569408,7,94532480,6,59072,0,0,0,0,0,0,0,0,0,0,0,0,0,0,0,0,0,0,0,0,0,0,0,0,0,0,0,0,0,0,0,0,0,0,0,0,0,0,0,0,0,0,0,0,0,0,
2,0,2560,10813440,0,0,0,94448640,4,3670016,2816,65732608,14336,11894784,7,94532992,6,60864,128,0,0,0,0,0,0,0,0,0,0,0,0,0,0,0,0,0,0,0,0,0,0,0,0,0,0,0,0,0,0,0,0,0,0,0,0,0,0,0,0,0,0,0,0,0,
2,0,3584,15138816,0,0,0,94452736,4,3670016,3840,65994752,14336,16220160,7,94533504,6,60608,128,0,0,0,0,0,0,0,0,0,0,0,0,0,0,0,0,0,0,0,0,0,0,0,0,0,0,0,0,0,0,0,0,0,0,0,0,0,0,0,0,0,0,0,0,0,
2,0,4608,19464192,0,0,0,94456832,4,3670016,4864,66256896,14336,20545536,7,94534016,6,62400,256,0,0,0,0,0,0,0,0,0,0,0,0,0,0,0,0,0,0,0,0,0,0,0,0,0,0,0,0,0,0,0,0,0,0,0,0,0,0,0,0,0,0,0,0,0,
2,0,5632,23789568,0,0,0,94460928,4,3670016,5888,66519040,14336,24870912,7,94534528,6,62144,256,0,0,0,0,0,0,0,0,0,0,0,0,0,0,0,0,0,0,0,0,0,0,0,0,0,0,0,0,0,0,0,0,0,0,0,0,0,0,0,0,0,0,0,0,0,
2,0,6656,28114944,0,0,0,94465024,4,3670016,6912,66781184,14336,29196288,7,94535040,6,63936,384,0,0,0,0,0,0,0,0,0,0,0,0,0,0,0,0,0,0,0,0,0,0,0,0,0,0,0,0,0,0,0,0,0,0,0,0,0,0,0,0,0,0,0,0,0,
2,0,7680,32440320,0,0,0,94469120,4,3670016,7936,67043328,14336,33521664,7,94535552,6,63680,384,0,0,0,0,0,0,0,0,0,0,0,0,0,0,0,0,0,0,0,0,0,0,0,0,0,0,0,0,0,0,0,0,0,0,0,0,0,0,0,0,0,0,0,0,0,
3,2097152,0,51380224,8192,0,0,94486528,4,3145728,0,61079552,12288,0,6,94527488,6,57280,0,0,0,0,0,0,0,0,0,0,0,0,0,0,0,0,0,0,0,0,0,0,0,0,0,0,0,0,0,0,0,0,0,0,0,0,0,0,0,0,0,0,0,0,0,0,
3,2621440,0,55312384,10240,1,0,94490624,4,3145728,1024,61341696,12288,4325376,6,94528000,6,57024,0,0,0,0,0,0,0,0,0,0,0,0,0,0,0,0,0,0,0,0,0,0,0,0,0,0,0,0,0,0,0,0,0,0,0,0,0,0,0,0,0,0,0,0,0,0,
3,2097152,1792,51838976,8192,2,0,94487424,4,3145728,2048,61603840,12288,8650752,6,94528512,6,58816,128,0,0,0,0,0,0,0,0,0,0,0,0,0,0,0,0,0,0,0,0,0,0,0,0,0,0,0,0,0,0,0,0,0,0,0,0,0,0,0,0,0,0,0,0,0,
3,2621440,1792,55771136,10240,3,0,94491520,4,3145728,3072,61865984,12288,12976128,6,94529024,6,58560,128,0,0,0,0,0,0,0,0,0,0,0,0,0,0,0,0,0,0,0,0,0,0,0,0,0,0,0,0,0,0,0,0,0,0,0,0,0,0,0,0,0,0,0,0,0,
3,2097152,3584,52297728,8192,4,0,94488320,4,3145728,4096,62128128,12288,17301504,6,94529536,6,60352,256,0,0,0,0,0,0,0,0,0,0,0,0,0,0,0,0,0,0,0,0,0,0,0,0,0,0,0,0,0,0,0,0,0,0,0,0,0,0,0,0,0,0,0,0,0,
3,2621440,3584,56229888,10240,5,0,94492416,4,3145728,5120,62390272,12288,21626880,6,94530048,6,60096,256,0,0,0,0,0,0,0,0,0,0,0,0,0,0,0,0,0,0,0,0,0,0,0,0,0,0,0,0,0,0,0,0,0,0,0,0,0,0,0,0,0,0,0,0,0,
3,2097152,5376,52756480,8192,6,0,94489216,4,3145728,6144,62652416,12288,25952256,6,94530560,6,61888,384,0,0,0,0,0,0,0,0,0,0,0,0,0,0,0,0,0,0,0,0,0,0,0,0,0,0,0,0,0,0,0,0,0,0,0,0,0,0,0,0,0,0,0,0,0,
3,2621440,5376,56688640,10240,7,0,94493312,4,3145728,7168,62914560,12288,30277632,6,94531072,6,61632,384,0,0,0,0,0,0,0,0,0,0,0,0,0,0,0,0,0,0,0,0,0,0,0,0,0,0,0,0,0,0,0,0,0,0,0,0,0,0,0,0,0,0,0,0,0,
3,3145728,0,59244544,12288,8,0,94494720,4,3145728,256,61145088,12288,1081344,6,94527616,6,55232,0,0,0,0,0,0,0,0,0,0,0,0,0,0,0,0,0,0,0,0,0,0,0,0,0,0,0,0,0,0,0,0,0,0,0,0,0,0,0,0,0,0,0,0,0,0,
3,3670016,0,63176704,14336,9,0,94498816,4,3145728,1280,61407232,12288,5406720,6,94528128,6,54976,0,0,0,0,0,0,0,0,0,0,0,0,0,0,0,0,0,0,0,0,0,0,0,0,0,0,0,0,0,0,0,0,0,0,0,0,0,0,0,0,0,0,0,0,0,0,
3,3145728,1792,59703296,12288,10,0,94495616,4,3145728,2304,61669376,12288,9732096,6,94528640,6,56768,128,0,0,0,0,0,0,0,0,0,0,0,0,0,0,0,0,0,0,0,0,0,0,0,0,0,0,0,0,0,0,0,0,0,0,0,0,0,0,0,0,0,0,0,0,0,
3,3670016,1792,63635456,14336,11,0,94499712,4,3145728,3328,61931520,12288,14057472,6,94529152,6,56512,128,0,0,0,0,0,0,0,0,0,0,0,0,0,0,0,0,0,0,0,0,0,0,0,0,0,0,0,0,0,0,0,0,0,0,0,0,0,0,0,0,0,0,0,0,0,
3,3145728,3584,60162048,12288,12,0,94496512,4,3145728,4352,62193664,12288,18382848,6,94529664,6,58304,256,0,0,0,0,0,0,0,0,0,0,0,0,0,0,0,0,0,0,0,0,0,0,0,0,0,0,0,0,0,0,0,0,0,0,0,0,0,0,0,0,0,0,0,0,0,
3,3670016,3584,64094208,14336,13,0,94500608,4,3145728,5376,62455808,12288,22708224,6,94530176,6,58048,256,0,0,0,0,0,0,0,0,0,0,0,0,0,0,0,0,0,0,0,0,0,0,0,0,0,0,0,0,0,0,0,0,0,0,0,0,0,0,0,0,0,0,0,0,0,
3,3145728,5376,60620800,12288,14,0,94497408,4,3145728,6400,62717952,12288,27033600,6,94530688,6,59840,384,0,0,0,0,0,0,0,0,0,0,0,0,0,0,0,0,0,0,0,0,0,0,0,0,0,0,0,0,0,0,0,0,0,0,0,0,0,0,0,0,0,0,0,0,0,
3,3670016,5376,64552960,14336,15,0,94501504,4,3145728,7424,62980096,12288,31358976,6,94531200,6,59584,384,0,0,0,0,0,0,0,0,0,0,0,0,0,0,0,0,0,0,0,0,0,0,0,0,0,0,0,0,0,0,0,0,0,0,0,0,0,0,0,0,0,0,0,0,0,
3,524288,0,39583744,2048,16,0,94474240,4,3145728,512,61210624,12288,2162688,6,94527744,6,53184,0,0,0,0,0,0,0,0,0,0,0,0,0,0,0,0,0,0,0,0,0,0,0,0,0,0,0,0,0,0,0,0,0,0,0,0,0,0,0,0,0,0,0,0,0,0,
3,1048576,0,43515904,4096,17,0,94478336,4,3145728,1536,61472768,12288,6488064,6,94528256,6,52928,0,0,0,0,0,0,0,0,0,0,0,0,0,0,0,0,0,0,0,0,0,0,0,0,0,0,0,0,0,0,0,0,0,0,0,0,0,0,0,0,0,0,0,0,0,0,
3,524288,1792,40042496,2048,18,0,94475136,4,3145728,2560,61734912,12288,10813440,6,94528768,6,54720,128,0,0,0,0,0,0,0,0,0,0,0,0,0,0,0,0,0,0,0,0,0,0,0,0,0,0,0,0,0,0,0,0,0,0,0,0,0,0,0,0,0,0,0,0,0,
3,1048576,1792,43974656,4096,19,0,94479232,4,3145728,3584,61997056,12288,15138816,6,94529280,6,54464,128,0,0,0,0,0,0,0,0,0,0,0,0,0,0,0,0,0,0,0,0,0,0,0,0,0,0,0,0,0,0,0,0,0,0,0,0,0,0,0,0,0,0,0,0,0,
3,524288,3584,40501248,2048,20,0,94476032,4,3145728,4608,62259200,12288,19464192,6,94529792,6,56256,256,0,0,0,0,0,0,0,0,0,0,0,0,0,0,0,0,0,0,0,0,0,0,0,0,0,0,0,0,0,0,0,0,0,0,0,0,0,0,0,0,0,0,0,0,0,
3,1048576,3584,44433408,4096,21,0,94480128,4,3145728,5632,62521344,12288,23789568,6,94530304,6,56000,256,0,0,0,0,0,0,0,0,0,0,0,0,0,0,0,0,0,0,0,0,0,0,0,0,0,0,0,0,0,0,0,0,0,0,0,0,0,0,0,0,0,0,0,0,0,
3,524288,5376,40960000,2048,22,0,94476928,4,3145728,6656,62783488,12288,28114944,6,94530816,6,57792,384,0,0,0,0,0,0,0,0,0,0,0,0,0,0,0,0,0,0,0,0,0,0,0,0,0,0,0,0,0,0,0,0,0,0,0,0,0,0,0,0,0,0,0,0,0,
3,1048576,5376,44892160,4096,23,0,94481024,4,3145728,7680,63045632,12288,32440320,6,94531328,6,57536,384,0,0,0,0,0,0,0,0,0,0,0,0,0,0,0,0,0,0,0,0,0,0,0,0,0,0,0,0,0,0,0,0,0,0,0,0,0,0,0,0,0,0,0,0,0,
3,1572864,0,47448064,6144,24,0,94482432,4,3145728,768,61276160,12288,3244032,6,94527872,6,51136,0,0,0,0,0,0,0,0,0,0,0,0,0,0,0,0,0,0,0,0,0,0,0,0,0,0,0,0,0,0,0,0,0,0,0,0,0,0,0,0,0,0,0,0,0,0,
3,0,0,35651584,0,25,0,94470144,4,3145728,1792,61538304,12288,7569408,6,94528384,6,50880,0,0,0,0,0,0,0,0,0,0,0,0,0,0,0,0,0,0,0,0,0,0,0,0,0,0,0,0,0,0,0,0,0,0,0,0,0,0,0,0,0,0,0,0,0,0,
3,1572864,1792,47906816,6144,26,0,94483328,4,3145728,2816,61800448,12288,11894784,6,94528896,6,52672,128,0,0,0,0,0,0,0,0,0,0,0,0,0,0,0,0,0,0,0,0,0,0,0,0,0,0,0,0,0,0,0,0,0,0,0,0,0,0,0,0,0,0,0,0,0,
3,0,1792,36110336,0,27,0,94471040,4,3145728,3840,62062592,12288,16220160,6,94529408,6,52416,128,0,0,0,0,0,0,0,0,0,0,0,0,0,0,0,0,0,0,0,0,0,0,0,0,0,0,0,0,0,0,0,0,0,0,0,0,0,0,0,0,0,0,0,0,0,
3,1572864,3584,48365568,6144,28,0,94484224,4,3145728,4864,62324736,12288,20545536,6,94529920,6,54208,256,0,0,0,0,0,0,0,0,0,0,0,0,0,0,0,0,0,0,0,0,0,0,0,0,0,0,0,0,0,0,0,0,0,0,0,0,0,0,0,0,0,0,0,0,0,
3,0,3584,36569088,0,29,0,94471936,4,3145728,5888,62586880,12288,24870912,6,94530432,6,53952,256,0,0,0,0,0,0,0,0,0,0,0,0,0,0,0,0,0,0,0,0,0,0,0,0,0,0,0,0,0,0,0,0,0,0,0,0,0,0,0,0,0,0,0,0,0,
3,1572864,5376,48824320,6144,30,0,94485120,4,3145728,6912,62849024,12288,29196288,6,94530944,6,55744,384,0,0,0,0,0,0,0,0,0,0,0,0,0,0,0,0,0,0,0,0,0,0,0,0,0,0,0,0,0,0,0,0,0,0,0,0,0,0,0,0,0,0,0,0,0,
3,0,5376,37027840,0,31,0,94472832,4,3145728,7936,63111168,12288,33521664,6,94531456,6,55488,384,0,0,0,0,0,0,0,0,0,0,0,0,0,0,0,0,0,0,0,0,0,0,0,0,0,0,0,0,0,0,0,0,0,0,0,0,0,0,0,0,0,0,0,0,0,
1,262144,0,131072,0,0,0,94437504,1,1048576,0,524288,0,0,0,94437888,4,2621440,0,57147392,10240,0,5,94523392,6,49056,0,0,0,0,0,0,0,0,0,0,0,0,0,0,0,0,0,0,0,0,0,0,0,0,0,0,0,0,0,0,0,0,0,0,0,0,0,0,
1,262144,1024,4456448,0,0,0,94441600,1,1048576,1024,4849664,0,0,0,94441984,4,2621440,1024,57409536,10240,4325376,5,94523904,6,48800,0,0,0,0,0,0,0,0,0,0,0,0,0,0,0,0,0,0,0,0,0,0,0,0,0,0,0,0,0,0,0,0,0,0,0,0,0,0,
1,262144,2048,8781824,0,0,0,94445696,1,1048576,2048,9175040,0,0,0,94446080,4,2621440,2048,57671680,10240,8650752,5,94524416,6,50592,128,0,0,0,0,0,0,0,0,0,0,0,0,0,0,0,0,0,0,0,0,0,0,0,0,0,0,0,0,0,0,0,0,0,0,0,0,0,
1,262144,3072,13107200,0,0,0,94449792,1,1048576,3072,13500416,0,0,0,94450176,4,2621440,3072,57933824,10240,12976128,5,94524928,6,50336,128,0,0,0,0,0,0,0,0,0,0,0,0,0,0,0,0,0,0,0,0,0,0,0,0,0,0,0,0,0,0,0,0,0,0,0,0,0,
1,262144,4096,17432576,0,0,0,94453888,1,1048576,4096,17825792,0,0,0,94454272,4,2621440,4096,58195968,10240,17301504,5,94525440,6,52128,256,0,0,0,0,0,0,0,0,0,0,0,0,0,0,0,0,0,0,0,0,0,0,0,0,0,0,0,0,0,0,0,0,0,0,0,0,0,
1,262144,5120,21757952,0,0,0,94457984,1,1048576,5120,22151168,0,0,0,94458368,4,2621440,5120,58458112,10240,21626880,5,94525952,6,51872,256,0,0,0,0,0,0,0,0,0,0,0,0,0,0,0,0,0,0,0,0,0,0,0,0,0,0,0,0,0,0,0,0,0,0,0,0,0,
1,262144,6144,26083328,0,0,0,94462080,1,1048576,6144,26476544,0,0,0,94462464,4,2621440,6144,58720256,10240,25952256,5,94526464,6,53664,384,0,0,0,0,0,0,0,0,0,0,0,0,0,0,0,0,0,0,0,0,0,0,0,0,0,0,0,0,0,0,0,0,0,0,0,0,0,
1,262144,7168,30408704,0,0,0,94466176,1,1048576,7168,30801920,0,0,0,94466560,4,2621440,7168,58982400,10240,30277632,5,94526976,6,53408,384,0,0,0,0,0,0,0,0,0,0,0,0,0,0,0,0,0,0,0,0,0,0,0,0,0,0,0,0,0,0,0,0,0,0,0,0,0,
1,262144,512,2293760,0,0,0,94439552,1,1048576,512,2686976,0,0,0,94439936,4,2621440,256,57212928,10240,1081344,5,94523520,6,47008,0,0,0,0,0,0,0,0,0,0,0,0,0,0,0,0,0,0,0,0,0,0,0,0,0,0,0,0,0,0,0,0,0,0,0,0,0,0,
1,262144,1536,6619136,0,0,0,94443648,1,1048576,1536,7012352,0,0,0,94444032,4,2621440,1280,57475072,10240,5406720,5,94524032,6,46752,0,0,0,0,0,0,0,0,0,0,0,0,0,0,0,0,0,0,0,0,0,0,0,0,0,0,0,0,0,0,0,0,0,0,0,0,0,0,
1,262144,2560,10944512,0,0,0,94447744,1,1048576,2560,11337728,0,0,0,94448128,4,2621440,2304,57737216,10240,9732096,5,94524544,6,48544,128,0,0,0,0,0,0,0,0,0,0,0,0,0,0,0,0,0,0,0,0,0,0,0,0,0,0,0,0,0,0,0,0,0,0,0,0,0,
1,262144,3584,15269888,0,0,0,94451840,1,1048576,3584,15663104,0,0,0,94452224,4,2621440,3328,57999360,10240,14057472,5,94525056,6,48288,128,0,0,0,0,0,0,0,0,0,0,0,0,0,0,0,0,0,0,0,0,0,0,0,0,0,0,0,0,0,0,0,0,0,0,0,0,0,
1,262144,4608,19595264,0,0,0,94455936,1,1048576,4608,19988480,0,0,0,94456320,4,2621440,4352,58261504,10240,18382848,5,94525568,6,50080,256,0,0,0,0,0,0,0,0,0,0,0,0,0,0,0,0,0,0,0,0,0,0,0,0,0,0,0,0,0,0,0,0,0,0,0,0,0,
1,262144,5632,23920640,0,0,0,94460032,1,1048576,5632,24313856,0,0,0,94460416,4,2621440,5376,58523648,10240,22708224,5,94526080,6,49824,256,0,0,0,0,0,0,0,0,0,0,0,0,0,0,0,0,0,0,0,0,0,0,0,0,0,0,0,0,0,0,0,0,0,0,0,0,0,
1,262144,6656,28246016,0,0,0,94464128,1,1048576,6656,28639232,0,0,0,94464512,4,2621440,6400,58785792,10240,27033600,5,94526592,6,51616,384,0,0,0,0,0,0,0,0,0,0,0,0,0,0,0,0,0,0,0,0,0,0,0,0,0,0,0,0,0,0,0,0,0,0,0,0,0,
1,262144,7680,32571392,0,0,0,94468224,1,1048576,7680,32964608,0,0,0,94468608,4,2621440,7424,59047936,10240,31358976,5,94527104,6,51360,384,0,0,0,0,0,0,0,0,0,0,0,0,0,0,0,0,0,0,0,0,0,0,0,0,0,0,0,0,0,0,0,0,0,0,0,0,0,
2,262144,0,131072,0,0,0,94438528,2,1048576,0,524288,0,0,0,94438912,4,2621440,512,57278464,10240,2162688,5,94523648,6,44960,0,0,0,0,0,0,0,0,0,0,0,0,0,0,0,0,0,0,0,0,0,0,0,0,0,0,0,0,0,0,0,0,0,0,0,0,0,0,
2,262144,1024,4456448,0,0,0,94442624,2,1048576,1024,4849664,0,0,0,94443008,4,2621440,1536,57540608,10240,6488064,5,94524160,6,44704,0,0,0,0,0,0,0,0,0,0,0,0,0,0,0,0,0,0,0,0,0,0,0,0,0,0,0,0,0,0,0,0,0,0,0,0,0,0,
2,262144,2048,8781824,0,0,0,94446720,2,1048576,2048,9175040,0,0,0,94447104,4,2621440,2560,57802752,10240,10813440,5,94524672,6,46496,128,0,0,0,0,0,0,0,0,0,0,0,0,0,0,0,0,0,0,0,0,0,0,0,0,0,0,0,0,0,0,0,0,0,0,0,0,0,
2,262144,3072,13107200,0,0,0,94450816,2,1048576,3072,13500416,0,0,0,94451200,4,2621440,3584,58064896,10240,15138816,5,94525184,6,46240,128,0,0,0,0,0,0,0,0,0,0,0,0,0,0,0,0,0,0,0,0,0,0,0,0,0,0,0,0,0,0,0,0,0,0,0,0,0,
2,262144,4096,17432576,0,0,0,94454912,2,1048576,4096,17825792,0,0,0,94455296,4,2621440,4608,58327040,10240,19464192,5,94525696,6,48032,256,0,0,0,0,0,0,0,0,0,0,0,0,0,0,0,0,0,0,0,0,0,0,0,0,0,0,0,0,0,0,0,0,0,0,0,0,0,
2,262144,5120,21757952,0,0,0,94459008,2,1048576,5120,22151168,0,0,0,94459392,4,2621440,5632,58589184,10240,23789568,5,94526208,6,47776,256,0,0,0,0,0,0,0,0,0,0,0,0,0,0,0,0,0,0,0,0,0,0,0,0,0,0,0,0,0,0,0,0,0,0,0,0,0,
2,262144,6144,26083328,0,0,0,94463104,2,1048576,6144,26476544,0,0,0,94463488,4,2621440,6656,58851328,10240,28114944,5,94526720,6,49568,384,0,0,0,0,0,0,0,0,0,0,0,0,0,0,0,0,0,0,0,0,0,0,0,0,0,0,0,0,0,0,0,0,0,0,0,0,0,
2,262144,7168,30408704,0,0,0,94467200,2,1048576,7168,30801920,0,0,0,94467584,4,2621440,7680,59113472,10240,32440320,5,94527232,6,49312,384,0,0,0,0,0,0,0,0,0,0,0,0,0,0,0,0,0,0,0,0,0,0,0,0,0,0,0,0,0,0,0,0,0,0,0,0,0,
2,262144,512,2293760,0,0,0,94440576,2,1048576,512,2686976,0,0,0,94440960,4,2621440,768,57344000,10240,3244032,5,94523776,6,42912,0,0,0,0,0,0,0,0,0,0,0,0,0,0,0,0,0,0,0,0,0,0,0,0,0,0,0,0,0,0,0,0,0,0,0,0,0,0,
2,262144,1536,6619136,0,0,0,94444672,2,1048576,1536,7012352,0,0,0,94445056,4,2621440,1792,57606144,10240,7569408,5,94524288,6,42656,0,0,0,0,0,0,0,0,0,0,0,0,0,0,0,0,0,0,0,0,0,0,0,0,0,0,0,0,0,0,0,0,0,0,0,0,0,0,
2,262144,2560,10944512,0,0,0,94448768,2,1048576,2560,11337728,0,0,0,94449152,4,2621440,2816,57868288,10240,11894784,5,94524800,6,44448,128,0,0,0,0,0,0,0,0,0,0,0,0,0,0,0,0,0,0,0,0,0,0,0,0,0,0,0,0,0,0,0,0,0,0,0,0,0,
2,262144,3584,15269888,0,0,0,94452864,2,1048576,3584,15663104,0,0,0,94453248,4,2621440,3840,58130432,10240,16220160,5,94525312,6,44192,128,0,0,0,0,0,0,0,0,0,0,0,0,0,0,0,0,0,0,0,0,0,0,0,0,0,0,0,0,0,0,0,0,0,0,0,0,0,
2,262144,4608,19595264,0,0,0,94456960,2,1048576,4608,19988480,0,0,0,94457344,4,2621440,4864,58392576,10240,20545536,5,94525824,6,45984,256,0,0,0,0,0,0,0,0,0,0,0,0,0,0,0,0,0,0,0,0,0,0,0,0,0,0,0,0,0,0,0,0,0,0,0,0,0,
2,262144,5632,23920640,0,0,0,94461056,2,1048576,5632,24313856,0,0,0,94461440,4,2621440,5888,58654720,10240,24870912,5,94526336,6,45728,256,0,0,0,0,0,0,0,0,0,0,0,0,0,0,0,0,0,0,0,0,0,0,0,0,0,0,0,0,0,0,0,0,0,0,0,0,0,
2,262144,6656,28246016,0,0,0,94465152,2,1048576,6656,28639232,0,0,0,94465536,4,2621440,6912,58916864,10240,29196288,5,94526848,6,47520,384,0,0,0,0,0,0,0,0,0,0,0,0,0,0,0,0,0,0,0,0,0,0,0,0,0,0,0,0,0,0,0,0,0,0,0,0,0,
2,262144,7680,32571392,0,0,0,94469248,2,1048576,7680,32964608,0,0,0,94469632,4,2621440,7936,59179008,10240,33521664,5,94527360,6,47264,384,0,0,0,0,0,0,0,0,0,0,0,0,0,0,0,0,0,0,0,0,0,0,0,0,0,0,0,0,0,0,0,0,0,0,0,0,0,
1,524288,0,262144,0,0,0,94437632,3,2097152,1024,51642368,8192,0,0,94487040,4,1048576,0,45350912,4096,0,2,94511104,6,40864,0,0,0,0,0,0,0,0,0,0,0,0,0,0,0,0,0,0,0,0,0,0,0,0,0,0,0,0,0,0,0,0,0,0,0,0,0,0,
1,524288,1024,4587520,0,0,0,94441728,3,2621440,1024,55574528,10240,0,0,94491136,4,1048576,1024,45613056,4096,4325376,2,94511616,6,40608,0,0,0,0,0,0,0,0,0,0,0,0,0,0,0,0,0,0,0,0,0,0,0,0,0,0,0,0,0,0,0,0,0,0,0,0,0,0,
1,524288,2048,8912896,0,0,0,94445824,3,2097152,2816,52101120,8192,0,0,94487936,4,1048576,2048,45875200,4096,8650752,2,94512128,6,42400,128,0,0,0,0,0,0,0,0,0,0,0,0,0,0,0,0,0,0,0,0,0,0,0,0,0,0,0,0,0,0,0,0,0,0,0,0,0,
1,524288,3072,13238272,0,0,0,94449920,3,2621440,2816,56033280,10240,0,0,94492032,4,1048576,3072,46137344,4096,12976128,2,94512640,6,42144,128,0,0,0,0,0,0,0,0,0,0,0,0,0,0,0,0,0,0,0,0,0,0,0,0,0,0,0,0,0,0,0,0,0,0,0,0,0,
1,524288,4096,17563648,0,0,0,94454016,3,2097152,4608,52559872,8192,0,0,94488832,4,1048576,4096,46399488,4096,17301504,2,94513152,6,43936,256,0,0,0,0,0,0,0,0,0,0,0,0,0,0,0,0,0,0,0,0,0,0,0,0,0,0,0,0,0,0,0,0,0,0,0,0,0,
1,524288,5120,21889024,0,0,0,94458112,3,2621440,4608,56492032,10240,0,0,94492928,4,1048576,5120,46661632,4096,21626880,2,94513664,6,43680,256,0,0,0,0,0,0,0,0,0,0,0,0,0,0,0,0,0,0,0,0,0,0,0,0,0,0,0,0,0,0,0,0,0,0,0,0,0,
1,524288,6144,26214400,0,0,0,94462208,3,2097152,6400,53018624,8192,0,0,94489728,4,1048576,6144,46923776,4096,25952256,2,94514176,6,45472,384,0,0,0,0,0,0,0,0,0,0,0,0,0,0,0,0,0,0,0,0,0,0,0,0,0,0,0,0,0,0,0,0,0,0,0,0,0,
1,524288,7168,30539776,0,0,0,94466304,3,2621440,6400,56950784,10240,0,0,94493824,4,1048576,7168,47185920,4096,30277632,2,94514688,6,45216,384,0,0,0,0,0,0,0,0,0,0,0,0,0,0,0,0,0,0,0,0,0,0,0,0,0,0,0,0,0,0,0,0,0,0,0,0,0,
1,524288,512,2424832,0,0,0,94439680,3,3145728,1024,59506688,12288,0,0,94495232,4,1048576,256,45416448,4096,1081344,2,94511232,6,38816,0,0,0,0,0,0,0,0,0,0,0,0,0,0,0,0,0,0,0,0,0,0,0,0,0,0,0,0,0,0,0,0,0,0,0,0,0,0,
1,524288,1536,6750208,0,0,0,94443776,3,3670016,1024,63438848,14336,0,0,94499328,4,1048576,1280,45678592,4096,5406720,2,94511744,6,38560,0,0,0,0,0,0,0,0,0,0,0,0,0,0,0,0,0,0,0,0,0,0,0,0,0,0,0,0,0,0,0,0,0,0,0,0,0,0,
1,524288,2560,11075584,0,0,0,94447872,3,3145728,2816,59965440,12288,0,0,94496128,4,1048576,2304,45940736,4096,9732096,2,94512256,6,40352,128,0,0,0,0,0,0,0,0,0,0,0,0,0,0,0,0,0,0,0,0,0,0,0,0,0,0,0,0,0,0,0,0,0,0,0,0,0,
1,524288,3584,15400960,0,0,0,94451968,3,3670016,2816,63897600,14336,0,0,94500224,4,1048576,3328,46202880,4096,14057472,2,94512768,6,40096,128,0,0,0,0,0,0,0,0,0,0,0,0,0,0,0,0,0,0,0,0,0,0,0,0,0,0,0,0,0,0,0,0,0,0,0,0,0,
1,524288,4608,19726336,0,0,0,94456064,3,3145728,4608,60424192,12288,0,0,94497024,4,1048576,4352,46465024,4096,18382848,2,94513280,6,41888,256,0,0,0,0,0,0,0,0,0,0,0,0,0,0,0,0,0,0,0,0,0,0,0,0,0,0,0,0,0,0,0,0,0,0,0,0,0,
1,524288,5632,24051712,0,0,0,94460160,3,3670016,4608,64356352,14336,0,0,94501120,4,1048576,5376,46727168,4096,22708224,2,94513792,6,41632,256,0,0,0,0,0,0,0,0,0,0,0,0,0,0,0,0,0,0,0,0,0,0,0,0,0,0,0,0,0,0,0,0,0,0,0,0,0,
1,524288,6656,28377088,0,0,0,94464256,3,3145728,6400,60882944,12288,0,0,94497920,4,1048576,6400,46989312,4096,27033600,2,94514304,6,43424,384,0,0,0,0,0,0,0,0,0,0,0,0,0,0,0,0,0,0,0,0,0,0,0,0,0,0,0,0,0,0,0,0,0,0,0,0,0,
1,524288,7680,32702464,0,0,0,94468352,3,3670016,6400,64815104,14336,0,0,94502016,4,1048576,7424,47251456,4096,31358976,2,94514816,6,43168,384,0,0,0,0,0,0,0,0,0,0,0,0,0,0,0,0,0,0,0,0,0,0,0,0,0,0,0,0,0,0,0,0,0,0,0,0,0,
2,524288,0,262144,0,0,0,94438656,3,524288,1024,39845888,2048,0,0,94474752,4,1048576,512,45481984,4096,2162688,2,94511360,6,36768,0,0,0,0,0,0,0,0,0,0,0,0,0,0,0,0,0,0,0,0,0,0,0,0,0,0,0,0,0,0,0,0,0,0,0,0,0,0,
2,524288,1024,4587520,0,0,0,94442752,3,1048576,1024,43778048,4096,0,0,94478848,4,1048576,1536,45744128,4096,6488064,2,94511872,6,36512,0,0,0,0,0,0,0,0,0,0,0,0,0,0,0,0,0,0,0,0,0,0,0,0,0,0,0,0,0,0,0,0,0,0,0,0,0,0,
2,524288,2048,8912896,0,0,0,94446848,3,524288,2816,40304640,2048,0,0,94475648,4,1048576,2560,46006272,4096,10813440,2,94512384,6,38304,128,0,0,0,0,0,0,0,0,0,0,0,0,0,0,0,0,0,0,0,0,0,0,0,0,0,0,0,0,0,0,0,0,0,0,0,0,0,
2,524288,3072,13238272,0,0,0,94450944,3,1048576,2816,44236800,4096,0,0,94479744,4,1048576,3584,46268416,4096,15138816,2,94512896,6,38048,128,0,0,0,0,0,0,0,0,0,0,0,0,0,0,0,0,0,0,0,0,0,0,0,0,0,0,0,0,0,0,0,0,0,0,0,0,0,
2,524288,4096,17563648,0,0,0,94455040,3,524288,4608,40763392,2048,0,0,94476544,4,1048576,4608,46530560,4096,19464192,2,94513408,6,39840,256,0,0,0,0,0,0,0,0,0,0,0,0,0,0,0,0,0,0,0,0,0,0,0,0,0,0,0,0,0,0,0,0,0,0,0,0,0,
2,524288,5120,21889024,0,0,0,94459136,3,1048576,4608,44695552,4096,0,0,94480640,4,1048576,5632,46792704,4096,23789568,2,94513920,6,39584,256,0,0,0,0,0,0,0,0,0,0,0,0,0,0,0,0,0,0,0,0,0,0,0,0,0,0,0,0,0,0,0,0,0,0,0,0,0,
2,524288,6144,26214400,0,0,0,94463232,3,524288,6400,41222144,2048,0,0,94477440,4,1048576,6656,47054848,4096,28114944,2,94514432,6,41376,384,0,0,0,0,0,0,0,0,0,0,0,0,0,0,0,0,0,0,0,0,0,0,0,0,0,0,0,0,0,0,0,0,0,0,0,0,0,
2,524288,7168,30539776,0,0,0,94467328,3,1048576,6400,45154304,4096,0,0,94481536,4,1048576,7680,47316992,4096,32440320,2,94514944,6,41120,384,0,0,0,0,0,0,0,0,0,0,0,0,0,0,0,0,0,0,0,0,0,0,0,0,0,0,0,0,0,0,0,0,0,0,0,0,0,
2,524288,512,2424832,0,0,0,94440704,3,1572864,1024,47710208,6144,0,0,94482944,4,1048576,768,45547520,4096,3244032,2,94511488,6,34720,0,0,0,0,0,0,0,0,0,0,0,0,0,0,0,0,0,0,0,0,0,0,0,0,0,0,0,0,0,0,0,0,0,0,0,0,0,0,
2,524288,1536,6750208,0,0,0,94444800,3,0,1024,35913728,0,0,0,94470656,4,1048576,1792,45809664,4096,7569408,2,94512000,6,34464,0,0,0,0,0,0,0,0,0,0,0,0,0,0,0,0,0,0,0,0,0,0,0,0,0,0,0,0,0,0,0,0,0,0,0,0,0,0,
2,524288,2560,11075584,0,0,0,94448896,3,1572864,2816,48168960,6144,0,0,94483840,4,1048576,2816,46071808,4096,11894784,2,94512512,6,36256,128,0,0,0,0,0,0,0,0,0,0,0,0,0,0,0,0,0,0,0,0,0,0,0,0,0,0,0,0,0,0,0,0,0,0,0,0,0,
2,524288,3584,15400960,0,0,0,94452992,3,0,2816,36372480,0,0,0,94471552,4,1048576,3840,46333952,4096,16220160,2,94513024,6,36000,128,0,0,0,0,0,0,0,0,0,0,0,0,0,0,0,0,0,0,0,0,0,0,0,0,0,0,0,0,0,0,0,0,0,0,0,0,0,
2,524288,4608,19726336,0,0,0,94457088,3,1572864,4608,48627712,6144,0,0,94484736,4,1048576,4864,46596096,4096,20545536,2,94513536,6,37792,256,0,0,0,0,0,0,0,0,0,0,0,0,0,0,0,0,0,0,0,0,0,0,0,0,0,0,0,0,0,0,0,0,0,0,0,0,0,
2,524288,5632,24051712,0,0,0,94461184,3,0,4608,36831232,0,0,0,94472448,4,1048576,5888,46858240,4096,24870912,2,94514048,6,37536,256,0,0,0,0,0,0,0,0,0,0,0,0,0,0,0,0,0,0,0,0,0,0,0,0,0,0,0,0,0,0,0,0,0,0,0,0,0,
2,524288,6656,28377088,0,0,0,94465280,3,1572864,6400,49086464,6144,0,0,94485632,4,1048576,6912,47120384,4096,29196288,2,94514560,6,39328,384,0,0,0,0,0,0,0,0,0,0,0,0,0,0,0,0,0,0,0,0,0,0,0,0,0,0,0,0,0,0,0,0,0,0,0,0,0,
2,524288,7680,32702464,0,0,0,94469376,3,0,6400,37289984,0,0,0,94473344,4,1048576,7936,47382528,4096,33521664,2,94515072,6,39072,384,0,0,0,0,0,0,0,0,0,0,0,0,0,0,0,0,0,0,0,0,0,0,0,0,0,0,0,0,0,0,0,0,0,0,0,0,0,
3,2097152,256,51445760,8192,32,0,94486656,1,1572864,0,786432,0,0,0,94438144,4,1572864,0,49283072,6144,0,3,94515200,6,32672,0,0,0,0,0,0,0,0,0,0,0,0,0,0,0,0,0,0,0,0,0,0,0,0,0,0,0,0,0,0,0,0,0,0,0,0,0,0,
3,2621440,256,55377920,10240,33,0,94490752,1,1572864,1024,5111808,0,0,0,94442240,4,1572864,1024,49545216,6144,4325376,3,94515712,6,32416,0,0,0,0,0,0,0,0,0,0,0,0,0,0,0,0,0,0,0,0,0,0,0,0,0,0,0,0,0,0,0,0,0,0,0,0,0,0,
3,2097152,2048,51904512,8192,34,0,94487552,1,1572864,2048,9437184,0,0,0,94446336,4,1572864,2048,49807360,6144,8650752,3,94516224,6,34208,128,0,0,0,0,0,0,0,0,0,0,0,0,0,0,0,0,0,0,0,0,0,0,0,0,0,0,0,0,0,0,0,0,0,0,0,0,0,
3,2621440,2048,55836672,10240,35,0,94491648,1,1572864,3072,13762560,0,0,0,94450432,4,1572864,3072,50069504,6144,12976128,3,94516736,6,33952,128,0,0,0,0,0,0,0,0,0,0,0,0,0,0,0,0,0,0,0,0,0,0,0,0,0,0,0,0,0,0,0,0,0,0,0,0,0,
3,2097152,3840,52363264,8192,36,0,94488448,1,1572864,4096,18087936,0,0,0,94454528,4,1572864,4096,50331648,6144,17301504,3,94517248,6,35744,256,0,0,0,0,0,0,0,0,0,0,0,0,0,0,0,0,0,0,0,0,0,0,0,0,0,0,0,0,0,0,0,0,0,0,0,0,0,
3,2621440,3840,56295424,10240,37,0,94492544,1,1572864,5120,22413312,0,0,0,94458624,4,1572864,5120,50593792,6144,21626880,3,94517760,6,35488,256,0,0,0,0,0,0,0,0,0,0,0,0,0,0,0,0,0,0,0,0,0,0,0,0,0,0,0,0,0,0,0,0,0,0,0,0,0,
3,2097152,5632,52822016,8192,38,0,94489344,1,1572864,6144,26738688,0,0,0,94462720,4,1572864,6144,50855936,6144,25952256,3,94518272,6,37280,384,0,0,0,0,0,0,0,0,0,0,0,0,0,0,0,0,0,0,0,0,0,0,0,0,0,0,0,0,0,0,0,0,0,0,0,0,0,
3,2621440,5632,56754176,10240,39,0,94493440,1,1572864,7168,31064064,0,0,0,94466816,4,1572864,7168,51118080,6144,30277632,3,94518784,6,37024,384,0,0,0,0,0,0,0,0,0,0,0,0,0,0,0,0,0,0,0,0,0,0,0,0,0,0,0,0,0,0,0,0,0,0,0,0,0,
3,3145728,256,59310080,12288,40,0,94494848,1,1572864,512,2949120,0,0,0,94440192,4,1572864,256,49348608,6144,1081344,3,94515328,6,30624,0,0,0,0,0,0,0,0,0,0,0,0,0,0,0,0,0,0,0,0,0,0,0,0,0,0,0,0,0,0,0,0,0,0,0,0,0,0,
3,3670016,256,63242240,14336,41,0,94498944,1,1572864,1536,7274496,0,0,0,94444288,4,1572864,1280,49610752,6144,5406720,3,94515840,6,30368,0,0,0,0,0,0,0,0,0,0,0,0,0,0,0,0,0,0,0,0,0,0,0,0,0,0,0,0,0,0,0,0,0,0,0,0,0,0,
3,3145728,2048,59768832,12288,42,0,94495744,1,1572864,2560,11599872,0,0,0,94448384,4,1572864,2304,49872896,6144,9732096,3,94516352,6,32160,128,0,0,0,0,0,0,0,0,0,0,0,0,0,0,0,0,0,0,0,0,0,0,0,0,0,0,0,0,0,0,0,0,0,0,0,0,0,
3,3670016,2048,63700992,14336,43,0,94499840,1,1572864,3584,15925248,0,0,0,94452480,4,1572864,3328,50135040,6144,14057472,3,94516864,6,31904,128,0,0,0,0,0,0,0,0,0,0,0,0,0,0,0,0,0,0,0,0,0,0,0,0,0,0,0,0,0,0,0,0,0,0,0,0,0,
3,3145728,3840,60227584,12288,44,0,94496640,1,1572864,4608,20250624,0,0,0,94456576,4,1572864,4352,50397184,6144,18382848,3,94517376,6,33696,256,0,0,0,0,0,0,0,0,0,0,0,0,0,0,0,0,0,0,0,0,0,0,0,0,0,0,0,0,0,0,0,0,0,0,0,0,0,
3,3670016,3840,64159744,14336,45,0,94500736,1,1572864,5632,24576000,0,0,0,94460672,4,1572864,5376,50659328,6144,22708224,3,94517888,6,33440,256,0,0,0,0,0,0,0,0,0,0,0,0,0,0,0,0,0,0,0,0,0,0,0,0,0,0,0,0,0,0,0,0,0,0,0,0,0,
3,3145728,5632,60686336,12288,46,0,94497536,1,1572864,6656,28901376,0,0,0,94464768,4,1572864,6400,50921472,6144,27033600,3,94518400,6,35232,384,0,0,0,0,0,0,0,0,0,0,0,0,0,0,0,0,0,0,0,0,0,0,0,0,0,0,0,0,0,0,0,0,0,0,0,0,0,
3,3670016,5632,64618496,14336,47,0,94501632,1,1572864,7680,33226752,0,0,0,94468864,4,1572864,7424,51183616,6144,31358976,3,94518912,6,34976,384,0,0,0,0,0,0,0,0,0,0,0,0,0,0,0,0,0,0,0,0,0,0,0,0,0,0,0,0,0,0,0,0,0,0,0,0,0,
3,524288,256,39649280,2048,48,0,94474368,2,1572864,0,786432,0,0,0,94439168,4,1572864,512,49414144,6144,2162688,3,94515456,6,28576,0,0,0,0,0,0,0,0,0,0,0,0,0,0,0,0,0,0,0,0,0,0,0,0,0,0,0,0,0,0,0,0,0,0,0,0,0,0,
3,1048576,256,43581440,4096,49,0,94478464,2,1572864,1024,5111808,0,0,0,94443264,4,1572864,1536,49676288,6144,6488064,3,94515968,6,28320,0,0,0,0,0,0,0,0,0,0,0,0,0,0,0,0,0,0,0,0,0,0,0,0,0,0,0,0,0,0,0,0,0,0,0,0,0,0,
3,524288,2048,40108032,2048,50,0,94475264,2,1572864,2048,9437184,0,0,0,94447360,4,1572864,2560,49938432,6144,10813440,3,94516480,6,30112,128,0,0,0,0,0,0,0,0,0,0,0,0,0,0,0,0,0,0,0,0,0,0,0,0,0,0,0,0,0,0,0,0,0,0,0,0,0,
3,1048576,2048,44040192,4096,51,0,94479360,2,1572864,3072,13762560,0,0,0,94451456,4,1572864,3584,50200576,6144,15138816,3,94516992,6,29856,128,0,0,0,0,0,0,0,0,0,0,0,0,0,0,0,0,0,0,0,0,0,0,0,0,0,0,0,0,0,0,0,0,0,0,0,0,0,
3,524288,3840,40566784,2048,52,0,94476160,2,1572864,4096,18087936,0,0,0,94455552,4,1572864,4608,50462720,6144,19464192,3,94517504,6,31648,256,0,0,0,0,0,0,0,0,0,0,0,0,0,0,0,0,0,0,0,0,0,0,0,0,0,0,0,0,0,0,0,0,0,0,0,0,0,
3,1048576,3840,44498944,4096,53,0,94480256,2,1572864,5120,22413312,0,0,0,94459648,4,1572864,5632,50724864,6144,23789568,3,94518016,6,31392,256,0,0,0,0,0,0,0,0,0,0,0,0,0,0,0,0,0,0,0,0,0,0,0,0,0,0,0,0,0,0,0,0,0,0,0,0,0,
3,524288,5632,41025536,2048,54,0,94477056,2,1572864,6144,26738688,0,0,0,94463744,4,1572864,6656,50987008,6144,28114944,3,94518528,6,33184,384,0,0,0,0,0,0,0,0,0,0,0,0,0,0,0,0,0,0,0,0,0,0,0,0,0,0,0,0,0,0,0,0,0,0,0,0,0,
3,1048576,5632,44957696,4096,55,0,94481152,2,1572864,7168,31064064,0,0,0,94467840,4,1572864,7680,51249152,6144,32440320,3,94519040,6,32928,384,0,0,0,0,0,0,0,0,0,0,0,0,0,0,0,0,0,0,0,0,0,0,0,0,0,0,0,0,0,0,0,0,0,0,0,0,0,
3,1572864,256,47513600,6144,56,0,94482560,2,1572864,512,2949120,0,0,0,94441216,4,1572864,768,49479680,6144,3244032,3,94515584,6,26528,0,0,0,0,0,0,0,0,0,0,0,0,0,0,0,0,0,0,0,0,0,0,0,0,0,0,0,0,0,0,0,0,0,0,0,0,0,0,
3,0,256,35717120,0,57,0,94470272,2,1572864,1536,7274496,0,0,0,94445312,4,1572864,1792,49741824,6144,7569408,3,94516096,6,26272,0,0,0,0,0,0,0,0,0,0,0,0,0,0,0,0,0,0,0,0,0,0,0,0,0,0,0,0,0,0,0,0,0,0,0,0,0,0,
3,1572864,2048,47972352,6144,58,0,94483456,2,1572864,2560,11599872,0,0,0,94449408,4,1572864,2816,50003968,6144,11894784,3,94516608,6,28064,128,0,0,0,0,0,0,0,0,0,0,0,0,0,0,0,0,0,0,0,0,0,0,0,0,0,0,0,0,0,0,0,0,0,0,0,0,0,
3,0,2048,36175872,0,59,0,94471168,2,1572864,3584,15925248,0,0,0,94453504,4,1572864,3840,50266112,6144,16220160,3,94517120,6,27808,128,0,0,0,0,0,0,0,0,0,0,0,0,0,0,0,0,0,0,0,0,0,0,0,0,0,0,0,0,0,0,0,0,0,0,0,0,0,
3,1572864,3840,48431104,6144,60,0,94484352,2,1572864,4608,20250624,0,0,0,94457600,4,1572864,4864,50528256,6144,20545536,3,94517632,6,29600,256,0,0,0,0,0,0,0,0,0,0,0,0,0,0,0,0,0,0,0,0,0,0,0,0,0,0,0,0,0,0,0,0,0,0,0,0,0,
3,0,3840,36634624,0,61,0,94472064,2,1572864,5632,24576000,0,0,0,94461696,4,1572864,5888,50790400,6144,24870912,3,94518144,6,29344,256,0,0,0,0,0,0,0,0,0,0,0,0,0,0,0,0,0,0,0,0,0,0,0,0,0,0,0,0,0,0,0,0,0,0,0,0,0,
3,1572864,5632,48889856,6144,62,0,94485248,2,1572864,6656,28901376,0,0,0,94465792,4,1572864,6912,51052544,6144,29196288,3,94518656,6,31136,384,0,0,0,0,0,0,0,0,0,0,0,0,0,0,0,0,0,0,0,0,0,0,0,0,0,0,0,0,0,0,0,0,0,0,0,0,0,
3,0,5632,37093376,0,63,0,94472960,2,1572864,7680,33226752,0,0,0,94469888,4,1572864,7936,51314688,6144,33521664,3,94519168,6,30880,384,0,0,0,0,0,0,0,0,0,0,0,0,0,0,0,0,0,0,0,0,0,0,0,0,0,0,0,0,0,0,0,0,0,0,0,0,0,
1,786432,0,393216,0,0,0,94437760,1,1310720,0,655360,0,0,0,94438016,1,1835008,0,917504,0,0,0,94438272,4,2097152,0,53215232,8192,0,4,94519296,6,24448,0,0,0,0,0,0,0,0,0,0,0,0,0,0,0,0,0,0,0,0,0,0,0,0,0,0,0,0,0,0,
1,786432,1024,4718592,0,0,0,94441856,1,1310720,1024,4980736,0,0,0,94442112,1,1835008,1024,5242880,0,0,0,94442368,4,2097152,1024,53477376,8192,4325376,4,94519808,6,24192,0,0,0,0,0,0,0,0,0,0,0,0,0,0,0,0,0,0,0,0,0,0,0,0,0,0,0,0,0,0,
1,786432,2048,9043968,0,0,0,94445952,1,1310720,2048,9306112,0,0,0,94446208,1,1835008,2048,9568256,0,0,0,94446464,4,2097152,2048,53739520,8192,8650752,4,94520320,6,25984,128,0,0,0,0,0,0,0,0,0,0,0,0,0,0,0,0,0,0,0,0,0,0,0,0,0,0,0,0,0,
1,786432,3072,13369344,0,0,0,94450048,1,1310720,3072,13631488,0,0,0,94450304,1,1835008,3072,13893632,0,0,0,94450560,4,2097152,3072,54001664,8192,12976128,4,94520832,6,25728,128,0,0,0,0,0,0,0,0,0,0,0,0,0,0,0,0,0,0,0,0,0,0,0,0,0,0,0,0,0,
1,786432,4096,17694720,0,0,0,94454144,1,1310720,4096,17956864,0,0,0,94454400,1,1835008,4096,18219008,0,0,0,94454656,4,2097152,4096,54263808,8192,17301504,4,94521344,6,27520,256,0,0,0,0,0,0,0,0,0,0,0,0,0,0,0,0,0,0,0,0,0,0,0,0,0,0,0,0,0,
1,786432,5120,22020096,0,0,0,94458240,1,1310720,5120,22282240,0,0,0,94458496,1,1835008,5120,22544384,0,0,0,94458752,4,2097152,5120,54525952,8192,21626880,4,94521856,6,27264,256,0,0,0,0,0,0,0,0,0,0,0,0,0,0,0,0,0,0,0,0,0,0,0,0,0,0,0,0,0,
1,786432,6144,26345472,0,0,0,94462336,1,1310720,6144,26607616,0,0,0,94462592,1,1835008,6144,26869760,0,0,0,94462848,4,2097152,6144,54788096,8192,25952256,4,94522368,6,29056,384,0,0,0,0,0,0,0,0,0,0,0,0,0,0,0,0,0,0,0,0,0,0,0,0,0,0,0,0,0,
1,786432,7168,30670848,0,0,0,94466432,1,1310720,7168,30932992,0,0,0,94466688,1,1835008,7168,31195136,0,0,0,94466944,4,2097152,7168,55050240,8192,30277632,4,94522880,6,28800,384,0,0,0,0,0,0,0,0,0,0,0,0,0,0,0,0,0,0,0,0,0,0,0,0,0,0,0,0,0,
1,786432,512,2555904,0,0,0,94439808,1,1310720,512,2818048,0,0,0,94440064,1,1835008,512,3080192,0,0,0,94440320,4,2097152,256,53280768,8192,1081344,4,94519424,6,22400,0,0,0,0,0,0,0,0,0,0,0,0,0,0,0,0,0,0,0,0,0,0,0,0,0,0,0,0,0,0,
1,786432,1536,6881280,0,0,0,94443904,1,1310720,1536,7143424,0,0,0,94444160,1,1835008,1536,7405568,0,0,0,94444416,4,2097152,1280,53542912,8192,5406720,4,94519936,6,22144,0,0,0,0,0,0,0,0,0,0,0,0,0,0,0,0,0,0,0,0,0,0,0,0,0,0,0,0,0,0,
1,786432,2560,11206656,0,0,0,94448000,1,1310720,2560,11468800,0,0,0,94448256,1,1835008,2560,11730944,0,0,0,94448512,4,2097152,2304,53805056,8192,9732096,4,94520448,6,23936,128,0,0,0,0,0,0,0,0,0,0,0,0,0,0,0,0,0,0,0,0,0,0,0,0,0,0,0,0,0,
1,786432,3584,15532032,0,0,0,94452096,1,1310720,3584,15794176,0,0,0,94452352,1,1835008,3584,16056320,0,0,0,94452608,4,2097152,3328,54067200,8192,14057472,4,94520960,6,23680,128,0,0,0,0,0,0,0,0,0,0,0,0,0,0,0,0,0,0,0,0,0,0,0,0,0,0,0,0,0,
1,786432,4608,19857408,0,0,0,94456192,1,1310720,4608,20119552,0,0,0,94456448,1,1835008,4608,20381696,0,0,0,94456704,4,2097152,4352,54329344,8192,18382848,4,94521472,6,25472,256,0,0,0,0,0,0,0,0,0,0,0,0,0,0,0,0,0,0,0,0,0,0,0,0,0,0,0,0,0,
1,786432,5632,24182784,0,0,0,94460288,1,1310720,5632,24444928,0,0,0,94460544,1,1835008,5632,24707072,0,0,0,94460800,4,2097152,5376,54591488,8192,22708224,4,94521984,6,25216,256,0,0,0,0,0,0,0,0,0,0,0,0,0,0,0,0,0,0,0,0,0,0,0,0,0,0,0,0,0,
1,786432,6656,28508160,0,0,0,94464384,1,1310720,6656,28770304,0,0,0,94464640,1,1835008,6656,29032448,0,0,0,94464896,4,2097152,6400,54853632,8192,27033600,4,94522496,6,27008,384,0,0,0,0,0,0,0,0,0,0,0,0,0,0,0,0,0,0,0,0,0,0,0,0,0,0,0,0,0,
1,786432,7680,32833536,0,0,0,94468480,1,1310720,7680,33095680,0,0,0,94468736,1,1835008,7680,33357824,0,0,0,94468992,4,2097152,7424,55115776,8192,31358976,4,94523008,6,26752,384,0,0,0,0,0,0,0,0,0,0,0,0,0,0,0,0,0,0,0,0,0,0,0,0,0,0,0,0,0,
2,786432,0,393216,0,0,0,94438784,2,1310720,0,655360,0,0,0,94439040,2,1835008,0,917504,0,0,0,94439296,4,2097152,512,53346304,8192,2162688,4,94519552,6,20352,0,0,0,0,0,0,0,0,0,0,0,0,0,0,0,0,0,0,0,0,0,0,0,0,0,0,0,0,0,0,
2,786432,1024,4718592,0,0,0,94442880,2,1310720,1024,4980736,0,0,0,94443136,2,1835008,1024,5242880,0,0,0,94443392,4,2097152,1536,53608448,8192,6488064,4,94520064,6,20096,0,0,0,0,0,0,0,0,0,0,0,0,0,0,0,0,0,0,0,0,0,0,0,0,0,0,0,0,0,0,
2,786432,2048,9043968,0,0,0,94446976,2,1310720,2048,9306112,0,0,0,94447232,2,1835008,2048,9568256,0,0,0,94447488,4,2097152,2560,53870592,8192,10813440,4,94520576,6,21888,128,0,0,0,0,0,0,0,0,0,0,0,0,0,0,0,0,0,0,0,0,0,0,0,0,0,0,0,0,0,
2,786432,3072,13369344,0,0,0,94451072,2,1310720,3072,13631488,0,0,0,94451328,2,1835008,3072,13893632,0,0,0,94451584,4,2097152,3584,54132736,8192,15138816,4,94521088,6,21632,128,0,0,0,0,0,0,0,0,0,0,0,0,0,0,0,0,0,0,0,0,0,0,0,0,0,0,0,0,0,
2,786432,4096,17694720,0,0,0,94455168,2,1310720,4096,17956864,0,0,0,94455424,2,1835008,4096,18219008,0,0,0,94455680,4,2097152,4608,54394880,8192,19464192,4,94521600,6,23424,256,0,0,0,0,0,0,0,0,0,0,0,0,0,0,0,0,0,0,0,0,0,0,0,0,0,0,0,0,0,
2,786432,5120,22020096,0,0,0,94459264,2,1310720,5120,22282240,0,0,0,94459520,2,1835008,5120,22544384,0,0,0,94459776,4,2097152,5632,54657024,8192,23789568,4,94522112,6,23168,256,0,0,0,0,0,0,0,0,0,0,0,0,0,0,0,0,0,0,0,0,0,0,0,0,0,0,0,0,0,
2,786432,6144,26345472,0,0,0,94463360,2,1310720,6144,26607616,0,0,0,94463616,2,1835008,6144,26869760,0,0,0,94463872,4,2097152,6656,54919168,8192,28114944,4,94522624,6,24960,384,0,0,0,0,0,0,0,0,0,0,0,0,0,0,0,0,0,0,0,0,0,0,0,0,0,0,0,0,0,
2,786432,7168,30670848,0,0,0,94467456,2,1310720,7168,30932992,0,0,0,94467712,2,1835008,7168,31195136,0,0,0,94467968,4,2097152,7680,55181312,8192,32440320,4,94523136,6,24704,384,0,0,0,0,0,0,0,0,0,0,0,0,0,0,0,0,0,0,0,0,0,0,0,0,0,0,0,0,0,
2,786432,512,2555904,0,0,0,94440832,2,1310720,512,2818048,0,0,0,94441088,2,1835008,512,3080192,0,0,0,94441344,4,2097152,768,53411840,8192,3244032,4,94519680,6,18304,0,0,0,0,0,0,0,0,0,0,0,0,0,0,0,0,0,0,0,0,0,0,0,0,0,0,0,0,0,0,
2,786432,1536,6881280,0,0,0,94444928,2,1310720,1536,7143424,0,0,0,94445184,2,1835008,1536,7405568,0,0,0,94445440,4,2097152,1792,53673984,8192,7569408,4,94520192,6,18048,0,0,0,0,0,0,0,0,0,0,0,0,0,0,0,0,0,0,0,0,0,0,0,0,0,0,0,0,0,0,
2,786432,2560,11206656,0,0,0,94449024,2,1310720,2560,11468800,0,0,0,94449280,2,1835008,2560,11730944,0,0,0,94449536,4,2097152,2816,53936128,8192,11894784,4,94520704,6,19840,128,0,0,0,0,0,0,0,0,0,0,0,0,0,0,0,0,0,0,0,0,0,0,0,0,0,0,0,0,0,
2,786432,3584,15532032,0,0,0,94453120,2,1310720,3584,15794176,0,0,0,94453376,2,1835008,3584,16056320,0,0,0,94453632,4,2097152,3840,54198272,8192,16220160,4,94521216,6,19584,128,0,0,0,0,0,0,0,0,0,0,0,0,0,0,0,0,0,0,0,0,0,0,0,0,0,0,0,0,0,
2,786432,4608,19857408,0,0,0,94457216,2,1310720,4608,20119552,0,0,0,94457472,2,1835008,4608,20381696,0,0,0,94457728,4,2097152,4864,54460416,8192,20545536,4,94521728,6,21376,256,0,0,0,0,0,0,0,0,0,0,0,0,0,0,0,0,0,0,0,0,0,0,0,0,0,0,0,0,0,
2,786432,5632,24182784,0,0,0,94461312,2,1310720,5632,24444928,0,0,0,94461568,2,1835008,5632,24707072,0,0,0,94461824,4,2097152,5888,54722560,8192,24870912,4,94522240,6,21120,256,0,0,0,0,0,0,0,0,0,0,0,0,0,0,0,0,0,0,0,0,0,0,0,0,0,0,0,0,0,
2,786432,6656,28508160,0,0,0,94465408,2,1310720,6656,28770304,0,0,0,94465664,2,1835008,6656,29032448,0,0,0,94465920,4,2097152,6912,54984704,8192,29196288,4,94522752,6,22912,384,0,0,0,0,0,0,0,0,0,0,0,0,0,0,0,0,0,0,0,0,0,0,0,0,0,0,0,0,0,
2,786432,7680,32833536,0,0,0,94469504,2,1310720,7680,33095680,0,0,0,94469760,2,1835008,7680,33357824,0,0,0,94470016,4,2097152,7936,55246848,8192,33521664,4,94523264,6,22656,384,0,0,0,0,0,0,0,0,0,0,0,0,0,0,0,0,0,0,0,0,0,0,0,0,0,0,0,0,0,
3,2097152,512,51511296,8192,64,0,94486784,3,2097152,1280,51707904,8192,0,0,94487168,4,524288,0,41418752,2048,0,1,94507008,6,16288,0,0,0,0,0,0,0,0,0,0,0,0,0,0,0,0,0,0,0,0,0,0,0,0,0,0,0,0,0,0,0,0,0,0,0,0,0,0,
3,2621440,512,55443456,10240,65,0,94490880,3,2621440,1280,55640064,10240,0,0,94491264,4,524288,1024,41680896,2048,4325376,1,94507520,6,16032,0,0,0,0,0,0,0,0,0,0,0,0,0,0,0,0,0,0,0,0,0,0,0,0,0,0,0,0,0,0,0,0,0,0,0,0,0,0,
3,2097152,2304,51970048,8192,66,0,94487680,3,2097152,3072,52166656,8192,0,0,94488064,4,524288,2048,41943040,2048,8650752,1,94508032,6,17824,128,0,0,0,0,0,0,0,0,0,0,0,0,0,0,0,0,0,0,0,0,0,0,0,0,0,0,0,0,0,0,0,0,0,0,0,0,0,
3,2621440,2304,55902208,10240,67,0,94491776,3,2621440,3072,56098816,10240,0,0,94492160,4,524288,3072,42205184,2048,12976128,1,94508544,6,17568,128,0,0,0,0,0,0,0,0,0,0,0,0,0,0,0,0,0,0,0,0,0,0,0,0,0,0,0,0,0,0,0,0,0,0,0,0,0,
3,2097152,4096,52428800,8192,68,0,94488576,3,2097152,4864,52625408,8192,0,0,94488960,4,524288,4096,42467328,2048,17301504,1,94509056,6,19360,256,0,0,0,0,0,0,0,0,0,0,0,0,0,0,0,0,0,0,0,0,0,0,0,0,0,0,0,0,0,0,0,0,0,0,0,0,0,
3,2621440,4096,56360960,10240,69,0,94492672,3,2621440,4864,56557568,10240,0,0,94493056,4,524288,5120,42729472,2048,21626880,1,94509568,6,19104,256,0,0,0,0,0,0,0,0,0,0,0,0,0,0,0,0,0,0,0,0,0,0,0,0,0,0,0,0,0,0,0,0,0,0,0,0,0,
3,2097152,5888,52887552,8192,70,0,94489472,3,2097152,6656,53084160,8192,0,0,94489856,4,524288,6144,42991616,2048,25952256,1,94510080,6,20896,384,0,0,0,0,0,0,0,0,0,0,0,0,0,0,0,0,0,0,0,0,0,0,0,0,0,0,0,0,0,0,0,0,0,0,0,0,0,
3,2621440,5888,56819712,10240,71,0,94493568,3,2621440,6656,57016320,10240,0,0,94493952,4,524288,7168,43253760,2048,30277632,1,94510592,6,20640,384,0,0,0,0,0,0,0,0,0,0,0,0,0,0,0,0,0,0,0,0,0,0,0,0,0,0,0,0,0,0,0,0,0,0,0,0,0,
3,3145728,512,59375616,12288,72,0,94494976,3,3145728,1280,59572224,12288,0,0,94495360,4,524288,256,41484288,2048,1081344,1,94507136,6,14240,0,0,0,0,0,0,0,0,0,0,0,0,0,0,0,0,0,0,0,0,0,0,0,0,0,0,0,0,0,0,0,0,0,0,0,0,0,0,
3,3670016,512,63307776,14336,73,0,94499072,3,3670016,1280,63504384,14336,0,0,94499456,4,524288,1280,41746432,2048,5406720,1,94507648,6,13984,0,0,0,0,0,0,0,0,0,0,0,0,0,0,0,0,0,0,0,0,0,0,0,0,0,0,0,0,0,0,0,0,0,0,0,0,0,0,
3,3145728,2304,59834368,12288,74,0,94495872,3,3145728,3072,60030976,12288,0,0,94496256,4,524288,2304,42008576,2048,9732096,1,94508160,6,15776,128,0,0,0,0,0,0,0,0,0,0,0,0,0,0,0,0,0,0,0,0,0,0,0,0,0,0,0,0,0,0,0,0,0,0,0,0,0,
3,3670016,2304,63766528,14336,75,0,94499968,3,3670016,3072,63963136,14336,0,0,94500352,4,524288,3328,42270720,2048,14057472,1,94508672,6,15520,128,0,0,0,0,0,0,0,0,0,0,0,0,0,0,0,0,0,0,0,0,0,0,0,0,0,0,0,0,0,0,0,0,0,0,0,0,0,
3,3145728,4096,60293120,12288,76,0,94496768,3,3145728,4864,60489728,12288,0,0,94497152,4,524288,4352,42532864,2048,18382848,1,94509184,6,17312,256,0,0,0,0,0,0,0,0,0,0,0,0,0,0,0,0,0,0,0,0,0,0,0,0,0,0,0,0,0,0,0,0,0,0,0,0,0,
3,3670016,4096,64225280,14336,77,0,94500864,3,3670016,4864,64421888,14336,0,0,94501248,4,524288,5376,42795008,2048,22708224,1,94509696,6,17056,256,0,0,0,0,0,0,0,0,0,0,0,0,0,0,0,0,0,0,0,0,0,0,0,0,0,0,0,0,0,0,0,0,0,0,0,0,0,
3,3145728,5888,60751872,12288,78,0,94497664,3,3145728,6656,60948480,12288,0,0,94498048,4,524288,6400,43057152,2048,27033600,1,94510208,6,18848,384,0,0,0,0,0,0,0,0,0,0,0,0,0,0,0,0,0,0,0,0,0,0,0,0,0,0,0,0,0,0,0,0,0,0,0,0,0,
3,3670016,5888,64684032,14336,79,0,94501760,3,3670016,6656,64880640,14336,0,0,94502144,4,524288,7424,43319296,2048,31358976,1,94510720,6,18592,384,0,0,0,0,0,0,0,0,0,0,0,0,0,0,0,0,0,0,0,0,0,0,0,0,0,0,0,0,0,0,0,0,0,0,0,0,0,
3,524288,512,39714816,2048,80,0,94474496,3,524288,1280,39911424,2048,0,0,94474880,4,524288,512,41549824,2048,2162688,1,94507264,6,12192,0,0,0,0,0,0,0,0,0,0,0,0,0,0,0,0,0,0,0,0,0,0,0,0,0,0,0,0,0,0,0,0,0,0,0,0,0,0,
3,1048576,512,43646976,4096,81,0,94478592,3,1048576,1280,43843584,4096,0,0,94478976,4,524288,1536,41811968,2048,6488064,1,94507776,6,11936,0,0,0,0,0,0,0,0,0,0,0,0,0,0,0,0,0,0,0,0,0,0,0,0,0,0,0,0,0,0,0,0,0,0,0,0,0,0,
3,524288,2304,40173568,2048,82,0,94475392,3,524288,3072,40370176,2048,0,0,94475776,4,524288,2560,42074112,2048,10813440,1,94508288,6,13728,128,0,0,0,0,0,0,0,0,0,0,0,0,0,0,0,0,0,0,0,0,0,0,0,0,0,0,0,0,0,0,0,0,0,0,0,0,0,
3,1048576,2304,44105728,4096,83,0,94479488,3,1048576,3072,44302336,4096,0,0,94479872,4,524288,3584,42336256,2048,15138816,1,94508800,6,13472,128,0,0,0,0,0,0,0,0,0,0,0,0,0,0,0,0,0,0,0,0,0,0,0,0,0,0,0,0,0,0,0,0,0,0,0,0,0,
3,524288,4096,40632320,2048,84,0,94476288,3,524288,4864,40828928,2048,0,0,94476672,4,524288,4608,42598400,2048,19464192,1,94509312,6,15264,256,0,0,0,0,0,0,0,0,0,0,0,0,0,0,0,0,0,0,0,0,0,0,0,0,0,0,0,0,0,0,0,0,0,0,0,0,0,
3,1048576,4096,44564480,4096,85,0,94480384,3,1048576,4864,44761088,4096,0,0,94480768,4,524288,5632,42860544,2048,23789568,1,94509824,6,15008,256,0,0,0,0,0,0,0,0,0,0,0,0,0,0,0,0,0,0,0,0,0,0,0,0,0,0,0,0,0,0,0,0,0,0,0,0,0,
3,524288,5888,41091072,2048,86,0,94477184,3,524288,6656,41287680,2048,0,0,94477568,4,524288,6656,43122688,2048,28114944,1,94510336,6,16800,384,0,0,0,0,0,0,0,0,0,0,0,0,0,0,0,0,0,0,0,0,0,0,0,0,0,0,0,0,0,0,0,0,0,0,0,0,0,
3,1048576,5888,45023232,4096,87,0,94481280,3,1048576,6656,45219840,4096,0,0,94481664,4,524288,7680,43384832,2048,32440320,1,94510848,6,16544,384,0,0,0,0,0,0,0,0,0,0,0,0,0,0,0,0,0,0,0,0,0,0,0,0,0,0,0,0,0,0,0,0,0,0,0,0,0,
3,1572864,512,47579136,6144,88,0,94482688,3,1572864,1280,47775744,6144,0,0,94483072,4,524288,768,41615360,2048,3244032,1,94507392,6,10144,0,0,0,0,0,0,0,0,0,0,0,0,0,0,0,0,0,0,0,0,0,0,0,0,0,0,0,0,0,0,0,0,0,0,0,0,0,0,
3,0,512,35782656,0,89,0,94470400,3,0,1280,35979264,0,0,0,94470784,4,524288,1792,41877504,2048,7569408,1,94507904,6,9888,0,0,0,0,0,0,0,0,0,0,0,0,0,0,0,0,0,0,0,0,0,0,0,0,0,0,0,0,0,0,0,0,0,0,0,0,0,0,
3,1572864,2304,48037888,6144,90,0,94483584,3,1572864,3072,48234496,6144,0,0,94483968,4,524288,2816,42139648,2048,11894784,1,94508416,6,11680,128,0,0,0,0,0,0,0,0,0,0,0,0,0,0,0,0,0,0,0,0,0,0,0,0,0,0,0,0,0,0,0,0,0,0,0,0,0,
3,0,2304,36241408,0,91,0,94471296,3,0,3072,36438016,0,0,0,94471680,4,524288,3840,42401792,2048,16220160,1,94508928,6,11424,128,0,0,0,0,0,0,0,0,0,0,0,0,0,0,0,0,0,0,0,0,0,0,0,0,0,0,0,0,0,0,0,0,0,0,0,0,0,
3,1572864,4096,48496640,6144,92,0,94484480,3,1572864,4864,48693248,6144,0,0,94484864,4,524288,4864,42663936,2048,20545536,1,94509440,6,13216,256,0,0,0,0,0,0,0,0,0,0,0,0,0,0,0,0,0,0,0,0,0,0,0,0,0,0,0,0,0,0,0,0,0,0,0,0,0,
3,0,4096,36700160,0,93,0,94472192,3,0,4864,36896768,0,0,0,94472576,4,524288,5888,42926080,2048,24870912,1,94509952,6,12960,256,0,0,0,0,0,0,0,0,0,0,0,0,0,0,0,0,0,0,0,0,0,0,0,0,0,0,0,0,0,0,0,0,0,0,0,0,0,
3,1572864,5888,48955392,6144,94,0,94485376,3,1572864,6656,49152000,6144,0,0,94485760,4,524288,6912,43188224,2048,29196288,1,94510464,6,14752,384,0,0,0,0,0,0,0,0,0,0,0,0,0,0,0,0,0,0,0,0,0,0,0,0,0,0,0,0,0,0,0,0,0,0,0,0,0,
3,0,5888,37158912,0,95,0,94473088,3,0,6656,37355520,0,0,0,94473472,4,524288,7936,43450368,2048,33521664,1,94510976,6,14496,384,0,0,0,0,0,0,0,0,0,0,0,0,0,0,0,0,0,0,0,0,0,0,0,0,0,0,0,0,0,0,0,0,0,0,0,0,0,
3,2097152,768,51576832,8192,96,0,94486912,3,2097152,1536,51773440,8192,0,0,94487296,4,0,0,37486592,0,0,0,94502912,6,8096,0,0,0,0,0,0,0,0,0,0,0,0,0,0,0,0,0,0,0,0,0,0,0,0,0,0,0,0,0,0,0,0,0,0,0,0,0,0,
3,2621440,768,55508992,10240,97,0,94491008,3,2621440,1536,55705600,10240,0,0,94491392,4,0,1024,37748736,0,4325376,0,94503424,6,7840,0,0,0,0,0,0,0,0,0,0,0,0,0,0,0,0,0,0,0,0,0,0,0,0,0,0,0,0,0,0,0,0,0,0,0,0,0,0,
3,2097152,2560,52035584,8192,98,0,94487808,3,2097152,3328,52232192,8192,0,0,94488192,4,0,2048,38010880,0,8650752,0,94503936,6,9632,128,0,0,0,0,0,0,0,0,0,0,0,0,0,0,0,0,0,0,0,0,0,0,0,0,0,0,0,0,0,0,0,0,0,0,0,0,0,
3,2621440,2560,55967744,10240,99,0,94491904,3,2621440,3328,56164352,10240,0,0,94492288,4,0,3072,38273024,0,12976128,0,94504448,6,9376,128,0,0,0,0,0,0,0,0,0,0,0,0,0,0,0,0,0,0,0,0,0,0,0,0,0,0,0,0,0,0,0,0,0,0,0,0,0,
3,2097152,4352,52494336,8192,100,0,94488704,3,2097152,5120,52690944,8192,0,0,94489088,4,0,4096,38535168,0,17301504,0,94504960,6,11168,256,0,0,0,0,0,0,0,0,0,0,0,0,0,0,0,0,0,0,0,0,0,0,0,0,0,0,0,0,0,0,0,0,0,0,0,0,0,
3,2621440,4352,56426496,10240,101,0,94492800,3,2621440,5120,56623104,10240,0,0,94493184,4,0,5120,38797312,0,21626880,0,94505472,6,10912,256,0,0,0,0,0,0,0,0,0,0,0,0,0,0,0,0,0,0,0,0,0,0,0,0,0,0,0,0,0,0,0,0,0,0,0,0,0,
3,2097152,6144,52953088,8192,102,0,94489600,3,2097152,6912,53149696,8192,0,0,94489984,4,0,6144,39059456,0,25952256,0,94505984,6,12704,384,0,0,0,0,0,0,0,0,0,0,0,0,0,0,0,0,0,0,0,0,0,0,0,0,0,0,0,0,0,0,0,0,0,0,0,0,0,
3,2621440,6144,56885248,10240,103,0,94493696,3,2621440,6912,57081856,10240,0,0,94494080,4,0,7168,39321600,0,30277632,0,94506496,6,12448,384,0,0,0,0,0,0,0,0,0,0,0,0,0,0,0,0,0,0,0,0,0,0,0,0,0,0,0,0,0,0,0,0,0,0,0,0,0,
3,3145728,768,59441152,12288,104,0,94495104,3,3145728,1536,59637760,12288,0,0,94495488,4,0,256,37552128,0,1081344,0,94503040,6,6048,0,0,0,0,0,0,0,0,0,0,0,0,0,0,0,0,0,0,0,0,0,0,0,0,0,0,0,0,0,0,0,0,0,0,0,0,0,0,
3,3670016,768,63373312,14336,105,0,94499200,3,3670016,1536,63569920,14336,0,0,94499584,4,0,1280,37814272,0,5406720,0,94503552,6,5792,0,0,0,0,0,0,0,0,0,0,0,0,0,0,0,0,0,0,0,0,0,0,0,0,0,0,0,0,0,0,0,0,0,0,0,0,0,0,
3,3145728,2560,59899904,12288,106,0,94496000,3,3145728,3328,60096512,12288,0,0,94496384,4,0,2304,38076416,0,9732096,0,94504064,6,7584,128,0,0,0,0,0,0,0,0,0,0,0,0,0,0,0,0,0,0,0,0,0,0,0,0,0,0,0,0,0,0,0,0,0,0,0,0,0,
3,3670016,2560,63832064,14336,107,0,94500096,3,3670016,3328,64028672,14336,0,0,94500480,4,0,3328,38338560,0,14057472,0,94504576,6,7328,128,0,0,0,0,0,0,0,0,0,0,0,0,0,0,0,0,0,0,0,0,0,0,0,0,0,0,0,0,0,0,0,0,0,0,0,0,0,
3,3145728,4352,60358656,12288,108,0,94496896,3,3145728,5120,60555264,12288,0,0,94497280,4,0,4352,38600704,0,18382848,0,94505088,6,9120,256,0,0,0,0,0,0,0,0,0,0,0,0,0,0,0,0,0,0,0,0,0,0,0,0,0,0,0,0,0,0,0,0,0,0,0,0,0,
3,3670016,4352,64290816,14336,109,0,94500992,3,3670016,5120,64487424,14336,0,0,94501376,4,0,5376,38862848,0,22708224,0,94505600,6,8864,256,0,0,0,0,0,0,0,0,0,0,0,0,0,0,0,0,0,0,0,0,0,0,0,0,0,0,0,0,0,0,0,0,0,0,0,0,0,
3,3145728,6144,60817408,12288,110,0,94497792,3,3145728,6912,61014016,12288,0,0,94498176,4,0,6400,39124992,0,27033600,0,94506112,6,10656,384,0,0,0,0,0,0,0,0,0,0,0,0,0,0,0,0,0,0,0,0,0,0,0,0,0,0,0,0,0,0,0,0,0,0,0,0,0,
3,3670016,6144,64749568,14336,111,0,94501888,3,3670016,6912,64946176,14336,0,0,94502272,4,0,7424,39387136,0,31358976,0,94506624,6,10400,384,0,0,0,0,0,0,0,0,0,0,0,0,0,0,0,0,0,0,0,0,0,0,0,0,0,0,0,0,0,0,0,0,0,0,0,0,0,
3,524288,768,39780352,2048,112,0,94474624,3,524288,1536,39976960,2048,0,0,94475008,4,0,512,37617664,0,2162688,0,94503168,6,4000,0,0,0,0,0,0,0,0,0,0,0,0,0,0,0,0,0,0,0,0,0,0,0,0,0,0,0,0,0,0,0,0,0,0,0,0,0,0,
3,1048576,768,43712512,4096,113,0,94478720,3,1048576,1536,43909120,4096,0,0,94479104,4,0,1536,37879808,0,6488064,0,94503680,6,3744,0,0,0,0,0,0,0,0,0,0,0,0,0,0,0,0,0,0,0,0,0,0,0,0,0,0,0,0,0,0,0,0,0,0,0,0,0,0,
3,524288,2560,40239104,2048,114,0,94475520,3,524288,3328,40435712,2048,0,0,94475904,4,0,2560,38141952,0,10813440,0,94504192,6,5536,128,0,0,0,0,0,0,0,0,0,0,0,0,0,0,0,0,0,0,0,0,0,0,0,0,0,0,0,0,0,0,0,0,0,0,0,0,0,
3,1048576,2560,44171264,4096,115,0,94479616,3,1048576,3328,44367872,4096,0,0,94480000,4,0,3584,38404096,0,15138816,0,94504704,6,5280,128,0,0,0,0,0,0,0,0,0,0,0,0,0,0,0,0,0,0,0,0,0,0,0,0,0,0,0,0,0,0,0,0,0,0,0,0,0,
3,524288,4352,40697856,2048,116,0,94476416,3,524288,5120,40894464,2048,0,0,94476800,4,0,4608,38666240,0,19464192,0,94505216,6,7072,256,0,0,0,0,0,0,0,0,0,0,0,0,0,0,0,0,0,0,0,0,0,0,0,0,0,0,0,0,0,0,0,0,0,0,0,0,0,
3,1048576,4352,44630016,4096,117,0,94480512,3,1048576,5120,44826624,4096,0,0,94480896,4,0,5632,38928384,0,23789568,0,94505728,6,6816,256,0,0,0,0,0,0,0,0,0,0,0,0,0,0,0,0,0,0,0,0,0,0,0,0,0,0,0,0,0,0,0,0,0,0,0,0,0,
3,524288,6144,41156608,2048,118,0,94477312,3,524288,6912,41353216,2048,0,0,94477696,4,0,6656,39190528,0,28114944,0,94506240,6,8608,384,0,0,0,0,0,0,0,0,0,0,0,0,0,0,0,0,0,0,0,0,0,0,0,0,0,0,0,0,0,0,0,0,0,0,0,0,0,
3,1048576,6144,45088768,4096,119,0,94481408,3,1048576,6912,45285376,4096,0,0,94481792,4,0,7680,39452672,0,32440320,0,94506752,6,8352,384,0,0,0,0,0,0,0,0,0,0,0,0,0,0,0,0,0,0,0,0,0,0,0,0,0,0,0,0,0,0,0,0,0,0,0,0,0,
3,1572864,768,47644672,6144,120,0,94482816,3,1572864,1536,47841280,6144,0,0,94483200,4,0,768,37683200,0,3244032,0,94503296,6,1952,0,0,0,0,0,0,0,0,0,0,0,0,0,0,0,0,0,0,0,0,0,0,0,0,0,0,0,0,0,0,0,0,0,0,0,0,0,0,
3,0,768,35848192,0,121,0,94470528,3,0,1536,36044800,0,0,0,94470912,4,0,1792,37945344,0,7569408,0,94503808,6,1696,0,0,0,0,0,0,0,0,0,0,0,0,0,0,0,0,0,0,0,0,0,0,0,0,0,0,0,0,0,0,0,0,0,0,0,0,0,0,
3,1572864,2560,48103424,6144,122,0,94483712,3,1572864,3328,48300032,6144,0,0,94484096,4,0,2816,38207488,0,11894784,0,94504320,6,3488,128,0,0,0,0,0,0,0,0,0,0,0,0,0,0,0,0,0,0,0,0,0,0,0,0,0,0,0,0,0,0,0,0,0,0,0,0,0,
3,0,2560,36306944,0,123,0,94471424,3,0,3328,36503552,0,0,0,94471808,4,0,3840,38469632,0,16220160,0,94504832,6,3232,128,0,0,0,0,0,0,0,0,0,0,0,0,0,0,0,0,0,0,0,0,0,0,0,0,0,0,0,0,0,0,0,0,0,0,0,0,0,
3,1572864,4352,48562176,6144,124,0,94484608,3,1572864,5120,48758784,6144,0,0,94484992,4,0,4864,38731776,0,20545536,0,94505344,6,5024,256,0,0,0,0,0,0,0,0,0,0,0,0,0,0,0,0,0,0,0,0,0,0,0,0,0,0,0,0,0,0,0,0,0,0,0,0,0,
3,0,4352,36765696,0,125,0,94472320,3,0,5120,36962304,0,0,0,94472704,4,0,5888,38993920,0,24870912,0,94505856,6,4768,256,0,0,0,0,0,0,0,0,0,0,0,0,0,0,0,0,0,0,0,0,0,0,0,0,0,0,0,0,0,0,0,0,0,0,0,0,0,
3,1572864,6144,49020928,6144,126,0,94485504,3,1572864,6912,49217536,6144,0,0,94485888,4,0,6912,39256064,0,29196288,0,94506368,6,6560,384,0,0,0,0,0,0,0,0,0,0,0,0,0,0,0,0,0,0,0,0,0,0,0,0,0,0,0,0,0,0,0,0,0,0,0,0,0,
3,0,6144,37224448,0,127,0,94473216,3,0,6912,37421056,0,0,0,94473600,4,0,7936,39518208,0,33521664,0,94506880,6,6304,384,0,0,0,0,0,0,0,0,0,0,0,0,0,0,0,0,0,0,0,0,0,0,0,0,0,0,0,0,0,0,0,0,0,0,0,0,0,
5,43515904,1024,1049600,2152736392u,1024,45350912,20,5,43515904,1280,1049856,2152736392u,1280,45350912,21,5,43515904,1536,1050112,1884300928,1536,45350912,22,5,43515904,1792,1050368,1884300928,1792,45350912,23,5,55312384,1024,2622464,2152212112u,1024,57147392,44,5,55312384,1280,2622720,2152211856u,1280,57147392,45,5,55312384,1536,2622976,2152211856u,1536,57147392,46,5,55312384,1792,2623232,2152211856u,1792,57147392,47,
5,47448064,0,1572864,2151687312u,0,49283072,24,5,47448064,256,1573120,2151687312u,256,49283072,25,5,47448064,512,1573376,2151687312u,512,49283072,26,5,47448064,768,1573632,2151687312u,768,49283072,27,5,35651584,0,0,2151687048u,0,37486592,0,5,35651584,256,256,2151687048u,256,37486592,1,5,35651584,512,512,2151686792u,512,37486592,2,5,35651584,768,768,1883251328,768,37486592,3,
5,59244544,0,3145728,1882726528,0,61079552,48,5,59244544,256,3145984,1882726528,256,61079552,49,5,59244544,512,3146240,1882726528,512,61079552,50,5,59244544,768,3146496,1882726528,768,61079552,51,5,59244544,1024,3146752,2151161992u,1024,61079552,52,5,59244544,1280,3147008,2151161992u,1280,61079552,53,5,59244544,1536,3147264,2151161992u,1536,61079552,54,5,59244544,1792,3147520,2150637448u,1792,61079552,55,
5,51380224,0,2097152,1882726528,0,53215232,32,5,51380224,256,2097408,1882726528,256,53215232,33,5,51380224,512,2097664,1882726272,512,53215232,34,5,51380224,768,2097920,1882201728,768,53215232,35,5,35651584,1024,1024,1882726272,1024,37486592,4,5,35651584,1280,1280,1882726272,1280,37486592,5,5,35651584,1536,1536,1882201728,1536,37486592,6,5,35651584,1792,1792,1882201728,1792,37486592,7,
5,39583744,1024,525312,2150637456u,1024,41418752,12,5,39583744,1280,525568,2150112912u,1280,41418752,13,5,39583744,1536,525824,2150112912u,1536,41418752,14,5,39583744,1792,526080,2150112912u,1792,41418752,15,5,47448064,1024,1573888,1882201472,1024,49283072,28,5,47448064,1280,1574144,1882201472,1280,49283072,29,5,47448064,1536,1574400,1882201472,1536,49283072,30,5,47448064,1792,1574656,1882201472,1792,49283072,31,
5,63176704,1024,3671040,1882201472,1024,65011712,60,5,63176704,1280,3671296,1881676928,1280,65011712,61,5,63176704,1536,3671552,1881676928,1536,65011712,62,5,63176704,1792,3671808,1881676928,1792,65011712,63,5,51380224,1024,2098176,1881676928,1024,53215232,36,5,51380224,1280,2098432,1881676928,1280,53215232,37,5,51380224,1536,2098688,1881676928,1536,53215232,38,5,51380224,1792,2098944,1881676928,1792,53215232,39,
5,55312384,0,2621440,1881676928,0,57147392,40,5,55312384,256,2621696,1881676672,256,57147392,41,5,55312384,512,2621952,1881676672,512,57147392,42,5,55312384,768,2622208,1881676672,768,57147392,43,5,39583744,0,524288,1881151872,0,41418752,8,5,39583744,256,524544,1881151872,256,41418752,9,5,39583744,512,524800,1881151872,512,41418752,10,5,39583744,768,525056,1880627328,768,41418752,11,
5,63176704,0,3670016,1880627072,0,65011712,56,5,63176704,256,3670272,1880627072,256,65011712,57,5,63176704,512,3670528,1880626808,512,65011712,58,5,63176704,768,3670784,1880626808,768,65011712,59,5,43515904,0,1048576,1343755104,0,45350912,16,5,43515904,256,1048832,1343755104,256,45350912,17,5,43515904,512,1049088,1343754840,512,45350912,18,5,43515904,768,1049344,1075319120,768,45350912,19,
5,47874048,1705728,1574656,1952032641,2098944,49807360,31,5,47874048,1705472,1574400,1952032385,2098688,49807360,30,5,47874048,1705216,1574144,1952032385,2098432,49807360,29,5,47874048,1704960,1573888,1952032385,2098176,49807360,28,5,36241408,2360064,768,1951556993,2097920,38010880,3,5,36208640,2228736,512,2219976065u,2228736,38043648,2,5,36241408,2359552,256,2219467905u,2228480,38043648,1,5,36241408,2359296,0,2219468169u,2228224,38043648,0,
5,59703296,1836800,3147520,2219434881u,2230016,61636608,55,5,59736064,1967616,3147264,2219434889u,2229760,61636608,54,5,59736064,1967360,3147008,2219434625u,2229504,61636608,53,5,59736064,1967104,3146752,2219434881u,2229248,61636608,52,5,36110336,1836800,1792,1950999425,2098944,38010880,7,5,36110336,1836544,1536,1950999425,2098688,38010880,6,5,36143104,1967360,1280,1950999169,2098432,38010880,5,5,36143104,1967104,1024,1950999169,2098176,38010880,4,
5,48037888,2360064,1573632,2226807441u,2360064,49872896,27,5,48037888,2359808,1573376,2226807441u,2359808,49872896,26,5,48037888,2359552,1573120,2226807177u,2359552,49872896,25,5,48037888,2359296,1572864,2226807177u,2359296,49872896,24,5,59768832,2097920,3146496,1949966209,2097920,61603840,51,5,59768832,2097664,3146240,1949966209,2097664,61603840,50,5,59768832,2097408,3145984,1949966209,2097408,61603840,49,5,59768832,2097152,3145728,1949966209,2097152,61603840,48,
5,63406080,918272,3670784,2209931401u,1966848,65503232,59,5,63406080,918016,3670528,2209931401u,1966592,65503232,58,5,63406080,917760,3670272,1949884289,2097408,65536000,57,5,63406080,917504,3670016,1949884289,2097152,65536000,56,5,55934976,2492160,2623232,2226806665u,2361088,57737216,47,5,55934976,2491904,2622976,2226806665u,2360832,57737216,46,5,55934976,2491648,2622720,2226282385u,2360576,57737216,45,5,55967744,2622464,2622464,2226298769u,2360320,57737216,44,
5,55672832,1442560,2622208,1949916801,2097920,57671680,43,5,55672832,1442304,2621952,1949916801,2097664,57671680,42,5,55672832,1442048,2621696,1949916801,2097408,57671680,41,5,55705600,1572864,2621440,1949408897,2097152,57671680,40,5,39845888,1049344,525056,1949376129,2097920,41943040,11,5,39878656,1180160,524800,1949375873,2097664,41943040,10,5,39878656,1179904,524544,1949375873,2097408,41943040,9,5,39878656,1179648,524288,1949375873,2097152,41943040,8,
5,43745280,918272,1049344,2192104849u,1311488,45678592,19,5,43745280,918016,1049088,2460540305u,1442304,45711360,18,5,43745280,917760,1048832,2200493457u,1573120,45744128,17,5,43745280,917504,1048576,2200493457u,1572864,45744128,16,5,51838976,1835776,2097920,1948900481,2097920,53739520,35,5,51871744,1966592,2097664,1948900225,2097664,53739520,34,5,51904512,2097408,2097408,1948916609,2097408,53739520,33,5,51904512,2097152,2097152,1948916609,2097152,53739520,32,
5,63569920,1574656,3671808,1948883841,2098944,65536000,63,5,63569920,1574400,3671552,1948883841,2098688,65536000,62,5,63569920,1574144,3671296,1948883841,2098432,65536000,61,5,63602688,1704960,3671040,1948883585,2098176,65536000,60,5,51773440,1574656,2098944,1948359041,2098944,53739520,39,5,51773440,1574400,2098688,1948359041,2098688,53739520,38,5,51773440,1574144,2098432,1948359041,2098432,53739520,37,5,51773440,1573888,2098176,1947834497,2098176,53739520,36,
5,44236800,2885376,1050368,1947916161,2098944,45875200,23,5,44236800,2885120,1050112,1947916161,2098688,45875200,22,5,44204032,2753792,1049856,1947899513,2229504,45907968,21,5,44204032,2753536,1049600,1947899513,2229248,45907968,20,5,39976960,1574656,526080,1419350881,2361088,42008576,15,5,39976960,1574400,525824,1419350881,2360832,42008576,14,5,39976960,1574144,525568,1419350617,2360576,42008576,13,5,40009728,1704960,525312,1150914897,2360320,42008576,12,
5,52035584,2622464,2098176,2019207042,4195328,54263808,36,5,52068352,2753792,2098432,2019206786,4195584,54263808,37,5,52068352,2754048,2098688,2019206786,4195840,54263808,38,5,52068352,2754304,2098944,2019206786,4196096,54263808,39,5,44105728,2359296,1048576,2278188426u,3932160,46333952,16,5,44105728,2359552,1048832,2278188426u,3932416,46333952,17,5,44105728,2359808,1049088,2538235282u,3801600,46301184,18,5,44105728,2360064,1049344,2269799826u,3670784,46268416,19,
5,52264960,3538944,2097152,2018206594,4194304,54263808,32,5,52264960,3539200,2097408,2018206594,4194560,54263808,33,5,52232192,3408384,2097664,2018206594,4194816,54263808,34,5,52232192,3408640,2097920,2018206594,4195072,54263808,35,5,40304640,2883584,524288,2018173826,4194304,42467328,8,5,40304640,2883840,524544,2018173826,4194560,42467328,9,5,40304640,2884096,524800,2018173826,4194816,42467328,10,5,40304640,2884352,525056,2018173826,4195072,42467328,11,
5,56393728,4326400,2622464,2302943890u,4719616,58327040,44,5,56360960,4195584,2622720,2302943882u,4719872,58327040,45,5,56393728,4326912,2622976,2294555018u,4589056,58294272,46,5,56393728,4327168,2623232,2294555018u,4589312,58294272,47,5,40239104,2622464,525312,2268766610u,3671040,42336256,12,5,40206336,2491648,525568,2537185682u,3802368,42369024,13,5,40206336,2491904,525824,2277663114u,3933696,42401792,14,5,40206336,2492160,526080,2277138570u,3933952,42401792,15,
5,63930368,3015680,3671040,2017648770,4195328,66060288,60,5,63930368,3015936,3671296,2017648770,4195584,66060288,61,5,63930368,3016192,3671552,2017124226,4195840,66060288,62,5,63930368,3016448,3671808,2017124226,4196096,66060288,63,5,44433408,3671040,1049600,2017173378,4195328,46399488,20,5,44433408,3671296,1049856,2017173378,4195584,46399488,21,5,44466176,3802624,1050112,2017173122,4195840,46399488,22,5,44466176,3802880,1050368,2017173122,4196096,46399488,23,
5,36732928,4326400,1024,2017206146,4195328,38535168,4,5,36732928,4326656,1280,2017205890,4195584,38535168,5,5,36732928,4326912,1536,2017205890,4195840,38535168,6,5,36732928,4327168,1792,2017205890,4196096,38535168,7,5,60260352,4063232,3145728,2016664706,4194304,62128128,48,5,60260352,4063488,3145984,2016664706,4194560,62128128,49,5,60260352,4063744,3146240,2016664706,4194816,62128128,50,5,60260352,4064000,3146496,2016664706,4195072,62128128,51,
5,63897600,2883584,3670016,2016599426,4194304,66060288,56,5,63897600,2883840,3670272,2016599426,4194560,66060288,57,5,63897600,2884096,3670528,2016599426,4194816,66060288,58,5,63897600,2884352,3670784,2016599426,4195072,66060288,59,5,56164352,3407872,2621440,2016107650,4194304,58195968,40,5,56131584,3277056,2621696,2016615554,4194560,58195968,41,5,56131584,3277312,2621952,2016615554,4194816,58195968,42,5,56131584,3277568,2622208,2016615554,4195072,58195968,43,
5,36896768,4980736,0,2024594306,4456448,38600704,0,5,36896768,4980992,256,2284641154u,4325632,38567936,1,5,36896768,4981248,512,2284641154u,4325888,38567936,2,5,36929536,5112576,768,2016205442,4195072,38535168,3,5,48594944,4588544,1573888,2015648130,4195328,50331648,28,5,48594944,4588800,1574144,2015648130,4195584,50331648,29,5,48594944,4589056,1574400,2015648130,4195840,50331648,30,5,48627712,4720384,1574656,2015139970,4196096,50331648,31,
5,60358656,4457472,3146752,2015123066,4326400,62160896,52,5,60325888,4326656,3147008,2015106682,4326656,62160896,53,5,60325888,4326912,3147264,1755059570,4457984,62193664,54,5,60325888,4327168,3147520,2015106682,4327168,62160896,55,5,48562176,4456448,1572864,1755075426,4587520,50429952,24,5,48562176,4456704,1573120,1755075426,4587776,50429952,25,5,48562176,4456960,1573376,1495028314,4719104,50462720,26,5,48562176,4457216,1573632,1226592594,4719360,50462720,27,
5,40697856,4458240,526080,2345953163u,6162176,42958848,15,5,40730624,4589056,525824,2345952907u,6161920,42958848,14,5,40697856,4457728,525568,2345953163u,6161664,42958848,13,5,40730624,4588544,525312,2345953171u,6030336,42926080,12,5,64323584,4588288,3670784,2085905795,6292224,66584576,59,5,64323584,4588032,3670528,2085905795,6291968,66584576,58,5,64323584,4587776,3670272,2085905795,6291712,66584576,57,5,64323584,4587520,3670016,2085905795,6291456,66584576,56,
5,44728320,4850432,1049344,2345444755u,6030080,46858240,19,5,44728320,4850176,1049088,2345444491u,6160896,46891008,18,5,44761088,4980992,1048832,2344936331u,6160640,46891008,17,5,44761088,4980736,1048576,2344936331u,6160384,46891008,16,5,56524800,4850432,2622208,2085397379,6292224,58720256,43,5,56524800,4850176,2621952,2085397379,6291968,58720256,42,5,56524800,4849920,2621696,2085397379,6291712,58720256,41,5,56557568,4980736,2621440,2084889219,6291456,58720256,40,
5,64421888,4982528,3671808,2084889219,6293248,66584576,63,5,64421888,4982272,3671552,2084889219,6292992,66584576,62,5,64454656,5113088,3671296,2084888963,6292736,66584576,61,5,64454656,5112832,3671040,2084888963,6292480,66584576,60,5,60555264,5244672,3147520,2084380803,6293248,62652416,55,5,60555264,5244416,3147264,2084380803,6292992,62652416,54,5,60555264,5244160,3147008,2084380803,6292736,62652416,53,5,60588032,5374976,3146752,2084380547,6292480,62652416,52,
5,48791552,5374720,1573632,2344427923u,6030080,50790400,27,5,48791552,5374464,1573376,2344427659u,6160896,50823168,26,5,48791552,5374208,1573120,2344427659u,6160640,50823168,25,5,48791552,5373952,1572864,2344427659u,6160384,50823168,24,5,40927232,5374720,525056,2084380547,6292224,42991616,11,5,40927232,5374464,524800,2084380547,6291968,42991616,10,5,40927232,5374208,524544,2084380547,6291712,42991616,9,5,40927232,5373952,524288,2084380547,6291456,42991616,8,
5,60653568,5636864,3146496,2083872131,6292224,62652416,51,5,60653568,5636608,3146240,2083872131,6291968,62652416,50,5,60653568,5636352,3145984,2083872131,6291712,62652416,49,5,60653568,5636096,3145728,2083872131,6291456,62652416,48,5,44924928,5637888,1050368,2083872131,6293248,46923776,23,5,44924928,5637632,1050112,2083872131,6292992,46923776,22,5,44924928,5637376,1049856,2083872131,6292736,46923776,21,5,44924928,5637120,1049600,2083872131,6292480,46923776,20,
5,52789248,5637888,2098944,2083872131,6293248,54788096,39,5,52789248,5637632,2098688,2083872131,6292992,54788096,38,5,52789248,5637376,2098432,2083872131,6292736,54788096,37,5,52789248,5637120,2098176,2083872131,6292480,54788096,36,5,48889856,5768960,1574656,2083363971,6293248,50855936,31,5,48889856,5768704,1574400,2083363971,6292992,50855936,30,5,48889856,5768448,1574144,2083363971,6292736,50855936,29,5,48889856,5768192,1573888,2083363971,6292480,50855936,28,
5,52854784,5899008,2097920,2083363715,6292224,54788096,35,5,52854784,5898752,2097664,2083363715,6291968,54788096,34,5,52887552,6029568,2097408,2082855555,6291712,54788096,33,5,52887552,6029312,2097152,2082855555,6291456,54788096,32,5,37191680,6162176,1792,2082855299,6293248,39059456,7,5,37191680,6161920,1536,2082855299,6292992,39059456,6,5,37191680,6161664,1280,2082855299,6292736,39059456,5,5,37224448,6292480,1024,2082347139,6292480,39059456,4,
5,37257216,6423296,768,2082346883,6292224,39059456,3,5,37257216,6423040,512,2082346619,6423040,39092224,2,5,37257216,6422784,256,2082346619,6422784,39092224,1,5,37257216,6422528,0,1822299507,6553600,39124992,0,5,56918016,6424320,2623232,1562252131,6817536,58851328,47,5,56918016,6424064,2622976,1562252131,6817280,58851328,46,5,56918016,6423808,2622720,1562251867,6948096,58884096,45,5,56918016,6423552,2622464,1302204755,7078912,58916864,44
};
__global__ __launch_bounds__(256, 1) void mega(MArgs args) {
  __shared__ __attribute__((aligned(1024))) char lds[163840];
  const unsigned lane = threadIdx.x & 63, w = __builtin_amdgcn_readfirstlane(threadIdx.x >> 6);
  const unsigned* tab = mtab + blockIdx.x*64;
  asm volatile("" :: "s"((unsigned)(size_t)lds));
  asm volatile(
  "s_load_dwordx16 s[8:23], %0, 0x0\n"
  "s_load_dwordx8 s[24:31], %0, 0x40\n"
  "s_load_dwordx8 s[44:51], %1, 0x0\n"
  "s_load_dwordx16 s[64:79], %1, 0x40\n"
  "s_load_dwordx16 s[80:95], %1, 0x80\n"
  "s_mov_b64 s[32:33], %1\n"
  "s_mov_b32 s34, %2\n"
  "s_mov_b32 s98, %4\n"
  "v_mov_b32 v255, %3\n"
  "s_getpc_b64 s[96:97]\n"
  "L_pc0%=:\n"
  "s_lshr_b32 s99, s98, 3\n"
  "s_mul_i32 s100, s99, 43\n"
  "s_lshr_b32 s100, s100, 7\n"
  "s_mul_i32 s100, s100, 3\n"
  "s_sub_u32 s99, s99, s100\n"
  "s_lshl_b32 s99, s99, 2\n"
  "s_add_u32 s99, s99, s34\n"
  "s_lshl_b32 s99, s99, 13\n"
  "v_lshl_add_u32 v217, v255, 7, s99\n"
  "s_mov_b32 s99, L_end%=-L_pc0%=\n"
  "v_min_u32 v217, s99, v217\n"
  "global_load_dword v216, v217, s[96:97]\n"
  "v_lshrrev_b32 v249, 3, v255\n"
  "v_lshrrev_b32 v0, 4, v255\n"
  "v_and_b32 v1, 7, v255\n"
  "v_xor_b32 v248, v0, v1\n"
  "v_lshlrev_b32 v248, 4, v248\n"
  "v_lshrrev_b32 v2, 1, v1\n"
  "v_and_b32 v3, 1, v0\n"
  "v_xor_b32 v2, v2, v3\n"
  "v_and_b32 v3, 1, v255\n"
  "v_lshlrev_b32 v3, 4, v3\n"
  "v_lshl_add_u32 v247, v2, 5, v3\n"
  "v_and_b32 v1, 15, v255\n"
  "v_lshlrev_b32 v1, 4, v1\n"
  "v_lshl_add_u32 v246, v0, 10, v1\n"
  "s_waitcnt lgkmcnt(0)\n"
  "s_mov_b32 s55, 0x20000\n"
  "s_mov_b32 s59, 0x20000\n"
  "s_mov_b32 s101, 1\n"
  "s_mov_b32 s86, 1\n"
  "s_mov_b32 s36, 0\n"
  "s_mov_b64 s[88:89], s[28:29]\n"
  "s_getpc_b64 s[96:97]\n"
  "s_add_u32 s96, s96, 12\n"
  "s_addc_u32 s97, s97, 0\n"
  "s_branch L_sregsx%=\n"
  "s_getpc_b64 s[96:97]\n"
  "s_add_u32 s96, s96, 12\n"
  "s_addc_u32 s97, s97, 0\n"
  "s_branch L_sregsw%=\n"
  "s_getpc_b64 s[96:97]\n"
  "s_add_u32 s96, s96, 12\n"
  "s_addc_u32 s97, s97, 0\n"
  "s_branch L_issue%=\n"
  "v_lshlrev_b32 v160, 4, v255\n"
  "global_load_dwordx4 v[180:183], v160, s[88:89] offset:0\n"
  "global_load_dwordx4 v[184:187], v160, s[88:89] offset:1024\n"
  "v_mov_b32 v190, 0x5a00000\n"
  "global_load_dword v191, v190, s[8:9] sc1\n"
  "s_lshl_b32 s87, s98, 2\n"
  "s_add_u32 s87, s87, s34\n"
  "s_lshl_b32 s87, s87, 12\n"
  "v_lshl_add_u32 v160, v255, 4, s87\n"
  "global_load_dwordx4 v[164:167], v160, s[10:11] offset:0\n"
  "global_load_dwordx4 v[168:171], v160, s[10:11] offset:1024\n"
  "global_load_dwordx4 v[172:175], v160, s[10:11] offset:2048\n"
  "global_load_dwordx4 v[176:179], v160, s[10:11] offset:3072\n"
  "v_and_b32 v217, 15, v255\n"
  "s_lshr_b32 s87, s87, 1\n"
  "v_lshl_add_u32 v217, v217, 7, s87\n"
  "global_load_dword v216, v217, s[12:13]\n"
  "s_cmp_lg_u32 s34, 3\n"
  "s_cbranch_scc1 L_tlb_d%=\n"
  "s_lshr_b32 s87, s98, 3\n"
  "s_cmp_ge_u32 s87, 22\n"
  "s_cbranch_scc1 L_tlb_d%=\n"
  "s_mov_b64 s[92:93], s[8:9]\n"
  "s_lshl_b32 s90, s87, 23\n"
  "s_mov_b32 s91, 0x20000\n"
  "s_cmp_eq_u32 s87, 12\n"
  "s_cselect_b64 s[92:93], s[14:15], s[92:93]\n"
  "s_cselect_b32 s90, 0, s90\n"
  "s_cselect_b32 s91, 0x20000, s91\n"
  "s_cmp_eq_u32 s87, 13\n"
  "s_cselect_b64 s[92:93], s[20:21], s[92:93]\n"
  "s_cselect_b32 s90, 0, s90\n"
  "s_cselect_b32 s91, 0x20000, s91\n"
  "s_cmp_eq_u32 s87, 14\n"
  "s_cselect_b64 s[92:93], s[26:27], s[92:93]\n"
  "s_cselect_b32 s90, 0, s90\n"
  "s_cselect_b32 s91, 0x1c000, s91\n"
  "s_cmp_eq_u32 s87, 15\n"
  "s_cselect_b64 s[92:93], s[22:23], s[92:93]\n"
  "s_cselect_b32 s90, 0, s90\n"
  "s_cselect_b32 s91, 0x1c000, s91\n"
  "s_cmp_eq_u32 s87, 16\n"
  "s_cselect_b64 s[92:93], s[24:25], s[92:93]\n"
  "s_cselect_b32 s90, 0, s90\n"
  "s_cselect_b32 s91, 0x1c000, s91\n"
  "s_cmp_eq_u32 s87, 17\n"
  "s_cselect_b64 s[92:93], s[30:31], s[92:93]\n"
  "s_cselect_b32 s90, 0, s90\n"
  "s_cselect_b32 s91, 0x10000, s91\n"
  "s_cmp_eq_u32 s87, 18\n"
  "s_cselect_b64 s[92:93], s[16:17], s[92:93]\n"
  "s_cselect_b32 s90, 0, s90\n"
  "s_cselect_b32 s91, 0x10000, s91\n"
  "s_cmp_eq_u32 s87, 19\n"
  "s_cselect_b64 s[92:93], s[18:19], s[92:93]\n"
  "s_cselect_b32 s90, 0, s90\n"
  "s_cselect_b32 s91, 0x10000, s91\n"
  "s_cmp_eq_u32 s87, 20\n"
  "s_cselect_b64 s[92:93], s[10:11], s[92:93]\n"
  "s_cselect_b32 s90, 0, s90\n"
  "s_cselect_b32 s91, 0x10000, s91\n"
  "s_cmp_eq_u32 s87, 21\n"
  "s_cselect_b64 s[92:93], s[12:13], s[92:93]\n"
  "s_cselect_b32 s90, 0, s90\n"
  "s_cselect_b32 s91, 0x8000, s91\n"
  "v_and_b32 v215, 56, v255\n"
  "v_mul_lo_u32 v215, v215, s91\n"
  "v_add_u32 v215, s90, v215\n"
  "global_load_dword v216, v215, s[92:93] sc1 nt\n"
  "L_tlb_d%=:\n"
  "s_and_b32 s87, s34, 1\n"
  "s_cselect_b64 s[92:93], s[24:25], s[22:23]\n"
  "s_lshr_b32 s87, s34, 1\n"
  "s_lshl1_add_u32 s87, s49, s87\n"
  "s_mul_i32 s87, s87, 0xe00\n"
  "s_add_u32 s87, s87, 0x230000\n"
  "v_min_u32 v214, 27, v255\n"
  "v_lshl_add_u32 v214, v214, 7, s87\n"
  "s_cmp_lg_u32 s44, 3\n"
  "s_cbranch_scc1 L_st_nw%=\n"
  "global_load_dword v216, v214, s[92:93]\n"
  "v_add_u32 v214, 0xe0000, v214\n"
  "v_min_u32 v214, 0x6fff80, v214\n"
  "L_st_nw%=:\n"
  "s_branch L_next%=\n"
  "L_sregsx%=:\n"
  "s_lshl_b32 s83, s34, 13\n"
  "s_cmp_eq_u32 s44, 5\n"
  "s_cbranch_scc1 L_sx_fin%=\n"
  "s_cmp_ge_u32 s44, 3\n"
  "s_cbranch_scc1 L_sx_x%=\n"
  "s_mul_i32 s99, s34, 0x10000\n"
  "s_add_u32 s99, s99, s45\n"
  "s_add_u32 s52, s12, s99\n"
  "s_addc_u32 s53, s13, 0\n"
  "s_sub_u32 s54, 0x200000, s99\n"
  "s_bitcmp1_b32 6, s36\n"
  "s_cbranch_scc1 L_skip_r%=\n"
  "s_mov_b32 s87, 0x400\n"
  "v_mad_u32_u24 v252, v249, s87, v248\n"
  "v_xor_b32 v253, 64, v252\n"
  "s_mov_b32 s64, 0\n"
  "s_mov_b32 s65, 0x1c00\n"
  "s_mov_b32 s66, 0x3800\n"
  "s_mov_b32 s67, 0x5400\n"
  "s_mov_b32 s68, 0x8000\n"
  "s_mov_b32 s69, 0x9c00\n"
  "s_mov_b32 s70, 0xb800\n"
  "s_mov_b32 s71, 0xd400\n"
  "s_mov_b32 s81, 0x80\n"
  "s_setpc_b64 s[96:97]\n"
  "L_sx_x%=:\n"
  "s_mul_i32 s99, s34, 0x20000\n"
  "s_add_u32 s99, s99, s45\n"
  "s_add_u32 s52, s10, s99\n"
  "s_addc_u32 s53, s11, 0\n"
  "s_sub_u32 s54, 0x400000, s99\n"
  "s_bitcmp1_b32 8, s36\n"
  "s_cbranch_scc1 L_skip_r%=\n"
  "s_mov_b32 s87, 0x800\n"
  "v_mad_u32_u24 v252, v249, s87, v248\n"
  "v_xor_b32 v253, 64, v252\n"
  "s_mov_b32 s64, 0\n"
  "s_mov_b32 s65, 0x3c00\n"
  "s_mov_b32 s66, 0x7800\n"
  "s_mov_b32 s67, 0xb400\n"
  "s_mov_b32 s68, 0x10000\n"
  "s_mov_b32 s69, 0x13c00\n"
  "s_mov_b32 s70, 0x17800\n"
  "s_mov_b32 s71, 0x1b400\n"
  "s_mov_b32 s81, 0x80\n"
  "s_setpc_b64 s[96:97]\n"
  "L_sx_fin%=:\n"
  "s_lshl_b32 s87, s34, 13\n"
  "s_add_u32 s90, s87, s45\n"
  "s_add_u32 s52, s8, s90\n"
  "s_addc_u32 s53, s9, 0\n"
  "s_mov_b32 s54, 0x2000\n"
  "v_lshlrev_b32 v252, 4, v255\n"
  "v_lshlrev_b32 v253, 4, v255\n"
  "s_mov_b32 s64, 0\n"
  "s_mov_b32 s65, 0\n"
  "s_mov_b32 s66, 0\n"
  "s_mov_b32 s67, 0\n"
  "s_mov_b32 s68, 0x1000\n"
  "s_mov_b32 s69, 0x1000\n"
  "s_mov_b32 s70, 0x1000\n"
  "s_mov_b32 s71, 0x1000\n"
  "s_mov_b32 s81, 0x8000\n"
  "s_setpc_b64 s[96:97]\n"
  "L_sregsw%=:\n"
  "s_cmp_eq_u32 s44, 3\n"
  "s_cbranch_scc1 L_su_gu%=\n"
  "s_cmp_eq_u32 s44, 4\n"
  "s_cbranch_scc1 L_su_at%=\n"
  "s_cmp_eq_u32 s44, 5\n"
  "s_cbranch_scc1 L_su_fin%=\n"
  "s_cmp_eq_u32 s44, 1\n"
  "s_cselect_b64 s[56:57], s[16:17], s[18:19]\n"
  "s_lshl_b32 s91, s34, 7\n"
  "s_add_u32 s91, s91, s46\n"
  "s_add_u32 s56, s56, s91\n"
  "s_addc_u32 s57, s57, 0\n"
  "s_sub_u32 s58, 0x400000, s91\n"
  "s_bitcmp1_b32 6, s36\n"
  "s_cbranch_scc1 L_skip_r%=\n"
  "s_mov_b32 s90, 0x2000\n"
  "v_mad_u32_u24 v250, v249, s90, v247\n"
  "v_xor_b32 v251, 64, v250\n"
  "s_mov_b32 s72, 0\n"
  "s_mov_b32 s73, 0xfc00\n"
  "s_mov_b32 s74, 0x1f800\n"
  "s_mov_b32 s75, 0x2f400\n"
  "s_mov_b32 s76, 0x40000\n"
  "s_mov_b32 s77, 0x4fc00\n"
  "s_mov_b32 s78, 0x5f800\n"
  "s_mov_b32 s79, 0x6f400\n"
  "s_mov_b32 s80, 0x80000\n"
  "s_branch L_su_256%=\n"
  "L_su_gu%=:\n"
  "s_and_b32 s91, s34, 1\n"
  "s_cselect_b64 s[56:57], s[24:25], s[22:23]\n"
  "s_lshr_b32 s91, s34, 1\n"
  "s_lshl_b32 s91, s91, 7\n"
  "s_add_u32 s91, s91, s46\n"
  "s_add_u32 s56, s56, s91\n"
  "s_addc_u32 s57, s57, 0\n"
  "s_sub_u32 s58, 0x700000, s91\n"
  "s_bitcmp1_b32 8, s36\n"
  "s_cbranch_scc1 L_skip_r%=\n"
  "s_mov_b32 s90, 0x1c00\n"
  "v_mad_u32_u24 v250, v249, s90, v247\n"
  "v_xor_b32 v251, 64, v250\n"
  "s_mov_b32 s72, 0\n"
  "s_mov_b32 s73, 0xdc00\n"
  "s_mov_b32 s74, 0x1b800\n"
  "s_mov_b32 s75, 0x29400\n"
  "s_mov_b32 s76, 0x38000\n"
  "s_mov_b32 s77, 0x45c00\n"
  "s_mov_b32 s78, 0x53800\n"
  "s_mov_b32 s79, 0x61400\n"
  "s_mov_b32 s80, 0x70000\n"
  "L_su_256%=:\n"
  "s_lshl_b32 s84, s34, 13\n"
  "s_add_u32 s84, s84, 0x10000\n"
  "s_mov_b32 s29, 0x1000\n"
  "s_mov_b32 s95, -1\n"
  "s_setpc_b64 s[96:97]\n"
  "L_su_at%=:\n"
  "s_mov_b64 s[56:57], s[14:15]\n"
  "s_and_b32 s91, s34, 1\n"
  "s_lshl_b32 s91, s91, 18\n"
  "s_lshr_b32 s99, s34, 1\n"
  "s_lshl_b32 s99, s99, 7\n"
  "s_add_u32 s91, s91, s99\n"
  "s_add_u32 s91, s91, s46\n"
  "s_add_u32 s56, s56, s91\n"
  "s_addc_u32 s57, s57, 0\n"
  "s_sub_u32 s58, 0x800000, s91\n"
  "s_mov_b32 s90, 0x2000\n"
  "v_mad_u32_u24 v250, v249, s90, v247\n"
  "v_xor_b32 v251, 64, v250\n"
  "s_mov_b32 s72, 0\n"
  "s_mov_b32 s73, 0xfc00\n"
  "s_mov_b32 s74, 0x1f800\n"
  "s_mov_b32 s75, 0x2f400\n"
  "s_mov_b32 s76, 0x80000\n"
  "s_mov_b32 s77, 0x8fc00\n"
  "s_mov_b32 s78, 0x9f800\n"
  "s_mov_b32 s79, 0xaf400\n"
  "s_mov_b32 s80, 0x80000\n"
  "s_branch L_su_128%=\n"
  "L_su_fin%=:\n"
  "s_and_b32 s91, s34, 1\n"
  "s_lshl_b32 s91, s91, 16\n"
  "s_lshr_b32 s99, s34, 1\n"
  "s_lshl_b32 s99, s99, 7\n"
  "s_add_u32 s91, s91, s99\n"
  "s_bitcmp1_b32 s48, 2\n"
  "s_cselect_b64 s[56:57], s[20:21], s[26:27]\n"
  "s_add_u32 s90, s91, s46\n"
  "s_add_u32 s56, s56, s90\n"
  "s_addc_u32 s57, s57, 0\n"
  "s_mov_b32 s58, 0x2f880\n"
  "v_and_b32 v250, 32, v255\n"
  "v_lshlrev_b32 v250, 1, v250\n"
  "v_xor_b32 v250, v250, v247\n"
  "v_lshl_add_u32 v250, v249, 11, v250\n"
  "v_mov_b32 v251, v250\n"
  "s_mov_b32 s72, 0\n"
  "s_mov_b32 s73, 0x3c00\n"
  "s_mov_b32 s74, 0x7800\n"
  "s_mov_b32 s75, 0xb400\n"
  "s_mov_b32 s76, 0x20000\n"
  "s_mov_b32 s77, 0x23c00\n"
  "s_mov_b32 s78, 0x27800\n"
  "s_mov_b32 s79, 0x2b400\n"
  "s_mov_b32 s80, 0x20000\n"
  "L_su_128%=:\n"
  "s_lshl_b32 s84, s34, 12\n"
  "s_add_u32 s84, s84, 0x18000\n"
  "s_mov_b32 s29, 0x4000\n"
  "s_mov_b32 s95, 0\n"
  "s_setpc_b64 s[96:97]\n"
  "L_issue%=:\n"
  "s_cmp_ge_u32 s44, 4\n"
  "s_cbranch_scc1 L_is_128%=\n"
  "s_mov_b32 s94, 0\n"
  "s_add_u32 m0, s84, 0\n"
  "s_nop 0\n"
  "buffer_load_dwordx4 v250, s[56:59], s72 offen lds\n"
  "buffer_load_dwordx4 v251, s[56:59], s73 offen offset:1024 lds\n"
  "buffer_load_dwordx4 v250, s[56:59], s74 offen offset:2048 lds\n"
  "buffer_load_dwordx4 v251, s[56:59], s75 offen offset:3072 lds\n"
  "s_add_u32 m0, m0, s29\n"
  "s_nop 0\n"
  "buffer_load_dwordx4 v250, s[56:59], s76 offen lds\n"
  "buffer_load_dwordx4 v251, s[56:59], s77 offen offset:1024 lds\n"
  "buffer_load_dwordx4 v250, s[56:59], s78 offen offset:2048 lds\n"
  "buffer_load_dwordx4 v251, s[56:59], s79 offen offset:3072 lds\n"
  "s_add_u32 m0, s83, 0\n"
  "s_nop 0\n"
  "buffer_load_dwordx4 v252, s[52:55], s64 offen sc1 lds\n"
  "buffer_load_dwordx4 v253, s[52:55], s65 offen offset:1024 sc1 lds\n"
  "buffer_load_dwordx4 v252, s[52:55], s66 offen offset:2048 sc1 lds\n"
  "buffer_load_dwordx4 v253, s[52:55], s67 offen offset:3072 sc1 lds\n"
  "s_add_u32 m0, s83, 0x1000\n"
  "s_nop 0\n"
  "buffer_load_dwordx4 v252, s[52:55], s68 offen sc1 lds\n"
  "buffer_load_dwordx4 v253, s[52:55], s69 offen offset:1024 sc1 lds\n"
  "buffer_load_dwordx4 v252, s[52:55], s70 offen offset:2048 sc1 lds\n"
  "buffer_load_dwordx4 v253, s[52:55], s71 offen offset:3072 sc1 lds\n"
  "s_add_u32 s52, s52, s81\n"
  "s_addc_u32 s53, s53, 0\n"
  "s_add_u32 s56, s56, s80\n"
  "s_addc_u32 s57, s57, 0\n"
  "s_add_u32 m0, s84, 0x8000\n"
  "s_nop 0\n"
  "buffer_load_dwordx4 v250, s[56:59], s72 offen lds\n"
  "buffer_load_dwordx4 v251, s[56:59], s73 offen offset:1024 lds\n"
  "buffer_load_dwordx4 v250, s[56:59], s74 offen offset:2048 lds\n"
  "buffer_load_dwordx4 v251, s[56:59], s75 offen offset:3072 lds\n"
  "s_add_u32 m0, m0, s29\n"
  "s_nop 0\n"
  "buffer_load_dwordx4 v250, s[56:59], s76 offen lds\n"
  "buffer_load_dwordx4 v251, s[56:59], s77 offen offset:1024 lds\n"
  "buffer_load_dwordx4 v250, s[56:59], s78 offen offset:2048 lds\n"
  "buffer_load_dwordx4 v251, s[56:59], s79 offen offset:3072 lds\n"
  "s_add_u32 m0, s83, 0x8000\n"
  "s_nop 0\n"
  "buffer_load_dwordx4 v252, s[52:55], s64 offen sc1 lds\n"
  "buffer_load_dwordx4 v253, s[52:55], s65 offen offset:1024 sc1 lds\n"
  "buffer_load_dwordx4 v252, s[52:55], s66 offen offset:2048 sc1 lds\n"
  "buffer_load_dwordx4 v253, s[52:55], s67 offen offset:3072 sc1 lds\n"
  "s_add_u32 m0, s83, 0x9000\n"
  "s_nop 0\n"
  "buffer_load_dwordx4 v252, s[52:55], s68 offen sc1 lds\n"
  "buffer_load_dwordx4 v253, s[52:55], s69 offen offset:1024 sc1 lds\n"
  "buffer_load_dwordx4 v252, s[52:55], s70 offen offset:2048 sc1 lds\n"
  "buffer_load_dwordx4 v253, s[52:55], s71 offen offset:3072 sc1 lds\n"
  "s_add_u32 s56, s56, s80\n"
  "s_addc_u32 s57, s57, 0\n"
  "s_add_u32 m0, s84, 0x10000\n"
  "s_nop 0\n"
  "buffer_load_dwordx4 v250, s[56:59], s72 offen lds\n"
  "buffer_load_dwordx4 v251, s[56:59], s73 offen offset:1024 lds\n"
  "buffer_load_dwordx4 v250, s[56:59], s74 offen offset:2048 lds\n"
  "buffer_load_dwordx4 v251, s[56:59], s75 offen offset:3072 lds\n"
  "s_add_u32 m0, m0, s29\n"
  "s_nop 0\n"
  "buffer_load_dwordx4 v250, s[56:59], s76 offen lds\n"
  "buffer_load_dwordx4 v251, s[56:59], s77 offen offset:1024 lds\n"
  "buffer_load_dwordx4 v250, s[56:59], s78 offen offset:2048 lds\n"
  "buffer_load_dwordx4 v251, s[56:59], s79 offen offset:3072 lds\n"
  "s_setpc_b64 s[96:97]\n"
  "L_is_128%=:\n"
  "s_add_u32 m0, s84, 0\n"
  "s_nop 0\n"
  "buffer_load_dwordx4 v250, s[56:59], s72 offen lds\n"
  "buffer_load_dwordx4 v251, s[56:59], s73 offen offset:1024 lds\n"
  "buffer_load_dwordx4 v250, s[56:59], s74 offen offset:2048 lds\n"
  "buffer_load_dwordx4 v251, s[56:59], s75 offen offset:3072 lds\n"
  "s_add_u32 m0, s83, 0\n"
  "s_nop 0\n"
  "buffer_load_dwordx4 v252, s[52:55], s64 offen sc1 lds\n"
  "buffer_load_dwordx4 v253, s[52:55], s65 offen offset:1024 sc1 lds\n"
  "buffer_load_dwordx4 v252, s[52:55], s66 offen offset:2048 sc1 lds\n"
  "buffer_load_dwordx4 v253, s[52:55], s67 offen offset:3072 sc1 lds\n"
  "s_add_u32 m0, s83, 0x1000\n"
  "s_nop 0\n"
  "buffer_load_dwordx4 v252, s[52:55], s68 offen sc1 lds\n"
  "buffer_load_dwordx4 v253, s[52:55], s69 offen offset:1024 sc1 lds\n"
  "buffer_load_dwordx4 v252, s[52:55], s70 offen offset:2048 sc1 lds\n"
  "buffer_load_dwordx4 v253, s[52:55], s71 offen offset:3072 sc1 lds\n"
  "s_add_u32 s52, s52, s81\n"
  "s_addc_u32 s53, s53, 0\n"
  "s_add_u32 s56, s56, s80\n"
  "s_addc_u32 s57, s57, 0\n"
  "s_add_u32 m0, s84, 0x4000\n"
  "s_nop 0\n"
  "buffer_load_dwordx4 v250, s[56:59], s72 offen lds\n"
  "buffer_load_dwordx4 v251, s[56:59], s73 offen offset:1024 lds\n"
  "buffer_load_dwordx4 v250, s[56:59], s74 offen offset:2048 lds\n"
  "buffer_load_dwordx4 v251, s[56:59], s75 offen offset:3072 lds\n"
  "s_add_u32 m0, s83, 0x8000\n"
  "s_nop 0\n"
  "buffer_load_dwordx4 v252, s[52:55], s64 offen sc1 lds\n"
  "buffer_load_dwordx4 v253, s[52:55], s65 offen offset:1024 sc1 lds\n"
  "buffer_load_dwordx4 v252, s[52:55], s66 offen offset:2048 sc1 lds\n"
  "buffer_load_dwordx4 v253, s[52:55], s67 offen offset:3072 sc1 lds\n"
  "s_add_u32 m0, s83, 0x9000\n"
  "s_nop 0\n"
  "buffer_load_dwordx4 v252, s[52:55], s68 offen sc1 lds\n"
  "buffer_load_dwordx4 v253, s[52:55], s69 offen offset:1024 sc1 lds\n"
  "buffer_load_dwordx4 v252, s[52:55], s70 offen offset:2048 sc1 lds\n"
  "buffer_load_dwordx4 v253, s[52:55], s71 offen offset:3072 sc1 lds\n"
  "s_add_u32 s52, s52, s81\n"
  "s_addc_u32 s53, s53, 0\n"
  "s_add_u32 s56, s56, s80\n"
  "s_addc_u32 s57, s57, 0\n"
  "s_add_u32 m0, s84, 0x8000\n"
  "s_nop 0\n"
  "buffer_load_dwordx4 v250, s[56:59], s72 offen lds\n"
  "buffer_load_dwordx4 v251, s[56:59], s73 offen offset:1024 lds\n"
  "buffer_load_dwordx4 v250, s[56:59], s74 offen offset:2048 lds\n"
  "buffer_load_dwordx4 v251, s[56:59], s75 offen offset:3072 lds\n"
  "s_add_u32 m0, s83, 0x10000\n"
  "s_nop 0\n"
  "buffer_load_dwordx4 v252, s[52:55], s64 offen sc1 lds\n"
  "buffer_load_dwordx4 v253, s[52:55], s65 offen offset:1024 sc1 lds\n"
  "buffer_load_dwordx4 v252, s[52:55], s66 offen offset:2048 sc1 lds\n"
  "buffer_load_dwordx4 v253, s[52:55], s67 offen offset:3072 sc1 lds\n"
  "s_setpc_b64 s[96:97]\n"
  "L_skip%=:\n"
  "s_add_u32 s52, s52, s81\n"
  "s_addc_u32 s53, s53, 0\n"
  "s_add_u32 s56, s56, s80\n"
  "s_addc_u32 s57, s57, 0\n"
  "s_add_u32 s56, s56, s80\n"
  "s_addc_u32 s57, s57, 0\n"
  "s_mov_b32 s94, 0\n"
  "s_cmp_lt_u32 s44, 4\n"
  "s_cbranch_scc1 L_skip_r%=\n"
  "s_add_u32 s52, s52, s81\n"
  "s_addc_u32 s53, s53, 0\n"
  "L_skip_r%=:\n"
  "s_setpc_b64 s[96:97]\n"
  "L_next%=:\n"
  "s_mov_b64 s[36:37], s[44:45]\n"
  "s_mov_b64 s[38:39], s[46:47]\n"
  "s_mov_b64 s[40:41], s[48:49]\n"
  "s_mov_b64 s[42:43], s[50:51]\n"
  "s_add_u32 s32, s32, 32\n"
  "s_addc_u32 s33, s33, 0\n"
  "s_load_dwordx8 s[44:51], s[32:33], 0x0\n"
  "s_cmp_eq_u32 s36, 1\n"
  "s_cbranch_scc1 L_u_k%=\n"
  "s_cmp_eq_u32 s36, 2\n"
  "s_cbranch_scc1 L_u_v%=\n"
  "s_cmp_eq_u32 s36, 3\n"
  "s_cbranch_scc1 L_u_gu%=\n"
  "s_cmp_eq_u32 s36, 4\n"
  "s_cbranch_scc1 L_u_at%=\n"
  "s_cmp_eq_u32 s36, 5\n"
  "s_cbranch_scc1 L_u_fin%=\n"
  "s_branch L_end%=\n"
  "L_u_k%=:\n"
  "s_mov_b32 s82, 4\n"
  "s_mov_b32 s100, 1\n"
  "s_branch L_g256%=\n"
  "L_u_gu%=:\n"
  "s_mov_b32 s82, 8\n"
  "s_mov_b32 s100, 0\n"
  "L_g256%=:\n"
  "s_mov_b32 s85, 4\n"
  "s_cmp_eq_u32 s101, 2\n"
  "s_cbranch_scc1 L_g_fast%=\n"
  "s_lshr_b32 s87, s34, 1\n"
  "s_lshl_b32 s87, s87, 14\n"
  "s_and_b32 s90, s34, 1\n"
  "s_lshl_b32 s90, s90, 14\n"
  "v_and_b32 v128, 15, v255\n"
  "v_lshrrev_b32 v129, 4, v255\n"
  "v_bfe_u32 v130, v255, 1, 3\n"
  "v_xor_b32 v130, v130, v129\n"
  "v_lshlrev_b32 v130, 4, v130\n"
  "v_lshl_add_u32 v130, v128, 7, v130\n"
  "v_add_u32 v130, s87, v130\n"
  "v_bfe_u32 v131, v255, 2, 2\n"
  "v_and_b32 v132, 3, v255\n"
  "v_lshlrev_b32 v132, 3, v132\n"
  "v_lshl_add_u32 v132, v131, 7, v132\n"
  "v_lshl_add_u32 v132, v129, 10, v132\n"
  "v_bfe_u32 v133, v255, 3, 2\n"
  "v_lshl_add_u32 v132, v133, 5, v132\n"
  "v_add_u32 v132, s90, v132\n"
  "v_add_u32 v228, 0, v130\n"
  "v_xor_b32 v229, 64, v228\n"
  "v_add_u32 v234, 0x8000, v130\n"
  "v_xor_b32 v235, 64, v234\n"
  "v_add_u32 v236, 0x10000, v132\n"
  "v_xor_b32 v237, 32, v236\n"
  "v_xor_b32 v238, 64, v236\n"
  "v_xor_b32 v239, 0x60, v236\n"
  "v_add_u32 v230, s94, v236\n"
  "v_add_u32 v231, s94, v237\n"
  "v_add_u32 v232, s94, v238\n"
  "v_add_u32 v233, s94, v239\n"
  "s_cmp_eq_u32 s101, 2\n"
  "s_cbranch_scc1 L_ew_g%=\n"
  "s_cmp_eq_u32 s101, 3\n"
  "s_cbranch_scc1 L_ewb_g%=\n"
  "s_waitcnt vmcnt(32)\n"
  "L_ewb_g%=:\n"
  "s_barrier\n"
  "L_ew_g%=:\n"
  "ds_read_b128 a[0:3], v228\n"
  "ds_read_b128 a[4:7], v229\n"
  "ds_read_b64_tr_b16 a[64:65], v230\n"
  "ds_read_b64_tr_b16 a[66:67], v230 offset:512\n"
  "ds_read_b64_tr_b16 a[68:69], v230 offset:4096\n"
  "ds_read_b64_tr_b16 a[70:71], v230 offset:4608\n"
  "ds_read_b64_tr_b16 a[72:73], v231\n"
  "ds_read_b64_tr_b16 a[74:75], v231 offset:512\n"
  "ds_read_b64_tr_b16 a[76:77], v231 offset:4096\n"
  "ds_read_b64_tr_b16 a[78:79], v231 offset:4608\n"
  "ds_read_b64_tr_b16 a[80:81], v232\n"
  "ds_read_b64_tr_b16 a[82:83], v232 offset:512\n"
  "ds_read_b64_tr_b16 a[84:85], v232 offset:4096\n"
  "ds_read_b64_tr_b16 a[86:87], v232 offset:4608\n"
  "ds_read_b64_tr_b16 a[88:89], v233\n"
  "ds_read_b64_tr_b16 a[90:91], v233 offset:512\n"
  "ds_read_b64_tr_b16 a[92:93], v233 offset:4096\n"
  "ds_read_b64_tr_b16 a[94:95], v233 offset:4608\n"
  "ds_read_b128 a[8:11], v228 offset:2048\n"
  "ds_read_b128 a[12:15], v229 offset:2048\n"
  "ds_read_b128 a[16:19], v228 offset:4096\n"
  "ds_read_b128 a[20:23], v229 offset:4096\n"
  "ds_read_b128 a[24:27], v228 offset:6144\n"
  "ds_read_b128 a[28:31], v229 offset:6144\n"
  "s_waitcnt lgkmcnt(0)\n"
  "L_g_fast%=:\n"
  "v_mfma_f32_16x16x32_bf16 a[128:131], a[64:67], a[0:3], 0\n"
  "ds_read_b64_tr_b16 a[96:97], v230 offset:8192\n"
  "v_mfma_f32_16x16x32_bf16 a[128:131], a[68:71], a[4:7], a[128:131]\n"
  "ds_read_b64_tr_b16 a[98:99], v230 offset:8704\n"
  "v_mfma_f32_16x16x32_bf16 a[132:135], a[76:79], a[4:7], 0\n"
  "ds_read_b64_tr_b16 a[100:101], v230 offset:12288\n"
  "v_mfma_f32_16x16x32_bf16 a[132:135], a[72:75], a[0:3], a[132:135]\n"
  "ds_read_b64_tr_b16 a[102:103], v230 offset:12800\n"
  "v_mfma_f32_16x16x32_bf16 a[136:139], a[80:83], a[0:3], 0\n"
  "ds_read_b64_tr_b16 a[104:105], v231 offset:8192\n"
  "v_mfma_f32_16x16x32_bf16 a[136:139], a[84:87], a[4:7], a[136:139]\n"
  "ds_read_b64_tr_b16 a[106:107], v231 offset:8704\n"
  "v_mfma_f32_16x16x32_bf16 a[140:143], a[92:95], a[4:7], 0\n"
  "ds_read_b64_tr_b16 a[108:109], v231 offset:12288\n"
  "v_mfma_f32_16x16x32_bf16 a[140:143], a[88:91], a[0:3], a[140:143]\n"
  "ds_read_b64_tr_b16 a[110:111], v231 offset:12800\n"
  "v_mfma_f32_16x16x32_bf16 a[156:159], a[88:91], a[8:11], 0\n"
  "ds_read_b64_tr_b16 a[112:113], v232 offset:8192\n"
  "v_mfma_f32_16x16x32_bf16 a[156:159], a[92:95], a[12:15], a[156:159]\n"
  "ds_read_b64_tr_b16 a[114:115], v232 offset:8704\n"
  "v_mfma_f32_16x16x32_bf16 a[152:155], a[84:87], a[12:15], 0\n"
  "ds_read_b64_tr_b16 a[116:117], v232 offset:12288\n"
  "v_mfma_f32_16x16x32_bf16 a[152:155], a[80:83], a[8:11], a[152:155]\n"
  "ds_read_b64_tr_b16 a[118:119], v232 offset:12800\n"
  "v_mfma_f32_16x16x32_bf16 a[148:151], a[72:75], a[8:11], 0\n"
  "ds_read_b64_tr_b16 a[120:121], v233 offset:8192\n"
  "v_mfma_f32_16x16x32_bf16 a[148:151], a[76:79], a[12:15], a[148:151]\n"
  "ds_read_b64_tr_b16 a[122:123], v233 offset:8704\n"
  "v_mfma_f32_16x16x32_bf16 a[144:147], a[68:71], a[12:15], 0\n"
  "ds_read_b64_tr_b16 a[124:125], v233 offset:12288\n"
  "v_mfma_f32_16x16x32_bf16 a[144:147], a[64:67], a[8:11], a[144:147]\n"
  "ds_read_b64_tr_b16 a[126:127], v233 offset:12800\n"
  "v_mfma_f32_16x16x32_bf16 a[160:163], a[64:67], a[16:19], 0\n"
  "ds_read_b128 a[32:35], v228 offset:8192\n"
  "v_mfma_f32_16x16x32_bf16 a[160:163], a[68:71], a[20:23], a[160:163]\n"
  "ds_read_b128 a[36:39], v229 offset:8192\n"
  "v_mfma_f32_16x16x32_bf16 a[164:167], a[76:79], a[20:23], 0\n"
  "ds_read_b128 a[40:43], v228 offset:10240\n"
  "v_mfma_f32_16x16x32_bf16 a[164:167], a[72:75], a[16:19], a[164:167]\n"
  "ds_read_b128 a[44:47], v229 offset:10240\n"
  "v_mfma_f32_16x16x32_bf16 a[168:171], a[80:83], a[16:19], 0\n"
  "ds_read_b128 a[48:51], v228 offset:12288\n"
  "v_mfma_f32_16x16x32_bf16 a[168:171], a[84:87], a[20:23], a[168:171]\n"
  "ds_read_b128 a[52:55], v229 offset:12288\n"
  "v_mfma_f32_16x16x32_bf16 a[172:175], a[92:95], a[20:23], 0\n"
  "ds_read_b128 a[56:59], v228 offset:14336\n"
  "v_mfma_f32_16x16x32_bf16 a[172:175], a[88:91], a[16:19], a[172:175]\n"
  "ds_read_b128 a[60:63], v229 offset:14336\n"
  "v_mfma_f32_16x16x32_bf16 a[188:191], a[88:91], a[24:27], 0\n"
  "s_add_u32 s52, s52, s81\n"
  "s_addc_u32 s53, s53, 0\n"
  "v_mfma_f32_16x16x32_bf16 a[188:191], a[92:95], a[28:31], a[188:191]\n"
  "s_add_u32 s56, s56, s80\n"
  "s_addc_u32 s57, s57, 0\n"
  "v_mfma_f32_16x16x32_bf16 a[184:187], a[84:87], a[28:31], 0\n"
  "v_mfma_f32_16x16x32_bf16 a[184:187], a[80:83], a[24:27], a[184:187]\n"
  "v_mfma_f32_16x16x32_bf16 a[180:183], a[72:75], a[24:27], 0\n"
  "s_waitcnt lgkmcnt(0)\n"
  "s_barrier\n"
  "v_mfma_f32_16x16x32_bf16 a[180:183], a[76:79], a[28:31], a[180:183]\n"
  "s_add_u32 m0, s83, 0\n"
  "v_mfma_f32_16x16x32_bf16 a[176:179], a[68:71], a[28:31], 0\n"
  "buffer_load_dwordx4 v252, s[52:55], s64 offen sc1 lds\n"
  "v_mfma_f32_16x16x32_bf16 a[176:179], a[64:67], a[24:27], a[176:179]\n"
  "v_mfma_f32_16x16x32_bf16 v[0:3], a[96:99], a[0:3], 0\n"
  "v_mfma_f32_16x16x32_bf16 v[0:3], a[100:103], a[4:7], v[0:3]\n"
  "v_mfma_f32_16x16x32_bf16 v[4:7], a[108:111], a[4:7], 0\n"
  "buffer_load_dwordx4 v253, s[52:55], s65 offen offset:1024 sc1 lds\n"
  "v_mfma_f32_16x16x32_bf16 v[4:7], a[104:107], a[0:3], v[4:7]\n"
  "v_mfma_f32_16x16x32_bf16 v[8:11], a[112:115], a[0:3], 0\n"
  "v_mfma_f32_16x16x32_bf16 v[8:11], a[116:119], a[4:7], v[8:11]\n"
  "v_mfma_f32_16x16x32_bf16 v[12:15], a[124:127], a[4:7], 0\n"
  "buffer_load_dwordx4 v252, s[52:55], s66 offen offset:2048 sc1 lds\n"
  "v_mfma_f32_16x16x32_bf16 v[12:15], a[120:123], a[0:3], v[12:15]\n"
  "v_mfma_f32_16x16x32_bf16 v[28:31], a[120:123], a[8:11], 0\n"
  "s_and_b32 s28, s94, s95\n"
  "v_mfma_f32_16x16x32_bf16 v[28:31], a[124:127], a[12:15], v[28:31]\n"
  "s_add_u32 s94, s94, 0x8000\n"
  "v_mfma_f32_16x16x32_bf16 v[24:27], a[116:119], a[12:15], 0\n"
  "buffer_load_dwordx4 v253, s[52:55], s67 offen offset:3072 sc1 lds\n"
  "v_mfma_f32_16x16x32_bf16 v[24:27], a[112:115], a[8:11], v[24:27]\n"
  "s_cmp_eq_u32 s94, 0x18000\n"
  "s_cselect_b32 s94, 0, s94\n"
  "v_mfma_f32_16x16x32_bf16 v[20:23], a[104:107], a[8:11], 0\n"
  "v_mfma_f32_16x16x32_bf16 v[20:23], a[108:111], a[12:15], v[20:23]\n"
  "s_add_u32 m0, s83, 0x1000\n"
  "v_mfma_f32_16x16x32_bf16 v[16:19], a[100:103], a[12:15], 0\n"
  "buffer_load_dwordx4 v252, s[52:55], s68 offen sc1 lds\n"
  "v_mfma_f32_16x16x32_bf16 v[16:19], a[96:99], a[8:11], v[16:19]\n"
  "v_add_u32 v230, s94, v236\n"
  "v_mfma_f32_16x16x32_bf16 v[32:35], a[96:99], a[16:19], 0\n"
  "v_mfma_f32_16x16x32_bf16 v[32:35], a[100:103], a[20:23], v[32:35]\n"
  "v_add_u32 v231, s94, v237\n"
  "v_mfma_f32_16x16x32_bf16 v[36:39], a[108:111], a[20:23], 0\n"
  "buffer_load_dwordx4 v253, s[52:55], s69 offen offset:1024 sc1 lds\n"
  "v_mfma_f32_16x16x32_bf16 v[36:39], a[104:107], a[16:19], v[36:39]\n"
  "v_mfma_f32_16x16x32_bf16 v[40:43], a[112:115], a[16:19], 0\n"
  "v_add_u32 v232, s94, v238\n"
  "v_mfma_f32_16x16x32_bf16 v[40:43], a[116:119], a[20:23], v[40:43]\n"
  "v_add_u32 v233, s94, v239\n"
  "v_mfma_f32_16x16x32_bf16 v[44:47], a[124:127], a[20:23], 0\n"
  "buffer_load_dwordx4 v252, s[52:55], s70 offen offset:2048 sc1 lds\n"
  "v_mfma_f32_16x16x32_bf16 v[44:47], a[120:123], a[16:19], v[44:47]\n"
  "v_mfma_f32_16x16x32_bf16 v[60:63], a[120:123], a[24:27], 0\n"
  "v_mfma_f32_16x16x32_bf16 v[60:63], a[124:127], a[28:31], v[60:63]\n"
  "v_mfma_f32_16x16x32_bf16 v[56:59], a[116:119], a[28:31], 0\n"
  "buffer_load_dwordx4 v253, s[52:55], s71 offen offset:3072 sc1 lds\n"
  "v_mfma_f32_16x16x32_bf16 v[56:59], a[112:115], a[24:27], v[56:59]\n"
  "v_mfma_f32_16x16x32_bf16 v[52:55], a[104:107], a[24:27], 0\n"
  "v_mfma_f32_16x16x32_bf16 v[52:55], a[108:111], a[28:31], v[52:55]\n"
  "s_add_u32 m0, s84, s28\n"
  "v_mfma_f32_16x16x32_bf16 v[48:51], a[100:103], a[28:31], 0\n"
  "buffer_load_dwordx4 v250, s[56:59], s72 offen lds\n"
  "v_mfma_f32_16x16x32_bf16 v[48:51], a[96:99], a[24:27], v[48:51]\n"
  "v_mfma_f32_16x16x32_bf16 v[64:67], a[96:99], a[32:35], 0\n"
  "v_mfma_f32_16x16x32_bf16 v[64:67], a[100:103], a[36:39], v[64:67]\n"
  "v_mfma_f32_16x16x32_bf16 v[68:71], a[108:111], a[36:39], 0\n"
  "buffer_load_dwordx4 v251, s[56:59], s73 offen offset:1024 lds\n"
  "v_mfma_f32_16x16x32_bf16 v[68:71], a[104:107], a[32:35], v[68:71]\n"
  "v_mfma_f32_16x16x32_bf16 v[72:75], a[112:115], a[32:35], 0\n"
  "v_mfma_f32_16x16x32_bf16 v[72:75], a[116:119], a[36:39], v[72:75]\n"
  "v_mfma_f32_16x16x32_bf16 v[76:79], a[124:127], a[36:39], 0\n"
  "buffer_load_dwordx4 v250, s[56:59], s74 offen offset:2048 lds\n"
  "v_mfma_f32_16x16x32_bf16 v[76:79], a[120:123], a[32:35], v[76:79]\n"
  "v_mfma_f32_16x16x32_bf16 v[92:95], a[120:123], a[40:43], 0\n"
  "v_mfma_f32_16x16x32_bf16 v[92:95], a[124:127], a[44:47], v[92:95]\n"
  "v_mfma_f32_16x16x32_bf16 v[88:91], a[116:119], a[44:47], 0\n"
  "buffer_load_dwordx4 v251, s[56:59], s75 offen offset:3072 lds\n"
  "v_mfma_f32_16x16x32_bf16 v[88:91], a[112:115], a[40:43], v[88:91]\n"
  "v_mfma_f32_16x16x32_bf16 v[84:87], a[104:107], a[40:43], 0\n"
  "v_mfma_f32_16x16x32_bf16 v[84:87], a[108:111], a[44:47], v[84:87]\n"
  "s_add_u32 m0, m0, s29\n"
  "v_mfma_f32_16x16x32_bf16 v[80:83], a[100:103], a[44:47], 0\n"
  "buffer_load_dwordx4 v250, s[56:59], s76 offen lds\n"
  "v_mfma_f32_16x16x32_bf16 v[80:83], a[96:99], a[40:43], v[80:83]\n"
  "v_mfma_f32_16x16x32_bf16 v[96:99], a[96:99], a[48:51], 0\n"
  "v_mfma_f32_16x16x32_bf16 v[96:99], a[100:103], a[52:55], v[96:99]\n"
  "v_mfma_f32_16x16x32_bf16 v[100:103], a[108:111], a[52:55], 0\n"
  "buffer_load_dwordx4 v251, s[56:59], s77 offen offset:1024 lds\n"
  "v_mfma_f32_16x16x32_bf16 v[100:103], a[104:107], a[48:51], v[100:103]\n"
  "v_mfma_f32_16x16x32_bf16 v[104:107], a[112:115], a[48:51], 0\n"
  "v_mfma_f32_16x16x32_bf16 v[104:107], a[116:119], a[52:55], v[104:107]\n"
  "v_mfma_f32_16x16x32_bf16 v[108:111], a[124:127], a[52:55], 0\n"
  "buffer_load_dwordx4 v250, s[56:59], s78 offen offset:2048 lds\n"
  "v_mfma_f32_16x16x32_bf16 v[108:111], a[120:123], a[48:51], v[108:111]\n"
  "v_mfma_f32_16x16x32_bf16 v[124:127], a[120:123], a[56:59], 0\n"
  "v_mfma_f32_16x16x32_bf16 v[124:127], a[124:127], a[60:63], v[124:127]\n"
  "v_mfma_f32_16x16x32_bf16 v[120:123], a[116:119], a[60:63], 0\n"
  "buffer_load_dwordx4 v251, s[56:59], s79 offen offset:3072 lds\n"
  "v_mfma_f32_16x16x32_bf16 v[120:123], a[112:115], a[56:59], v[120:123]\n"
  "v_mfma_f32_16x16x32_bf16 v[116:119], a[104:107], a[56:59], 0\n"
  "v_mfma_f32_16x16x32_bf16 v[116:119], a[108:111], a[60:63], v[116:119]\n"
  "v_mfma_f32_16x16x32_bf16 v[112:115], a[100:103], a[60:63], 0\n"
  "v_mfma_f32_16x16x32_bf16 v[112:115], a[96:99], a[56:59], v[112:115]\n"
  "s_waitcnt vmcnt(24)\n"
  "s_barrier\n"
  "v_mfma_f32_16x16x32_bf16 a[192:195], a[64:67], a[32:35], 0\n"
  "v_mfma_f32_16x16x32_bf16 a[192:195], a[68:71], a[36:39], a[192:195]\n"
  "ds_read_b128 a[0:3], v234\n"
  "v_mfma_f32_16x16x32_bf16 a[196:199], a[76:79], a[36:39], 0\n"
  "ds_read_b128 a[4:7], v235\n"
  "v_mfma_f32_16x16x32_bf16 a[196:199], a[72:75], a[32:35], a[196:199]\n"
  "ds_read_b64_tr_b16 a[96:97], v230 offset:8192\n"
  "v_mfma_f32_16x16x32_bf16 a[200:203], a[80:83], a[32:35], 0\n"
  "ds_read_b64_tr_b16 a[98:99], v230 offset:8704\n"
  "v_mfma_f32_16x16x32_bf16 a[200:203], a[84:87], a[36:39], a[200:203]\n"
  "ds_read_b64_tr_b16 a[100:101], v230 offset:12288\n"
  "v_mfma_f32_16x16x32_bf16 a[204:207], a[92:95], a[36:39], 0\n"
  "ds_read_b64_tr_b16 a[102:103], v230 offset:12800\n"
  "v_mfma_f32_16x16x32_bf16 a[204:207], a[88:91], a[32:35], a[204:207]\n"
  "ds_read_b64_tr_b16 a[104:105], v231 offset:8192\n"
  "v_mfma_f32_16x16x32_bf16 a[220:223], a[88:91], a[40:43], 0\n"
  "ds_read_b64_tr_b16 a[106:107], v231 offset:8704\n"
  "v_mfma_f32_16x16x32_bf16 a[220:223], a[92:95], a[44:47], a[220:223]\n"
  "ds_read_b64_tr_b16 a[108:109], v231 offset:12288\n"
  "v_mfma_f32_16x16x32_bf16 a[216:219], a[84:87], a[44:47], 0\n"
  "ds_read_b64_tr_b16 a[110:111], v231 offset:12800\n"
  "v_mfma_f32_16x16x32_bf16 a[216:219], a[80:83], a[40:43], a[216:219]\n"
  "ds_read_b64_tr_b16 a[112:113], v232 offset:8192\n"
  "v_mfma_f32_16x16x32_bf16 a[212:215], a[72:75], a[40:43], 0\n"
  "ds_read_b64_tr_b16 a[114:115], v232 offset:8704\n"
  "v_mfma_f32_16x16x32_bf16 a[212:215], a[76:79], a[44:47], a[212:215]\n"
  "ds_read_b64_tr_b16 a[116:117], v232 offset:12288\n"
  "v_mfma_f32_16x16x32_bf16 a[208:211], a[68:71], a[44:47], 0\n"
  "ds_read_b64_tr_b16 a[118:119], v232 offset:12800\n"
  "v_mfma_f32_16x16x32_bf16 a[208:211], a[64:67], a[40:43], a[208:211]\n"
  "ds_read_b64_tr_b16 a[120:121], v233 offset:8192\n"
  "v_mfma_f32_16x16x32_bf16 a[224:227], a[64:67], a[48:51], 0\n"
  "ds_read_b64_tr_b16 a[122:123], v233 offset:8704\n"
  "v_mfma_f32_16x16x32_bf16 a[224:227], a[68:71], a[52:55], a[224:227]\n"
  "ds_read_b64_tr_b16 a[124:125], v233 offset:12288\n"
  "v_mfma_f32_16x16x32_bf16 a[228:231], a[76:79], a[52:55], 0\n"
  "ds_read_b64_tr_b16 a[126:127], v233 offset:12800\n"
  "v_mfma_f32_16x16x32_bf16 a[228:231], a[72:75], a[48:51], a[228:231]\n"
  "ds_read_b128 a[8:11], v234 offset:2048\n"
  "v_mfma_f32_16x16x32_bf16 a[232:235], a[80:83], a[48:51], 0\n"
  "ds_read_b128 a[12:15], v235 offset:2048\n"
  "v_mfma_f32_16x16x32_bf16 a[232:235], a[84:87], a[52:55], a[232:235]\n"
  "ds_read_b128 a[16:19], v234 offset:4096\n"
  "v_mfma_f32_16x16x32_bf16 a[236:239], a[92:95], a[52:55], 0\n"
  "ds_read_b128 a[20:23], v235 offset:4096\n"
  "v_mfma_f32_16x16x32_bf16 a[236:239], a[88:91], a[48:51], a[236:239]\n"
  "ds_read_b128 a[24:27], v234 offset:6144\n"
  "v_mfma_f32_16x16x32_bf16 a[252:255], a[88:91], a[56:59], 0\n"
  "ds_read_b128 a[28:31], v235 offset:6144\n"
  "v_mfma_f32_16x16x32_bf16 a[252:255], a[92:95], a[60:63], a[252:255]\n"
  "v_mfma_f32_16x16x32_bf16 a[248:251], a[84:87], a[60:63], 0\n"
  "v_mfma_f32_16x16x32_bf16 a[248:251], a[80:83], a[56:59], a[248:251]\n"
  "v_mfma_f32_16x16x32_bf16 a[244:247], a[72:75], a[56:59], 0\n"
  "v_mfma_f32_16x16x32_bf16 a[244:247], a[76:79], a[60:63], a[244:247]\n"
  "v_mfma_f32_16x16x32_bf16 a[240:243], a[68:71], a[60:63], 0\n"
  "v_mfma_f32_16x16x32_bf16 a[240:243], a[64:67], a[56:59], a[240:243]\n"
  "s_waitcnt lgkmcnt(0)\n"
  "s_branch L_g_mid%=\n"
  "L_g_loop%=:\n"
  "v_mfma_f32_16x16x32_bf16 a[128:131], a[64:67], a[0:3], a[128:131]\n"
  "ds_read_b64_tr_b16 a[96:97], v230 offset:8192\n"
  "v_mfma_f32_16x16x32_bf16 a[128:131], a[68:71], a[4:7], a[128:131]\n"
  "ds_read_b64_tr_b16 a[98:99], v230 offset:8704\n"
  "v_mfma_f32_16x16x32_bf16 a[132:135], a[76:79], a[4:7], a[132:135]\n"
  "ds_read_b64_tr_b16 a[100:101], v230 offset:12288\n"
  "v_mfma_f32_16x16x32_bf16 a[132:135], a[72:75], a[0:3], a[132:135]\n"
  "ds_read_b64_tr_b16 a[102:103], v230 offset:12800\n"
  "v_mfma_f32_16x16x32_bf16 a[136:139], a[80:83], a[0:3], a[136:139]\n"
  "ds_read_b64_tr_b16 a[104:105], v231 offset:8192\n"
  "v_mfma_f32_16x16x32_bf16 a[136:139], a[84:87], a[4:7], a[136:139]\n"
  "ds_read_b64_tr_b16 a[106:107], v231 offset:8704\n"
  "v_mfma_f32_16x16x32_bf16 a[140:143], a[92:95], a[4:7], a[140:143]\n"
  "ds_read_b64_tr_b16 a[108:109], v231 offset:12288\n"
  "v_mfma_f32_16x16x32_bf16 a[140:143], a[88:91], a[0:3], a[140:143]\n"
  "ds_read_b64_tr_b16 a[110:111], v231 offset:12800\n"
  "v_mfma_f32_16x16x32_bf16 a[156:159], a[88:91], a[8:11], a[156:159]\n"
  "ds_read_b64_tr_b16 a[112:113], v232 offset:8192\n"
  "v_mfma_f32_16x16x32_bf16 a[156:159], a[92:95], a[12:15], a[156:159]\n"
  "ds_read_b64_tr_b16 a[114:115], v232 offset:8704\n"
  "v_mfma_f32_16x16x32_bf16 a[152:155], a[84:87], a[12:15], a[152:155]\n"
  "ds_read_b64_tr_b16 a[116:117], v232 offset:12288\n"
  "v_mfma_f32_16x16x32_bf16 a[152:155], a[80:83], a[8:11], a[152:155]\n"
  "ds_read_b64_tr_b16 a[118:119], v232 offset:12800\n"
  "v_mfma_f32_16x16x32_bf16 a[148:151], a[72:75], a[8:11], a[148:151]\n"
  "ds_read_b64_tr_b16 a[120:121], v233 offset:8192\n"
  "v_mfma_f32_16x16x32_bf16 a[148:151], a[76:79], a[12:15], a[148:151]\n"
  "ds_read_b64_tr_b16 a[122:123], v233 offset:8704\n"
  "v_mfma_f32_16x16x32_bf16 a[144:147], a[68:71], a[12:15], a[144:147]\n"
  "ds_read_b64_tr_b16 a[124:125], v233 offset:12288\n"
  "v_mfma_f32_16x16x32_bf16 a[144:147], a[64:67], a[8:11], a[144:147]\n"
  "ds_read_b64_tr_b16 a[126:127], v233 offset:12800\n"
  "v_mfma_f32_16x16x32_bf16 a[160:163], a[64:67], a[16:19], a[160:163]\n"
  "ds_read_b128 a[32:35], v228 offset:8192\n"
  "v_mfma_f32_16x16x32_bf16 a[160:163], a[68:71], a[20:23], a[160:163]\n"
  "ds_read_b128 a[36:39], v229 offset:8192\n"
  "v_mfma_f32_16x16x32_bf16 a[164:167], a[76:79], a[20:23], a[164:167]\n"
  "ds_read_b128 a[40:43], v228 offset:10240\n"
  "v_mfma_f32_16x16x32_bf16 a[164:167], a[72:75], a[16:19], a[164:167]\n"
  "ds_read_b128 a[44:47], v229 offset:10240\n"
  "v_mfma_f32_16x16x32_bf16 a[168:171], a[80:83], a[16:19], a[168:171]\n"
  "ds_read_b128 a[48:51], v228 offset:12288\n"
  "v_mfma_f32_16x16x32_bf16 a[168:171], a[84:87], a[20:23], a[168:171]\n"
  "ds_read_b128 a[52:55], v229 offset:12288\n"
  "v_mfma_f32_16x16x32_bf16 a[172:175], a[92:95], a[20:23], a[172:175]\n"
  "ds_read_b128 a[56:59], v228 offset:14336\n"
  "v_mfma_f32_16x16x32_bf16 a[172:175], a[88:91], a[16:19], a[172:175]\n"
  "ds_read_b128 a[60:63], v229 offset:14336\n"
  "v_mfma_f32_16x16x32_bf16 a[188:191], a[88:91], a[24:27], a[188:191]\n"
  "s_add_u32 s52, s52, s81\n"
  "s_addc_u32 s53, s53, 0\n"
  "v_mfma_f32_16x16x32_bf16 a[188:191], a[92:95], a[28:31], a[188:191]\n"
  "s_add_u32 s56, s56, s80\n"
  "s_addc_u32 s57, s57, 0\n"
  "v_mfma_f32_16x16x32_bf16 a[184:187], a[84:87], a[28:31], a[184:187]\n"
  "v_mfma_f32_16x16x32_bf16 a[184:187], a[80:83], a[24:27], a[184:187]\n"
  "s_cmp_eq_u32 s82, 1\n"
  "v_mfma_f32_16x16x32_bf16 a[180:183], a[72:75], a[24:27], a[180:183]\n"
  "s_waitcnt lgkmcnt(0)\n"
  "s_barrier\n"
  "v_mfma_f32_16x16x32_bf16 a[180:183], a[76:79], a[28:31], a[180:183]\n"
  "s_cbranch_scc1 L_gev0_g%=\n"
  "L_gevr0_g%=:\n"
  "s_add_u32 m0, s83, 0\n"
  "v_mfma_f32_16x16x32_bf16 a[176:179], a[68:71], a[28:31], a[176:179]\n"
  "buffer_load_dwordx4 v252, s[52:55], s64 offen sc1 lds\n"
  "v_mfma_f32_16x16x32_bf16 a[176:179], a[64:67], a[24:27], a[176:179]\n"
  "v_mfma_f32_16x16x32_bf16 v[0:3], a[96:99], a[0:3], v[0:3]\n"
  "v_mfma_f32_16x16x32_bf16 v[0:3], a[100:103], a[4:7], v[0:3]\n"
  "v_mfma_f32_16x16x32_bf16 v[4:7], a[108:111], a[4:7], v[4:7]\n"
  "buffer_load_dwordx4 v253, s[52:55], s65 offen offset:1024 sc1 lds\n"
  "v_mfma_f32_16x16x32_bf16 v[4:7], a[104:107], a[0:3], v[4:7]\n"
  "v_mfma_f32_16x16x32_bf16 v[8:11], a[112:115], a[0:3], v[8:11]\n"
  "v_mfma_f32_16x16x32_bf16 v[8:11], a[116:119], a[4:7], v[8:11]\n"
  "v_mfma_f32_16x16x32_bf16 v[12:15], a[124:127], a[4:7], v[12:15]\n"
  "buffer_load_dwordx4 v252, s[52:55], s66 offen offset:2048 sc1 lds\n"
  "v_mfma_f32_16x16x32_bf16 v[12:15], a[120:123], a[0:3], v[12:15]\n"
  "v_mfma_f32_16x16x32_bf16 v[28:31], a[120:123], a[8:11], v[28:31]\n"
  "s_and_b32 s28, s94, s95\n"
  "v_mfma_f32_16x16x32_bf16 v[28:31], a[124:127], a[12:15], v[28:31]\n"
  "s_add_u32 s94, s94, 0x8000\n"
  "v_mfma_f32_16x16x32_bf16 v[24:27], a[116:119], a[12:15], v[24:27]\n"
  "buffer_load_dwordx4 v253, s[52:55], s67 offen offset:3072 sc1 lds\n"
  "v_mfma_f32_16x16x32_bf16 v[24:27], a[112:115], a[8:11], v[24:27]\n"
  "s_cmp_eq_u32 s94, 0x18000\n"
  "s_cselect_b32 s94, 0, s94\n"
  "v_mfma_f32_16x16x32_bf16 v[20:23], a[104:107], a[8:11], v[20:23]\n"
  "v_mfma_f32_16x16x32_bf16 v[20:23], a[108:111], a[12:15], v[20:23]\n"
  "s_add_u32 m0, s83, 0x1000\n"
  "v_mfma_f32_16x16x32_bf16 v[16:19], a[100:103], a[12:15], v[16:19]\n"
  "buffer_load_dwordx4 v252, s[52:55], s68 offen sc1 lds\n"
  "v_mfma_f32_16x16x32_bf16 v[16:19], a[96:99], a[8:11], v[16:19]\n"
  "v_add_u32 v230, s94, v236\n"
  "v_mfma_f32_16x16x32_bf16 v[32:35], a[96:99], a[16:19], v[32:35]\n"
  "v_mfma_f32_16x16x32_bf16 v[32:35], a[100:103], a[20:23], v[32:35]\n"
  "v_add_u32 v231, s94, v237\n"
  "v_mfma_f32_16x16x32_bf16 v[36:39], a[108:111], a[20:23], v[36:39]\n"
  "buffer_load_dwordx4 v253, s[52:55], s69 offen offset:1024 sc1 lds\n"
  "v_mfma_f32_16x16x32_bf16 v[36:39], a[104:107], a[16:19], v[36:39]\n"
  "v_mfma_f32_16x16x32_bf16 v[40:43], a[112:115], a[16:19], v[40:43]\n"
  "v_add_u32 v232, s94, v238\n"
  "v_mfma_f32_16x16x32_bf16 v[40:43], a[116:119], a[20:23], v[40:43]\n"
  "v_add_u32 v233, s94, v239\n"
  "v_mfma_f32_16x16x32_bf16 v[44:47], a[124:127], a[20:23], v[44:47]\n"
  "buffer_load_dwordx4 v252, s[52:55], s70 offen offset:2048 sc1 lds\n"
  "v_mfma_f32_16x16x32_bf16 v[44:47], a[120:123], a[16:19], v[44:47]\n"
  "v_mfma_f32_16x16x32_bf16 v[60:63], a[120:123], a[24:27], v[60:63]\n"
  "v_mfma_f32_16x16x32_bf16 v[60:63], a[124:127], a[28:31], v[60:63]\n"
  "v_mfma_f32_16x16x32_bf16 v[56:59], a[116:119], a[28:31], v[56:59]\n"
  "buffer_load_dwordx4 v253, s[52:55], s71 offen offset:3072 sc1 lds\n"
  "v_mfma_f32_16x16x32_bf16 v[56:59], a[112:115], a[24:27], v[56:59]\n"
  "v_mfma_f32_16x16x32_bf16 v[52:55], a[104:107], a[24:27], v[52:55]\n"
  "v_mfma_f32_16x16x32_bf16 v[52:55], a[108:111], a[28:31], v[52:55]\n"
  "s_add_u32 m0, s84, s28\n"
  "v_mfma_f32_16x16x32_bf16 v[48:51], a[100:103], a[28:31], v[48:51]\n"
  "buffer_load_dwordx4 v250, s[56:59], s72 offen lds\n"
  "v_mfma_f32_16x16x32_bf16 v[48:51], a[96:99], a[24:27], v[48:51]\n"
  "v_mfma_f32_16x16x32_bf16 v[64:67], a[96:99], a[32:35], v[64:67]\n"
  "v_mfma_f32_16x16x32_bf16 v[64:67], a[100:103], a[36:39], v[64:67]\n"
  "v_mfma_f32_16x16x32_bf16 v[68:71], a[108:111], a[36:39], v[68:71]\n"
  "buffer_load_dwordx4 v251, s[56:59], s73 offen offset:1024 lds\n"
  "v_mfma_f32_16x16x32_bf16 v[68:71], a[104:107], a[32:35], v[68:71]\n"
  "v_mfma_f32_16x16x32_bf16 v[72:75], a[112:115], a[32:35], v[72:75]\n"
  "v_mfma_f32_16x16x32_bf16 v[72:75], a[116:119], a[36:39], v[72:75]\n"
  "v_mfma_f32_16x16x32_bf16 v[76:79], a[124:127], a[36:39], v[76:79]\n"
  "buffer_load_dwordx4 v250, s[56:59], s74 offen offset:2048 lds\n"
  "v_mfma_f32_16x16x32_bf16 v[76:79], a[120:123], a[32:35], v[76:79]\n"
  "v_mfma_f32_16x16x32_bf16 v[92:95], a[120:123], a[40:43], v[92:95]\n"
  "v_mfma_f32_16x16x32_bf16 v[92:95], a[124:127], a[44:47], v[92:95]\n"
  "v_mfma_f32_16x16x32_bf16 v[88:91], a[116:119], a[44:47], v[88:91]\n"
  "buffer_load_dwordx4 v251, s[56:59], s75 offen offset:3072 lds\n"
  "v_mfma_f32_16x16x32_bf16 v[88:91], a[112:115], a[40:43], v[88:91]\n"
  "v_mfma_f32_16x16x32_bf16 v[84:87], a[104:107], a[40:43], v[84:87]\n"
  "v_mfma_f32_16x16x32_bf16 v[84:87], a[108:111], a[44:47], v[84:87]\n"
  "s_add_u32 m0, m0, s29\n"
  "v_mfma_f32_16x16x32_bf16 v[80:83], a[100:103], a[44:47], v[80:83]\n"
  "buffer_load_dwordx4 v250, s[56:59], s76 offen lds\n"
  "v_mfma_f32_16x16x32_bf16 v[80:83], a[96:99], a[40:43], v[80:83]\n"
  "v_mfma_f32_16x16x32_bf16 v[96:99], a[96:99], a[48:51], v[96:99]\n"
  "v_mfma_f32_16x16x32_bf16 v[96:99], a[100:103], a[52:55], v[96:99]\n"
  "v_mfma_f32_16x16x32_bf16 v[100:103], a[108:111], a[52:55], v[100:103]\n"
  "buffer_load_dwordx4 v251, s[56:59], s77 offen offset:1024 lds\n"
  "v_mfma_f32_16x16x32_bf16 v[100:103], a[104:107], a[48:51], v[100:103]\n"
  "v_mfma_f32_16x16x32_bf16 v[104:107], a[112:115], a[48:51], v[104:107]\n"
  "v_mfma_f32_16x16x32_bf16 v[104:107], a[116:119], a[52:55], v[104:107]\n"
  "v_mfma_f32_16x16x32_bf16 v[108:111], a[124:127], a[52:55], v[108:111]\n"
  "buffer_load_dwordx4 v250, s[56:59], s78 offen offset:2048 lds\n"
  "v_mfma_f32_16x16x32_bf16 v[108:111], a[120:123], a[48:51], v[108:111]\n"
  "v_mfma_f32_16x16x32_bf16 v[124:127], a[120:123], a[56:59], v[124:127]\n"
  "v_mfma_f32_16x16x32_bf16 v[124:127], a[124:127], a[60:63], v[124:127]\n"
  "v_mfma_f32_16x16x32_bf16 v[120:123], a[116:119], a[60:63], v[120:123]\n"
  "buffer_load_dwordx4 v251, s[56:59], s79 offen offset:3072 lds\n"
  "v_mfma_f32_16x16x32_bf16 v[120:123], a[112:115], a[56:59], v[120:123]\n"
  "v_mfma_f32_16x16x32_bf16 v[116:119], a[104:107], a[56:59], v[116:119]\n"
  "v_mfma_f32_16x16x32_bf16 v[116:119], a[108:111], a[60:63], v[116:119]\n"
  "v_mfma_f32_16x16x32_bf16 v[112:115], a[100:103], a[60:63], v[112:115]\n"
  "v_mfma_f32_16x16x32_bf16 v[112:115], a[96:99], a[56:59], v[112:115]\n"
  "s_waitcnt vmcnt(24)\n"
  "s_barrier\n"
  "v_mfma_f32_16x16x32_bf16 a[192:195], a[64:67], a[32:35], a[192:195]\n"
  "v_mfma_f32_16x16x32_bf16 a[192:195], a[68:71], a[36:39], a[192:195]\n"
  "ds_read_b128 a[0:3], v234\n"
  "v_mfma_f32_16x16x32_bf16 a[196:199], a[76:79], a[36:39], a[196:199]\n"
  "ds_read_b128 a[4:7], v235\n"
  "v_mfma_f32_16x16x32_bf16 a[196:199], a[72:75], a[32:35], a[196:199]\n"
  "ds_read_b64_tr_b16 a[96:97], v230 offset:8192\n"
  "v_mfma_f32_16x16x32_bf16 a[200:203], a[80:83], a[32:35], a[200:203]\n"
  "ds_read_b64_tr_b16 a[98:99], v230 offset:8704\n"
  "v_mfma_f32_16x16x32_bf16 a[200:203], a[84:87], a[36:39], a[200:203]\n"
  "ds_read_b64_tr_b16 a[100:101], v230 offset:12288\n"
  "v_mfma_f32_16x16x32_bf16 a[204:207], a[92:95], a[36:39], a[204:207]\n"
  "ds_read_b64_tr_b16 a[102:103], v230 offset:12800\n"
  "v_mfma_f32_16x16x32_bf16 a[204:207], a[88:91], a[32:35], a[204:207]\n"
  "ds_read_b64_tr_b16 a[104:105], v231 offset:8192\n"
  "v_mfma_f32_16x16x32_bf16 a[220:223], a[88:91], a[40:43], a[220:223]\n"
  "ds_read_b64_tr_b16 a[106:107], v231 offset:8704\n"
  "v_mfma_f32_16x16x32_bf16 a[220:223], a[92:95], a[44:47], a[220:223]\n"
  "ds_read_b64_tr_b16 a[108:109], v231 offset:12288\n"
  "v_mfma_f32_16x16x32_bf16 a[216:219], a[84:87], a[44:47], a[216:219]\n"
  "ds_read_b64_tr_b16 a[110:111], v231 offset:12800\n"
  "v_mfma_f32_16x16x32_bf16 a[216:219], a[80:83], a[40:43], a[216:219]\n"
  "ds_read_b64_tr_b16 a[112:113], v232 offset:8192\n"
  "v_mfma_f32_16x16x32_bf16 a[212:215], a[72:75], a[40:43], a[212:215]\n"
  "ds_read_b64_tr_b16 a[114:115], v232 offset:8704\n"
  "v_mfma_f32_16x16x32_bf16 a[212:215], a[76:79], a[44:47], a[212:215]\n"
  "ds_read_b64_tr_b16 a[116:117], v232 offset:12288\n"
  "v_mfma_f32_16x16x32_bf16 a[208:211], a[68:71], a[44:47], a[208:211]\n"
  "ds_read_b64_tr_b16 a[118:119], v232 offset:12800\n"
  "v_mfma_f32_16x16x32_bf16 a[208:211], a[64:67], a[40:43], a[208:211]\n"
  "ds_read_b64_tr_b16 a[120:121], v233 offset:8192\n"
  "v_mfma_f32_16x16x32_bf16 a[224:227], a[64:67], a[48:51], a[224:227]\n"
  "ds_read_b64_tr_b16 a[122:123], v233 offset:8704\n"
  "v_mfma_f32_16x16x32_bf16 a[224:227], a[68:71], a[52:55], a[224:227]\n"
  "ds_read_b64_tr_b16 a[124:125], v233 offset:12288\n"
  "v_mfma_f32_16x16x32_bf16 a[228:231], a[76:79], a[52:55], a[228:231]\n"
  "ds_read_b64_tr_b16 a[126:127], v233 offset:12800\n"
  "v_mfma_f32_16x16x32_bf16 a[228:231], a[72:75], a[48:51], a[228:231]\n"
  "ds_read_b128 a[8:11], v234 offset:2048\n"
  "v_mfma_f32_16x16x32_bf16 a[232:235], a[80:83], a[48:51], a[232:235]\n"
  "ds_read_b128 a[12:15], v235 offset:2048\n"
  "v_mfma_f32_16x16x32_bf16 a[232:235], a[84:87], a[52:55], a[232:235]\n"
  "ds_read_b128 a[16:19], v234 offset:4096\n"
  "v_mfma_f32_16x16x32_bf16 a[236:239], a[92:95], a[52:55], a[236:239]\n"
  "ds_read_b128 a[20:23], v235 offset:4096\n"
  "v_mfma_f32_16x16x32_bf16 a[236:239], a[88:91], a[48:51], a[236:239]\n"
  "ds_read_b128 a[24:27], v234 offset:6144\n"
  "v_mfma_f32_16x16x32_bf16 a[252:255], a[88:91], a[56:59], a[252:255]\n"
  "ds_read_b128 a[28:31], v235 offset:6144\n"
  "v_mfma_f32_16x16x32_bf16 a[252:255], a[92:95], a[60:63], a[252:255]\n"
  "v_mfma_f32_16x16x32_bf16 a[248:251], a[84:87], a[60:63], a[248:251]\n"
  "v_mfma_f32_16x16x32_bf16 a[248:251], a[80:83], a[56:59], a[248:251]\n"
  "v_mfma_f32_16x16x32_bf16 a[244:247], a[72:75], a[56:59], a[244:247]\n"
  "v_mfma_f32_16x16x32_bf16 a[244:247], a[76:79], a[60:63], a[244:247]\n"
  "v_mfma_f32_16x16x32_bf16 a[240:243], a[68:71], a[60:63], a[240:243]\n"
  "v_mfma_f32_16x16x32_bf16 a[240:243], a[64:67], a[56:59], a[240:243]\n"
  "s_waitcnt lgkmcnt(0)\n"
  "s_cmp_eq_u32 s82, s100\n"
  "s_cbranch_scc1 L_g_last%=\n"
  "L_g_mid%=:\n"
  "v_mfma_f32_16x16x32_bf16 v[0:3], a[96:99], a[0:3], v[0:3]\n"
  "ds_read_b64_tr_b16 a[64:65], v230\n"
  "v_mfma_f32_16x16x32_bf16 v[0:3], a[100:103], a[4:7], v[0:3]\n"
  "ds_read_b64_tr_b16 a[66:67], v230 offset:512\n"
  "v_mfma_f32_16x16x32_bf16 v[4:7], a[108:111], a[4:7], v[4:7]\n"
  "ds_read_b64_tr_b16 a[68:69], v230 offset:4096\n"
  "v_mfma_f32_16x16x32_bf16 v[4:7], a[104:107], a[0:3], v[4:7]\n"
  "ds_read_b64_tr_b16 a[70:71], v230 offset:4608\n"
  "v_mfma_f32_16x16x32_bf16 v[8:11], a[112:115], a[0:3], v[8:11]\n"
  "ds_read_b64_tr_b16 a[72:73], v231\n"
  "v_mfma_f32_16x16x32_bf16 v[8:11], a[116:119], a[4:7], v[8:11]\n"
  "ds_read_b64_tr_b16 a[74:75], v231 offset:512\n"
  "v_mfma_f32_16x16x32_bf16 v[12:15], a[124:127], a[4:7], v[12:15]\n"
  "ds_read_b64_tr_b16 a[76:77], v231 offset:4096\n"
  "v_mfma_f32_16x16x32_bf16 v[12:15], a[120:123], a[0:3], v[12:15]\n"
  "ds_read_b64_tr_b16 a[78:79], v231 offset:4608\n"
  "v_mfma_f32_16x16x32_bf16 v[28:31], a[120:123], a[8:11], v[28:31]\n"
  "ds_read_b64_tr_b16 a[80:81], v232\n"
  "v_mfma_f32_16x16x32_bf16 v[28:31], a[124:127], a[12:15], v[28:31]\n"
  "ds_read_b64_tr_b16 a[82:83], v232 offset:512\n"
  "v_mfma_f32_16x16x32_bf16 v[24:27], a[116:119], a[12:15], v[24:27]\n"
  "ds_read_b64_tr_b16 a[84:85], v232 offset:4096\n"
  "v_mfma_f32_16x16x32_bf16 v[24:27], a[112:115], a[8:11], v[24:27]\n"
  "ds_read_b64_tr_b16 a[86:87], v232 offset:4608\n"
  "v_mfma_f32_16x16x32_bf16 v[20:23], a[104:107], a[8:11], v[20:23]\n"
  "ds_read_b64_tr_b16 a[88:89], v233\n"
  "v_mfma_f32_16x16x32_bf16 v[20:23], a[108:111], a[12:15], v[20:23]\n"
  "ds_read_b64_tr_b16 a[90:91], v233 offset:512\n"
  "v_mfma_f32_16x16x32_bf16 v[16:19], a[100:103], a[12:15], v[16:19]\n"
  "ds_read_b64_tr_b16 a[92:93], v233 offset:4096\n"
  "v_mfma_f32_16x16x32_bf16 v[16:19], a[96:99], a[8:11], v[16:19]\n"
  "ds_read_b64_tr_b16 a[94:95], v233 offset:4608\n"
  "v_mfma_f32_16x16x32_bf16 v[32:35], a[96:99], a[16:19], v[32:35]\n"
  "ds_read_b128 a[32:35], v234 offset:8192\n"
  "v_mfma_f32_16x16x32_bf16 v[32:35], a[100:103], a[20:23], v[32:35]\n"
  "ds_read_b128 a[36:39], v235 offset:8192\n"
  "v_mfma_f32_16x16x32_bf16 v[36:39], a[108:111], a[20:23], v[36:39]\n"
  "ds_read_b128 a[40:43], v234 offset:10240\n"
  "v_mfma_f32_16x16x32_bf16 v[36:39], a[104:107], a[16:19], v[36:39]\n"
  "ds_read_b128 a[44:47], v235 offset:10240\n"
  "v_mfma_f32_16x16x32_bf16 v[40:43], a[112:115], a[16:19], v[40:43]\n"
  "ds_read_b128 a[48:51], v234 offset:12288\n"
  "v_mfma_f32_16x16x32_bf16 v[40:43], a[116:119], a[20:23], v[40:43]\n"
  "ds_read_b128 a[52:55], v235 offset:12288\n"
  "v_mfma_f32_16x16x32_bf16 v[44:47], a[124:127], a[20:23], v[44:47]\n"
  "ds_read_b128 a[56:59], v234 offset:14336\n"
  "v_mfma_f32_16x16x32_bf16 v[44:47], a[120:123], a[16:19], v[44:47]\n"
  "ds_read_b128 a[60:63], v235 offset:14336\n"
  "v_mfma_f32_16x16x32_bf16 v[60:63], a[120:123], a[24:27], v[60:63]\n"
  "s_add_u32 s52, s52, s81\n"
  "s_addc_u32 s53, s53, 0\n"
  "v_mfma_f32_16x16x32_bf16 v[60:63], a[124:127], a[28:31], v[60:63]\n"
  "s_add_u32 s56, s56, s80\n"
  "s_addc_u32 s57, s57, 0\n"
  "v_mfma_f32_16x16x32_bf16 v[56:59], a[116:119], a[28:31], v[56:59]\n"
  "v_mfma_f32_16x16x32_bf16 v[56:59], a[112:115], a[24:27], v[56:59]\n"
  "s_bitcmp1_b32 s85, s82\n"
  "v_mfma_f32_16x16x32_bf16 v[52:55], a[104:107], a[24:27], v[52:55]\n"
  "s_waitcnt lgkmcnt(0)\n"
  "s_barrier\n"
  "v_mfma_f32_16x16x32_bf16 v[52:55], a[108:111], a[28:31], v[52:55]\n"
  "s_cbranch_scc1 L_gev1_g%=\n"
  "L_gevr1_g%=:\n"
  "s_add_u32 m0, s83, 0x8000\n"
  "v_mfma_f32_16x16x32_bf16 v[48:51], a[100:103], a[28:31], v[48:51]\n"
  "buffer_load_dwordx4 v252, s[52:55], s64 offen sc1 lds\n"
  "v_mfma_f32_16x16x32_bf16 v[48:51], a[96:99], a[24:27], v[48:51]\n"
  "v_mfma_f32_16x16x32_bf16 a[128:131], a[64:67], a[0:3], a[128:131]\n"
  "v_mfma_f32_16x16x32_bf16 a[128:131], a[68:71], a[4:7], a[128:131]\n"
  "v_mfma_f32_16x16x32_bf16 a[132:135], a[76:79], a[4:7], a[132:135]\n"
  "buffer_load_dwordx4 v253, s[52:55], s65 offen offset:1024 sc1 lds\n"
  "v_mfma_f32_16x16x32_bf16 a[132:135], a[72:75], a[0:3], a[132:135]\n"
  "v_mfma_f32_16x16x32_bf16 a[136:139], a[80:83], a[0:3], a[136:139]\n"
  "v_mfma_f32_16x16x32_bf16 a[136:139], a[84:87], a[4:7], a[136:139]\n"
  "v_mfma_f32_16x16x32_bf16 a[140:143], a[92:95], a[4:7], a[140:143]\n"
  "buffer_load_dwordx4 v252, s[52:55], s66 offen offset:2048 sc1 lds\n"
  "v_mfma_f32_16x16x32_bf16 a[140:143], a[88:91], a[0:3], a[140:143]\n"
  "v_mfma_f32_16x16x32_bf16 a[156:159], a[88:91], a[8:11], a[156:159]\n"
  "s_and_b32 s28, s94, s95\n"
  "v_mfma_f32_16x16x32_bf16 a[156:159], a[92:95], a[12:15], a[156:159]\n"
  "s_add_u32 s94, s94, 0x8000\n"
  "v_mfma_f32_16x16x32_bf16 a[152:155], a[84:87], a[12:15], a[152:155]\n"
  "buffer_load_dwordx4 v253, s[52:55], s67 offen offset:3072 sc1 lds\n"
  "v_mfma_f32_16x16x32_bf16 a[152:155], a[80:83], a[8:11], a[152:155]\n"
  "s_cmp_eq_u32 s94, 0x18000\n"
  "s_cselect_b32 s94, 0, s94\n"
  "v_mfma_f32_16x16x32_bf16 a[148:151], a[72:75], a[8:11], a[148:151]\n"
  "v_mfma_f32_16x16x32_bf16 a[148:151], a[76:79], a[12:15], a[148:151]\n"
  "s_add_u32 m0, s83, 0x9000\n"
  "v_mfma_f32_16x16x32_bf16 a[144:147], a[68:71], a[12:15], a[144:147]\n"
  "buffer_load_dwordx4 v252, s[52:55], s68 offen sc1 lds\n"
  "v_mfma_f32_16x16x32_bf16 a[144:147], a[64:67], a[8:11], a[144:147]\n"
  "v_add_u32 v230, s94, v236\n"
  "v_mfma_f32_16x16x32_bf16 a[160:163], a[64:67], a[16:19], a[160:163]\n"
  "v_mfma_f32_16x16x32_bf16 a[160:163], a[68:71], a[20:23], a[160:163]\n"
  "v_add_u32 v231, s94, v237\n"
  "v_mfma_f32_16x16x32_bf16 a[164:167], a[76:79], a[20:23], a[164:167]\n"
  "buffer_load_dwordx4 v253, s[52:55], s69 offen offset:1024 sc1 lds\n"
  "v_mfma_f32_16x16x32_bf16 a[164:167], a[72:75], a[16:19], a[164:167]\n"
  "v_mfma_f32_16x16x32_bf16 a[168:171], a[80:83], a[16:19], a[168:171]\n"
  "v_add_u32 v232, s94, v238\n"
  "v_mfma_f32_16x16x32_bf16 a[168:171], a[84:87], a[20:23], a[168:171]\n"
  "v_add_u32 v233, s94, v239\n"
  "v_mfma_f32_16x16x32_bf16 a[172:175], a[92:95], a[20:23], a[172:175]\n"
  "buffer_load_dwordx4 v252, s[52:55], s70 offen offset:2048 sc1 lds\n"
  "v_mfma_f32_16x16x32_bf16 a[172:175], a[88:91], a[16:19], a[172:175]\n"
  "v_mfma_f32_16x16x32_bf16 a[188:191], a[88:91], a[24:27], a[188:191]\n"
  "v_mfma_f32_16x16x32_bf16 a[188:191], a[92:95], a[28:31], a[188:191]\n"
  "v_mfma_f32_16x16x32_bf16 a[184:187], a[84:87], a[28:31], a[184:187]\n"
  "buffer_load_dwordx4 v253, s[52:55], s71 offen offset:3072 sc1 lds\n"
  "v_mfma_f32_16x16x32_bf16 a[184:187], a[80:83], a[24:27], a[184:187]\n"
  "v_mfma_f32_16x16x32_bf16 a[180:183], a[72:75], a[24:27], a[180:183]\n"
  "v_mfma_f32_16x16x32_bf16 a[180:183], a[76:79], a[28:31], a[180:183]\n"
  "s_add_u32 m0, s84, s28\n"
  "v_mfma_f32_16x16x32_bf16 a[176:179], a[68:71], a[28:31], a[176:179]\n"
  "buffer_load_dwordx4 v250, s[56:59], s72 offen lds\n"
  "v_mfma_f32_16x16x32_bf16 a[176:179], a[64:67], a[24:27], a[176:179]\n"
  "v_mfma_f32_16x16x32_bf16 a[192:195], a[64:67], a[32:35], a[192:195]\n"
  "v_mfma_f32_16x16x32_bf16 a[192:195], a[68:71], a[36:39], a[192:195]\n"
  "v_mfma_f32_16x16x32_bf16 a[196:199], a[76:79], a[36:39], a[196:199]\n"
  "buffer_load_dwordx4 v251, s[56:59], s73 offen offset:1024 lds\n"
  "v_mfma_f32_16x16x32_bf16 a[196:199], a[72:75], a[32:35], a[196:199]\n"
  "v_mfma_f32_16x16x32_bf16 a[200:203], a[80:83], a[32:35], a[200:203]\n"
  "v_mfma_f32_16x16x32_bf16 a[200:203], a[84:87], a[36:39], a[200:203]\n"
  "v_mfma_f32_16x16x32_bf16 a[204:207], a[92:95], a[36:39], a[204:207]\n"
  "buffer_load_dwordx4 v250, s[56:59], s74 offen offset:2048 lds\n"
  "v_mfma_f32_16x16x32_bf16 a[204:207], a[88:91], a[32:35], a[204:207]\n"
  "v_mfma_f32_16x16x32_bf16 a[220:223], a[88:91], a[40:43], a[220:223]\n"
  "v_mfma_f32_16x16x32_bf16 a[220:223], a[92:95], a[44:47], a[220:223]\n"
  "v_mfma_f32_16x16x32_bf16 a[216:219], a[84:87], a[44:47], a[216:219]\n"
  "buffer_load_dwordx4 v251, s[56:59], s75 offen offset:3072 lds\n"
  "v_mfma_f32_16x16x32_bf16 a[216:219], a[80:83], a[40:43], a[216:219]\n"
  "v_mfma_f32_16x16x32_bf16 a[212:215], a[72:75], a[40:43], a[212:215]\n"
  "v_mfma_f32_16x16x32_bf16 a[212:215], a[76:79], a[44:47], a[212:215]\n"
  "s_add_u32 m0, m0, s29\n"
  "v_mfma_f32_16x16x32_bf16 a[208:211], a[68:71], a[44:47], a[208:211]\n"
  "buffer_load_dwordx4 v250, s[56:59], s76 offen lds\n"
  "v_mfma_f32_16x16x32_bf16 a[208:211], a[64:67], a[40:43], a[208:211]\n"
  "v_mfma_f32_16x16x32_bf16 a[224:227], a[64:67], a[48:51], a[224:227]\n"
  "v_mfma_f32_16x16x32_bf16 a[224:227], a[68:71], a[52:55], a[224:227]\n"
  "v_mfma_f32_16x16x32_bf16 a[228:231], a[76:79], a[52:55], a[228:231]\n"
  "buffer_load_dwordx4 v251, s[56:59], s77 offen offset:1024 lds\n"
  "v_mfma_f32_16x16x32_bf16 a[228:231], a[72:75], a[48:51], a[228:231]\n"
  "v_mfma_f32_16x16x32_bf16 a[232:235], a[80:83], a[48:51], a[232:235]\n"
  "v_mfma_f32_16x16x32_bf16 a[232:235], a[84:87], a[52:55], a[232:235]\n"
  "v_mfma_f32_16x16x32_bf16 a[236:239], a[92:95], a[52:55], a[236:239]\n"
  "buffer_load_dwordx4 v250, s[56:59], s78 offen offset:2048 lds\n"
  "v_mfma_f32_16x16x32_bf16 a[236:239], a[88:91], a[48:51], a[236:239]\n"
  "v_mfma_f32_16x16x32_bf16 a[252:255], a[88:91], a[56:59], a[252:255]\n"
  "v_mfma_f32_16x16x32_bf16 a[252:255], a[92:95], a[60:63], a[252:255]\n"
  "v_mfma_f32_16x16x32_bf16 a[248:251], a[84:87], a[60:63], a[248:251]\n"
  "buffer_load_dwordx4 v251, s[56:59], s79 offen offset:3072 lds\n"
  "v_mfma_f32_16x16x32_bf16 a[248:251], a[80:83], a[56:59], a[248:251]\n"
  "v_mfma_f32_16x16x32_bf16 a[244:247], a[72:75], a[56:59], a[244:247]\n"
  "v_mfma_f32_16x16x32_bf16 a[244:247], a[76:79], a[60:63], a[244:247]\n"
  "v_mfma_f32_16x16x32_bf16 a[240:243], a[68:71], a[60:63], a[240:243]\n"
  "s_cmp_lg_u32 s86, 0\n"
  "v_mfma_f32_16x16x32_bf16 a[240:243], a[64:67], a[56:59], a[240:243]\n"
  "s_waitcnt vmcnt(24)\n"
  "s_barrier\n"
  "v_mfma_f32_16x16x32_bf16 v[64:67], a[96:99], a[32:35], v[64:67]\n"
  "s_cbranch_scc1 L_hk_g%=\n"
  "L_hkr_g%=:\n"
  "v_mfma_f32_16x16x32_bf16 v[64:67], a[100:103], a[36:39], v[64:67]\n"
  "ds_read_b128 a[0:3], v228\n"
  "v_mfma_f32_16x16x32_bf16 v[68:71], a[108:111], a[36:39], v[68:71]\n"
  "ds_read_b128 a[4:7], v229\n"
  "v_mfma_f32_16x16x32_bf16 v[68:71], a[104:107], a[32:35], v[68:71]\n"
  "ds_read_b64_tr_b16 a[64:65], v230\n"
  "v_mfma_f32_16x16x32_bf16 v[72:75], a[112:115], a[32:35], v[72:75]\n"
  "ds_read_b64_tr_b16 a[66:67], v230 offset:512\n"
  "v_mfma_f32_16x16x32_bf16 v[72:75], a[116:119], a[36:39], v[72:75]\n"
  "ds_read_b64_tr_b16 a[68:69], v230 offset:4096\n"
  "v_mfma_f32_16x16x32_bf16 v[76:79], a[124:127], a[36:39], v[76:79]\n"
  "ds_read_b64_tr_b16 a[70:71], v230 offset:4608\n"
  "v_mfma_f32_16x16x32_bf16 v[76:79], a[120:123], a[32:35], v[76:79]\n"
  "ds_read_b64_tr_b16 a[72:73], v231\n"
  "v_mfma_f32_16x16x32_bf16 v[92:95], a[120:123], a[40:43], v[92:95]\n"
  "ds_read_b64_tr_b16 a[74:75], v231 offset:512\n"
  "v_mfma_f32_16x16x32_bf16 v[92:95], a[124:127], a[44:47], v[92:95]\n"
  "ds_read_b64_tr_b16 a[76:77], v231 offset:4096\n"
  "v_mfma_f32_16x16x32_bf16 v[88:91], a[116:119], a[44:47], v[88:91]\n"
  "ds_read_b64_tr_b16 a[78:79], v231 offset:4608\n"
  "v_mfma_f32_16x16x32_bf16 v[88:91], a[112:115], a[40:43], v[88:91]\n"
  "ds_read_b64_tr_b16 a[80:81], v232\n"
  "v_mfma_f32_16x16x32_bf16 v[84:87], a[104:107], a[40:43], v[84:87]\n"
  "ds_read_b64_tr_b16 a[82:83], v232 offset:512\n"
  "v_mfma_f32_16x16x32_bf16 v[84:87], a[108:111], a[44:47], v[84:87]\n"
  "ds_read_b64_tr_b16 a[84:85], v232 offset:4096\n"
  "v_mfma_f32_16x16x32_bf16 v[80:83], a[100:103], a[44:47], v[80:83]\n"
  "ds_read_b64_tr_b16 a[86:87], v232 offset:4608\n"
  "v_mfma_f32_16x16x32_bf16 v[80:83], a[96:99], a[40:43], v[80:83]\n"
  "ds_read_b64_tr_b16 a[88:89], v233\n"
  "v_mfma_f32_16x16x32_bf16 v[96:99], a[96:99], a[48:51], v[96:99]\n"
  "ds_read_b64_tr_b16 a[90:91], v233 offset:512\n"
  "v_mfma_f32_16x16x32_bf16 v[96:99], a[100:103], a[52:55], v[96:99]\n"
  "ds_read_b64_tr_b16 a[92:93], v233 offset:4096\n"
  "v_mfma_f32_16x16x32_bf16 v[100:103], a[108:111], a[52:55], v[100:103]\n"
  "ds_read_b64_tr_b16 a[94:95], v233 offset:4608\n"
  "v_mfma_f32_16x16x32_bf16 v[100:103], a[104:107], a[48:51], v[100:103]\n"
  "ds_read_b128 a[8:11], v228 offset:2048\n"
  "v_mfma_f32_16x16x32_bf16 v[104:107], a[112:115], a[48:51], v[104:107]\n"
  "ds_read_b128 a[12:15], v229 offset:2048\n"
  "v_mfma_f32_16x16x32_bf16 v[104:107], a[116:119], a[52:55], v[104:107]\n"
  "ds_read_b128 a[16:19], v228 offset:4096\n"
  "v_mfma_f32_16x16x32_bf16 v[108:111], a[124:127], a[52:55], v[108:111]\n"
  "ds_read_b128 a[20:23], v229 offset:4096\n"
  "v_mfma_f32_16x16x32_bf16 v[108:111], a[120:123], a[48:51], v[108:111]\n"
  "ds_read_b128 a[24:27], v228 offset:6144\n"
  "v_mfma_f32_16x16x32_bf16 v[124:127], a[120:123], a[56:59], v[124:127]\n"
  "ds_read_b128 a[28:31], v229 offset:6144\n"
  "v_mfma_f32_16x16x32_bf16 v[124:127], a[124:127], a[60:63], v[124:127]\n"
  "v_mfma_f32_16x16x32_bf16 v[120:123], a[116:119], a[60:63], v[120:123]\n"
  "v_mfma_f32_16x16x32_bf16 v[120:123], a[112:115], a[56:59], v[120:123]\n"
  "v_mfma_f32_16x16x32_bf16 v[116:119], a[104:107], a[56:59], v[116:119]\n"
  "v_mfma_f32_16x16x32_bf16 v[116:119], a[108:111], a[60:63], v[116:119]\n"
  "v_mfma_f32_16x16x32_bf16 v[112:115], a[100:103], a[60:63], v[112:115]\n"
  "v_mfma_f32_16x16x32_bf16 v[112:115], a[96:99], a[56:59], v[112:115]\n"
  "s_waitcnt lgkmcnt(0)\n"
  "s_sub_u32 s82, s82, 1\n"
  "s_cmp_lg_u32 s82, 0\n"
  "s_cbranch_scc1 L_g_loop%=\n"
  "s_cmp_lt_u32 s44, 4\n"
  "s_cbranch_scc1 L_al_g%=\n"
  "s_add_u32 s52, s52, s81\n"
  "s_addc_u32 s53, s53, 0\n"
  "s_add_u32 s56, s56, s80\n"
  "s_addc_u32 s57, s57, 0\n"
  "s_add_u32 s56, s56, s80\n"
  "s_addc_u32 s57, s57, 0\n"
  "s_add_u32 m0, s84, 0x8000\n"
  "s_nop 0\n"
  "buffer_load_dwordx4 v250, s[56:59], s72 offen lds\n"
  "buffer_load_dwordx4 v251, s[56:59], s73 offen offset:1024 lds\n"
  "buffer_load_dwordx4 v250, s[56:59], s74 offen offset:2048 lds\n"
  "buffer_load_dwordx4 v251, s[56:59], s75 offen offset:3072 lds\n"
  "s_add_u32 m0, s83, 0x10000\n"
  "s_nop 0\n"
  "buffer_load_dwordx4 v252, s[52:55], s64 offen sc1 lds\n"
  "buffer_load_dwordx4 v253, s[52:55], s65 offen offset:1024 sc1 lds\n"
  "buffer_load_dwordx4 v252, s[52:55], s66 offen offset:2048 sc1 lds\n"
  "buffer_load_dwordx4 v253, s[52:55], s67 offen offset:3072 sc1 lds\n"
  "L_al_g%=:\n"
  "s_mov_b32 s86, s43\n"
  "s_branch L_e_gu%=\n"
  "L_g_last%=:\n"
  "v_mfma_f32_16x16x32_bf16 v[0:3], a[96:99], a[0:3], v[0:3]\n"
  "ds_read_b64_tr_b16 a[64:65], v230\n"
  "v_mfma_f32_16x16x32_bf16 v[0:3], a[100:103], a[4:7], v[0:3]\n"
  "ds_read_b64_tr_b16 a[66:67], v230 offset:512\n"
  "v_mfma_f32_16x16x32_bf16 v[4:7], a[108:111], a[4:7], v[4:7]\n"
  "ds_read_b64_tr_b16 a[68:69], v230 offset:4096\n"
  "v_mfma_f32_16x16x32_bf16 v[4:7], a[104:107], a[0:3], v[4:7]\n"
  "ds_read_b64_tr_b16 a[70:71], v230 offset:4608\n"
  "v_mfma_f32_16x16x32_bf16 v[8:11], a[112:115], a[0:3], v[8:11]\n"
  "ds_read_b64_tr_b16 a[72:73], v231\n"
  "v_mfma_f32_16x16x32_bf16 v[8:11], a[116:119], a[4:7], v[8:11]\n"
  "ds_read_b64_tr_b16 a[74:75], v231 offset:512\n"
  "v_mfma_f32_16x16x32_bf16 v[12:15], a[124:127], a[4:7], v[12:15]\n"
  "ds_read_b64_tr_b16 a[76:77], v231 offset:4096\n"
  "v_mfma_f32_16x16x32_bf16 v[12:15], a[120:123], a[0:3], v[12:15]\n"
  "ds_read_b64_tr_b16 a[78:79], v231 offset:4608\n"
  "v_mfma_f32_16x16x32_bf16 v[28:31], a[120:123], a[8:11], v[28:31]\n"
  "ds_read_b64_tr_b16 a[80:81], v232\n"
  "v_mfma_f32_16x16x32_bf16 v[28:31], a[124:127], a[12:15], v[28:31]\n"
  "ds_read_b64_tr_b16 a[82:83], v232 offset:512\n"
  "v_mfma_f32_16x16x32_bf16 v[24:27], a[116:119], a[12:15], v[24:27]\n"
  "ds_read_b64_tr_b16 a[84:85], v232 offset:4096\n"
  "v_mfma_f32_16x16x32_bf16 v[24:27], a[112:115], a[8:11], v[24:27]\n"
  "ds_read_b64_tr_b16 a[86:87], v232 offset:4608\n"
  "v_mfma_f32_16x16x32_bf16 v[20:23], a[104:107], a[8:11], v[20:23]\n"
  "ds_read_b64_tr_b16 a[88:89], v233\n"
  "v_mfma_f32_16x16x32_bf16 v[20:23], a[108:111], a[12:15], v[20:23]\n"
  "ds_read_b64_tr_b16 a[90:91], v233 offset:512\n"
  "v_mfma_f32_16x16x32_bf16 v[16:19], a[100:103], a[12:15], v[16:19]\n"
  "ds_read_b64_tr_b16 a[92:93], v233 offset:4096\n"
  "v_mfma_f32_16x16x32_bf16 v[16:19], a[96:99], a[8:11], v[16:19]\n"
  "ds_read_b64_tr_b16 a[94:95], v233 offset:4608\n"
  "v_mfma_f32_16x16x32_bf16 v[32:35], a[96:99], a[16:19], v[32:35]\n"
  "ds_read_b128 a[32:35], v234 offset:8192\n"
  "v_mfma_f32_16x16x32_bf16 v[32:35], a[100:103], a[20:23], v[32:35]\n"
  "ds_read_b128 a[36:39], v235 offset:8192\n"
  "v_mfma_f32_16x16x32_bf16 v[36:39], a[108:111], a[20:23], v[36:39]\n"
  "ds_read_b128 a[40:43], v234 offset:10240\n"
  "v_mfma_f32_16x16x32_bf16 v[36:39], a[104:107], a[16:19], v[36:39]\n"
  "ds_read_b128 a[44:47], v235 offset:10240\n"
  "v_mfma_f32_16x16x32_bf16 v[40:43], a[112:115], a[16:19], v[40:43]\n"
  "ds_read_b128 a[48:51], v234 offset:12288\n"
  "v_mfma_f32_16x16x32_bf16 v[40:43], a[116:119], a[20:23], v[40:43]\n"
  "ds_read_b128 a[52:55], v235 offset:12288\n"
  "v_mfma_f32_16x16x32_bf16 v[44:47], a[124:127], a[20:23], v[44:47]\n"
  "ds_read_b128 a[56:59], v234 offset:14336\n"
  "v_mfma_f32_16x16x32_bf16 v[44:47], a[120:123], a[16:19], v[44:47]\n"
  "ds_read_b128 a[60:63], v235 offset:14336\n"
  "v_mfma_f32_16x16x32_bf16 v[60:63], a[120:123], a[24:27], v[60:63]\n"
  "s_add_u32 s52, s52, s81\n"
  "s_addc_u32 s53, s53, 0\n"
  "v_mfma_f32_16x16x32_bf16 v[60:63], a[124:127], a[28:31], v[60:63]\n"
  "s_add_u32 s56, s56, s80\n"
  "s_addc_u32 s57, s57, 0\n"
  "v_mfma_f32_16x16x32_bf16 v[56:59], a[116:119], a[28:31], v[56:59]\n"
  "v_mfma_f32_16x16x32_bf16 v[56:59], a[112:115], a[24:27], v[56:59]\n"
  "s_cmp_lt_u32 s44, 4\n"
  "v_mfma_f32_16x16x32_bf16 v[52:55], a[104:107], a[24:27], v[52:55]\n"
  "s_waitcnt lgkmcnt(0)\n"
  "s_barrier\n"
  "v_mfma_f32_16x16x32_bf16 v[52:55], a[108:111], a[28:31], v[52:55]\n"
  "s_cbranch_scc1 L_kl_k%=\n"
  "s_getpc_b64 s[96:97]\n"
  "s_add_u32 s96, s96, 12\n"
  "s_addc_u32 s97, s97, 0\n"
  "s_branch L_sregsw%=\n"
  "L_kl_k%=:\n"
  "s_add_u32 m0, s83, 0x8000\n"
  "v_mfma_f32_16x16x32_bf16 v[48:51], a[100:103], a[28:31], v[48:51]\n"
  "buffer_load_dwordx4 v252, s[52:55], s64 offen sc1 lds\n"
  "v_mfma_f32_16x16x32_bf16 v[48:51], a[96:99], a[24:27], v[48:51]\n"
  "s_and_b32 s87, s34, 1\n"
  "s_mul_i32 s87, s87, 0x108000\n"
  "v_mfma_f32_16x16x32_bf16 a[128:131], a[64:67], a[0:3], a[128:131]\n"
  "s_lshr_b32 s90, s34, 1\n"
  "s_lshl_b32 s90, s90, 16\n"
  "v_mfma_f32_16x16x32_bf16 a[128:131], a[68:71], a[4:7], a[128:131]\n"
  "s_add_u32 s87, s87, s90\n"
  "s_add_u32 s87, s87, s39\n"
  "v_mfma_f32_16x16x32_bf16 a[132:135], a[76:79], a[4:7], a[132:135]\n"
  "buffer_load_dwordx4 v253, s[52:55], s65 offen offset:1024 sc1 lds\n"
  "s_add_u32 s87, s87, 0x4000\n"
  "v_mfma_f32_16x16x32_bf16 a[132:135], a[72:75], a[0:3], a[132:135]\n"
  "s_add_u32 s60, s8, s87\n"
  "s_addc_u32 s61, s9, 0\n"
  "v_mfma_f32_16x16x32_bf16 a[136:139], a[80:83], a[0:3], a[136:139]\n"
  "s_sub_u32 s62, 0x5b00000, s87\n"
  "s_mov_b32 s63, 0x20000\n"
  "v_mfma_f32_16x16x32_bf16 a[136:139], a[84:87], a[4:7], a[136:139]\n"
  "s_mov_b32 s88, 0x3e0293ee\n"
  "s_mov_b32 s89, 0x3e0293ee\n"
  "v_mfma_f32_16x16x32_bf16 a[140:143], a[92:95], a[4:7], a[140:143]\n"
  "buffer_load_dwordx4 v252, s[52:55], s66 offen offset:2048 sc1 lds\n"
  "s_mov_b32 s90, 0x2000\n"
  "v_mfma_f32_16x16x32_bf16 a[140:143], a[88:91], a[0:3], a[140:143]\n"
  "v_mul_f32 v0, s88, v0\n"
  "v_mul_f32 v1, s88, v1\n"
  "v_mfma_f32_16x16x32_bf16 a[156:159], a[88:91], a[8:11], a[156:159]\n"
  "s_and_b32 s28, s94, s95\n"
  "v_mul_f32 v2, s88, v2\n"
  "v_mfma_f32_16x16x32_bf16 a[156:159], a[92:95], a[12:15], a[156:159]\n"
  "s_add_u32 s94, s94, 0x8000\n"
  "v_mul_f32 v3, s88, v3\n"
  "v_mfma_f32_16x16x32_bf16 a[152:155], a[84:87], a[12:15], a[152:155]\n"
  "buffer_load_dwordx4 v253, s[52:55], s67 offen offset:3072 sc1 lds\n"
  "v_mul_f32 v4, s88, v4\n"
  "v_mfma_f32_16x16x32_bf16 a[152:155], a[80:83], a[8:11], a[152:155]\n"
  "s_cmp_eq_u32 s94, 0x18000\n"
  "s_cselect_b32 s94, 0, s94\n"
  "v_mfma_f32_16x16x32_bf16 a[148:151], a[72:75], a[8:11], a[148:151]\n"
  "v_mul_f32 v5, s88, v5\n"
  "v_mul_f32 v6, s88, v6\n"
  "v_mfma_f32_16x16x32_bf16 a[148:151], a[76:79], a[12:15], a[148:151]\n"
  "s_add_u32 m0, s83, 0x9000\n"
  "v_mul_f32 v7, s88, v7\n"
  "v_mfma_f32_16x16x32_bf16 a[144:147], a[68:71], a[12:15], a[144:147]\n"
  "buffer_load_dwordx4 v252, s[52:55], s68 offen sc1 lds\n"
  "v_cvt_pk_bf16_f32 v136, v0, v1\n"
  "v_mfma_f32_16x16x32_bf16 a[144:147], a[64:67], a[8:11], a[144:147]\n"
  "v_add_u32 v230, s94, v236\n"
  "v_cvt_pk_bf16_f32 v137, v2, v3\n"
  "v_mfma_f32_16x16x32_bf16 a[160:163], a[64:67], a[16:19], a[160:163]\n"
  "v_cvt_pk_bf16_f32 v138, v4, v5\n"
  "v_cvt_pk_bf16_f32 v139, v6, v7\n"
  "v_mfma_f32_16x16x32_bf16 a[160:163], a[68:71], a[20:23], a[160:163]\n"
  "buffer_store_dwordx4 v[136:139], v246, s[60:63], s90 offen sc1\n"
  "v_add_u32 v231, s94, v237\n"
  "v_mfma_f32_16x16x32_bf16 a[164:167], a[76:79], a[20:23], a[164:167]\n"
  "buffer_load_dwordx4 v253, s[52:55], s69 offen offset:1024 sc1 lds\n"
  "v_mul_f32 v16, s88, v16\n"
  "v_mfma_f32_16x16x32_bf16 a[164:167], a[72:75], a[16:19], a[164:167]\n"
  "v_mul_f32 v17, s88, v17\n"
  "v_mul_f32 v18, s88, v18\n"
  "v_mfma_f32_16x16x32_bf16 a[168:171], a[80:83], a[16:19], a[168:171]\n"
  "v_add_u32 v232, s94, v238\n"
  "v_mul_f32 v19, s88, v19\n"
  "v_mfma_f32_16x16x32_bf16 a[168:171], a[84:87], a[20:23], a[168:171]\n"
  "v_add_u32 v233, s94, v239\n"
  "v_mul_f32 v20, s88, v20\n"
  "v_mfma_f32_16x16x32_bf16 a[172:175], a[92:95], a[20:23], a[172:175]\n"
  "buffer_load_dwordx4 v252, s[52:55], s70 offen offset:2048 sc1 lds\n"
  "v_mul_f32 v21, s88, v21\n"
  "v_mfma_f32_16x16x32_bf16 a[172:175], a[88:91], a[16:19], a[172:175]\n"
  "v_mul_f32 v22, s88, v22\n"
  "v_mul_f32 v23, s88, v23\n"
  "v_mfma_f32_16x16x32_bf16 a[188:191], a[88:91], a[24:27], a[188:191]\n"
  "v_cvt_pk_bf16_f32 v148, v16, v17\n"
  "v_cvt_pk_bf16_f32 v149, v18, v19\n"
  "v_mfma_f32_16x16x32_bf16 a[188:191], a[92:95], a[28:31], a[188:191]\n"
  "v_cvt_pk_bf16_f32 v150, v20, v21\n"
  "v_cvt_pk_bf16_f32 v151, v22, v23\n"
  "v_mfma_f32_16x16x32_bf16 a[184:187], a[84:87], a[28:31], a[184:187]\n"
  "buffer_load_dwordx4 v253, s[52:55], s71 offen offset:3072 sc1 lds\n"
  "v_mul_f32 v32, s88, v32\n"
  "v_mfma_f32_16x16x32_bf16 a[184:187], a[80:83], a[24:27], a[184:187]\n"
  "buffer_store_dwordx4 v[148:151], v246, s[60:63], s90 offen offset:256 sc1\n"
  "v_mul_f32 v33, s88, v33\n"
  "v_mfma_f32_16x16x32_bf16 a[180:183], a[72:75], a[24:27], a[180:183]\n"
  "v_mul_f32 v34, s88, v34\n"
  "v_mul_f32 v35, s88, v35\n"
  "v_mfma_f32_16x16x32_bf16 a[180:183], a[76:79], a[28:31], a[180:183]\n"
  "s_add_u32 m0, s84, s28\n"
  "v_mul_f32 v36, s88, v36\n"
  "v_mfma_f32_16x16x32_bf16 a[176:179], a[68:71], a[28:31], a[176:179]\n"
  "buffer_load_dwordx4 v250, s[56:59], s72 offen lds\n"
  "v_mul_f32 v37, s88, v37\n"
  "v_mfma_f32_16x16x32_bf16 a[176:179], a[64:67], a[24:27], a[176:179]\n"
  "v_mul_f32 v38, s88, v38\n"
  "v_mul_f32 v39, s88, v39\n"
  "v_mfma_f32_16x16x32_bf16 a[192:195], a[64:67], a[32:35], a[192:195]\n"
  "v_cvt_pk_bf16_f32 v160, v32, v33\n"
  "v_cvt_pk_bf16_f32 v161, v34, v35\n"
  "v_mfma_f32_16x16x32_bf16 a[192:195], a[68:71], a[36:39], a[192:195]\n"
  "v_cvt_pk_bf16_f32 v162, v36, v37\n"
  "v_cvt_pk_bf16_f32 v163, v38, v39\n"
  "v_mfma_f32_16x16x32_bf16 a[196:199], a[76:79], a[36:39], a[196:199]\n"
  "buffer_load_dwordx4 v251, s[56:59], s73 offen offset:1024 lds\n"
  "v_mul_f32 v48, s88, v48\n"
  "v_mfma_f32_16x16x32_bf16 a[196:199], a[72:75], a[32:35], a[196:199]\n"
  "buffer_store_dwordx4 v[160:163], v246, s[60:63], s90 offen offset:512 sc1\n"
  "v_mul_f32 v49, s88, v49\n"
  "v_mfma_f32_16x16x32_bf16 a[200:203], a[80:83], a[32:35], a[200:203]\n"
  "v_mul_f32 v50, s88, v50\n"
  "v_mul_f32 v51, s88, v51\n"
  "v_mfma_f32_16x16x32_bf16 a[200:203], a[84:87], a[36:39], a[200:203]\n"
  "v_mul_f32 v52, s88, v52\n"
  "v_mul_f32 v53, s88, v53\n"
  "v_mfma_f32_16x16x32_bf16 a[204:207], a[92:95], a[36:39], a[204:207]\n"
  "buffer_load_dwordx4 v250, s[56:59], s74 offen offset:2048 lds\n"
  "v_mul_f32 v54, s88, v54\n"
  "v_mfma_f32_16x16x32_bf16 a[204:207], a[88:91], a[32:35], a[204:207]\n"
  "v_mul_f32 v55, s88, v55\n"
  "v_cvt_pk_bf16_f32 v172, v48, v49\n"
  "v_mfma_f32_16x16x32_bf16 a[220:223], a[88:91], a[40:43], a[220:223]\n"
  "v_cvt_pk_bf16_f32 v173, v50, v51\n"
  "v_cvt_pk_bf16_f32 v174, v52, v53\n"
  "v_mfma_f32_16x16x32_bf16 a[220:223], a[92:95], a[44:47], a[220:223]\n"
  "v_cvt_pk_bf16_f32 v175, v54, v55\n"
  "v_mfma_f32_16x16x32_bf16 a[216:219], a[84:87], a[44:47], a[216:219]\n"
  "buffer_load_dwordx4 v251, s[56:59], s75 offen offset:3072 lds\n"
  "v_mfma_f32_16x16x32_bf16 a[216:219], a[80:83], a[40:43], a[216:219]\n"
  "buffer_store_dwordx4 v[172:175], v246, s[60:63], s90 offen offset:768 sc1\n"
  "s_mov_b32 s90, 0x3000\n"
  "v_mfma_f32_16x16x32_bf16 a[212:215], a[72:75], a[40:43], a[212:215]\n"
  "v_mul_f32 v8, s88, v8\n"
  "v_mul_f32 v9, s88, v9\n"
  "v_mfma_f32_16x16x32_bf16 a[212:215], a[76:79], a[44:47], a[212:215]\n"
  "s_add_u32 m0, m0, s29\n"
  "v_mul_f32 v10, s88, v10\n"
  "v_mfma_f32_16x16x32_bf16 a[208:211], a[68:71], a[44:47], a[208:211]\n"
  "buffer_load_dwordx4 v250, s[56:59], s76 offen lds\n"
  "v_mul_f32 v11, s88, v11\n"
  "v_mfma_f32_16x16x32_bf16 a[208:211], a[64:67], a[40:43], a[208:211]\n"
  "v_mul_f32 v12, s88, v12\n"
  "v_mul_f32 v13, s88, v13\n"
  "v_mfma_f32_16x16x32_bf16 a[224:227], a[64:67], a[48:51], a[224:227]\n"
  "v_mul_f32 v14, s88, v14\n"
  "v_mul_f32 v15, s88, v15\n"
  "v_mfma_f32_16x16x32_bf16 a[224:227], a[68:71], a[52:55], a[224:227]\n"
  "v_cvt_pk_bf16_f32 v136, v8, v9\n"
  "v_cvt_pk_bf16_f32 v137, v10, v11\n"
  "v_mfma_f32_16x16x32_bf16 a[228:231], a[76:79], a[52:55], a[228:231]\n"
  "buffer_load_dwordx4 v251, s[56:59], s77 offen offset:1024 lds\n"
  "v_cvt_pk_bf16_f32 v138, v12, v13\n"
  "v_mfma_f32_16x16x32_bf16 a[228:231], a[72:75], a[48:51], a[228:231]\n"
  "v_cvt_pk_bf16_f32 v139, v14, v15\n"
  "v_mul_f32 v24, s88, v24\n"
  "v_mfma_f32_16x16x32_bf16 a[232:235], a[80:83], a[48:51], a[232:235]\n"
  "buffer_store_dwordx4 v[136:139], v246, s[60:63], s90 offen sc1\n"
  "v_mul_f32 v25, s88, v25\n"
  "v_mfma_f32_16x16x32_bf16 a[232:235], a[84:87], a[52:55], a[232:235]\n"
  "v_mul_f32 v26, s88, v26\n"
  "v_mul_f32 v27, s88, v27\n"
  "v_mfma_f32_16x16x32_bf16 a[236:239], a[92:95], a[52:55], a[236:239]\n"
  "buffer_load_dwordx4 v250, s[56:59], s78 offen offset:2048 lds\n"
  "v_mul_f32 v28, s88, v28\n"
  "v_mfma_f32_16x16x32_bf16 a[236:239], a[88:91], a[48:51], a[236:239]\n"
  "v_mul_f32 v29, s88, v29\n"
  "v_mul_f32 v30, s88, v30\n"
  "v_mfma_f32_16x16x32_bf16 a[252:255], a[88:91], a[56:59], a[252:255]\n"
  "v_mul_f32 v31, s88, v31\n"
  "v_cvt_pk_bf16_f32 v148, v24, v25\n"
  "v_mfma_f32_16x16x32_bf16 a[252:255], a[92:95], a[60:63], a[252:255]\n"
  "v_cvt_pk_bf16_f32 v149, v26, v27\n"
  "v_cvt_pk_bf16_f32 v150, v28, v29\n"
  "v_mfma_f32_16x16x32_bf16 a[248:251], a[84:87], a[60:63], a[248:251]\n"
  "buffer_load_dwordx4 v251, s[56:59], s79 offen offset:3072 lds\n"
  "v_cvt_pk_bf16_f32 v151, v30, v31\n"
  "v_mfma_f32_16x16x32_bf16 a[248:251], a[80:83], a[56:59], a[248:251]\n"
  "v_mul_f32 v40, s88, v40\n"
  "v_mul_f32 v41, s88, v41\n"
  "v_mfma_f32_16x16x32_bf16 a[244:247], a[72:75], a[56:59], a[244:247]\n"
  "buffer_store_dwordx4 v[148:151], v246, s[60:63], s90 offen offset:256 sc1\n"
  "v_mul_f32 v42, s88, v42\n"
  "v_mfma_f32_16x16x32_bf16 a[244:247], a[76:79], a[60:63], a[244:247]\n"
  "v_mul_f32 v43, s88, v43\n"
  "v_mul_f32 v44, s88, v44\n"
  "v_mfma_f32_16x16x32_bf16 a[240:243], a[68:71], a[60:63], a[240:243]\n"
  "v_mul_f32 v45, s88, v45\n"
  "v_mul_f32 v46, s88, v46\n"
  "v_mfma_f32_16x16x32_bf16 a[240:243], a[64:67], a[56:59], a[240:243]\n"
  "s_waitcnt vmcnt(30)\n"
  "s_barrier\n"
  "v_mfma_f32_16x16x32_bf16 v[64:67], a[96:99], a[32:35], v[64:67]\n"
  "v_mul_f32 v47, s88, v47\n"
  "v_cvt_pk_bf16_f32 v160, v40, v41\n"
  "v_mfma_f32_16x16x32_bf16 v[64:67], a[100:103], a[36:39], v[64:67]\n"
  "ds_read_b128 a[0:3], v228\n"
  "v_cvt_pk_bf16_f32 v161, v42, v43\n"
  "v_mfma_f32_16x16x32_bf16 v[68:71], a[108:111], a[36:39], v[68:71]\n"
  "ds_read_b128 a[4:7], v229\n"
  "v_cvt_pk_bf16_f32 v162, v44, v45\n"
  "v_mfma_f32_16x16x32_bf16 v[68:71], a[104:107], a[32:35], v[68:71]\n"
  "ds_read_b64_tr_b16 a[64:65], v230\n"
  "v_cvt_pk_bf16_f32 v163, v46, v47\n"
  "v_mfma_f32_16x16x32_bf16 v[72:75], a[112:115], a[32:35], v[72:75]\n"
  "buffer_store_dwordx4 v[160:163], v246, s[60:63], s90 offen offset:512 sc1\n"
  "ds_read_b64_tr_b16 a[66:67], v230 offset:512\n"
  "v_mfma_f32_16x16x32_bf16 v[72:75], a[116:119], a[36:39], v[72:75]\n"
  "ds_read_b64_tr_b16 a[68:69], v230 offset:4096\n"
  "v_mul_f32 v56, s88, v56\n"
  "v_mfma_f32_16x16x32_bf16 v[76:79], a[124:127], a[36:39], v[76:79]\n"
  "ds_read_b64_tr_b16 a[70:71], v230 offset:4608\n"
  "v_mul_f32 v57, s88, v57\n"
  "v_mfma_f32_16x16x32_bf16 v[76:79], a[120:123], a[32:35], v[76:79]\n"
  "ds_read_b64_tr_b16 a[72:73], v231\n"
  "v_mul_f32 v58, s88, v58\n"
  "v_mfma_f32_16x16x32_bf16 v[92:95], a[120:123], a[40:43], v[92:95]\n"
  "ds_read_b64_tr_b16 a[74:75], v231 offset:512\n"
  "v_mul_f32 v59, s88, v59\n"
  "v_mfma_f32_16x16x32_bf16 v[92:95], a[124:127], a[44:47], v[92:95]\n"
  "ds_read_b64_tr_b16 a[76:77], v231 offset:4096\n"
  "v_mul_f32 v60, s88, v60\n"
  "v_mfma_f32_16x16x32_bf16 v[88:91], a[116:119], a[44:47], v[88:91]\n"
  "ds_read_b64_tr_b16 a[78:79], v231 offset:4608\n"
  "v_mul_f32 v61, s88, v61\n"
  "v_mfma_f32_16x16x32_bf16 v[88:91], a[112:115], a[40:43], v[88:91]\n"
  "ds_read_b64_tr_b16 a[80:81], v232\n"
  "v_mul_f32 v62, s88, v62\n"
  "v_mfma_f32_16x16x32_bf16 v[84:87], a[104:107], a[40:43], v[84:87]\n"
  "ds_read_b64_tr_b16 a[82:83], v232 offset:512\n"
  "v_mul_f32 v63, s88, v63\n"
  "v_mfma_f32_16x16x32_bf16 v[84:87], a[108:111], a[44:47], v[84:87]\n"
  "ds_read_b64_tr_b16 a[84:85], v232 offset:4096\n"
  "v_cvt_pk_bf16_f32 v172, v56, v57\n"
  "v_mfma_f32_16x16x32_bf16 v[80:83], a[100:103], a[44:47], v[80:83]\n"
  "ds_read_b64_tr_b16 a[86:87], v232 offset:4608\n"
  "v_cvt_pk_bf16_f32 v173, v58, v59\n"
  "v_mfma_f32_16x16x32_bf16 v[80:83], a[96:99], a[40:43], v[80:83]\n"
  "ds_read_b64_tr_b16 a[88:89], v233\n"
  "v_cvt_pk_bf16_f32 v174, v60, v61\n"
  "v_mfma_f32_16x16x32_bf16 v[96:99], a[96:99], a[48:51], v[96:99]\n"
  "ds_read_b64_tr_b16 a[90:91], v233 offset:512\n"
  "v_cvt_pk_bf16_f32 v175, v62, v63\n"
  "v_mfma_f32_16x16x32_bf16 v[96:99], a[100:103], a[52:55], v[96:99]\n"
  "buffer_store_dwordx4 v[172:175], v246, s[60:63], s90 offen offset:768 sc1\n"
  "ds_read_b64_tr_b16 a[92:93], v233 offset:4096\n"
  "v_mfma_f32_16x16x32_bf16 v[100:103], a[108:111], a[52:55], v[100:103]\n"
  "ds_read_b64_tr_b16 a[94:95], v233 offset:4608\n"
  "v_mfma_f32_16x16x32_bf16 v[100:103], a[104:107], a[48:51], v[100:103]\n"
  "ds_read_b128 a[8:11], v228 offset:2048\n"
  "v_mfma_f32_16x16x32_bf16 v[104:107], a[112:115], a[48:51], v[104:107]\n"
  "ds_read_b128 a[12:15], v229 offset:2048\n"
  "v_mfma_f32_16x16x32_bf16 v[104:107], a[116:119], a[52:55], v[104:107]\n"
  "ds_read_b128 a[16:19], v228 offset:4096\n"
  "v_mfma_f32_16x16x32_bf16 v[108:111], a[124:127], a[52:55], v[108:111]\n"
  "ds_read_b128 a[20:23], v229 offset:4096\n"
  "v_mfma_f32_16x16x32_bf16 v[108:111], a[120:123], a[48:51], v[108:111]\n"
  "ds_read_b128 a[24:27], v228 offset:6144\n"
  "v_mfma_f32_16x16x32_bf16 v[124:127], a[120:123], a[56:59], v[124:127]\n"
  "ds_read_b128 a[28:31], v229 offset:6144\n"
  "v_mfma_f32_16x16x32_bf16 v[124:127], a[124:127], a[60:63], v[124:127]\n"
  "v_mfma_f32_16x16x32_bf16 v[120:123], a[116:119], a[60:63], v[120:123]\n"
  "v_mfma_f32_16x16x32_bf16 v[120:123], a[112:115], a[56:59], v[120:123]\n"
  "v_mfma_f32_16x16x32_bf16 v[116:119], a[104:107], a[56:59], v[116:119]\n"
  "v_mfma_f32_16x16x32_bf16 v[116:119], a[108:111], a[60:63], v[116:119]\n"
  "v_mfma_f32_16x16x32_bf16 v[112:115], a[100:103], a[60:63], v[112:115]\n"
  "v_mfma_f32_16x16x32_bf16 v[112:115], a[96:99], a[56:59], v[112:115]\n"
  "s_waitcnt lgkmcnt(0)\n"
  "s_cmp_lt_u32 s44, 4\n"
  "s_cbranch_scc1 L_al_gk%=\n"
  "s_add_u32 s52, s52, s81\n"
  "s_addc_u32 s53, s53, 0\n"
  "s_add_u32 s56, s56, s80\n"
  "s_addc_u32 s57, s57, 0\n"
  "s_add_u32 s56, s56, s80\n"
  "s_addc_u32 s57, s57, 0\n"
  "s_add_u32 m0, s84, 0x8000\n"
  "s_nop 0\n"
  "buffer_load_dwordx4 v250, s[56:59], s72 offen lds\n"
  "buffer_load_dwordx4 v251, s[56:59], s73 offen offset:1024 lds\n"
  "buffer_load_dwordx4 v250, s[56:59], s74 offen offset:2048 lds\n"
  "buffer_load_dwordx4 v251, s[56:59], s75 offen offset:3072 lds\n"
  "s_add_u32 m0, s83, 0x10000\n"
  "s_nop 0\n"
  "buffer_load_dwordx4 v252, s[52:55], s64 offen sc1 lds\n"
  "buffer_load_dwordx4 v253, s[52:55], s65 offen offset:1024 sc1 lds\n"
  "buffer_load_dwordx4 v252, s[52:55], s66 offen offset:2048 sc1 lds\n"
  "buffer_load_dwordx4 v253, s[52:55], s67 offen offset:3072 sc1 lds\n"
  "L_al_gk%=:\n"
  "s_mov_b32 s86, s43\n"
  "s_mov_b32 s90, 0\n"
  "v_accvgpr_read_b32 v128, a128\n"
  "v_accvgpr_read_b32 v129, a129\n"
  "v_accvgpr_read_b32 v130, a130\n"
  "v_accvgpr_read_b32 v131, a131\n"
  "v_accvgpr_read_b32 v132, a132\n"
  "v_accvgpr_read_b32 v133, a133\n"
  "v_accvgpr_read_b32 v134, a134\n"
  "v_accvgpr_read_b32 v135, a135\n"
  "v_pk_mul_f32 v[128:129], v[128:129], s[88:89]\n"
  "v_pk_mul_f32 v[130:131], v[130:131], s[88:89]\n"
  "v_pk_mul_f32 v[132:133], v[132:133], s[88:89]\n"
  "v_pk_mul_f32 v[134:135], v[134:135], s[88:89]\n"
  "v_cvt_pk_bf16_f32 v136, v128, v129\n"
  "v_cvt_pk_bf16_f32 v137, v130, v131\n"
  "v_cvt_pk_bf16_f32 v138, v132, v133\n"
  "v_cvt_pk_bf16_f32 v139, v134, v135\n"
  "buffer_store_dwordx4 v[136:139], v246, s[60:63], s90 offen sc1\n"
  "v_accvgpr_read_b32 v140, a144\n"
  "v_accvgpr_read_b32 v141, a145\n"
  "v_accvgpr_read_b32 v142, a146\n"
  "v_accvgpr_read_b32 v143, a147\n"
  "v_accvgpr_read_b32 v144, a148\n"
  "v_accvgpr_read_b32 v145, a149\n"
  "v_accvgpr_read_b32 v146, a150\n"
  "v_accvgpr_read_b32 v147, a151\n"
  "v_pk_mul_f32 v[140:141], v[140:141], s[88:89]\n"
  "v_pk_mul_f32 v[142:143], v[142:143], s[88:89]\n"
  "v_pk_mul_f32 v[144:145], v[144:145], s[88:89]\n"
  "v_pk_mul_f32 v[146:147], v[146:147], s[88:89]\n"
  "v_cvt_pk_bf16_f32 v148, v140, v141\n"
  "v_cvt_pk_bf16_f32 v149, v142, v143\n"
  "v_cvt_pk_bf16_f32 v150, v144, v145\n"
  "v_cvt_pk_bf16_f32 v151, v146, v147\n"
  "buffer_store_dwordx4 v[148:151], v246, s[60:63], s90 offen offset:256 sc1\n"
  "v_accvgpr_read_b32 v152, a160\n"
  "v_accvgpr_read_b32 v153, a161\n"
  "v_accvgpr_read_b32 v154, a162\n"
  "v_accvgpr_read_b32 v155, a163\n"
  "v_accvgpr_read_b32 v156, a164\n"
  "v_accvgpr_read_b32 v157, a165\n"
  "v_accvgpr_read_b32 v158, a166\n"
  "v_accvgpr_read_b32 v159, a167\n"
  "v_pk_mul_f32 v[152:153], v[152:153], s[88:89]\n"
  "v_pk_mul_f32 v[154:155], v[154:155], s[88:89]\n"
  "v_pk_mul_f32 v[156:157], v[156:157], s[88:89]\n"
  "v_pk_mul_f32 v[158:159], v[158:159], s[88:89]\n"
  "v_cvt_pk_bf16_f32 v160, v152, v153\n"
  "v_cvt_pk_bf16_f32 v161, v154, v155\n"
  "v_cvt_pk_bf16_f32 v162, v156, v157\n"
  "v_cvt_pk_bf16_f32 v163, v158, v159\n"
  "buffer_store_dwordx4 v[160:163], v246, s[60:63], s90 offen offset:512 sc1\n"
  "v_accvgpr_read_b32 v164, a176\n"
  "v_accvgpr_read_b32 v165, a177\n"
  "v_accvgpr_read_b32 v166, a178\n"
  "v_accvgpr_read_b32 v167, a179\n"
  "v_accvgpr_read_b32 v168, a180\n"
  "v_accvgpr_read_b32 v169, a181\n"
  "v_accvgpr_read_b32 v170, a182\n"
  "v_accvgpr_read_b32 v171, a183\n"
  "v_pk_mul_f32 v[164:165], v[164:165], s[88:89]\n"
  "v_pk_mul_f32 v[166:167], v[166:167], s[88:89]\n"
  "v_pk_mul_f32 v[168:169], v[168:169], s[88:89]\n"
  "v_pk_mul_f32 v[170:171], v[170:171], s[88:89]\n"
  "v_cvt_pk_bf16_f32 v172, v164, v165\n"
  "v_cvt_pk_bf16_f32 v173, v166, v167\n"
  "v_cvt_pk_bf16_f32 v174, v168, v169\n"
  "v_cvt_pk_bf16_f32 v175, v170, v171\n"
  "buffer_store_dwordx4 v[172:175], v246, s[60:63], s90 offen offset:768 sc1\n"
  "s_mov_b32 s90, 0x1000\n"
  "v_accvgpr_read_b32 v128, a136\n"
  "v_accvgpr_read_b32 v129, a137\n"
  "v_accvgpr_read_b32 v130, a138\n"
  "v_accvgpr_read_b32 v131, a139\n"
  "v_accvgpr_read_b32 v132, a140\n"
  "v_accvgpr_read_b32 v133, a141\n"
  "v_accvgpr_read_b32 v134, a142\n"
  "v_accvgpr_read_b32 v135, a143\n"
  "v_pk_mul_f32 v[128:129], v[128:129], s[88:89]\n"
  "v_pk_mul_f32 v[130:131], v[130:131], s[88:89]\n"
  "v_pk_mul_f32 v[132:133], v[132:133], s[88:89]\n"
  "v_pk_mul_f32 v[134:135], v[134:135], s[88:89]\n"
  "v_cvt_pk_bf16_f32 v136, v128, v129\n"
  "v_cvt_pk_bf16_f32 v137, v130, v131\n"
  "v_cvt_pk_bf16_f32 v138, v132, v133\n"
  "v_cvt_pk_bf16_f32 v139, v134, v135\n"
  "buffer_store_dwordx4 v[136:139], v246, s[60:63], s90 offen sc1\n"
  "v_accvgpr_read_b32 v140, a152\n"
  "v_accvgpr_read_b32 v141, a153\n"
  "v_accvgpr_read_b32 v142, a154\n"
  "v_accvgpr_read_b32 v143, a155\n"
  "v_accvgpr_read_b32 v144, a156\n"
  "v_accvgpr_read_b32 v145, a157\n"
  "v_accvgpr_read_b32 v146, a158\n"
  "v_accvgpr_read_b32 v147, a159\n"
  "v_pk_mul_f32 v[140:141], v[140:141], s[88:89]\n"
  "v_pk_mul_f32 v[142:143], v[142:143], s[88:89]\n"
  "v_pk_mul_f32 v[144:145], v[144:145], s[88:89]\n"
  "v_pk_mul_f32 v[146:147], v[146:147], s[88:89]\n"
  "v_cvt_pk_bf16_f32 v148, v140, v141\n"
  "v_cvt_pk_bf16_f32 v149, v142, v143\n"
  "v_cvt_pk_bf16_f32 v150, v144, v145\n"
  "v_cvt_pk_bf16_f32 v151, v146, v147\n"
  "buffer_store_dwordx4 v[148:151], v246, s[60:63], s90 offen offset:256 sc1\n"
  "v_accvgpr_read_b32 v152, a168\n"
  "v_accvgpr_read_b32 v153, a169\n"
  "v_accvgpr_read_b32 v154, a170\n"
  "v_accvgpr_read_b32 v155, a171\n"
  "v_accvgpr_read_b32 v156, a172\n"
  "v_accvgpr_read_b32 v157, a173\n"
  "v_accvgpr_read_b32 v158, a174\n"
  "v_accvgpr_read_b32 v159, a175\n"
  "v_pk_mul_f32 v[152:153], v[152:153], s[88:89]\n"
  "v_pk_mul_f32 v[154:155], v[154:155], s[88:89]\n"
  "v_pk_mul_f32 v[156:157], v[156:157], s[88:89]\n"
  "v_pk_mul_f32 v[158:159], v[158:159], s[88:89]\n"
  "v_cvt_pk_bf16_f32 v160, v152, v153\n"
  "v_cvt_pk_bf16_f32 v161, v154, v155\n"
  "v_cvt_pk_bf16_f32 v162, v156, v157\n"
  "v_cvt_pk_bf16_f32 v163, v158, v159\n"
  "buffer_store_dwordx4 v[160:163], v246, s[60:63], s90 offen offset:512 sc1\n"
  "v_accvgpr_read_b32 v164, a184\n"
  "v_accvgpr_read_b32 v165, a185\n"
  "v_accvgpr_read_b32 v166, a186\n"
  "v_accvgpr_read_b32 v167, a187\n"
  "v_accvgpr_read_b32 v168, a188\n"
  "v_accvgpr_read_b32 v169, a189\n"
  "v_accvgpr_read_b32 v170, a190\n"
  "v_accvgpr_read_b32 v171, a191\n"
  "v_pk_mul_f32 v[164:165], v[164:165], s[88:89]\n"
  "v_pk_mul_f32 v[166:167], v[166:167], s[88:89]\n"
  "v_pk_mul_f32 v[168:169], v[168:169], s[88:89]\n"
  "v_pk_mul_f32 v[170:171], v[170:171], s[88:89]\n"
  "v_cvt_pk_bf16_f32 v172, v164, v165\n"
  "v_cvt_pk_bf16_f32 v173, v166, v167\n"
  "v_cvt_pk_bf16_f32 v174, v168, v169\n"
  "v_cvt_pk_bf16_f32 v175, v170, v171\n"
  "buffer_store_dwordx4 v[172:175], v246, s[60:63], s90 offen offset:768 sc1\n"
  "s_mov_b32 s90, 0x8000\n"
  "v_accvgpr_read_b32 v128, a192\n"
  "v_accvgpr_read_b32 v129, a193\n"
  "v_accvgpr_read_b32 v130, a194\n"
  "v_accvgpr_read_b32 v131, a195\n"
  "v_accvgpr_read_b32 v132, a196\n"
  "v_accvgpr_read_b32 v133, a197\n"
  "v_accvgpr_read_b32 v134, a198\n"
  "v_accvgpr_read_b32 v135, a199\n"
  "v_pk_mul_f32 v[128:129], v[128:129], s[88:89]\n"
  "v_pk_mul_f32 v[130:131], v[130:131], s[88:89]\n"
  "v_pk_mul_f32 v[132:133], v[132:133], s[88:89]\n"
  "v_pk_mul_f32 v[134:135], v[134:135], s[88:89]\n"
  "v_cvt_pk_bf16_f32 v136, v128, v129\n"
  "v_cvt_pk_bf16_f32 v137, v130, v131\n"
  "v_cvt_pk_bf16_f32 v138, v132, v133\n"
  "v_cvt_pk_bf16_f32 v139, v134, v135\n"
  "buffer_store_dwordx4 v[136:139], v246, s[60:63], s90 offen sc1\n"
  "v_accvgpr_read_b32 v140, a208\n"
  "v_accvgpr_read_b32 v141, a209\n"
  "v_accvgpr_read_b32 v142, a210\n"
  "v_accvgpr_read_b32 v143, a211\n"
  "v_accvgpr_read_b32 v144, a212\n"
  "v_accvgpr_read_b32 v145, a213\n"
  "v_accvgpr_read_b32 v146, a214\n"
  "v_accvgpr_read_b32 v147, a215\n"
  "v_pk_mul_f32 v[140:141], v[140:141], s[88:89]\n"
  "v_pk_mul_f32 v[142:143], v[142:143], s[88:89]\n"
  "v_pk_mul_f32 v[144:145], v[144:145], s[88:89]\n"
  "v_pk_mul_f32 v[146:147], v[146:147], s[88:89]\n"
  "v_cvt_pk_bf16_f32 v148, v140, v141\n"
  "v_cvt_pk_bf16_f32 v149, v142, v143\n"
  "v_cvt_pk_bf16_f32 v150, v144, v145\n"
  "v_cvt_pk_bf16_f32 v151, v146, v147\n"
  "buffer_store_dwordx4 v[148:151], v246, s[60:63], s90 offen offset:256 sc1\n"
  "v_accvgpr_read_b32 v152, a224\n"
  "v_accvgpr_read_b32 v153, a225\n"
  "v_accvgpr_read_b32 v154, a226\n"
  "v_accvgpr_read_b32 v155, a227\n"
  "v_accvgpr_read_b32 v156, a228\n"
  "v_accvgpr_read_b32 v157, a229\n"
  "v_accvgpr_read_b32 v158, a230\n"
  "v_accvgpr_read_b32 v159, a231\n"
  "v_pk_mul_f32 v[152:153], v[152:153], s[88:89]\n"
  "v_pk_mul_f32 v[154:155], v[154:155], s[88:89]\n"
  "v_pk_mul_f32 v[156:157], v[156:157], s[88:89]\n"
  "v_pk_mul_f32 v[158:159], v[158:159], s[88:89]\n"
  "v_cvt_pk_bf16_f32 v160, v152, v153\n"
  "v_cvt_pk_bf16_f32 v161, v154, v155\n"
  "v_cvt_pk_bf16_f32 v162, v156, v157\n"
  "v_cvt_pk_bf16_f32 v163, v158, v159\n"
  "buffer_store_dwordx4 v[160:163], v246, s[60:63], s90 offen offset:512 sc1\n"
  "v_accvgpr_read_b32 v164, a240\n"
  "v_accvgpr_read_b32 v165, a241\n"
  "v_accvgpr_read_b32 v166, a242\n"
  "v_accvgpr_read_b32 v167, a243\n"
  "v_accvgpr_read_b32 v168, a244\n"
  "v_accvgpr_read_b32 v169, a245\n"
  "v_accvgpr_read_b32 v170, a246\n"
  "v_accvgpr_read_b32 v171, a247\n"
  "v_pk_mul_f32 v[164:165], v[164:165], s[88:89]\n"
  "v_pk_mul_f32 v[166:167], v[166:167], s[88:89]\n"
  "v_pk_mul_f32 v[168:169], v[168:169], s[88:89]\n"
  "v_pk_mul_f32 v[170:171], v[170:171], s[88:89]\n"
  "v_cvt_pk_bf16_f32 v172, v164, v165\n"
  "v_cvt_pk_bf16_f32 v173, v166, v167\n"
  "v_cvt_pk_bf16_f32 v174, v168, v169\n"
  "v_cvt_pk_bf16_f32 v175, v170, v171\n"
  "buffer_store_dwordx4 v[172:175], v246, s[60:63], s90 offen offset:768 sc1\n"
  "s_mov_b32 s90, 0x9000\n"
  "v_accvgpr_read_b32 v128, a200\n"
  "v_accvgpr_read_b32 v129, a201\n"
  "v_accvgpr_read_b32 v130, a202\n"
  "v_accvgpr_read_b32 v131, a203\n"
  "v_accvgpr_read_b32 v132, a204\n"
  "v_accvgpr_read_b32 v133, a205\n"
  "v_accvgpr_read_b32 v134, a206\n"
  "v_accvgpr_read_b32 v135, a207\n"
  "v_pk_mul_f32 v[128:129], v[128:129], s[88:89]\n"
  "v_pk_mul_f32 v[130:131], v[130:131], s[88:89]\n"
  "v_pk_mul_f32 v[132:133], v[132:133], s[88:89]\n"
  "v_pk_mul_f32 v[134:135], v[134:135], s[88:89]\n"
  "v_cvt_pk_bf16_f32 v136, v128, v129\n"
  "v_cvt_pk_bf16_f32 v137, v130, v131\n"
  "v_cvt_pk_bf16_f32 v138, v132, v133\n"
  "v_cvt_pk_bf16_f32 v139, v134, v135\n"
  "buffer_store_dwordx4 v[136:139], v246, s[60:63], s90 offen sc1\n"
  "v_accvgpr_read_b32 v140, a216\n"
  "v_accvgpr_read_b32 v141, a217\n"
  "v_accvgpr_read_b32 v142, a218\n"
  "v_accvgpr_read_b32 v143, a219\n"
  "v_accvgpr_read_b32 v144, a220\n"
  "v_accvgpr_read_b32 v145, a221\n"
  "v_accvgpr_read_b32 v146, a222\n"
  "v_accvgpr_read_b32 v147, a223\n"
  "v_pk_mul_f32 v[140:141], v[140:141], s[88:89]\n"
  "v_pk_mul_f32 v[142:143], v[142:143], s[88:89]\n"
  "v_pk_mul_f32 v[144:145], v[144:145], s[88:89]\n"
  "v_pk_mul_f32 v[146:147], v[146:147], s[88:89]\n"
  "v_cvt_pk_bf16_f32 v148, v140, v141\n"
  "v_cvt_pk_bf16_f32 v149, v142, v143\n"
  "v_cvt_pk_bf16_f32 v150, v144, v145\n"
  "v_cvt_pk_bf16_f32 v151, v146, v147\n"
  "buffer_store_dwordx4 v[148:151], v246, s[60:63], s90 offen offset:256 sc1\n"
  "v_accvgpr_read_b32 v152, a232\n"
  "v_accvgpr_read_b32 v153, a233\n"
  "v_accvgpr_read_b32 v154, a234\n"
  "v_accvgpr_read_b32 v155, a235\n"
  "v_accvgpr_read_b32 v156, a236\n"
  "v_accvgpr_read_b32 v157, a237\n"
  "v_accvgpr_read_b32 v158, a238\n"
  "v_accvgpr_read_b32 v159, a239\n"
  "v_pk_mul_f32 v[152:153], v[152:153], s[88:89]\n"
  "v_pk_mul_f32 v[154:155], v[154:155], s[88:89]\n"
  "v_pk_mul_f32 v[156:157], v[156:157], s[88:89]\n"
  "v_pk_mul_f32 v[158:159], v[158:159], s[88:89]\n"
  "v_cvt_pk_bf16_f32 v160, v152, v153\n"
  "v_cvt_pk_bf16_f32 v161, v154, v155\n"
  "v_cvt_pk_bf16_f32 v162, v156, v157\n"
  "v_cvt_pk_bf16_f32 v163, v158, v159\n"
  "buffer_store_dwordx4 v[160:163], v246, s[60:63], s90 offen offset:512 sc1\n"
  "v_accvgpr_read_b32 v164, a248\n"
  "v_accvgpr_read_b32 v165, a249\n"
  "v_accvgpr_read_b32 v166, a250\n"
  "v_accvgpr_read_b32 v167, a251\n"
  "v_accvgpr_read_b32 v168, a252\n"
  "v_accvgpr_read_b32 v169, a253\n"
  "v_accvgpr_read_b32 v170, a254\n"
  "v_accvgpr_read_b32 v171, a255\n"
  "v_pk_mul_f32 v[164:165], v[164:165], s[88:89]\n"
  "v_pk_mul_f32 v[166:167], v[166:167], s[88:89]\n"
  "v_pk_mul_f32 v[168:169], v[168:169], s[88:89]\n"
  "v_pk_mul_f32 v[170:171], v[170:171], s[88:89]\n"
  "v_cvt_pk_bf16_f32 v172, v164, v165\n"
  "v_cvt_pk_bf16_f32 v173, v166, v167\n"
  "v_cvt_pk_bf16_f32 v174, v168, v169\n"
  "v_cvt_pk_bf16_f32 v175, v170, v171\n"
  "buffer_store_dwordx4 v[172:175], v246, s[60:63], s90 offen offset:768 sc1\n"
  "s_mov_b32 s90, 0xa000\n"
  "v_pk_mul_f32 v[64:65], v[64:65], s[88:89]\n"
  "v_pk_mul_f32 v[66:67], v[66:67], s[88:89]\n"
  "v_pk_mul_f32 v[68:69], v[68:69], s[88:89]\n"
  "v_pk_mul_f32 v[70:71], v[70:71], s[88:89]\n"
  "v_cvt_pk_bf16_f32 v136, v64, v65\n"
  "v_cvt_pk_bf16_f32 v137, v66, v67\n"
  "v_cvt_pk_bf16_f32 v138, v68, v69\n"
  "v_cvt_pk_bf16_f32 v139, v70, v71\n"
  "buffer_store_dwordx4 v[136:139], v246, s[60:63], s90 offen sc1\n"
  "v_pk_mul_f32 v[80:81], v[80:81], s[88:89]\n"
  "v_pk_mul_f32 v[82:83], v[82:83], s[88:89]\n"
  "v_pk_mul_f32 v[84:85], v[84:85], s[88:89]\n"
  "v_pk_mul_f32 v[86:87], v[86:87], s[88:89]\n"
  "v_cvt_pk_bf16_f32 v148, v80, v81\n"
  "v_cvt_pk_bf16_f32 v149, v82, v83\n"
  "v_cvt_pk_bf16_f32 v150, v84, v85\n"
  "v_cvt_pk_bf16_f32 v151, v86, v87\n"
  "buffer_store_dwordx4 v[148:151], v246, s[60:63], s90 offen offset:256 sc1\n"
  "v_pk_mul_f32 v[96:97], v[96:97], s[88:89]\n"
  "v_pk_mul_f32 v[98:99], v[98:99], s[88:89]\n"
  "v_pk_mul_f32 v[100:101], v[100:101], s[88:89]\n"
  "v_pk_mul_f32 v[102:103], v[102:103], s[88:89]\n"
  "v_cvt_pk_bf16_f32 v160, v96, v97\n"
  "v_cvt_pk_bf16_f32 v161, v98, v99\n"
  "v_cvt_pk_bf16_f32 v162, v100, v101\n"
  "v_cvt_pk_bf16_f32 v163, v102, v103\n"
  "buffer_store_dwordx4 v[160:163], v246, s[60:63], s90 offen offset:512 sc1\n"
  "v_pk_mul_f32 v[112:113], v[112:113], s[88:89]\n"
  "v_pk_mul_f32 v[114:115], v[114:115], s[88:89]\n"
  "v_pk_mul_f32 v[116:117], v[116:117], s[88:89]\n"
  "v_pk_mul_f32 v[118:119], v[118:119], s[88:89]\n"
  "v_cvt_pk_bf16_f32 v172, v112, v113\n"
  "v_cvt_pk_bf16_f32 v173, v114, v115\n"
  "v_cvt_pk_bf16_f32 v174, v116, v117\n"
  "v_cvt_pk_bf16_f32 v175, v118, v119\n"
  "buffer_store_dwordx4 v[172:175], v246, s[60:63], s90 offen offset:768 sc1\n"
  "s_mov_b32 s90, 0xb000\n"
  "v_pk_mul_f32 v[72:73], v[72:73], s[88:89]\n"
  "v_pk_mul_f32 v[74:75], v[74:75], s[88:89]\n"
  "v_pk_mul_f32 v[76:77], v[76:77], s[88:89]\n"
  "v_pk_mul_f32 v[78:79], v[78:79], s[88:89]\n"
  "v_cvt_pk_bf16_f32 v136, v72, v73\n"
  "v_cvt_pk_bf16_f32 v137, v74, v75\n"
  "v_cvt_pk_bf16_f32 v138, v76, v77\n"
  "v_cvt_pk_bf16_f32 v139, v78, v79\n"
  "buffer_store_dwordx4 v[136:139], v246, s[60:63], s90 offen sc1\n"
  "v_pk_mul_f32 v[88:89], v[88:89], s[88:89]\n"
  "v_pk_mul_f32 v[90:91], v[90:91], s[88:89]\n"
  "v_pk_mul_f32 v[92:93], v[92:93], s[88:89]\n"
  "v_pk_mul_f32 v[94:95], v[94:95], s[88:89]\n"
  "v_cvt_pk_bf16_f32 v148, v88, v89\n"
  "v_cvt_pk_bf16_f32 v149, v90, v91\n"
  "v_cvt_pk_bf16_f32 v150, v92, v93\n"
  "v_cvt_pk_bf16_f32 v151, v94, v95\n"
  "buffer_store_dwordx4 v[148:151], v246, s[60:63], s90 offen offset:256 sc1\n"
  "v_pk_mul_f32 v[104:105], v[104:105], s[88:89]\n"
  "v_pk_mul_f32 v[106:107], v[106:107], s[88:89]\n"
  "v_pk_mul_f32 v[108:109], v[108:109], s[88:89]\n"
  "v_pk_mul_f32 v[110:111], v[110:111], s[88:89]\n"
  "v_cvt_pk_bf16_f32 v160, v104, v105\n"
  "v_cvt_pk_bf16_f32 v161, v106, v107\n"
  "v_cvt_pk_bf16_f32 v162, v108, v109\n"
  "v_cvt_pk_bf16_f32 v163, v110, v111\n"
  "buffer_store_dwordx4 v[160:163], v246, s[60:63], s90 offen offset:512 sc1\n"
  "v_pk_mul_f32 v[120:121], v[120:121], s[88:89]\n"
  "v_pk_mul_f32 v[122:123], v[122:123], s[88:89]\n"
  "v_pk_mul_f32 v[124:125], v[124:125], s[88:89]\n"
  "v_pk_mul_f32 v[126:127], v[126:127], s[88:89]\n"
  "v_cvt_pk_bf16_f32 v172, v120, v121\n"
  "v_cvt_pk_bf16_f32 v173, v122, v123\n"
  "v_cvt_pk_bf16_f32 v174, v124, v125\n"
  "v_cvt_pk_bf16_f32 v175, v126, v127\n"
  "buffer_store_dwordx4 v[172:175], v246, s[60:63], s90 offen offset:768 sc1\n"
  "s_waitcnt vmcnt(24)\n"
  "s_branch L_next%=\n"
  "L_early%=:\n"
  "s_cmp_lg_u32 s36, 3\n"
  "s_cbranch_scc1 L_early0%=\n"
  "global_load_dword v216, v214, s[92:93]\n"
  "v_add_u32 v214, 0xe0000, v214\n"
  "v_min_u32 v214, 0x6fff80, v214\n"
  "s_cmp_lt_u32 s86, 4\n"
  "s_cbranch_scc1 L_early0%=\n"
  "s_add_u32 s86, s86, 1\n"
  "s_cmp_eq_u32 s86, 6\n"
  "s_cselect_b32 s86, 0, s86\n"
  "s_setpc_b64 s[96:97]\n"
  "L_early0%=:\n"
  "s_cmp_eq_u32 s86, 2\n"
  "s_cbranch_scc1 L_early2%=\n"
  "s_cmp_eq_u32 s86, 3\n"
  "s_cbranch_scc1 L_early3%=\n"
  "v_readfirstlane_b32 s35, v191\n"
  "s_nop 3\n"
  "s_add_u32 s35, s35, 1\n"
  "v_mov_b32 v161, 0\n"
  "v_mov_b32 v162, 0\n"
  "v_mov_b32 v188, 0\n"
  "v_mov_b32 v189, 0\n"
  "v_dot2_f32_bf16 v161, v164, v180, v161\n"
  "v_dot2_f32_bf16 v162, v172, v180, v162\n"
  "v_dot2_f32_bf16 v188, v165, v181, v188\n"
  "v_dot2_f32_bf16 v189, v173, v181, v189\n"
  "v_dot2_f32_bf16 v161, v166, v182, v161\n"
  "v_dot2_f32_bf16 v162, v174, v182, v162\n"
  "v_dot2_f32_bf16 v188, v167, v183, v188\n"
  "v_dot2_f32_bf16 v189, v175, v183, v189\n"
  "v_dot2_f32_bf16 v161, v168, v184, v161\n"
  "v_dot2_f32_bf16 v162, v176, v184, v162\n"
  "v_dot2_f32_bf16 v188, v169, v185, v188\n"
  "v_dot2_f32_bf16 v189, v177, v185, v189\n"
  "v_dot2_f32_bf16 v161, v170, v186, v161\n"
  "v_dot2_f32_bf16 v162, v178, v186, v162\n"
  "v_dot2_f32_bf16 v188, v171, v187, v188\n"
  "v_dot2_f32_bf16 v189, v179, v187, v189\n"
  "s_nop 3\n"
  "v_add_f32 v161, v161, v188\n"
  "v_add_f32 v162, v162, v189\n"
  "s_nop 1\n"
  "v_add_f32_dpp v161, v161, v161 row_shr:1 bound_ctrl:0\n"
  "v_add_f32_dpp v162, v162, v162 row_shr:1 bound_ctrl:0\n"
  "s_nop 0\n"
  "v_add_f32_dpp v161, v161, v161 row_shr:2 bound_ctrl:0\n"
  "v_add_f32_dpp v162, v162, v162 row_shr:2 bound_ctrl:0\n"
  "s_nop 0\n"
  "v_add_f32_dpp v161, v161, v161 row_shr:4 bound_ctrl:0\n"
  "v_add_f32_dpp v162, v162, v162 row_shr:4 bound_ctrl:0\n"
  "s_nop 0\n"
  "v_add_f32_dpp v161, v161, v161 row_shr:8 bound_ctrl:0\n"
  "v_add_f32_dpp v162, v162, v162 row_shr:8 bound_ctrl:0\n"
  "s_nop 0\n"
  "v_add_f32_dpp v161, v161, v161 row_bcast:15 row_mask:0xa\n"
  "v_add_f32_dpp v162, v162, v162 row_bcast:15 row_mask:0xa\n"
  "s_nop 0\n"
  "v_add_f32_dpp v161, v161, v161 row_bcast:31 row_mask:0xc\n"
  "v_add_f32_dpp v162, v162, v162 row_bcast:31 row_mask:0xc\n"
  "s_nop 0\n"
  "v_mul_f32 v161, 0xbfb8aa3b, v161\n"
  "v_mul_f32 v162, 0xbfb8aa3b, v162\n"
  "v_exp_f32 v161, v161\n"
  "v_exp_f32 v162, v162\n"
  "s_nop 0\n"
  "v_add_f32 v161, 1.0, v161\n"
  "v_add_f32 v162, 1.0, v162\n"
  "v_rcp_f32 v164, v161\n"
  "v_rcp_f32 v166, v162\n"
  "v_mov_b32 v165, s35\n"
  "v_mov_b32 v167, s35\n"
  "v_sub_u32 v163, 63, v255\n"
  "v_lshlrev_b32 v163, 20, v163\n"
  "s_mov_b32 s91, 0x4000000\n"
  "s_add_u32 s60, s8, s91\n"
  "s_addc_u32 s61, s9, 0\n"
  "s_mov_b32 s62, 0x4000\n"
  "s_mov_b32 s63, 0x20000\n"
  "s_lshl_b32 s87, s98, 2\n"
  "s_add_u32 s87, s87, s34\n"
  "s_lshl_b32 s87, s87, 4\n"
  "buffer_store_dwordx4 v[164:167], v163, s[60:63], s87 offen sc1\n"
  "s_mov_b32 s86, 2\n"
  "s_lshr_b32 s87, s98, 2\n"
  "s_or_b32 s87, s87, s34\n"
  "s_cbranch_scc1 L_early1r%=\n"
  "s_lshl_b32 s87, s98, 7\n"
  "s_add_u32 s87, s87, 0x5a00800\n"
  "v_mov_b32 v170, s87\n"
  "v_mov_b32 v172, 0\n"
  "v_mov_b32 v173, s35\n"
  "global_store_dwordx2 v170, v[172:173], s[8:9] sc1\n"
  "L_early1r%=:\n"
  "s_setpc_b64 s[96:97]\n"
  "; covered\n"
  "L_early2%=:\n"
  "s_lshr_b32 s87, s98, 3\n"
  "s_lshl_b32 s87, s87, 2\n"
  "s_add_u32 s87, s87, s34\n"
  "s_lshl_b32 s87, s87, 15\n"
  "s_and_b32 s90, s98, 7\n"
  "s_lshl_b32 s90, s90, 10\n"
  "s_add_u32 s87, s87, s90\n"
  "v_and_b32 v217, 7, v255\n"
  "v_lshl_add_u32 v217, v217, 7, s87\n"
  "v_and_b32 v215, 3, v249\n"
  "v_lshl_add_u32 v217, v215, 13, v217\n"
  "global_load_dword v216, v217, s[14:15]\n"
  "s_mov_b32 s86, 3\n"
  "s_setpc_b64 s[96:97]\n"
  "L_early3%=:\n"
  "s_lshr_b32 s87, s98, 3\n"
  "s_lshl_b32 s87, s87, 2\n"
  "s_add_u32 s87, s87, s34\n"
  "s_lshl_b32 s87, s87, 15\n"
  "s_and_b32 s90, s98, 7\n"
  "s_lshl_b32 s90, s90, 10\n"
  "s_add_u32 s87, s87, s90\n"
  "s_add_u32 s87, s87, 0x400000\n"
  "v_and_b32 v217, 7, v255\n"
  "v_lshl_add_u32 v217, v217, 7, s87\n"
  "v_and_b32 v215, 3, v249\n"
  "v_lshl_add_u32 v217, v215, 13, v217\n"
  "global_load_dword v216, v217, s[14:15]\n"
  "s_cmp_eq_u32 s36, 3\n"
  "s_cselect_b32 s86, 4, 0\n"
  "s_setpc_b64 s[96:97]\n"
  "L_hk_g%=:\n"
  "s_cmp_le_u32 s86, 8\n"
  "s_cbranch_scc1 L_hkg_g%=\n"
  "s_cmp_eq_u32 s86, 0\n"
  "s_cbranch_scc1 L_sg_g%=\n"
  "s_lshl2_add_u32 s86, s34, s86\n"
  "v_mov_b32 v220, s86\n"
  "v_mov_b32 v221, s35\n"
  "global_store_dword v220, v221, s[8:9] sc1\n"
  "s_mov_b32 s86, 0\n"
  "L_sg_g%=:\n"
  "s_branch L_hkr_g%=\n"
  "L_hkg_g%=:\n"
  "s_getpc_b64 s[96:97]\n"
  "s_add_u32 s96, s96, 12\n"
  "s_addc_u32 s97, s97, 0\n"
  "s_branch L_early%=\n"
  "s_branch L_hkr_g%=\n"
  "L_hk_gs%=:\n"
  "s_cmp_le_u32 s86, 8\n"
  "s_cbranch_scc1 L_hkg_gs%=\n"
  "s_cmp_eq_u32 s86, 0\n"
  "s_cbranch_scc1 L_sg_gs%=\n"
  "s_lshl2_add_u32 s86, s34, s86\n"
  "v_mov_b32 v220, s86\n"
  "v_mov_b32 v221, s35\n"
  "global_store_dword v220, v221, s[8:9] sc1\n"
  "s_mov_b32 s86, 0\n"
  "L_sg_gs%=:\n"
  "s_branch L_hkr_gs%=\n"
  "L_hkg_gs%=:\n"
  "s_getpc_b64 s[96:97]\n"
  "s_add_u32 s96, s96, 12\n"
  "s_addc_u32 s97, s97, 0\n"
  "s_branch L_early%=\n"
  "s_branch L_hkr_gs%=\n"
  "L_gev0_g%=:\n"
  "s_cmp_lg_u32 s36, 3\n"
  "s_cbranch_scc1 L_gsw2_g%=\n"
  "s_lshr_b32 s90, s34, 1\n"
  "s_lshl_b32 s87, s90, 10\n"
  "s_add_u32 s87, s87, s40\n"
  "v_and_b32 v199, 15, v255\n"
  "v_lshl_add_u32 v199, v199, 3, s87\n"
  "v_add_u32 v199, 0x4000000, v199\n"
  "global_load_dwordx2 v[200:201], v199, s[8:9] offset:0 sc1\n"
  "global_load_dwordx2 v[202:203], v199, s[8:9] offset:128 sc1\n"
  "global_load_dwordx2 v[204:205], v199, s[8:9] offset:256 sc1\n"
  "global_load_dwordx2 v[206:207], v199, s[8:9] offset:384 sc1\n"
  "global_load_dwordx2 v[208:209], v199, s[8:9] offset:512 sc1\n"
  "global_load_dwordx2 v[210:211], v199, s[8:9] offset:640 sc1\n"
  "global_load_dwordx2 v[212:213], v199, s[8:9] offset:768 sc1\n"
  "global_load_dwordx2 v[214:215], v199, s[8:9] offset:896 sc1\n"
  "L_gsw2_g%=:\n"
  "s_cmp_eq_u32 s44, 4\n"
  "s_cselect_b32 s101, 3, 2\n"
  "s_getpc_b64 s[96:97]\n"
  "s_add_u32 s96, s96, 12\n"
  "s_addc_u32 s97, s97, 0\n"
  "s_branch L_sregsx%=\n"
  "s_branch L_gevr0_g%=\n"
  "L_gev1_g%=:\n"
  "s_cmp_ge_u32 s44, 4\n"
  "s_cselect_b32 s87, 1, 2\n"
  "s_cmp_eq_u32 s82, s87\n"
  "s_cbranch_scc1 L_gsww_g%=\n"
  "s_mov_b32 s58, 0\n"
  "s_mov_b32 s84, 0x30000\n"
  "s_mov_b32 s95, 0\n"
  "s_mov_b32 s85, 2\n"
  "s_branch L_gevr1_g%=\n"
  "L_gsww_g%=:\n"
  "s_getpc_b64 s[96:97]\n"
  "s_add_u32 s96, s96, 12\n"
  "s_addc_u32 s97, s97, 0\n"
  "s_branch L_sregsw%=\n"
  "s_branch L_gevr1_g%=\n"
  "L_gev0_gs%=:\n"
  "s_cmp_eq_u32 s44, 4\n"
  "s_cselect_b32 s101, 3, 2\n"
  "s_getpc_b64 s[96:97]\n"
  "s_add_u32 s96, s96, 12\n"
  "s_addc_u32 s97, s97, 0\n"
  "s_branch L_sregsx%=\n"
  "s_branch L_gevr0_gs%=\n"
  "L_gev1_gs%=:\n"
  "s_cmp_ge_u32 s44, 4\n"
  "s_cselect_b32 s87, 1, 2\n"
  "s_cmp_eq_u32 s82, s87\n"
  "s_cbranch_scc1 L_gsww_gs%=\n"
  "s_mov_b32 s58, 0\n"
  "s_mov_b32 s84, 0x30000\n"
  "s_mov_b32 s95, 0\n"
  "s_mov_b32 s85, 2\n"
  "s_branch L_gevr1_gs%=\n"
  "L_gsww_gs%=:\n"
  "s_getpc_b64 s[96:97]\n"
  "s_add_u32 s96, s96, 12\n"
  "s_addc_u32 s97, s97, 0\n"
  "s_branch L_sregsw%=\n"
  "s_branch L_gevr1_gs%=\n"
  "L_e_gu%=:\n"
  "s_and_b32 s87, s34, 1\n"
  "s_lshl_b32 s87, s87, 15\n"
  "s_lshr_b32 s90, s34, 1\n"
  "s_lshl_b32 s90, s90, 11\n"
  "s_add_u32 s87, s87, s90\n"
  "s_add_u32 s87, s87, s39\n"
  "s_add_u32 s60, s8, s87\n"
  "s_addc_u32 s61, s9, 0\n"
  "s_sub_u32 s62, 0x5b00000, s87\n"
  "s_mov_b32 s63, 0x20000\n"
  "v_lshrrev_b32 v198, 4, v255\n"
  "v_lshlrev_b32 v198, 12, v198\n"
  "v_and_b32 v197, 15, v255\n"
  "v_lshl_add_u32 v198, v197, 4, v198\n"
  "s_mov_b32 s88, 0xbdd2d3e8\n"
  "s_mov_b32 s89, 0xbdd2d3e8\n"
  "v_mov_b32 v194, 0xc0135761\n"
  "v_mov_b32 v195, 0xc0135761\n"
  "s_mov_b32 s90, 0x4000\n"
  "s_mov_b32 s99, 0x4000\n"
  "L_gc_gu%=:\n"
  "v_xor_b32 v196, s35, v201\n"
  "v_xor_b32 v203, s35, v203\n"
  "v_or_b32 v196, v196, v203\n"
  "v_xor_b32 v205, s35, v205\n"
  "v_or_b32 v196, v196, v205\n"
  "v_xor_b32 v207, s35, v207\n"
  "v_or_b32 v196, v196, v207\n"
  "v_xor_b32 v209, s35, v209\n"
  "v_or_b32 v196, v196, v209\n"
  "v_xor_b32 v211, s35, v211\n"
  "v_or_b32 v196, v196, v211\n"
  "v_xor_b32 v213, s35, v213\n"
  "v_or_b32 v196, v196, v213\n"
  "v_xor_b32 v215, s35, v215\n"
  "v_or_b32 v196, v196, v215\n"
  "v_cmp_ne_u32 vcc, 0, v196\n"
  "s_nop 4\n"
  "s_cbranch_vccz L_gd_gu%=\n"
  "s_sleep 2\n"
  "global_load_dwordx2 v[200:201], v199, s[8:9] offset:0 sc1\n"
  "global_load_dwordx2 v[202:203], v199, s[8:9] offset:128 sc1\n"
  "global_load_dwordx2 v[204:205], v199, s[8:9] offset:256 sc1\n"
  "global_load_dwordx2 v[206:207], v199, s[8:9] offset:384 sc1\n"
  "global_load_dwordx2 v[208:209], v199, s[8:9] offset:512 sc1\n"
  "global_load_dwordx2 v[210:211], v199, s[8:9] offset:640 sc1\n"
  "global_load_dwordx2 v[212:213], v199, s[8:9] offset:768 sc1\n"
  "global_load_dwordx2 v[214:215], v199, s[8:9] offset:896 sc1\n"
  "s_waitcnt vmcnt(0)\n"
  "s_sub_u32 s99, s99, 1\n"
  "s_cbranch_scc0 L_gc_gu%=\n"
  "L_gd_gu%=:\n"
  "v_sub_f32 v200, 1.0, v200\n"
  "v_sub_f32 v202, 1.0, v202\n"
  "v_sub_f32 v204, 1.0, v204\n"
  "v_sub_f32 v206, 1.0, v206\n"
  "v_sub_f32 v208, 1.0, v208\n"
  "v_sub_f32 v210, 1.0, v210\n"
  "v_sub_f32 v212, 1.0, v212\n"
  "v_sub_f32 v214, 1.0, v214\n"
  "v_rcp_f32 v200, v200\n"
  "v_rcp_f32 v202, v202\n"
  "v_rcp_f32 v204, v204\n"
  "v_rcp_f32 v206, v206\n"
  "v_rcp_f32 v208, v208\n"
  "v_rcp_f32 v210, v210\n"
  "v_rcp_f32 v212, v212\n"
  "v_rcp_f32 v214, v214\n"
  "v_min_f32 v200, 0x7149f2ca, v200\n"
  "v_mov_b32 v201, v200\n"
  "v_min_f32 v202, 0x7149f2ca, v202\n"
  "v_mov_b32 v203, v202\n"
  "v_min_f32 v204, 0x7149f2ca, v204\n"
  "v_mov_b32 v205, v204\n"
  "v_min_f32 v206, 0x7149f2ca, v206\n"
  "v_mov_b32 v207, v206\n"
  "v_min_f32 v208, 0x7149f2ca, v208\n"
  "v_mov_b32 v209, v208\n"
  "v_min_f32 v210, 0x7149f2ca, v210\n"
  "v_mov_b32 v211, v210\n"
  "v_min_f32 v212, 0x7149f2ca, v212\n"
  "v_mov_b32 v213, v212\n"
  "v_min_f32 v214, 0x7149f2ca, v214\n"
  "v_mov_b32 v215, v214\n"
  "v_accvgpr_read_b32 v128, a128\n"
  "v_accvgpr_read_b32 v129, a129\n"
  "v_accvgpr_read_b32 v130, a130\n"
  "v_accvgpr_read_b32 v131, a131\n"
  "v_accvgpr_read_b32 v132, a132\n"
  "v_accvgpr_read_b32 v133, a133\n"
  "v_accvgpr_read_b32 v134, a134\n"
  "v_accvgpr_read_b32 v135, a135\n"
  "v_pk_mul_f32 v[144:145], v[128:129], v[128:129]\n"
  "v_pk_mul_f32 v[146:147], v[130:131], v[130:131]\n"
  "v_pk_mul_f32 v[148:149], v[132:133], v[132:133]\n"
  "v_pk_mul_f32 v[150:151], v[134:135], v[134:135]\n"
  "v_pk_fma_f32 v[144:145], v[144:145], s[88:89], v[194:195]\n"
  "v_pk_fma_f32 v[146:147], v[146:147], s[88:89], v[194:195]\n"
  "v_pk_fma_f32 v[148:149], v[148:149], s[88:89], v[194:195]\n"
  "v_pk_fma_f32 v[150:151], v[150:151], s[88:89], v[194:195]\n"
  "v_pk_mul_f32 v[144:145], v[144:145], v[128:129]\n"
  "v_pk_mul_f32 v[146:147], v[146:147], v[130:131]\n"
  "v_pk_mul_f32 v[148:149], v[148:149], v[132:133]\n"
  "v_pk_mul_f32 v[150:151], v[150:151], v[134:135]\n"
  "v_exp_f32 v144, v144\n"
  "v_pk_mul_f32 v[0:1], v[0:1], v[128:129]\n"
  "v_exp_f32 v145, v145\n"
  "v_pk_mul_f32 v[2:3], v[2:3], v[130:131]\n"
  "v_exp_f32 v146, v146\n"
  "v_pk_mul_f32 v[4:5], v[4:5], v[132:133]\n"
  "v_exp_f32 v147, v147\n"
  "v_pk_mul_f32 v[6:7], v[6:7], v[134:135]\n"
  "v_accvgpr_read_b32 v136, a144\n"
  "v_exp_f32 v148, v148\n"
  "v_accvgpr_read_b32 v137, a145\n"
  "v_exp_f32 v149, v149\n"
  "v_accvgpr_read_b32 v138, a146\n"
  "v_exp_f32 v150, v150\n"
  "v_accvgpr_read_b32 v139, a147\n"
  "v_exp_f32 v151, v151\n"
  "v_accvgpr_read_b32 v140, a148\n"
  "v_accvgpr_read_b32 v141, a149\n"
  "v_accvgpr_read_b32 v142, a150\n"
  "v_accvgpr_read_b32 v143, a151\n"
  "v_pk_mul_f32 v[152:153], v[136:137], v[136:137]\n"
  "v_pk_mul_f32 v[154:155], v[138:139], v[138:139]\n"
  "v_pk_mul_f32 v[156:157], v[140:141], v[140:141]\n"
  "v_pk_mul_f32 v[158:159], v[142:143], v[142:143]\n"
  "v_pk_fma_f32 v[152:153], v[152:153], s[88:89], v[194:195]\n"
  "v_pk_fma_f32 v[154:155], v[154:155], s[88:89], v[194:195]\n"
  "v_pk_fma_f32 v[156:157], v[156:157], s[88:89], v[194:195]\n"
  "v_pk_fma_f32 v[158:159], v[158:159], s[88:89], v[194:195]\n"
  "v_pk_mul_f32 v[152:153], v[152:153], v[136:137]\n"
  "v_pk_mul_f32 v[154:155], v[154:155], v[138:139]\n"
  "v_pk_mul_f32 v[156:157], v[156:157], v[140:141]\n"
  "v_pk_mul_f32 v[158:159], v[158:159], v[142:143]\n"
  "v_pk_fma_f32 v[144:145], v[144:145], v[200:201], v[200:201]\n"
  "v_pk_fma_f32 v[146:147], v[146:147], v[200:201], v[200:201]\n"
  "v_pk_fma_f32 v[148:149], v[148:149], v[200:201], v[200:201]\n"
  "v_pk_fma_f32 v[150:151], v[150:151], v[200:201], v[200:201]\n"
  "v_exp_f32 v152, v152\n"
  "v_pk_mul_f32 v[16:17], v[16:17], v[136:137]\n"
  "v_exp_f32 v153, v153\n"
  "v_pk_mul_f32 v[18:19], v[18:19], v[138:139]\n"
  "v_exp_f32 v154, v154\n"
  "v_pk_mul_f32 v[20:21], v[20:21], v[140:141]\n"
  "v_exp_f32 v155, v155\n"
  "v_pk_mul_f32 v[22:23], v[22:23], v[142:143]\n"
  "v_accvgpr_read_b32 v128, a160\n"
  "v_exp_f32 v156, v156\n"
  "v_accvgpr_read_b32 v129, a161\n"
  "v_exp_f32 v157, v157\n"
  "v_accvgpr_read_b32 v130, a162\n"
  "v_exp_f32 v158, v158\n"
  "v_accvgpr_read_b32 v131, a163\n"
  "v_exp_f32 v159, v159\n"
  "v_accvgpr_read_b32 v132, a164\n"
  "v_accvgpr_read_b32 v133, a165\n"
  "v_rcp_f32 v144, v144\n"
  "v_accvgpr_read_b32 v134, a166\n"
  "v_accvgpr_read_b32 v135, a167\n"
  "v_rcp_f32 v145, v145\n"
  "v_pk_mul_f32 v[160:161], v[128:129], v[128:129]\n"
  "v_pk_mul_f32 v[162:163], v[130:131], v[130:131]\n"
  "v_rcp_f32 v146, v146\n"
  "v_pk_mul_f32 v[164:165], v[132:133], v[132:133]\n"
  "v_pk_mul_f32 v[166:167], v[134:135], v[134:135]\n"
  "v_rcp_f32 v147, v147\n"
  "v_pk_fma_f32 v[160:161], v[160:161], s[88:89], v[194:195]\n"
  "v_pk_fma_f32 v[162:163], v[162:163], s[88:89], v[194:195]\n"
  "v_pk_fma_f32 v[164:165], v[164:165], s[88:89], v[194:195]\n"
  "v_rcp_f32 v148, v148\n"
  "v_pk_fma_f32 v[166:167], v[166:167], s[88:89], v[194:195]\n"
  "v_pk_mul_f32 v[160:161], v[160:161], v[128:129]\n"
  "v_rcp_f32 v149, v149\n"
  "v_pk_mul_f32 v[162:163], v[162:163], v[130:131]\n"
  "v_pk_mul_f32 v[164:165], v[164:165], v[132:133]\n"
  "v_rcp_f32 v150, v150\n"
  "v_pk_mul_f32 v[166:167], v[166:167], v[134:135]\n"
  "v_pk_fma_f32 v[152:153], v[152:153], v[202:203], v[202:203]\n"
  "v_rcp_f32 v151, v151\n"
  "v_pk_fma_f32 v[154:155], v[154:155], v[202:203], v[202:203]\n"
  "v_pk_fma_f32 v[156:157], v[156:157], v[202:203], v[202:203]\n"
  "v_pk_fma_f32 v[158:159], v[158:159], v[202:203], v[202:203]\n"
  "v_exp_f32 v160, v160\n"
  "v_pk_mul_f32 v[0:1], v[0:1], v[144:145]\n"
  "v_pk_mul_f32 v[2:3], v[2:3], v[146:147]\n"
  "v_exp_f32 v161, v161\n"
  "v_pk_mul_f32 v[4:5], v[4:5], v[148:149]\n"
  "v_pk_mul_f32 v[6:7], v[6:7], v[150:151]\n"
  "v_exp_f32 v162, v162\n"
  "v_cvt_pk_bf16_f32 v0, v0, v1\n"
  "v_cvt_pk_bf16_f32 v1, v2, v3\n"
  "v_cvt_pk_bf16_f32 v2, v4, v5\n"
  "v_exp_f32 v163, v163\n"
  "v_cvt_pk_bf16_f32 v3, v6, v7\n"
  "buffer_store_dwordx4 v[0:3], v198, s[60:63], 0 offen sc1\n"
  "v_exp_f32 v164, v164\n"
  "v_pk_mul_f32 v[32:33], v[32:33], v[128:129]\n"
  "v_pk_mul_f32 v[34:35], v[34:35], v[130:131]\n"
  "v_exp_f32 v165, v165\n"
  "v_pk_mul_f32 v[36:37], v[36:37], v[132:133]\n"
  "v_pk_mul_f32 v[38:39], v[38:39], v[134:135]\n"
  "v_accvgpr_read_b32 v136, a176\n"
  "v_exp_f32 v166, v166\n"
  "v_accvgpr_read_b32 v137, a177\n"
  "v_accvgpr_read_b32 v138, a178\n"
  "v_exp_f32 v167, v167\n"
  "v_accvgpr_read_b32 v139, a179\n"
  "v_accvgpr_read_b32 v140, a180\n"
  "v_accvgpr_read_b32 v141, a181\n"
  "v_rcp_f32 v152, v152\n"
  "v_accvgpr_read_b32 v142, a182\n"
  "v_accvgpr_read_b32 v143, a183\n"
  "v_rcp_f32 v153, v153\n"
  "v_pk_mul_f32 v[168:169], v[136:137], v[136:137]\n"
  "v_pk_mul_f32 v[170:171], v[138:139], v[138:139]\n"
  "v_rcp_f32 v154, v154\n"
  "v_pk_mul_f32 v[172:173], v[140:141], v[140:141]\n"
  "v_pk_mul_f32 v[174:175], v[142:143], v[142:143]\n"
  "v_rcp_f32 v155, v155\n"
  "v_pk_fma_f32 v[168:169], v[168:169], s[88:89], v[194:195]\n"
  "v_pk_fma_f32 v[170:171], v[170:171], s[88:89], v[194:195]\n"
  "v_pk_fma_f32 v[172:173], v[172:173], s[88:89], v[194:195]\n"
  "v_rcp_f32 v156, v156\n"
  "v_pk_fma_f32 v[174:175], v[174:175], s[88:89], v[194:195]\n"
  "v_pk_mul_f32 v[168:169], v[168:169], v[136:137]\n"
  "v_rcp_f32 v157, v157\n"
  "v_pk_mul_f32 v[170:171], v[170:171], v[138:139]\n"
  "v_pk_mul_f32 v[172:173], v[172:173], v[140:141]\n"
  "v_rcp_f32 v158, v158\n"
  "v_pk_mul_f32 v[174:175], v[174:175], v[142:143]\n"
  "v_pk_fma_f32 v[160:161], v[160:161], v[204:205], v[204:205]\n"
  "v_rcp_f32 v159, v159\n"
  "v_pk_fma_f32 v[162:163], v[162:163], v[204:205], v[204:205]\n"
  "v_pk_fma_f32 v[164:165], v[164:165], v[204:205], v[204:205]\n"
  "v_pk_fma_f32 v[166:167], v[166:167], v[204:205], v[204:205]\n"
  "v_exp_f32 v168, v168\n"
  "v_pk_mul_f32 v[16:17], v[16:17], v[152:153]\n"
  "v_pk_mul_f32 v[18:19], v[18:19], v[154:155]\n"
  "v_exp_f32 v169, v169\n"
  "v_pk_mul_f32 v[20:21], v[20:21], v[156:157]\n"
  "v_pk_mul_f32 v[22:23], v[22:23], v[158:159]\n"
  "v_exp_f32 v170, v170\n"
  "v_cvt_pk_bf16_f32 v16, v16, v17\n"
  "v_cvt_pk_bf16_f32 v17, v18, v19\n"
  "v_cvt_pk_bf16_f32 v18, v20, v21\n"
  "v_exp_f32 v171, v171\n"
  "v_cvt_pk_bf16_f32 v19, v22, v23\n"
  "buffer_store_dwordx4 v[16:19], v198, s[60:63], 0 offen offset:256 sc1\n"
  "v_exp_f32 v172, v172\n"
  "v_pk_mul_f32 v[48:49], v[48:49], v[136:137]\n"
  "v_pk_mul_f32 v[50:51], v[50:51], v[138:139]\n"
  "v_exp_f32 v173, v173\n"
  "v_pk_mul_f32 v[52:53], v[52:53], v[140:141]\n"
  "v_pk_mul_f32 v[54:55], v[54:55], v[142:143]\n"
  "v_accvgpr_read_b32 v128, a192\n"
  "v_exp_f32 v174, v174\n"
  "v_accvgpr_read_b32 v129, a193\n"
  "v_accvgpr_read_b32 v130, a194\n"
  "v_exp_f32 v175, v175\n"
  "v_accvgpr_read_b32 v131, a195\n"
  "v_accvgpr_read_b32 v132, a196\n"
  "v_accvgpr_read_b32 v133, a197\n"
  "v_rcp_f32 v160, v160\n"
  "v_accvgpr_read_b32 v134, a198\n"
  "v_accvgpr_read_b32 v135, a199\n"
  "v_rcp_f32 v161, v161\n"
  "v_pk_mul_f32 v[144:145], v[128:129], v[128:129]\n"
  "v_pk_mul_f32 v[146:147], v[130:131], v[130:131]\n"
  "v_rcp_f32 v162, v162\n"
  "v_pk_mul_f32 v[148:149], v[132:133], v[132:133]\n"
  "v_pk_mul_f32 v[150:151], v[134:135], v[134:135]\n"
  "v_rcp_f32 v163, v163\n"
  "v_pk_fma_f32 v[144:145], v[144:145], s[88:89], v[194:195]\n"
  "v_pk_fma_f32 v[146:147], v[146:147], s[88:89], v[194:195]\n"
  "v_pk_fma_f32 v[148:149], v[148:149], s[88:89], v[194:195]\n"
  "v_rcp_f32 v164, v164\n"
  "v_pk_fma_f32 v[150:151], v[150:151], s[88:89], v[194:195]\n"
  "v_pk_mul_f32 v[144:145], v[144:145], v[128:129]\n"
  "v_rcp_f32 v165, v165\n"
  "v_pk_mul_f32 v[146:147], v[146:147], v[130:131]\n"
  "v_pk_mul_f32 v[148:149], v[148:149], v[132:133]\n"
  "v_rcp_f32 v166, v166\n"
  "v_pk_mul_f32 v[150:151], v[150:151], v[134:135]\n"
  "v_pk_fma_f32 v[168:169], v[168:169], v[206:207], v[206:207]\n"
  "v_rcp_f32 v167, v167\n"
  "v_pk_fma_f32 v[170:171], v[170:171], v[206:207], v[206:207]\n"
  "v_pk_fma_f32 v[172:173], v[172:173], v[206:207], v[206:207]\n"
  "v_pk_fma_f32 v[174:175], v[174:175], v[206:207], v[206:207]\n"
  "v_exp_f32 v144, v144\n"
  "v_pk_mul_f32 v[32:33], v[32:33], v[160:161]\n"
  "v_pk_mul_f32 v[34:35], v[34:35], v[162:163]\n"
  "v_exp_f32 v145, v145\n"
  "v_pk_mul_f32 v[36:37], v[36:37], v[164:165]\n"
  "v_pk_mul_f32 v[38:39], v[38:39], v[166:167]\n"
  "v_exp_f32 v146, v146\n"
  "v_cvt_pk_bf16_f32 v32, v32, v33\n"
  "v_cvt_pk_bf16_f32 v33, v34, v35\n"
  "v_cvt_pk_bf16_f32 v34, v36, v37\n"
  "v_exp_f32 v147, v147\n"
  "v_cvt_pk_bf16_f32 v35, v38, v39\n"
  "buffer_store_dwordx4 v[32:35], v198, s[60:63], 0 offen offset:512 sc1\n"
  "v_exp_f32 v148, v148\n"
  "v_pk_mul_f32 v[64:65], v[64:65], v[128:129]\n"
  "v_pk_mul_f32 v[66:67], v[66:67], v[130:131]\n"
  "v_exp_f32 v149, v149\n"
  "v_pk_mul_f32 v[68:69], v[68:69], v[132:133]\n"
  "v_pk_mul_f32 v[70:71], v[70:71], v[134:135]\n"
  "v_accvgpr_read_b32 v136, a208\n"
  "v_exp_f32 v150, v150\n"
  "v_accvgpr_read_b32 v137, a209\n"
  "v_accvgpr_read_b32 v138, a210\n"
  "v_exp_f32 v151, v151\n"
  "v_accvgpr_read_b32 v139, a211\n"
  "v_accvgpr_read_b32 v140, a212\n"
  "v_accvgpr_read_b32 v141, a213\n"
  "v_rcp_f32 v168, v168\n"
  "v_accvgpr_read_b32 v142, a214\n"
  "v_accvgpr_read_b32 v143, a215\n"
  "v_rcp_f32 v169, v169\n"
  "v_pk_mul_f32 v[152:153], v[136:137], v[136:137]\n"
  "v_pk_mul_f32 v[154:155], v[138:139], v[138:139]\n"
  "v_rcp_f32 v170, v170\n"
  "v_pk_mul_f32 v[156:157], v[140:141], v[140:141]\n"
  "v_pk_mul_f32 v[158:159], v[142:143], v[142:143]\n"
  "v_rcp_f32 v171, v171\n"
  "v_pk_fma_f32 v[152:153], v[152:153], s[88:89], v[194:195]\n"
  "v_pk_fma_f32 v[154:155], v[154:155], s[88:89], v[194:195]\n"
  "v_pk_fma_f32 v[156:157], v[156:157], s[88:89], v[194:195]\n"
  "v_rcp_f32 v172, v172\n"
  "v_pk_fma_f32 v[158:159], v[158:159], s[88:89], v[194:195]\n"
  "v_pk_mul_f32 v[152:153], v[152:153], v[136:137]\n"
  "v_rcp_f32 v173, v173\n"
  "v_pk_mul_f32 v[154:155], v[154:155], v[138:139]\n"
  "v_pk_mul_f32 v[156:157], v[156:157], v[140:141]\n"
  "v_rcp_f32 v174, v174\n"
  "v_pk_mul_f32 v[158:159], v[158:159], v[142:143]\n"
  "v_pk_fma_f32 v[144:145], v[144:145], v[208:209], v[208:209]\n"
  "v_rcp_f32 v175, v175\n"
  "v_pk_fma_f32 v[146:147], v[146:147], v[208:209], v[208:209]\n"
  "v_pk_fma_f32 v[148:149], v[148:149], v[208:209], v[208:209]\n"
  "v_pk_fma_f32 v[150:151], v[150:151], v[208:209], v[208:209]\n"
  "v_exp_f32 v152, v152\n"
  "v_pk_mul_f32 v[48:49], v[48:49], v[168:169]\n"
  "v_pk_mul_f32 v[50:51], v[50:51], v[170:171]\n"
  "v_exp_f32 v153, v153\n"
  "v_pk_mul_f32 v[52:53], v[52:53], v[172:173]\n"
  "v_pk_mul_f32 v[54:55], v[54:55], v[174:175]\n"
  "v_exp_f32 v154, v154\n"
  "v_cvt_pk_bf16_f32 v48, v48, v49\n"
  "v_cvt_pk_bf16_f32 v49, v50, v51\n"
  "v_cvt_pk_bf16_f32 v50, v52, v53\n"
  "v_exp_f32 v155, v155\n"
  "v_cvt_pk_bf16_f32 v51, v54, v55\n"
  "buffer_store_dwordx4 v[48:51], v198, s[60:63], 0 offen offset:768 sc1\n"
  "v_exp_f32 v156, v156\n"
  "v_pk_mul_f32 v[80:81], v[80:81], v[136:137]\n"
  "v_pk_mul_f32 v[82:83], v[82:83], v[138:139]\n"
  "v_exp_f32 v157, v157\n"
  "v_pk_mul_f32 v[84:85], v[84:85], v[140:141]\n"
  "v_pk_mul_f32 v[86:87], v[86:87], v[142:143]\n"
  "v_accvgpr_read_b32 v128, a224\n"
  "v_exp_f32 v158, v158\n"
  "v_accvgpr_read_b32 v129, a225\n"
  "v_accvgpr_read_b32 v130, a226\n"
  "v_exp_f32 v159, v159\n"
  "v_accvgpr_read_b32 v131, a227\n"
  "v_accvgpr_read_b32 v132, a228\n"
  "v_accvgpr_read_b32 v133, a229\n"
  "v_rcp_f32 v144, v144\n"
  "v_accvgpr_read_b32 v134, a230\n"
  "v_accvgpr_read_b32 v135, a231\n"
  "v_rcp_f32 v145, v145\n"
  "v_pk_mul_f32 v[160:161], v[128:129], v[128:129]\n"
  "v_pk_mul_f32 v[162:163], v[130:131], v[130:131]\n"
  "v_rcp_f32 v146, v146\n"
  "v_pk_mul_f32 v[164:165], v[132:133], v[132:133]\n"
  "v_pk_mul_f32 v[166:167], v[134:135], v[134:135]\n"
  "v_rcp_f32 v147, v147\n"
  "v_pk_fma_f32 v[160:161], v[160:161], s[88:89], v[194:195]\n"
  "v_pk_fma_f32 v[162:163], v[162:163], s[88:89], v[194:195]\n"
  "v_pk_fma_f32 v[164:165], v[164:165], s[88:89], v[194:195]\n"
  "v_rcp_f32 v148, v148\n"
  "v_pk_fma_f32 v[166:167], v[166:167], s[88:89], v[194:195]\n"
  "v_pk_mul_f32 v[160:161], v[160:161], v[128:129]\n"
  "v_rcp_f32 v149, v149\n"
  "v_pk_mul_f32 v[162:163], v[162:163], v[130:131]\n"
  "v_pk_mul_f32 v[164:165], v[164:165], v[132:133]\n"
  "v_rcp_f32 v150, v150\n"
  "v_pk_mul_f32 v[166:167], v[166:167], v[134:135]\n"
  "v_pk_fma_f32 v[152:153], v[152:153], v[210:211], v[210:211]\n"
  "v_rcp_f32 v151, v151\n"
  "v_pk_fma_f32 v[154:155], v[154:155], v[210:211], v[210:211]\n"
  "v_pk_fma_f32 v[156:157], v[156:157], v[210:211], v[210:211]\n"
  "v_pk_fma_f32 v[158:159], v[158:159], v[210:211], v[210:211]\n"
  "v_exp_f32 v160, v160\n"
  "v_pk_mul_f32 v[64:65], v[64:65], v[144:145]\n"
  "v_pk_mul_f32 v[66:67], v[66:67], v[146:147]\n"
  "v_exp_f32 v161, v161\n"
  "v_pk_mul_f32 v[68:69], v[68:69], v[148:149]\n"
  "v_pk_mul_f32 v[70:71], v[70:71], v[150:151]\n"
  "v_exp_f32 v162, v162\n"
  "v_cvt_pk_bf16_f32 v64, v64, v65\n"
  "v_cvt_pk_bf16_f32 v65, v66, v67\n"
  "v_cvt_pk_bf16_f32 v66, v68, v69\n"
  "v_exp_f32 v163, v163\n"
  "v_cvt_pk_bf16_f32 v67, v70, v71\n"
  "buffer_store_dwordx4 v[64:67], v198, s[60:63], 0 offen offset:1024 sc1\n"
  "v_exp_f32 v164, v164\n"
  "v_pk_mul_f32 v[96:97], v[96:97], v[128:129]\n"
  "v_pk_mul_f32 v[98:99], v[98:99], v[130:131]\n"
  "v_exp_f32 v165, v165\n"
  "v_pk_mul_f32 v[100:101], v[100:101], v[132:133]\n"
  "v_pk_mul_f32 v[102:103], v[102:103], v[134:135]\n"
  "v_accvgpr_read_b32 v136, a240\n"
  "v_exp_f32 v166, v166\n"
  "v_accvgpr_read_b32 v137, a241\n"
  "v_accvgpr_read_b32 v138, a242\n"
  "v_exp_f32 v167, v167\n"
  "v_accvgpr_read_b32 v139, a243\n"
  "v_accvgpr_read_b32 v140, a244\n"
  "v_accvgpr_read_b32 v141, a245\n"
  "v_rcp_f32 v152, v152\n"
  "v_accvgpr_read_b32 v142, a246\n"
  "v_accvgpr_read_b32 v143, a247\n"
  "v_rcp_f32 v153, v153\n"
  "v_pk_mul_f32 v[168:169], v[136:137], v[136:137]\n"
  "v_pk_mul_f32 v[170:171], v[138:139], v[138:139]\n"
  "v_rcp_f32 v154, v154\n"
  "v_pk_mul_f32 v[172:173], v[140:141], v[140:141]\n"
  "v_pk_mul_f32 v[174:175], v[142:143], v[142:143]\n"
  "v_rcp_f32 v155, v155\n"
  "v_pk_fma_f32 v[168:169], v[168:169], s[88:89], v[194:195]\n"
  "v_pk_fma_f32 v[170:171], v[170:171], s[88:89], v[194:195]\n"
  "v_pk_fma_f32 v[172:173], v[172:173], s[88:89], v[194:195]\n"
  "v_rcp_f32 v156, v156\n"
  "v_pk_fma_f32 v[174:175], v[174:175], s[88:89], v[194:195]\n"
  "v_pk_mul_f32 v[168:169], v[168:169], v[136:137]\n"
  "v_rcp_f32 v157, v157\n"
  "v_pk_mul_f32 v[170:171], v[170:171], v[138:139]\n"
  "v_pk_mul_f32 v[172:173], v[172:173], v[140:141]\n"
  "v_rcp_f32 v158, v158\n"
  "v_pk_mul_f32 v[174:175], v[174:175], v[142:143]\n"
  "v_pk_fma_f32 v[160:161], v[160:161], v[212:213], v[212:213]\n"
  "v_rcp_f32 v159, v159\n"
  "v_pk_fma_f32 v[162:163], v[162:163], v[212:213], v[212:213]\n"
  "v_pk_fma_f32 v[164:165], v[164:165], v[212:213], v[212:213]\n"
  "v_pk_fma_f32 v[166:167], v[166:167], v[212:213], v[212:213]\n"
  "v_exp_f32 v168, v168\n"
  "v_pk_mul_f32 v[80:81], v[80:81], v[152:153]\n"
  "v_pk_mul_f32 v[82:83], v[82:83], v[154:155]\n"
  "v_exp_f32 v169, v169\n"
  "v_pk_mul_f32 v[84:85], v[84:85], v[156:157]\n"
  "v_pk_mul_f32 v[86:87], v[86:87], v[158:159]\n"
  "v_exp_f32 v170, v170\n"
  "v_cvt_pk_bf16_f32 v80, v80, v81\n"
  "v_cvt_pk_bf16_f32 v81, v82, v83\n"
  "v_cvt_pk_bf16_f32 v82, v84, v85\n"
  "v_exp_f32 v171, v171\n"
  "v_cvt_pk_bf16_f32 v83, v86, v87\n"
  "buffer_store_dwordx4 v[80:83], v198, s[60:63], 0 offen offset:1280 sc1\n"
  "v_exp_f32 v172, v172\n"
  "v_pk_mul_f32 v[112:113], v[112:113], v[136:137]\n"
  "v_pk_mul_f32 v[114:115], v[114:115], v[138:139]\n"
  "v_exp_f32 v173, v173\n"
  "v_pk_mul_f32 v[116:117], v[116:117], v[140:141]\n"
  "v_pk_mul_f32 v[118:119], v[118:119], v[142:143]\n"
  "v_accvgpr_read_b32 v128, a136\n"
  "v_exp_f32 v174, v174\n"
  "v_accvgpr_read_b32 v129, a137\n"
  "v_accvgpr_read_b32 v130, a138\n"
  "v_exp_f32 v175, v175\n"
  "v_accvgpr_read_b32 v131, a139\n"
  "v_accvgpr_read_b32 v132, a140\n"
  "v_accvgpr_read_b32 v133, a141\n"
  "v_rcp_f32 v160, v160\n"
  "v_accvgpr_read_b32 v134, a142\n"
  "v_accvgpr_read_b32 v135, a143\n"
  "v_rcp_f32 v161, v161\n"
  "v_pk_mul_f32 v[144:145], v[128:129], v[128:129]\n"
  "v_pk_mul_f32 v[146:147], v[130:131], v[130:131]\n"
  "v_rcp_f32 v162, v162\n"
  "v_pk_mul_f32 v[148:149], v[132:133], v[132:133]\n"
  "v_pk_mul_f32 v[150:151], v[134:135], v[134:135]\n"
  "v_rcp_f32 v163, v163\n"
  "v_pk_fma_f32 v[144:145], v[144:145], s[88:89], v[194:195]\n"
  "v_pk_fma_f32 v[146:147], v[146:147], s[88:89], v[194:195]\n"
  "v_pk_fma_f32 v[148:149], v[148:149], s[88:89], v[194:195]\n"
  "v_rcp_f32 v164, v164\n"
  "v_pk_fma_f32 v[150:151], v[150:151], s[88:89], v[194:195]\n"
  "v_pk_mul_f32 v[144:145], v[144:145], v[128:129]\n"
  "v_rcp_f32 v165, v165\n"
  "v_pk_mul_f32 v[146:147], v[146:147], v[130:131]\n"
  "v_pk_mul_f32 v[148:149], v[148:149], v[132:133]\n"
  "v_rcp_f32 v166, v166\n"
  "v_pk_mul_f32 v[150:151], v[150:151], v[134:135]\n"
  "v_pk_fma_f32 v[168:169], v[168:169], v[214:215], v[214:215]\n"
  "v_rcp_f32 v167, v167\n"
  "v_pk_fma_f32 v[170:171], v[170:171], v[214:215], v[214:215]\n"
  "v_pk_fma_f32 v[172:173], v[172:173], v[214:215], v[214:215]\n"
  "v_pk_fma_f32 v[174:175], v[174:175], v[214:215], v[214:215]\n"
  "v_exp_f32 v144, v144\n"
  "v_pk_mul_f32 v[96:97], v[96:97], v[160:161]\n"
  "v_pk_mul_f32 v[98:99], v[98:99], v[162:163]\n"
  "v_exp_f32 v145, v145\n"
  "v_pk_mul_f32 v[100:101], v[100:101], v[164:165]\n"
  "v_pk_mul_f32 v[102:103], v[102:103], v[166:167]\n"
  "v_exp_f32 v146, v146\n"
  "v_cvt_pk_bf16_f32 v96, v96, v97\n"
  "v_cvt_pk_bf16_f32 v97, v98, v99\n"
  "v_cvt_pk_bf16_f32 v98, v100, v101\n"
  "v_exp_f32 v147, v147\n"
  "v_cvt_pk_bf16_f32 v99, v102, v103\n"
  "buffer_store_dwordx4 v[96:99], v198, s[60:63], 0 offen offset:1536 sc1\n"
  "v_exp_f32 v148, v148\n"
  "v_pk_mul_f32 v[8:9], v[8:9], v[128:129]\n"
  "v_pk_mul_f32 v[10:11], v[10:11], v[130:131]\n"
  "v_exp_f32 v149, v149\n"
  "v_pk_mul_f32 v[12:13], v[12:13], v[132:133]\n"
  "v_pk_mul_f32 v[14:15], v[14:15], v[134:135]\n"
  "v_accvgpr_read_b32 v136, a152\n"
  "v_exp_f32 v150, v150\n"
  "v_accvgpr_read_b32 v137, a153\n"
  "v_accvgpr_read_b32 v138, a154\n"
  "v_exp_f32 v151, v151\n"
  "v_accvgpr_read_b32 v139, a155\n"
  "v_accvgpr_read_b32 v140, a156\n"
  "v_accvgpr_read_b32 v141, a157\n"
  "v_rcp_f32 v168, v168\n"
  "v_accvgpr_read_b32 v142, a158\n"
  "v_accvgpr_read_b32 v143, a159\n"
  "v_rcp_f32 v169, v169\n"
  "v_pk_mul_f32 v[152:153], v[136:137], v[136:137]\n"
  "v_pk_mul_f32 v[154:155], v[138:139], v[138:139]\n"
  "v_rcp_f32 v170, v170\n"
  "v_pk_mul_f32 v[156:157], v[140:141], v[140:141]\n"
  "v_pk_mul_f32 v[158:159], v[142:143], v[142:143]\n"
  "v_rcp_f32 v171, v171\n"
  "v_pk_fma_f32 v[152:153], v[152:153], s[88:89], v[194:195]\n"
  "v_pk_fma_f32 v[154:155], v[154:155], s[88:89], v[194:195]\n"
  "v_pk_fma_f32 v[156:157], v[156:157], s[88:89], v[194:195]\n"
  "v_rcp_f32 v172, v172\n"
  "v_pk_fma_f32 v[158:159], v[158:159], s[88:89], v[194:195]\n"
  "v_pk_mul_f32 v[152:153], v[152:153], v[136:137]\n"
  "v_rcp_f32 v173, v173\n"
  "v_pk_mul_f32 v[154:155], v[154:155], v[138:139]\n"
  "v_pk_mul_f32 v[156:157], v[156:157], v[140:141]\n"
  "v_rcp_f32 v174, v174\n"
  "v_pk_mul_f32 v[158:159], v[158:159], v[142:143]\n"
  "v_pk_fma_f32 v[144:145], v[144:145], v[200:201], v[200:201]\n"
  "v_rcp_f32 v175, v175\n"
  "v_pk_fma_f32 v[146:147], v[146:147], v[200:201], v[200:201]\n"
  "v_pk_fma_f32 v[148:149], v[148:149], v[200:201], v[200:201]\n"
  "v_pk_fma_f32 v[150:151], v[150:151], v[200:201], v[200:201]\n"
  "v_exp_f32 v152, v152\n"
  "v_pk_mul_f32 v[112:113], v[112:113], v[168:169]\n"
  "v_pk_mul_f32 v[114:115], v[114:115], v[170:171]\n"
  "v_exp_f32 v153, v153\n"
  "v_pk_mul_f32 v[116:117], v[116:117], v[172:173]\n"
  "v_pk_mul_f32 v[118:119], v[118:119], v[174:175]\n"
  "v_exp_f32 v154, v154\n"
  "v_cvt_pk_bf16_f32 v112, v112, v113\n"
  "v_cvt_pk_bf16_f32 v113, v114, v115\n"
  "v_cvt_pk_bf16_f32 v114, v116, v117\n"
  "v_exp_f32 v155, v155\n"
  "v_cvt_pk_bf16_f32 v115, v118, v119\n"
  "buffer_store_dwordx4 v[112:115], v198, s[60:63], 0 offen offset:1792 sc1\n"
  "v_exp_f32 v156, v156\n"
  "v_pk_mul_f32 v[24:25], v[24:25], v[136:137]\n"
  "v_pk_mul_f32 v[26:27], v[26:27], v[138:139]\n"
  "v_exp_f32 v157, v157\n"
  "v_pk_mul_f32 v[28:29], v[28:29], v[140:141]\n"
  "v_pk_mul_f32 v[30:31], v[30:31], v[142:143]\n"
  "v_accvgpr_read_b32 v128, a168\n"
  "v_exp_f32 v158, v158\n"
  "v_accvgpr_read_b32 v129, a169\n"
  "v_accvgpr_read_b32 v130, a170\n"
  "v_exp_f32 v159, v159\n"
  "v_accvgpr_read_b32 v131, a171\n"
  "v_accvgpr_read_b32 v132, a172\n"
  "v_accvgpr_read_b32 v133, a173\n"
  "v_rcp_f32 v144, v144\n"
  "v_accvgpr_read_b32 v134, a174\n"
  "v_accvgpr_read_b32 v135, a175\n"
  "v_rcp_f32 v145, v145\n"
  "v_pk_mul_f32 v[160:161], v[128:129], v[128:129]\n"
  "v_pk_mul_f32 v[162:163], v[130:131], v[130:131]\n"
  "v_rcp_f32 v146, v146\n"
  "v_pk_mul_f32 v[164:165], v[132:133], v[132:133]\n"
  "v_pk_mul_f32 v[166:167], v[134:135], v[134:135]\n"
  "v_rcp_f32 v147, v147\n"
  "v_pk_fma_f32 v[160:161], v[160:161], s[88:89], v[194:195]\n"
  "v_pk_fma_f32 v[162:163], v[162:163], s[88:89], v[194:195]\n"
  "v_pk_fma_f32 v[164:165], v[164:165], s[88:89], v[194:195]\n"
  "v_rcp_f32 v148, v148\n"
  "v_pk_fma_f32 v[166:167], v[166:167], s[88:89], v[194:195]\n"
  "v_pk_mul_f32 v[160:161], v[160:161], v[128:129]\n"
  "v_rcp_f32 v149, v149\n"
  "v_pk_mul_f32 v[162:163], v[162:163], v[130:131]\n"
  "v_pk_mul_f32 v[164:165], v[164:165], v[132:133]\n"
  "v_rcp_f32 v150, v150\n"
  "v_pk_mul_f32 v[166:167], v[166:167], v[134:135]\n"
  "v_pk_fma_f32 v[152:153], v[152:153], v[202:203], v[202:203]\n"
  "v_rcp_f32 v151, v151\n"
  "v_pk_fma_f32 v[154:155], v[154:155], v[202:203], v[202:203]\n"
  "v_pk_fma_f32 v[156:157], v[156:157], v[202:203], v[202:203]\n"
  "v_pk_fma_f32 v[158:159], v[158:159], v[202:203], v[202:203]\n"
  "v_exp_f32 v160, v160\n"
  "v_pk_mul_f32 v[8:9], v[8:9], v[144:145]\n"
  "v_pk_mul_f32 v[10:11], v[10:11], v[146:147]\n"
  "v_exp_f32 v161, v161\n"
  "v_pk_mul_f32 v[12:13], v[12:13], v[148:149]\n"
  "v_pk_mul_f32 v[14:15], v[14:15], v[150:151]\n"
  "v_exp_f32 v162, v162\n"
  "v_cvt_pk_bf16_f32 v8, v8, v9\n"
  "v_cvt_pk_bf16_f32 v9, v10, v11\n"
  "v_cvt_pk_bf16_f32 v10, v12, v13\n"
  "v_exp_f32 v163, v163\n"
  "v_cvt_pk_bf16_f32 v11, v14, v15\n"
  "buffer_store_dwordx4 v[8:11], v198, s[60:63], s90 offen sc1\n"
  "v_exp_f32 v164, v164\n"
  "v_pk_mul_f32 v[40:41], v[40:41], v[128:129]\n"
  "v_pk_mul_f32 v[42:43], v[42:43], v[130:131]\n"
  "v_exp_f32 v165, v165\n"
  "v_pk_mul_f32 v[44:45], v[44:45], v[132:133]\n"
  "v_pk_mul_f32 v[46:47], v[46:47], v[134:135]\n"
  "v_accvgpr_read_b32 v136, a184\n"
  "v_exp_f32 v166, v166\n"
  "v_accvgpr_read_b32 v137, a185\n"
  "v_accvgpr_read_b32 v138, a186\n"
  "v_exp_f32 v167, v167\n"
  "v_accvgpr_read_b32 v139, a187\n"
  "v_accvgpr_read_b32 v140, a188\n"
  "v_accvgpr_read_b32 v141, a189\n"
  "v_rcp_f32 v152, v152\n"
  "v_accvgpr_read_b32 v142, a190\n"
  "v_accvgpr_read_b32 v143, a191\n"
  "v_rcp_f32 v153, v153\n"
  "v_pk_mul_f32 v[168:169], v[136:137], v[136:137]\n"
  "v_pk_mul_f32 v[170:171], v[138:139], v[138:139]\n"
  "v_rcp_f32 v154, v154\n"
  "v_pk_mul_f32 v[172:173], v[140:141], v[140:141]\n"
  "v_pk_mul_f32 v[174:175], v[142:143], v[142:143]\n"
  "v_rcp_f32 v155, v155\n"
  "v_pk_fma_f32 v[168:169], v[168:169], s[88:89], v[194:195]\n"
  "v_pk_fma_f32 v[170:171], v[170:171], s[88:89], v[194:195]\n"
  "v_pk_fma_f32 v[172:173], v[172:173], s[88:89], v[194:195]\n"
  "v_rcp_f32 v156, v156\n"
  "v_pk_fma_f32 v[174:175], v[174:175], s[88:89], v[194:195]\n"
  "v_pk_mul_f32 v[168:169], v[168:169], v[136:137]\n"
  "v_rcp_f32 v157, v157\n"
  "v_pk_mul_f32 v[170:171], v[170:171], v[138:139]\n"
  "v_pk_mul_f32 v[172:173], v[172:173], v[140:141]\n"
  "v_rcp_f32 v158, v158\n"
  "v_pk_mul_f32 v[174:175], v[174:175], v[142:143]\n"
  "v_pk_fma_f32 v[160:161], v[160:161], v[204:205], v[204:205]\n"
  "v_rcp_f32 v159, v159\n"
  "v_pk_fma_f32 v[162:163], v[162:163], v[204:205], v[204:205]\n"
  "v_pk_fma_f32 v[164:165], v[164:165], v[204:205], v[204:205]\n"
  "v_pk_fma_f32 v[166:167], v[166:167], v[204:205], v[204:205]\n"
  "v_exp_f32 v168, v168\n"
  "v_pk_mul_f32 v[24:25], v[24:25], v[152:153]\n"
  "v_pk_mul_f32 v[26:27], v[26:27], v[154:155]\n"
  "v_exp_f32 v169, v169\n"
  "v_pk_mul_f32 v[28:29], v[28:29], v[156:157]\n"
  "v_pk_mul_f32 v[30:31], v[30:31], v[158:159]\n"
  "v_exp_f32 v170, v170\n"
  "v_cvt_pk_bf16_f32 v24, v24, v25\n"
  "v_cvt_pk_bf16_f32 v25, v26, v27\n"
  "v_cvt_pk_bf16_f32 v26, v28, v29\n"
  "v_exp_f32 v171, v171\n"
  "v_cvt_pk_bf16_f32 v27, v30, v31\n"
  "buffer_store_dwordx4 v[24:27], v198, s[60:63], s90 offen offset:256 sc1\n"
  "v_exp_f32 v172, v172\n"
  "v_pk_mul_f32 v[56:57], v[56:57], v[136:137]\n"
  "v_pk_mul_f32 v[58:59], v[58:59], v[138:139]\n"
  "v_exp_f32 v173, v173\n"
  "v_pk_mul_f32 v[60:61], v[60:61], v[140:141]\n"
  "v_pk_mul_f32 v[62:63], v[62:63], v[142:143]\n"
  "v_accvgpr_read_b32 v128, a200\n"
  "v_exp_f32 v174, v174\n"
  "v_accvgpr_read_b32 v129, a201\n"
  "v_accvgpr_read_b32 v130, a202\n"
  "v_exp_f32 v175, v175\n"
  "v_accvgpr_read_b32 v131, a203\n"
  "v_accvgpr_read_b32 v132, a204\n"
  "v_accvgpr_read_b32 v133, a205\n"
  "v_rcp_f32 v160, v160\n"
  "v_accvgpr_read_b32 v134, a206\n"
  "v_accvgpr_read_b32 v135, a207\n"
  "v_rcp_f32 v161, v161\n"
  "v_pk_mul_f32 v[144:145], v[128:129], v[128:129]\n"
  "v_pk_mul_f32 v[146:147], v[130:131], v[130:131]\n"
  "v_rcp_f32 v162, v162\n"
  "v_pk_mul_f32 v[148:149], v[132:133], v[132:133]\n"
  "v_pk_mul_f32 v[150:151], v[134:135], v[134:135]\n"
  "v_rcp_f32 v163, v163\n"
  "v_pk_fma_f32 v[144:145], v[144:145], s[88:89], v[194:195]\n"
  "v_pk_fma_f32 v[146:147], v[146:147], s[88:89], v[194:195]\n"
  "v_pk_fma_f32 v[148:149], v[148:149], s[88:89], v[194:195]\n"
  "v_rcp_f32 v164, v164\n"
  "v_pk_fma_f32 v[150:151], v[150:151], s[88:89], v[194:195]\n"
  "v_pk_mul_f32 v[144:145], v[144:145], v[128:129]\n"
  "v_rcp_f32 v165, v165\n"
  "v_pk_mul_f32 v[146:147], v[146:147], v[130:131]\n"
  "v_pk_mul_f32 v[148:149], v[148:149], v[132:133]\n"
  "v_rcp_f32 v166, v166\n"
  "v_pk_mul_f32 v[150:151], v[150:151], v[134:135]\n"
  "v_pk_fma_f32 v[168:169], v[168:169], v[206:207], v[206:207]\n"
  "v_rcp_f32 v167, v167\n"
  "v_pk_fma_f32 v[170:171], v[170:171], v[206:207], v[206:207]\n"
  "v_pk_fma_f32 v[172:173], v[172:173], v[206:207], v[206:207]\n"
  "v_pk_fma_f32 v[174:175], v[174:175], v[206:207], v[206:207]\n"
  "v_exp_f32 v144, v144\n"
  "v_pk_mul_f32 v[40:41], v[40:41], v[160:161]\n"
  "v_pk_mul_f32 v[42:43], v[42:43], v[162:163]\n"
  "v_exp_f32 v145, v145\n"
  "v_pk_mul_f32 v[44:45], v[44:45], v[164:165]\n"
  "v_pk_mul_f32 v[46:47], v[46:47], v[166:167]\n"
  "v_exp_f32 v146, v146\n"
  "v_cvt_pk_bf16_f32 v40, v40, v41\n"
  "v_cvt_pk_bf16_f32 v41, v42, v43\n"
  "v_cvt_pk_bf16_f32 v42, v44, v45\n"
  "v_exp_f32 v147, v147\n"
  "v_cvt_pk_bf16_f32 v43, v46, v47\n"
  "buffer_store_dwordx4 v[40:43], v198, s[60:63], s90 offen offset:512 sc1\n"
  "v_exp_f32 v148, v148\n"
  "v_pk_mul_f32 v[72:73], v[72:73], v[128:129]\n"
  "v_pk_mul_f32 v[74:75], v[74:75], v[130:131]\n"
  "v_exp_f32 v149, v149\n"
  "v_pk_mul_f32 v[76:77], v[76:77], v[132:133]\n"
  "v_pk_mul_f32 v[78:79], v[78:79], v[134:135]\n"
  "v_accvgpr_read_b32 v136, a216\n"
  "v_exp_f32 v150, v150\n"
  "v_accvgpr_read_b32 v137, a217\n"
  "v_accvgpr_read_b32 v138, a218\n"
  "v_exp_f32 v151, v151\n"
  "v_accvgpr_read_b32 v139, a219\n"
  "v_accvgpr_read_b32 v140, a220\n"
  "v_accvgpr_read_b32 v141, a221\n"
  "v_rcp_f32 v168, v168\n"
  "v_accvgpr_read_b32 v142, a222\n"
  "v_accvgpr_read_b32 v143, a223\n"
  "v_rcp_f32 v169, v169\n"
  "v_pk_mul_f32 v[152:153], v[136:137], v[136:137]\n"
  "v_pk_mul_f32 v[154:155], v[138:139], v[138:139]\n"
  "v_rcp_f32 v170, v170\n"
  "v_pk_mul_f32 v[156:157], v[140:141], v[140:141]\n"
  "v_pk_mul_f32 v[158:159], v[142:143], v[142:143]\n"
  "v_rcp_f32 v171, v171\n"
  "v_pk_fma_f32 v[152:153], v[152:153], s[88:89], v[194:195]\n"
  "v_pk_fma_f32 v[154:155], v[154:155], s[88:89], v[194:195]\n"
  "v_pk_fma_f32 v[156:157], v[156:157], s[88:89], v[194:195]\n"
  "v_rcp_f32 v172, v172\n"
  "v_pk_fma_f32 v[158:159], v[158:159], s[88:89], v[194:195]\n"
  "v_pk_mul_f32 v[152:153], v[152:153], v[136:137]\n"
  "v_rcp_f32 v173, v173\n"
  "v_pk_mul_f32 v[154:155], v[154:155], v[138:139]\n"
  "v_pk_mul_f32 v[156:157], v[156:157], v[140:141]\n"
  "v_rcp_f32 v174, v174\n"
  "v_pk_mul_f32 v[158:159], v[158:159], v[142:143]\n"
  "v_pk_fma_f32 v[144:145], v[144:145], v[208:209], v[208:209]\n"
  "v_rcp_f32 v175, v175\n"
  "v_pk_fma_f32 v[146:147], v[146:147], v[208:209], v[208:209]\n"
  "v_pk_fma_f32 v[148:149], v[148:149], v[208:209], v[208:209]\n"
  "v_pk_fma_f32 v[150:151], v[150:151], v[208:209], v[208:209]\n"
  "v_exp_f32 v152, v152\n"
  "v_pk_mul_f32 v[56:57], v[56:57], v[168:169]\n"
  "v_pk_mul_f32 v[58:59], v[58:59], v[170:171]\n"
  "v_exp_f32 v153, v153\n"
  "v_pk_mul_f32 v[60:61], v[60:61], v[172:173]\n"
  "v_pk_mul_f32 v[62:63], v[62:63], v[174:175]\n"
  "v_exp_f32 v154, v154\n"
  "v_cvt_pk_bf16_f32 v56, v56, v57\n"
  "v_cvt_pk_bf16_f32 v57, v58, v59\n"
  "v_cvt_pk_bf16_f32 v58, v60, v61\n"
  "v_exp_f32 v155, v155\n"
  "v_cvt_pk_bf16_f32 v59, v62, v63\n"
  "buffer_store_dwordx4 v[56:59], v198, s[60:63], s90 offen offset:768 sc1\n"
  "v_exp_f32 v156, v156\n"
  "v_pk_mul_f32 v[88:89], v[88:89], v[136:137]\n"
  "v_pk_mul_f32 v[90:91], v[90:91], v[138:139]\n"
  "v_exp_f32 v157, v157\n"
  "v_pk_mul_f32 v[92:93], v[92:93], v[140:141]\n"
  "v_pk_mul_f32 v[94:95], v[94:95], v[142:143]\n"
  "v_accvgpr_read_b32 v128, a232\n"
  "v_exp_f32 v158, v158\n"
  "v_accvgpr_read_b32 v129, a233\n"
  "v_accvgpr_read_b32 v130, a234\n"
  "v_exp_f32 v159, v159\n"
  "v_accvgpr_read_b32 v131, a235\n"
  "v_accvgpr_read_b32 v132, a236\n"
  "v_accvgpr_read_b32 v133, a237\n"
  "v_rcp_f32 v144, v144\n"
  "v_accvgpr_read_b32 v134, a238\n"
  "v_accvgpr_read_b32 v135, a239\n"
  "v_rcp_f32 v145, v145\n"
  "v_pk_mul_f32 v[160:161], v[128:129], v[128:129]\n"
  "v_pk_mul_f32 v[162:163], v[130:131], v[130:131]\n"
  "v_rcp_f32 v146, v146\n"
  "v_pk_mul_f32 v[164:165], v[132:133], v[132:133]\n"
  "v_pk_mul_f32 v[166:167], v[134:135], v[134:135]\n"
  "v_rcp_f32 v147, v147\n"
  "v_pk_fma_f32 v[160:161], v[160:161], s[88:89], v[194:195]\n"
  "v_pk_fma_f32 v[162:163], v[162:163], s[88:89], v[194:195]\n"
  "v_pk_fma_f32 v[164:165], v[164:165], s[88:89], v[194:195]\n"
  "v_rcp_f32 v148, v148\n"
  "v_pk_fma_f32 v[166:167], v[166:167], s[88:89], v[194:195]\n"
  "v_pk_mul_f32 v[160:161], v[160:161], v[128:129]\n"
  "v_rcp_f32 v149, v149\n"
  "v_pk_mul_f32 v[162:163], v[162:163], v[130:131]\n"
  "v_pk_mul_f32 v[164:165], v[164:165], v[132:133]\n"
  "v_rcp_f32 v150, v150\n"
  "v_pk_mul_f32 v[166:167], v[166:167], v[134:135]\n"
  "v_pk_fma_f32 v[152:153], v[152:153], v[210:211], v[210:211]\n"
  "v_rcp_f32 v151, v151\n"
  "v_pk_fma_f32 v[154:155], v[154:155], v[210:211], v[210:211]\n"
  "v_pk_fma_f32 v[156:157], v[156:157], v[210:211], v[210:211]\n"
  "v_pk_fma_f32 v[158:159], v[158:159], v[210:211], v[210:211]\n"
  "v_exp_f32 v160, v160\n"
  "v_pk_mul_f32 v[72:73], v[72:73], v[144:145]\n"
  "v_pk_mul_f32 v[74:75], v[74:75], v[146:147]\n"
  "v_exp_f32 v161, v161\n"
  "v_pk_mul_f32 v[76:77], v[76:77], v[148:149]\n"
  "v_pk_mul_f32 v[78:79], v[78:79], v[150:151]\n"
  "v_exp_f32 v162, v162\n"
  "v_cvt_pk_bf16_f32 v72, v72, v73\n"
  "v_cvt_pk_bf16_f32 v73, v74, v75\n"
  "v_cvt_pk_bf16_f32 v74, v76, v77\n"
  "v_exp_f32 v163, v163\n"
  "v_cvt_pk_bf16_f32 v75, v78, v79\n"
  "buffer_store_dwordx4 v[72:75], v198, s[60:63], s90 offen offset:1024 sc1\n"
  "v_exp_f32 v164, v164\n"
  "v_pk_mul_f32 v[104:105], v[104:105], v[128:129]\n"
  "v_pk_mul_f32 v[106:107], v[106:107], v[130:131]\n"
  "v_exp_f32 v165, v165\n"
  "v_pk_mul_f32 v[108:109], v[108:109], v[132:133]\n"
  "v_pk_mul_f32 v[110:111], v[110:111], v[134:135]\n"
  "v_accvgpr_read_b32 v136, a248\n"
  "v_exp_f32 v166, v166\n"
  "v_accvgpr_read_b32 v137, a249\n"
  "v_accvgpr_read_b32 v138, a250\n"
  "v_exp_f32 v167, v167\n"
  "v_accvgpr_read_b32 v139, a251\n"
  "v_accvgpr_read_b32 v140, a252\n"
  "v_accvgpr_read_b32 v141, a253\n"
  "v_rcp_f32 v152, v152\n"
  "v_accvgpr_read_b32 v142, a254\n"
  "v_accvgpr_read_b32 v143, a255\n"
  "v_rcp_f32 v153, v153\n"
  "v_pk_mul_f32 v[168:169], v[136:137], v[136:137]\n"
  "v_pk_mul_f32 v[170:171], v[138:139], v[138:139]\n"
  "v_rcp_f32 v154, v154\n"
  "v_pk_mul_f32 v[172:173], v[140:141], v[140:141]\n"
  "v_pk_mul_f32 v[174:175], v[142:143], v[142:143]\n"
  "v_rcp_f32 v155, v155\n"
  "v_pk_fma_f32 v[168:169], v[168:169], s[88:89], v[194:195]\n"
  "v_pk_fma_f32 v[170:171], v[170:171], s[88:89], v[194:195]\n"
  "v_pk_fma_f32 v[172:173], v[172:173], s[88:89], v[194:195]\n"
  "v_rcp_f32 v156, v156\n"
  "v_pk_fma_f32 v[174:175], v[174:175], s[88:89], v[194:195]\n"
  "v_pk_mul_f32 v[168:169], v[168:169], v[136:137]\n"
  "v_rcp_f32 v157, v157\n"
  "v_pk_mul_f32 v[170:171], v[170:171], v[138:139]\n"
  "v_pk_mul_f32 v[172:173], v[172:173], v[140:141]\n"
  "v_rcp_f32 v158, v158\n"
  "v_pk_mul_f32 v[174:175], v[174:175], v[142:143]\n"
  "v_pk_fma_f32 v[160:161], v[160:161], v[212:213], v[212:213]\n"
  "v_rcp_f32 v159, v159\n"
  "v_pk_fma_f32 v[162:163], v[162:163], v[212:213], v[212:213]\n"
  "v_pk_fma_f32 v[164:165], v[164:165], v[212:213], v[212:213]\n"
  "v_pk_fma_f32 v[166:167], v[166:167], v[212:213], v[212:213]\n"
  "v_exp_f32 v168, v168\n"
  "v_pk_mul_f32 v[88:89], v[88:89], v[152:153]\n"
  "v_exp_f32 v169, v169\n"
  "v_pk_mul_f32 v[90:91], v[90:91], v[154:155]\n"
  "v_pk_mul_f32 v[92:93], v[92:93], v[156:157]\n"
  "v_exp_f32 v170, v170\n"
  "v_pk_mul_f32 v[94:95], v[94:95], v[158:159]\n"
  "v_exp_f32 v171, v171\n"
  "v_cvt_pk_bf16_f32 v88, v88, v89\n"
  "v_cvt_pk_bf16_f32 v89, v90, v91\n"
  "v_exp_f32 v172, v172\n"
  "v_cvt_pk_bf16_f32 v90, v92, v93\n"
  "v_cvt_pk_bf16_f32 v91, v94, v95\n"
  "v_exp_f32 v173, v173\n"
  "buffer_store_dwordx4 v[88:91], v198, s[60:63], s90 offen offset:1280 sc1\n"
  "v_exp_f32 v174, v174\n"
  "v_pk_mul_f32 v[120:121], v[120:121], v[136:137]\n"
  "v_pk_mul_f32 v[122:123], v[122:123], v[138:139]\n"
  "v_exp_f32 v175, v175\n"
  "v_pk_mul_f32 v[124:125], v[124:125], v[140:141]\n"
  "v_pk_mul_f32 v[126:127], v[126:127], v[142:143]\n"
  "v_rcp_f32 v160, v160\n"
  "v_rcp_f32 v161, v161\n"
  "v_pk_fma_f32 v[168:169], v[168:169], v[214:215], v[214:215]\n"
  "v_rcp_f32 v162, v162\n"
  "v_rcp_f32 v163, v163\n"
  "v_pk_fma_f32 v[170:171], v[170:171], v[214:215], v[214:215]\n"
  "v_rcp_f32 v164, v164\n"
  "v_rcp_f32 v165, v165\n"
  "v_pk_fma_f32 v[172:173], v[172:173], v[214:215], v[214:215]\n"
  "v_rcp_f32 v166, v166\n"
  "v_rcp_f32 v167, v167\n"
  "v_pk_fma_f32 v[174:175], v[174:175], v[214:215], v[214:215]\n"
  "v_pk_mul_f32 v[104:105], v[104:105], v[160:161]\n"
  "v_pk_mul_f32 v[106:107], v[106:107], v[162:163]\n"
  "v_pk_mul_f32 v[108:109], v[108:109], v[164:165]\n"
  "v_pk_mul_f32 v[110:111], v[110:111], v[166:167]\n"
  "v_cvt_pk_bf16_f32 v104, v104, v105\n"
  "v_cvt_pk_bf16_f32 v105, v106, v107\n"
  "v_cvt_pk_bf16_f32 v106, v108, v109\n"
  "v_cvt_pk_bf16_f32 v107, v110, v111\n"
  "buffer_store_dwordx4 v[104:107], v198, s[60:63], s90 offen offset:1536 sc1\n"
  "v_rcp_f32 v168, v168\n"
  "v_rcp_f32 v169, v169\n"
  "v_rcp_f32 v170, v170\n"
  "v_rcp_f32 v171, v171\n"
  "v_rcp_f32 v172, v172\n"
  "v_rcp_f32 v173, v173\n"
  "v_rcp_f32 v174, v174\n"
  "v_rcp_f32 v175, v175\n"
  "s_nop 0\n"
  "v_pk_mul_f32 v[120:121], v[120:121], v[168:169]\n"
  "v_pk_mul_f32 v[122:123], v[122:123], v[170:171]\n"
  "v_pk_mul_f32 v[124:125], v[124:125], v[172:173]\n"
  "v_pk_mul_f32 v[126:127], v[126:127], v[174:175]\n"
  "v_cvt_pk_bf16_f32 v120, v120, v121\n"
  "v_cvt_pk_bf16_f32 v121, v122, v123\n"
  "v_cvt_pk_bf16_f32 v122, v124, v125\n"
  "v_cvt_pk_bf16_f32 v123, v126, v127\n"
  "buffer_store_dwordx4 v[120:123], v198, s[60:63], s90 offen offset:1792 sc1\n"
  "s_waitcnt vmcnt(16)\n"
  "s_branch L_next%=\n"
  "L_u_v%=:\n"
  "s_mov_b32 s82, 4\n"
  "s_mov_b32 s100, 1\n"
  "s_mov_b32 s85, 4\n"
  "s_cmp_eq_u32 s101, 2\n"
  "s_cbranch_scc1 L_gs_fast%=\n"
  "s_lshr_b32 s87, s34, 1\n"
  "s_lshl_b32 s87, s87, 14\n"
  "s_and_b32 s90, s34, 1\n"
  "s_lshl_b32 s90, s90, 14\n"
  "v_and_b32 v128, 15, v255\n"
  "v_lshrrev_b32 v129, 4, v255\n"
  "v_bfe_u32 v130, v255, 1, 3\n"
  "v_xor_b32 v130, v130, v129\n"
  "v_lshlrev_b32 v130, 4, v130\n"
  "v_lshl_add_u32 v130, v128, 7, v130\n"
  "v_add_u32 v130, s87, v130\n"
  "v_bfe_u32 v131, v255, 2, 2\n"
  "v_and_b32 v132, 3, v255\n"
  "v_lshlrev_b32 v132, 3, v132\n"
  "v_lshl_add_u32 v132, v131, 7, v132\n"
  "v_lshl_add_u32 v132, v129, 10, v132\n"
  "v_bfe_u32 v133, v255, 3, 2\n"
  "v_lshl_add_u32 v132, v133, 5, v132\n"
  "v_add_u32 v132, s90, v132\n"
  "v_add_u32 v228, 0, v130\n"
  "v_xor_b32 v229, 64, v228\n"
  "v_add_u32 v234, 0x8000, v130\n"
  "v_xor_b32 v235, 64, v234\n"
  "v_add_u32 v236, 0x10000, v132\n"
  "v_xor_b32 v237, 32, v236\n"
  "v_xor_b32 v238, 64, v236\n"
  "v_xor_b32 v239, 0x60, v236\n"
  "v_add_u32 v230, s94, v236\n"
  "v_add_u32 v231, s94, v237\n"
  "v_add_u32 v232, s94, v238\n"
  "v_add_u32 v233, s94, v239\n"
  "s_cmp_eq_u32 s101, 2\n"
  "s_cbranch_scc1 L_ew_gs%=\n"
  "s_cmp_eq_u32 s101, 3\n"
  "s_cbranch_scc1 L_ewb_gs%=\n"
  "s_waitcnt vmcnt(32)\n"
  "L_ewb_gs%=:\n"
  "s_barrier\n"
  "L_ew_gs%=:\n"
  "ds_read_b128 a[0:3], v228\n"
  "ds_read_b128 a[4:7], v229\n"
  "ds_read_b64_tr_b16 a[64:65], v230\n"
  "ds_read_b64_tr_b16 a[66:67], v230 offset:512\n"
  "ds_read_b64_tr_b16 a[68:69], v230 offset:4096\n"
  "ds_read_b64_tr_b16 a[70:71], v230 offset:4608\n"
  "ds_read_b64_tr_b16 a[72:73], v231\n"
  "ds_read_b64_tr_b16 a[74:75], v231 offset:512\n"
  "ds_read_b64_tr_b16 a[76:77], v231 offset:4096\n"
  "ds_read_b64_tr_b16 a[78:79], v231 offset:4608\n"
  "ds_read_b64_tr_b16 a[80:81], v232\n"
  "ds_read_b64_tr_b16 a[82:83], v232 offset:512\n"
  "ds_read_b64_tr_b16 a[84:85], v232 offset:4096\n"
  "ds_read_b64_tr_b16 a[86:87], v232 offset:4608\n"
  "ds_read_b64_tr_b16 a[88:89], v233\n"
  "ds_read_b64_tr_b16 a[90:91], v233 offset:512\n"
  "ds_read_b64_tr_b16 a[92:93], v233 offset:4096\n"
  "ds_read_b64_tr_b16 a[94:95], v233 offset:4608\n"
  "ds_read_b128 a[8:11], v228 offset:2048\n"
  "ds_read_b128 a[12:15], v229 offset:2048\n"
  "ds_read_b128 a[16:19], v228 offset:4096\n"
  "ds_read_b128 a[20:23], v229 offset:4096\n"
  "ds_read_b128 a[24:27], v228 offset:6144\n"
  "ds_read_b128 a[28:31], v229 offset:6144\n"
  "s_waitcnt lgkmcnt(0)\n"
  "L_gs_fast%=:\n"
  "v_mfma_f32_16x16x32_bf16 a[128:131], a[0:3], a[64:67], 0\n"
  "ds_read_b64_tr_b16 a[96:97], v230 offset:8192\n"
  "v_mfma_f32_16x16x32_bf16 a[128:131], a[4:7], a[68:71], a[128:131]\n"
  "ds_read_b64_tr_b16 a[98:99], v230 offset:8704\n"
  "v_mfma_f32_16x16x32_bf16 a[132:135], a[4:7], a[76:79], 0\n"
  "ds_read_b64_tr_b16 a[100:101], v230 offset:12288\n"
  "v_mfma_f32_16x16x32_bf16 a[132:135], a[0:3], a[72:75], a[132:135]\n"
  "ds_read_b64_tr_b16 a[102:103], v230 offset:12800\n"
  "v_mfma_f32_16x16x32_bf16 a[136:139], a[0:3], a[80:83], 0\n"
  "ds_read_b64_tr_b16 a[104:105], v231 offset:8192\n"
  "v_mfma_f32_16x16x32_bf16 a[136:139], a[4:7], a[84:87], a[136:139]\n"
  "ds_read_b64_tr_b16 a[106:107], v231 offset:8704\n"
  "v_mfma_f32_16x16x32_bf16 a[140:143], a[4:7], a[92:95], 0\n"
  "ds_read_b64_tr_b16 a[108:109], v231 offset:12288\n"
  "v_mfma_f32_16x16x32_bf16 a[140:143], a[0:3], a[88:91], a[140:143]\n"
  "ds_read_b64_tr_b16 a[110:111], v231 offset:12800\n"
  "v_mfma_f32_16x16x32_bf16 a[156:159], a[8:11], a[88:91], 0\n"
  "ds_read_b64_tr_b16 a[112:113], v232 offset:8192\n"
  "v_mfma_f32_16x16x32_bf16 a[156:159], a[12:15], a[92:95], a[156:159]\n"
  "ds_read_b64_tr_b16 a[114:115], v232 offset:8704\n"
  "v_mfma_f32_16x16x32_bf16 a[152:155], a[12:15], a[84:87], 0\n"
  "ds_read_b64_tr_b16 a[116:117], v232 offset:12288\n"
  "v_mfma_f32_16x16x32_bf16 a[152:155], a[8:11], a[80:83], a[152:155]\n"
  "ds_read_b64_tr_b16 a[118:119], v232 offset:12800\n"
  "v_mfma_f32_16x16x32_bf16 a[148:151], a[8:11], a[72:75], 0\n"
  "ds_read_b64_tr_b16 a[120:121], v233 offset:8192\n"
  "v_mfma_f32_16x16x32_bf16 a[148:151], a[12:15], a[76:79], a[148:151]\n"
  "ds_read_b64_tr_b16 a[122:123], v233 offset:8704\n"
  "v_mfma_f32_16x16x32_bf16 a[144:147], a[12:15], a[68:71], 0\n"
  "ds_read_b64_tr_b16 a[124:125], v233 offset:12288\n"
  "v_mfma_f32_16x16x32_bf16 a[144:147], a[8:11], a[64:67], a[144:147]\n"
  "ds_read_b64_tr_b16 a[126:127], v233 offset:12800\n"
  "v_mfma_f32_16x16x32_bf16 a[160:163], a[16:19], a[64:67], 0\n"
  "ds_read_b128 a[32:35], v228 offset:8192\n"
  "v_mfma_f32_16x16x32_bf16 a[160:163], a[20:23], a[68:71], a[160:163]\n"
  "ds_read_b128 a[36:39], v229 offset:8192\n"
  "v_mfma_f32_16x16x32_bf16 a[164:167], a[20:23], a[76:79], 0\n"
  "ds_read_b128 a[40:43], v228 offset:10240\n"
  "v_mfma_f32_16x16x32_bf16 a[164:167], a[16:19], a[72:75], a[164:167]\n"
  "ds_read_b128 a[44:47], v229 offset:10240\n"
  "v_mfma_f32_16x16x32_bf16 a[168:171], a[16:19], a[80:83], 0\n"
  "ds_read_b128 a[48:51], v228 offset:12288\n"
  "v_mfma_f32_16x16x32_bf16 a[168:171], a[20:23], a[84:87], a[168:171]\n"
  "ds_read_b128 a[52:55], v229 offset:12288\n"
  "v_mfma_f32_16x16x32_bf16 a[172:175], a[20:23], a[92:95], 0\n"
  "ds_read_b128 a[56:59], v228 offset:14336\n"
  "v_mfma_f32_16x16x32_bf16 a[172:175], a[16:19], a[88:91], a[172:175]\n"
  "ds_read_b128 a[60:63], v229 offset:14336\n"
  "v_mfma_f32_16x16x32_bf16 a[188:191], a[24:27], a[88:91], 0\n"
  "s_add_u32 s52, s52, s81\n"
  "s_addc_u32 s53, s53, 0\n"
  "v_mfma_f32_16x16x32_bf16 a[188:191], a[28:31], a[92:95], a[188:191]\n"
  "s_add_u32 s56, s56, s80\n"
  "s_addc_u32 s57, s57, 0\n"
  "v_mfma_f32_16x16x32_bf16 a[184:187], a[28:31], a[84:87], 0\n"
  "v_mfma_f32_16x16x32_bf16 a[184:187], a[24:27], a[80:83], a[184:187]\n"
  "v_mfma_f32_16x16x32_bf16 a[180:183], a[24:27], a[72:75], 0\n"
  "s_waitcnt lgkmcnt(0)\n"
  "s_barrier\n"
  "v_mfma_f32_16x16x32_bf16 a[180:183], a[28:31], a[76:79], a[180:183]\n"
  "s_add_u32 m0, s83, 0\n"
  "v_mfma_f32_16x16x32_bf16 a[176:179], a[28:31], a[68:71], 0\n"
  "buffer_load_dwordx4 v252, s[52:55], s64 offen sc1 lds\n"
  "v_mfma_f32_16x16x32_bf16 a[176:179], a[24:27], a[64:67], a[176:179]\n"
  "v_mfma_f32_16x16x32_bf16 v[0:3], a[0:3], a[96:99], 0\n"
  "v_mfma_f32_16x16x32_bf16 v[0:3], a[4:7], a[100:103], v[0:3]\n"
  "v_mfma_f32_16x16x32_bf16 v[4:7], a[4:7], a[108:111], 0\n"
  "buffer_load_dwordx4 v253, s[52:55], s65 offen offset:1024 sc1 lds\n"
  "v_mfma_f32_16x16x32_bf16 v[4:7], a[0:3], a[104:107], v[4:7]\n"
  "v_mfma_f32_16x16x32_bf16 v[8:11], a[0:3], a[112:115], 0\n"
  "v_mfma_f32_16x16x32_bf16 v[8:11], a[4:7], a[116:119], v[8:11]\n"
  "v_mfma_f32_16x16x32_bf16 v[12:15], a[4:7], a[124:127], 0\n"
  "buffer_load_dwordx4 v252, s[52:55], s66 offen offset:2048 sc1 lds\n"
  "v_mfma_f32_16x16x32_bf16 v[12:15], a[0:3], a[120:123], v[12:15]\n"
  "v_mfma_f32_16x16x32_bf16 v[28:31], a[8:11], a[120:123], 0\n"
  "s_and_b32 s28, s94, s95\n"
  "v_mfma_f32_16x16x32_bf16 v[28:31], a[12:15], a[124:127], v[28:31]\n"
  "s_add_u32 s94, s94, 0x8000\n"
  "v_mfma_f32_16x16x32_bf16 v[24:27], a[12:15], a[116:119], 0\n"
  "buffer_load_dwordx4 v253, s[52:55], s67 offen offset:3072 sc1 lds\n"
  "v_mfma_f32_16x16x32_bf16 v[24:27], a[8:11], a[112:115], v[24:27]\n"
  "s_cmp_eq_u32 s94, 0x18000\n"
  "s_cselect_b32 s94, 0, s94\n"
  "v_mfma_f32_16x16x32_bf16 v[20:23], a[8:11], a[104:107], 0\n"
  "v_mfma_f32_16x16x32_bf16 v[20:23], a[12:15], a[108:111], v[20:23]\n"
  "s_add_u32 m0, s83, 0x1000\n"
  "v_mfma_f32_16x16x32_bf16 v[16:19], a[12:15], a[100:103], 0\n"
  "buffer_load_dwordx4 v252, s[52:55], s68 offen sc1 lds\n"
  "v_mfma_f32_16x16x32_bf16 v[16:19], a[8:11], a[96:99], v[16:19]\n"
  "v_add_u32 v230, s94, v236\n"
  "v_mfma_f32_16x16x32_bf16 v[32:35], a[16:19], a[96:99], 0\n"
  "v_mfma_f32_16x16x32_bf16 v[32:35], a[20:23], a[100:103], v[32:35]\n"
  "v_add_u32 v231, s94, v237\n"
  "v_mfma_f32_16x16x32_bf16 v[36:39], a[20:23], a[108:111], 0\n"
  "buffer_load_dwordx4 v253, s[52:55], s69 offen offset:1024 sc1 lds\n"
  "v_mfma_f32_16x16x32_bf16 v[36:39], a[16:19], a[104:107], v[36:39]\n"
  "v_mfma_f32_16x16x32_bf16 v[40:43], a[16:19], a[112:115], 0\n"
  "v_add_u32 v232, s94, v238\n"
  "v_mfma_f32_16x16x32_bf16 v[40:43], a[20:23], a[116:119], v[40:43]\n"
  "v_add_u32 v233, s94, v239\n"
  "v_mfma_f32_16x16x32_bf16 v[44:47], a[20:23], a[124:127], 0\n"
  "buffer_load_dwordx4 v252, s[52:55], s70 offen offset:2048 sc1 lds\n"
  "v_mfma_f32_16x16x32_bf16 v[44:47], a[16:19], a[120:123], v[44:47]\n"
  "v_mfma_f32_16x16x32_bf16 v[60:63], a[24:27], a[120:123], 0\n"
  "v_mfma_f32_16x16x32_bf16 v[60:63], a[28:31], a[124:127], v[60:63]\n"
  "v_mfma_f32_16x16x32_bf16 v[56:59], a[28:31], a[116:119], 0\n"
  "buffer_load_dwordx4 v253, s[52:55], s71 offen offset:3072 sc1 lds\n"
  "v_mfma_f32_16x16x32_bf16 v[56:59], a[24:27], a[112:115], v[56:59]\n"
  "v_mfma_f32_16x16x32_bf16 v[52:55], a[24:27], a[104:107], 0\n"
  "v_mfma_f32_16x16x32_bf16 v[52:55], a[28:31], a[108:111], v[52:55]\n"
  "s_add_u32 m0, s84, s28\n"
  "v_mfma_f32_16x16x32_bf16 v[48:51], a[28:31], a[100:103], 0\n"
  "buffer_load_dwordx4 v250, s[56:59], s72 offen lds\n"
  "v_mfma_f32_16x16x32_bf16 v[48:51], a[24:27], a[96:99], v[48:51]\n"
  "v_mfma_f32_16x16x32_bf16 v[64:67], a[32:35], a[96:99], 0\n"
  "v_mfma_f32_16x16x32_bf16 v[64:67], a[36:39], a[100:103], v[64:67]\n"
  "v_mfma_f32_16x16x32_bf16 v[68:71], a[36:39], a[108:111], 0\n"
  "buffer_load_dwordx4 v251, s[56:59], s73 offen offset:1024 lds\n"
  "v_mfma_f32_16x16x32_bf16 v[68:71], a[32:35], a[104:107], v[68:71]\n"
  "v_mfma_f32_16x16x32_bf16 v[72:75], a[32:35], a[112:115], 0\n"
  "v_mfma_f32_16x16x32_bf16 v[72:75], a[36:39], a[116:119], v[72:75]\n"
  "v_mfma_f32_16x16x32_bf16 v[76:79], a[36:39], a[124:127], 0\n"
  "buffer_load_dwordx4 v250, s[56:59], s74 offen offset:2048 lds\n"
  "v_mfma_f32_16x16x32_bf16 v[76:79], a[32:35], a[120:123], v[76:79]\n"
  "v_mfma_f32_16x16x32_bf16 v[92:95], a[40:43], a[120:123], 0\n"
  "v_mfma_f32_16x16x32_bf16 v[92:95], a[44:47], a[124:127], v[92:95]\n"
  "v_mfma_f32_16x16x32_bf16 v[88:91], a[44:47], a[116:119], 0\n"
  "buffer_load_dwordx4 v251, s[56:59], s75 offen offset:3072 lds\n"
  "v_mfma_f32_16x16x32_bf16 v[88:91], a[40:43], a[112:115], v[88:91]\n"
  "v_mfma_f32_16x16x32_bf16 v[84:87], a[40:43], a[104:107], 0\n"
  "v_mfma_f32_16x16x32_bf16 v[84:87], a[44:47], a[108:111], v[84:87]\n"
  "s_add_u32 m0, m0, s29\n"
  "v_mfma_f32_16x16x32_bf16 v[80:83], a[44:47], a[100:103], 0\n"
  "buffer_load_dwordx4 v250, s[56:59], s76 offen lds\n"
  "v_mfma_f32_16x16x32_bf16 v[80:83], a[40:43], a[96:99], v[80:83]\n"
  "v_mfma_f32_16x16x32_bf16 v[96:99], a[48:51], a[96:99], 0\n"
  "v_mfma_f32_16x16x32_bf16 v[96:99], a[52:55], a[100:103], v[96:99]\n"
  "v_mfma_f32_16x16x32_bf16 v[100:103], a[52:55], a[108:111], 0\n"
  "buffer_load_dwordx4 v251, s[56:59], s77 offen offset:1024 lds\n"
  "v_mfma_f32_16x16x32_bf16 v[100:103], a[48:51], a[104:107], v[100:103]\n"
  "v_mfma_f32_16x16x32_bf16 v[104:107], a[48:51], a[112:115], 0\n"
  "v_mfma_f32_16x16x32_bf16 v[104:107], a[52:55], a[116:119], v[104:107]\n"
  "v_mfma_f32_16x16x32_bf16 v[108:111], a[52:55], a[124:127], 0\n"
  "buffer_load_dwordx4 v250, s[56:59], s78 offen offset:2048 lds\n"
  "v_mfma_f32_16x16x32_bf16 v[108:111], a[48:51], a[120:123], v[108:111]\n"
  "v_mfma_f32_16x16x32_bf16 v[124:127], a[56:59], a[120:123], 0\n"
  "v_mfma_f32_16x16x32_bf16 v[124:127], a[60:63], a[124:127], v[124:127]\n"
  "v_mfma_f32_16x16x32_bf16 v[120:123], a[60:63], a[116:119], 0\n"
  "buffer_load_dwordx4 v251, s[56:59], s79 offen offset:3072 lds\n"
  "v_mfma_f32_16x16x32_bf16 v[120:123], a[56:59], a[112:115], v[120:123]\n"
  "v_mfma_f32_16x16x32_bf16 v[116:119], a[56:59], a[104:107], 0\n"
  "v_mfma_f32_16x16x32_bf16 v[116:119], a[60:63], a[108:111], v[116:119]\n"
  "v_mfma_f32_16x16x32_bf16 v[112:115], a[60:63], a[100:103], 0\n"
  "v_mfma_f32_16x16x32_bf16 v[112:115], a[56:59], a[96:99], v[112:115]\n"
  "s_waitcnt vmcnt(24)\n"
  "s_barrier\n"
  "v_mfma_f32_16x16x32_bf16 a[192:195], a[32:35], a[64:67], 0\n"
  "v_mfma_f32_16x16x32_bf16 a[192:195], a[36:39], a[68:71], a[192:195]\n"
  "ds_read_b128 a[0:3], v234\n"
  "v_mfma_f32_16x16x32_bf16 a[196:199], a[36:39], a[76:79], 0\n"
  "ds_read_b128 a[4:7], v235\n"
  "v_mfma_f32_16x16x32_bf16 a[196:199], a[32:35], a[72:75], a[196:199]\n"
  "ds_read_b64_tr_b16 a[96:97], v230 offset:8192\n"
  "v_mfma_f32_16x16x32_bf16 a[200:203], a[32:35], a[80:83], 0\n"
  "ds_read_b64_tr_b16 a[98:99], v230 offset:8704\n"
  "v_mfma_f32_16x16x32_bf16 a[200:203], a[36:39], a[84:87], a[200:203]\n"
  "ds_read_b64_tr_b16 a[100:101], v230 offset:12288\n"
  "v_mfma_f32_16x16x32_bf16 a[204:207], a[36:39], a[92:95], 0\n"
  "ds_read_b64_tr_b16 a[102:103], v230 offset:12800\n"
  "v_mfma_f32_16x16x32_bf16 a[204:207], a[32:35], a[88:91], a[204:207]\n"
  "ds_read_b64_tr_b16 a[104:105], v231 offset:8192\n"
  "v_mfma_f32_16x16x32_bf16 a[220:223], a[40:43], a[88:91], 0\n"
  "ds_read_b64_tr_b16 a[106:107], v231 offset:8704\n"
  "v_mfma_f32_16x16x32_bf16 a[220:223], a[44:47], a[92:95], a[220:223]\n"
  "ds_read_b64_tr_b16 a[108:109], v231 offset:12288\n"
  "v_mfma_f32_16x16x32_bf16 a[216:219], a[44:47], a[84:87], 0\n"
  "ds_read_b64_tr_b16 a[110:111], v231 offset:12800\n"
  "v_mfma_f32_16x16x32_bf16 a[216:219], a[40:43], a[80:83], a[216:219]\n"
  "ds_read_b64_tr_b16 a[112:113], v232 offset:8192\n"
  "v_mfma_f32_16x16x32_bf16 a[212:215], a[40:43], a[72:75], 0\n"
  "ds_read_b64_tr_b16 a[114:115], v232 offset:8704\n"
  "v_mfma_f32_16x16x32_bf16 a[212:215], a[44:47], a[76:79], a[212:215]\n"
  "ds_read_b64_tr_b16 a[116:117], v232 offset:12288\n"
  "v_mfma_f32_16x16x32_bf16 a[208:211], a[44:47], a[68:71], 0\n"
  "ds_read_b64_tr_b16 a[118:119], v232 offset:12800\n"
  "v_mfma_f32_16x16x32_bf16 a[208:211], a[40:43], a[64:67], a[208:211]\n"
  "ds_read_b64_tr_b16 a[120:121], v233 offset:8192\n"
  "v_mfma_f32_16x16x32_bf16 a[224:227], a[48:51], a[64:67], 0\n"
  "ds_read_b64_tr_b16 a[122:123], v233 offset:8704\n"
  "v_mfma_f32_16x16x32_bf16 a[224:227], a[52:55], a[68:71], a[224:227]\n"
  "ds_read_b64_tr_b16 a[124:125], v233 offset:12288\n"
  "v_mfma_f32_16x16x32_bf16 a[228:231], a[52:55], a[76:79], 0\n"
  "ds_read_b64_tr_b16 a[126:127], v233 offset:12800\n"
  "v_mfma_f32_16x16x32_bf16 a[228:231], a[48:51], a[72:75], a[228:231]\n"
  "ds_read_b128 a[8:11], v234 offset:2048\n"
  "v_mfma_f32_16x16x32_bf16 a[232:235], a[48:51], a[80:83], 0\n"
  "ds_read_b128 a[12:15], v235 offset:2048\n"
  "v_mfma_f32_16x16x32_bf16 a[232:235], a[52:55], a[84:87], a[232:235]\n"
  "ds_read_b128 a[16:19], v234 offset:4096\n"
  "v_mfma_f32_16x16x32_bf16 a[236:239], a[52:55], a[92:95], 0\n"
  "ds_read_b128 a[20:23], v235 offset:4096\n"
  "v_mfma_f32_16x16x32_bf16 a[236:239], a[48:51], a[88:91], a[236:239]\n"
  "ds_read_b128 a[24:27], v234 offset:6144\n"
  "v_mfma_f32_16x16x32_bf16 a[252:255], a[56:59], a[88:91], 0\n"
  "ds_read_b128 a[28:31], v235 offset:6144\n"
  "v_mfma_f32_16x16x32_bf16 a[252:255], a[60:63], a[92:95], a[252:255]\n"
  "v_mfma_f32_16x16x32_bf16 a[248:251], a[60:63], a[84:87], 0\n"
  "v_mfma_f32_16x16x32_bf16 a[248:251], a[56:59], a[80:83], a[248:251]\n"
  "v_mfma_f32_16x16x32_bf16 a[244:247], a[56:59], a[72:75], 0\n"
  "v_mfma_f32_16x16x32_bf16 a[244:247], a[60:63], a[76:79], a[244:247]\n"
  "v_mfma_f32_16x16x32_bf16 a[240:243], a[60:63], a[68:71], 0\n"
  "v_mfma_f32_16x16x32_bf16 a[240:243], a[56:59], a[64:67], a[240:243]\n"
  "s_waitcnt lgkmcnt(0)\n"
  "s_branch L_gs_mid%=\n"
  "L_gs_loop%=:\n"
  "v_mfma_f32_16x16x32_bf16 a[128:131], a[0:3], a[64:67], a[128:131]\n"
  "ds_read_b64_tr_b16 a[96:97], v230 offset:8192\n"
  "v_mfma_f32_16x16x32_bf16 a[128:131], a[4:7], a[68:71], a[128:131]\n"
  "ds_read_b64_tr_b16 a[98:99], v230 offset:8704\n"
  "v_mfma_f32_16x16x32_bf16 a[132:135], a[4:7], a[76:79], a[132:135]\n"
  "ds_read_b64_tr_b16 a[100:101], v230 offset:12288\n"
  "v_mfma_f32_16x16x32_bf16 a[132:135], a[0:3], a[72:75], a[132:135]\n"
  "ds_read_b64_tr_b16 a[102:103], v230 offset:12800\n"
  "v_mfma_f32_16x16x32_bf16 a[136:139], a[0:3], a[80:83], a[136:139]\n"
  "ds_read_b64_tr_b16 a[104:105], v231 offset:8192\n"
  "v_mfma_f32_16x16x32_bf16 a[136:139], a[4:7], a[84:87], a[136:139]\n"
  "ds_read_b64_tr_b16 a[106:107], v231 offset:8704\n"
  "v_mfma_f32_16x16x32_bf16 a[140:143], a[4:7], a[92:95], a[140:143]\n"
  "ds_read_b64_tr_b16 a[108:109], v231 offset:12288\n"
  "v_mfma_f32_16x16x32_bf16 a[140:143], a[0:3], a[88:91], a[140:143]\n"
  "ds_read_b64_tr_b16 a[110:111], v231 offset:12800\n"
  "v_mfma_f32_16x16x32_bf16 a[156:159], a[8:11], a[88:91], a[156:159]\n"
  "ds_read_b64_tr_b16 a[112:113], v232 offset:8192\n"
  "v_mfma_f32_16x16x32_bf16 a[156:159], a[12:15], a[92:95], a[156:159]\n"
  "ds_read_b64_tr_b16 a[114:115], v232 offset:8704\n"
  "v_mfma_f32_16x16x32_bf16 a[152:155], a[12:15], a[84:87], a[152:155]\n"
  "ds_read_b64_tr_b16 a[116:117], v232 offset:12288\n"
  "v_mfma_f32_16x16x32_bf16 a[152:155], a[8:11], a[80:83], a[152:155]\n"
  "ds_read_b64_tr_b16 a[118:119], v232 offset:12800\n"
  "v_mfma_f32_16x16x32_bf16 a[148:151], a[8:11], a[72:75], a[148:151]\n"
  "ds_read_b64_tr_b16 a[120:121], v233 offset:8192\n"
  "v_mfma_f32_16x16x32_bf16 a[148:151], a[12:15], a[76:79], a[148:151]\n"
  "ds_read_b64_tr_b16 a[122:123], v233 offset:8704\n"
  "v_mfma_f32_16x16x32_bf16 a[144:147], a[12:15], a[68:71], a[144:147]\n"
  "ds_read_b64_tr_b16 a[124:125], v233 offset:12288\n"
  "v_mfma_f32_16x16x32_bf16 a[144:147], a[8:11], a[64:67], a[144:147]\n"
  "ds_read_b64_tr_b16 a[126:127], v233 offset:12800\n"
  "v_mfma_f32_16x16x32_bf16 a[160:163], a[16:19], a[64:67], a[160:163]\n"
  "ds_read_b128 a[32:35], v228 offset:8192\n"
  "v_mfma_f32_16x16x32_bf16 a[160:163], a[20:23], a[68:71], a[160:163]\n"
  "ds_read_b128 a[36:39], v229 offset:8192\n"
  "v_mfma_f32_16x16x32_bf16 a[164:167], a[20:23], a[76:79], a[164:167]\n"
  "ds_read_b128 a[40:43], v228 offset:10240\n"
  "v_mfma_f32_16x16x32_bf16 a[164:167], a[16:19], a[72:75], a[164:167]\n"
  "ds_read_b128 a[44:47], v229 offset:10240\n"
  "v_mfma_f32_16x16x32_bf16 a[168:171], a[16:19], a[80:83], a[168:171]\n"
  "ds_read_b128 a[48:51], v228 offset:12288\n"
  "v_mfma_f32_16x16x32_bf16 a[168:171], a[20:23], a[84:87], a[168:171]\n"
  "ds_read_b128 a[52:55], v229 offset:12288\n"
  "v_mfma_f32_16x16x32_bf16 a[172:175], a[20:23], a[92:95], a[172:175]\n"
  "ds_read_b128 a[56:59], v228 offset:14336\n"
  "v_mfma_f32_16x16x32_bf16 a[172:175], a[16:19], a[88:91], a[172:175]\n"
  "ds_read_b128 a[60:63], v229 offset:14336\n"
  "v_mfma_f32_16x16x32_bf16 a[188:191], a[24:27], a[88:91], a[188:191]\n"
  "s_add_u32 s52, s52, s81\n"
  "s_addc_u32 s53, s53, 0\n"
  "v_mfma_f32_16x16x32_bf16 a[188:191], a[28:31], a[92:95], a[188:191]\n"
  "s_add_u32 s56, s56, s80\n"
  "s_addc_u32 s57, s57, 0\n"
  "v_mfma_f32_16x16x32_bf16 a[184:187], a[28:31], a[84:87], a[184:187]\n"
  "v_mfma_f32_16x16x32_bf16 a[184:187], a[24:27], a[80:83], a[184:187]\n"
  "s_cmp_eq_u32 s82, 1\n"
  "v_mfma_f32_16x16x32_bf16 a[180:183], a[24:27], a[72:75], a[180:183]\n"
  "s_waitcnt lgkmcnt(0)\n"
  "s_barrier\n"
  "v_mfma_f32_16x16x32_bf16 a[180:183], a[28:31], a[76:79], a[180:183]\n"
  "s_cbranch_scc1 L_gev0_gs%=\n"
  "L_gevr0_gs%=:\n"
  "s_add_u32 m0, s83, 0\n"
  "v_mfma_f32_16x16x32_bf16 a[176:179], a[28:31], a[68:71], a[176:179]\n"
  "buffer_load_dwordx4 v252, s[52:55], s64 offen sc1 lds\n"
  "v_mfma_f32_16x16x32_bf16 a[176:179], a[24:27], a[64:67], a[176:179]\n"
  "v_mfma_f32_16x16x32_bf16 v[0:3], a[0:3], a[96:99], v[0:3]\n"
  "v_mfma_f32_16x16x32_bf16 v[0:3], a[4:7], a[100:103], v[0:3]\n"
  "v_mfma_f32_16x16x32_bf16 v[4:7], a[4:7], a[108:111], v[4:7]\n"
  "buffer_load_dwordx4 v253, s[52:55], s65 offen offset:1024 sc1 lds\n"
  "v_mfma_f32_16x16x32_bf16 v[4:7], a[0:3], a[104:107], v[4:7]\n"
  "v_mfma_f32_16x16x32_bf16 v[8:11], a[0:3], a[112:115], v[8:11]\n"
  "v_mfma_f32_16x16x32_bf16 v[8:11], a[4:7], a[116:119], v[8:11]\n"
  "v_mfma_f32_16x16x32_bf16 v[12:15], a[4:7], a[124:127], v[12:15]\n"
  "buffer_load_dwordx4 v252, s[52:55], s66 offen offset:2048 sc1 lds\n"
  "v_mfma_f32_16x16x32_bf16 v[12:15], a[0:3], a[120:123], v[12:15]\n"
  "v_mfma_f32_16x16x32_bf16 v[28:31], a[8:11], a[120:123], v[28:31]\n"
  "s_and_b32 s28, s94, s95\n"
  "v_mfma_f32_16x16x32_bf16 v[28:31], a[12:15], a[124:127], v[28:31]\n"
  "s_add_u32 s94, s94, 0x8000\n"
  "v_mfma_f32_16x16x32_bf16 v[24:27], a[12:15], a[116:119], v[24:27]\n"
  "buffer_load_dwordx4 v253, s[52:55], s67 offen offset:3072 sc1 lds\n"
  "v_mfma_f32_16x16x32_bf16 v[24:27], a[8:11], a[112:115], v[24:27]\n"
  "s_cmp_eq_u32 s94, 0x18000\n"
  "s_cselect_b32 s94, 0, s94\n"
  "v_mfma_f32_16x16x32_bf16 v[20:23], a[8:11], a[104:107], v[20:23]\n"
  "v_mfma_f32_16x16x32_bf16 v[20:23], a[12:15], a[108:111], v[20:23]\n"
  "s_add_u32 m0, s83, 0x1000\n"
  "v_mfma_f32_16x16x32_bf16 v[16:19], a[12:15], a[100:103], v[16:19]\n"
  "buffer_load_dwordx4 v252, s[52:55], s68 offen sc1 lds\n"
  "v_mfma_f32_16x16x32_bf16 v[16:19], a[8:11], a[96:99], v[16:19]\n"
  "v_add_u32 v230, s94, v236\n"
  "v_mfma_f32_16x16x32_bf16 v[32:35], a[16:19], a[96:99], v[32:35]\n"
  "v_mfma_f32_16x16x32_bf16 v[32:35], a[20:23], a[100:103], v[32:35]\n"
  "v_add_u32 v231, s94, v237\n"
  "v_mfma_f32_16x16x32_bf16 v[36:39], a[20:23], a[108:111], v[36:39]\n"
  "buffer_load_dwordx4 v253, s[52:55], s69 offen offset:1024 sc1 lds\n"
  "v_mfma_f32_16x16x32_bf16 v[36:39], a[16:19], a[104:107], v[36:39]\n"
  "v_mfma_f32_16x16x32_bf16 v[40:43], a[16:19], a[112:115], v[40:43]\n"
  "v_add_u32 v232, s94, v238\n"
  "v_mfma_f32_16x16x32_bf16 v[40:43], a[20:23], a[116:119], v[40:43]\n"
  "v_add_u32 v233, s94, v239\n"
  "v_mfma_f32_16x16x32_bf16 v[44:47], a[20:23], a[124:127], v[44:47]\n"
  "buffer_load_dwordx4 v252, s[52:55], s70 offen offset:2048 sc1 lds\n"
  "v_mfma_f32_16x16x32_bf16 v[44:47], a[16:19], a[120:123], v[44:47]\n"
  "v_mfma_f32_16x16x32_bf16 v[60:63], a[24:27], a[120:123], v[60:63]\n"
  "v_mfma_f32_16x16x32_bf16 v[60:63], a[28:31], a[124:127], v[60:63]\n"
  "v_mfma_f32_16x16x32_bf16 v[56:59], a[28:31], a[116:119], v[56:59]\n"
  "buffer_load_dwordx4 v253, s[52:55], s71 offen offset:3072 sc1 lds\n"
  "v_mfma_f32_16x16x32_bf16 v[56:59], a[24:27], a[112:115], v[56:59]\n"
  "v_mfma_f32_16x16x32_bf16 v[52:55], a[24:27], a[104:107], v[52:55]\n"
  "v_mfma_f32_16x16x32_bf16 v[52:55], a[28:31], a[108:111], v[52:55]\n"
  "s_add_u32 m0, s84, s28\n"
  "v_mfma_f32_16x16x32_bf16 v[48:51], a[28:31], a[100:103], v[48:51]\n"
  "buffer_load_dwordx4 v250, s[56:59], s72 offen lds\n"
  "v_mfma_f32_16x16x32_bf16 v[48:51], a[24:27], a[96:99], v[48:51]\n"
  "v_mfma_f32_16x16x32_bf16 v[64:67], a[32:35], a[96:99], v[64:67]\n"
  "v_mfma_f32_16x16x32_bf16 v[64:67], a[36:39], a[100:103], v[64:67]\n"
  "v_mfma_f32_16x16x32_bf16 v[68:71], a[36:39], a[108:111], v[68:71]\n"
  "buffer_load_dwordx4 v251, s[56:59], s73 offen offset:1024 lds\n"
  "v_mfma_f32_16x16x32_bf16 v[68:71], a[32:35], a[104:107], v[68:71]\n"
  "v_mfma_f32_16x16x32_bf16 v[72:75], a[32:35], a[112:115], v[72:75]\n"
  "v_mfma_f32_16x16x32_bf16 v[72:75], a[36:39], a[116:119], v[72:75]\n"
  "v_mfma_f32_16x16x32_bf16 v[76:79], a[36:39], a[124:127], v[76:79]\n"
  "buffer_load_dwordx4 v250, s[56:59], s74 offen offset:2048 lds\n"
  "v_mfma_f32_16x16x32_bf16 v[76:79], a[32:35], a[120:123], v[76:79]\n"
  "v_mfma_f32_16x16x32_bf16 v[92:95], a[40:43], a[120:123], v[92:95]\n"
  "v_mfma_f32_16x16x32_bf16 v[92:95], a[44:47], a[124:127], v[92:95]\n"
  "v_mfma_f32_16x16x32_bf16 v[88:91], a[44:47], a[116:119], v[88:91]\n"
  "buffer_load_dwordx4 v251, s[56:59], s75 offen offset:3072 lds\n"
  "v_mfma_f32_16x16x32_bf16 v[88:91], a[40:43], a[112:115], v[88:91]\n"
  "v_mfma_f32_16x16x32_bf16 v[84:87], a[40:43], a[104:107], v[84:87]\n"
  "v_mfma_f32_16x16x32_bf16 v[84:87], a[44:47], a[108:111], v[84:87]\n"
  "s_add_u32 m0, m0, s29\n"
  "v_mfma_f32_16x16x32_bf16 v[80:83], a[44:47], a[100:103], v[80:83]\n"
  "buffer_load_dwordx4 v250, s[56:59], s76 offen lds\n"
  "v_mfma_f32_16x16x32_bf16 v[80:83], a[40:43], a[96:99], v[80:83]\n"
  "v_mfma_f32_16x16x32_bf16 v[96:99], a[48:51], a[96:99], v[96:99]\n"
  "v_mfma_f32_16x16x32_bf16 v[96:99], a[52:55], a[100:103], v[96:99]\n"
  "v_mfma_f32_16x16x32_bf16 v[100:103], a[52:55], a[108:111], v[100:103]\n"
  "buffer_load_dwordx4 v251, s[56:59], s77 offen offset:1024 lds\n"
  "v_mfma_f32_16x16x32_bf16 v[100:103], a[48:51], a[104:107], v[100:103]\n"
  "v_mfma_f32_16x16x32_bf16 v[104:107], a[48:51], a[112:115], v[104:107]\n"
  "v_mfma_f32_16x16x32_bf16 v[104:107], a[52:55], a[116:119], v[104:107]\n"
  "v_mfma_f32_16x16x32_bf16 v[108:111], a[52:55], a[124:127], v[108:111]\n"
  "buffer_load_dwordx4 v250, s[56:59], s78 offen offset:2048 lds\n"
  "v_mfma_f32_16x16x32_bf16 v[108:111], a[48:51], a[120:123], v[108:111]\n"
  "v_mfma_f32_16x16x32_bf16 v[124:127], a[56:59], a[120:123], v[124:127]\n"
  "v_mfma_f32_16x16x32_bf16 v[124:127], a[60:63], a[124:127], v[124:127]\n"
  "v_mfma_f32_16x16x32_bf16 v[120:123], a[60:63], a[116:119], v[120:123]\n"
  "buffer_load_dwordx4 v251, s[56:59], s79 offen offset:3072 lds\n"
  "v_mfma_f32_16x16x32_bf16 v[120:123], a[56:59], a[112:115], v[120:123]\n"
  "v_mfma_f32_16x16x32_bf16 v[116:119], a[56:59], a[104:107], v[116:119]\n"
  "v_mfma_f32_16x16x32_bf16 v[116:119], a[60:63], a[108:111], v[116:119]\n"
  "v_mfma_f32_16x16x32_bf16 v[112:115], a[60:63], a[100:103], v[112:115]\n"
  "v_mfma_f32_16x16x32_bf16 v[112:115], a[56:59], a[96:99], v[112:115]\n"
  "s_waitcnt vmcnt(24)\n"
  "s_barrier\n"
  "v_mfma_f32_16x16x32_bf16 a[192:195], a[32:35], a[64:67], a[192:195]\n"
  "v_mfma_f32_16x16x32_bf16 a[192:195], a[36:39], a[68:71], a[192:195]\n"
  "ds_read_b128 a[0:3], v234\n"
  "v_mfma_f32_16x16x32_bf16 a[196:199], a[36:39], a[76:79], a[196:199]\n"
  "ds_read_b128 a[4:7], v235\n"
  "v_mfma_f32_16x16x32_bf16 a[196:199], a[32:35], a[72:75], a[196:199]\n"
  "ds_read_b64_tr_b16 a[96:97], v230 offset:8192\n"
  "v_mfma_f32_16x16x32_bf16 a[200:203], a[32:35], a[80:83], a[200:203]\n"
  "ds_read_b64_tr_b16 a[98:99], v230 offset:8704\n"
  "v_mfma_f32_16x16x32_bf16 a[200:203], a[36:39], a[84:87], a[200:203]\n"
  "ds_read_b64_tr_b16 a[100:101], v230 offset:12288\n"
  "v_mfma_f32_16x16x32_bf16 a[204:207], a[36:39], a[92:95], a[204:207]\n"
  "ds_read_b64_tr_b16 a[102:103], v230 offset:12800\n"
  "v_mfma_f32_16x16x32_bf16 a[204:207], a[32:35], a[88:91], a[204:207]\n"
  "ds_read_b64_tr_b16 a[104:105], v231 offset:8192\n"
  "v_mfma_f32_16x16x32_bf16 a[220:223], a[40:43], a[88:91], a[220:223]\n"
  "ds_read_b64_tr_b16 a[106:107], v231 offset:8704\n"
  "v_mfma_f32_16x16x32_bf16 a[220:223], a[44:47], a[92:95], a[220:223]\n"
  "ds_read_b64_tr_b16 a[108:109], v231 offset:12288\n"
  "v_mfma_f32_16x16x32_bf16 a[216:219], a[44:47], a[84:87], a[216:219]\n"
  "ds_read_b64_tr_b16 a[110:111], v231 offset:12800\n"
  "v_mfma_f32_16x16x32_bf16 a[216:219], a[40:43], a[80:83], a[216:219]\n"
  "ds_read_b64_tr_b16 a[112:113], v232 offset:8192\n"
  "v_mfma_f32_16x16x32_bf16 a[212:215], a[40:43], a[72:75], a[212:215]\n"
  "ds_read_b64_tr_b16 a[114:115], v232 offset:8704\n"
  "v_mfma_f32_16x16x32_bf16 a[212:215], a[44:47], a[76:79], a[212:215]\n"
  "ds_read_b64_tr_b16 a[116:117], v232 offset:12288\n"
  "v_mfma_f32_16x16x32_bf16 a[208:211], a[44:47], a[68:71], a[208:211]\n"
  "ds_read_b64_tr_b16 a[118:119], v232 offset:12800\n"
  "v_mfma_f32_16x16x32_bf16 a[208:211], a[40:43], a[64:67], a[208:211]\n"
  "ds_read_b64_tr_b16 a[120:121], v233 offset:8192\n"
  "v_mfma_f32_16x16x32_bf16 a[224:227], a[48:51], a[64:67], a[224:227]\n"
  "ds_read_b64_tr_b16 a[122:123], v233 offset:8704\n"
  "v_mfma_f32_16x16x32_bf16 a[224:227], a[52:55], a[68:71], a[224:227]\n"
  "ds_read_b64_tr_b16 a[124:125], v233 offset:12288\n"
  "v_mfma_f32_16x16x32_bf16 a[228:231], a[52:55], a[76:79], a[228:231]\n"
  "ds_read_b64_tr_b16 a[126:127], v233 offset:12800\n"
  "v_mfma_f32_16x16x32_bf16 a[228:231], a[48:51], a[72:75], a[228:231]\n"
  "ds_read_b128 a[8:11], v234 offset:2048\n"
  "v_mfma_f32_16x16x32_bf16 a[232:235], a[48:51], a[80:83], a[232:235]\n"
  "ds_read_b128 a[12:15], v235 offset:2048\n"
  "v_mfma_f32_16x16x32_bf16 a[232:235], a[52:55], a[84:87], a[232:235]\n"
  "ds_read_b128 a[16:19], v234 offset:4096\n"
  "v_mfma_f32_16x16x32_bf16 a[236:239], a[52:55], a[92:95], a[236:239]\n"
  "ds_read_b128 a[20:23], v235 offset:4096\n"
  "v_mfma_f32_16x16x32_bf16 a[236:239], a[48:51], a[88:91], a[236:239]\n"
  "ds_read_b128 a[24:27], v234 offset:6144\n"
  "v_mfma_f32_16x16x32_bf16 a[252:255], a[56:59], a[88:91], a[252:255]\n"
  "ds_read_b128 a[28:31], v235 offset:6144\n"
  "v_mfma_f32_16x16x32_bf16 a[252:255], a[60:63], a[92:95], a[252:255]\n"
  "v_mfma_f32_16x16x32_bf16 a[248:251], a[60:63], a[84:87], a[248:251]\n"
  "v_mfma_f32_16x16x32_bf16 a[248:251], a[56:59], a[80:83], a[248:251]\n"
  "v_mfma_f32_16x16x32_bf16 a[244:247], a[56:59], a[72:75], a[244:247]\n"
  "v_mfma_f32_16x16x32_bf16 a[244:247], a[60:63], a[76:79], a[244:247]\n"
  "v_mfma_f32_16x16x32_bf16 a[240:243], a[60:63], a[68:71], a[240:243]\n"
  "v_mfma_f32_16x16x32_bf16 a[240:243], a[56:59], a[64:67], a[240:243]\n"
  "s_waitcnt lgkmcnt(0)\n"
  "s_cmp_eq_u32 s82, s100\n"
  "s_cbranch_scc1 L_gs_last%=\n"
  "L_gs_mid%=:\n"
  "v_mfma_f32_16x16x32_bf16 v[0:3], a[0:3], a[96:99], v[0:3]\n"
  "ds_read_b64_tr_b16 a[64:65], v230\n"
  "v_mfma_f32_16x16x32_bf16 v[0:3], a[4:7], a[100:103], v[0:3]\n"
  "ds_read_b64_tr_b16 a[66:67], v230 offset:512\n"
  "v_mfma_f32_16x16x32_bf16 v[4:7], a[4:7], a[108:111], v[4:7]\n"
  "ds_read_b64_tr_b16 a[68:69], v230 offset:4096\n"
  "v_mfma_f32_16x16x32_bf16 v[4:7], a[0:3], a[104:107], v[4:7]\n"
  "ds_read_b64_tr_b16 a[70:71], v230 offset:4608\n"
  "v_mfma_f32_16x16x32_bf16 v[8:11], a[0:3], a[112:115], v[8:11]\n"
  "ds_read_b64_tr_b16 a[72:73], v231\n"
  "v_mfma_f32_16x16x32_bf16 v[8:11], a[4:7], a[116:119], v[8:11]\n"
  "ds_read_b64_tr_b16 a[74:75], v231 offset:512\n"
  "v_mfma_f32_16x16x32_bf16 v[12:15], a[4:7], a[124:127], v[12:15]\n"
  "ds_read_b64_tr_b16 a[76:77], v231 offset:4096\n"
  "v_mfma_f32_16x16x32_bf16 v[12:15], a[0:3], a[120:123], v[12:15]\n"
  "ds_read_b64_tr_b16 a[78:79], v231 offset:4608\n"
  "v_mfma_f32_16x16x32_bf16 v[28:31], a[8:11], a[120:123], v[28:31]\n"
  "ds_read_b64_tr_b16 a[80:81], v232\n"
  "v_mfma_f32_16x16x32_bf16 v[28:31], a[12:15], a[124:127], v[28:31]\n"
  "ds_read_b64_tr_b16 a[82:83], v232 offset:512\n"
  "v_mfma_f32_16x16x32_bf16 v[24:27], a[12:15], a[116:119], v[24:27]\n"
  "ds_read_b64_tr_b16 a[84:85], v232 offset:4096\n"
  "v_mfma_f32_16x16x32_bf16 v[24:27], a[8:11], a[112:115], v[24:27]\n"
  "ds_read_b64_tr_b16 a[86:87], v232 offset:4608\n"
  "v_mfma_f32_16x16x32_bf16 v[20:23], a[8:11], a[104:107], v[20:23]\n"
  "ds_read_b64_tr_b16 a[88:89], v233\n"
  "v_mfma_f32_16x16x32_bf16 v[20:23], a[12:15], a[108:111], v[20:23]\n"
  "ds_read_b64_tr_b16 a[90:91], v233 offset:512\n"
  "v_mfma_f32_16x16x32_bf16 v[16:19], a[12:15], a[100:103], v[16:19]\n"
  "ds_read_b64_tr_b16 a[92:93], v233 offset:4096\n"
  "v_mfma_f32_16x16x32_bf16 v[16:19], a[8:11], a[96:99], v[16:19]\n"
  "ds_read_b64_tr_b16 a[94:95], v233 offset:4608\n"
  "v_mfma_f32_16x16x32_bf16 v[32:35], a[16:19], a[96:99], v[32:35]\n"
  "ds_read_b128 a[32:35], v234 offset:8192\n"
  "v_mfma_f32_16x16x32_bf16 v[32:35], a[20:23], a[100:103], v[32:35]\n"
  "ds_read_b128 a[36:39], v235 offset:8192\n"
  "v_mfma_f32_16x16x32_bf16 v[36:39], a[20:23], a[108:111], v[36:39]\n"
  "ds_read_b128 a[40:43], v234 offset:10240\n"
  "v_mfma_f32_16x16x32_bf16 v[36:39], a[16:19], a[104:107], v[36:39]\n"
  "ds_read_b128 a[44:47], v235 offset:10240\n"
  "v_mfma_f32_16x16x32_bf16 v[40:43], a[16:19], a[112:115], v[40:43]\n"
  "ds_read_b128 a[48:51], v234 offset:12288\n"
  "v_mfma_f32_16x16x32_bf16 v[40:43], a[20:23], a[116:119], v[40:43]\n"
  "ds_read_b128 a[52:55], v235 offset:12288\n"
  "v_mfma_f32_16x16x32_bf16 v[44:47], a[20:23], a[124:127], v[44:47]\n"
  "ds_read_b128 a[56:59], v234 offset:14336\n"
  "v_mfma_f32_16x16x32_bf16 v[44:47], a[16:19], a[120:123], v[44:47]\n"
  "ds_read_b128 a[60:63], v235 offset:14336\n"
  "v_mfma_f32_16x16x32_bf16 v[60:63], a[24:27], a[120:123], v[60:63]\n"
  "s_add_u32 s52, s52, s81\n"
  "s_addc_u32 s53, s53, 0\n"
  "v_mfma_f32_16x16x32_bf16 v[60:63], a[28:31], a[124:127], v[60:63]\n"
  "s_add_u32 s56, s56, s80\n"
  "s_addc_u32 s57, s57, 0\n"
  "v_mfma_f32_16x16x32_bf16 v[56:59], a[28:31], a[116:119], v[56:59]\n"
  "v_mfma_f32_16x16x32_bf16 v[56:59], a[24:27], a[112:115], v[56:59]\n"
  "s_bitcmp1_b32 s85, s82\n"
  "v_mfma_f32_16x16x32_bf16 v[52:55], a[24:27], a[104:107], v[52:55]\n"
  "s_waitcnt lgkmcnt(0)\n"
  "s_barrier\n"
  "v_mfma_f32_16x16x32_bf16 v[52:55], a[28:31], a[108:111], v[52:55]\n"
  "s_cbranch_scc1 L_gev1_gs%=\n"
  "L_gevr1_gs%=:\n"
  "s_add_u32 m0, s83, 0x8000\n"
  "v_mfma_f32_16x16x32_bf16 v[48:51], a[28:31], a[100:103], v[48:51]\n"
  "buffer_load_dwordx4 v252, s[52:55], s64 offen sc1 lds\n"
  "v_mfma_f32_16x16x32_bf16 v[48:51], a[24:27], a[96:99], v[48:51]\n"
  "v_mfma_f32_16x16x32_bf16 a[128:131], a[0:3], a[64:67], a[128:131]\n"
  "v_mfma_f32_16x16x32_bf16 a[128:131], a[4:7], a[68:71], a[128:131]\n"
  "v_mfma_f32_16x16x32_bf16 a[132:135], a[4:7], a[76:79], a[132:135]\n"
  "buffer_load_dwordx4 v253, s[52:55], s65 offen offset:1024 sc1 lds\n"
  "v_mfma_f32_16x16x32_bf16 a[132:135], a[0:3], a[72:75], a[132:135]\n"
  "v_mfma_f32_16x16x32_bf16 a[136:139], a[0:3], a[80:83], a[136:139]\n"
  "v_mfma_f32_16x16x32_bf16 a[136:139], a[4:7], a[84:87], a[136:139]\n"
  "v_mfma_f32_16x16x32_bf16 a[140:143], a[4:7], a[92:95], a[140:143]\n"
  "buffer_load_dwordx4 v252, s[52:55], s66 offen offset:2048 sc1 lds\n"
  "v_mfma_f32_16x16x32_bf16 a[140:143], a[0:3], a[88:91], a[140:143]\n"
  "v_mfma_f32_16x16x32_bf16 a[156:159], a[8:11], a[88:91], a[156:159]\n"
  "s_and_b32 s28, s94, s95\n"
  "v_mfma_f32_16x16x32_bf16 a[156:159], a[12:15], a[92:95], a[156:159]\n"
  "s_add_u32 s94, s94, 0x8000\n"
  "v_mfma_f32_16x16x32_bf16 a[152:155], a[12:15], a[84:87], a[152:155]\n"
  "buffer_load_dwordx4 v253, s[52:55], s67 offen offset:3072 sc1 lds\n"
  "v_mfma_f32_16x16x32_bf16 a[152:155], a[8:11], a[80:83], a[152:155]\n"
  "s_cmp_eq_u32 s94, 0x18000\n"
  "s_cselect_b32 s94, 0, s94\n"
  "v_mfma_f32_16x16x32_bf16 a[148:151], a[8:11], a[72:75], a[148:151]\n"
  "v_mfma_f32_16x16x32_bf16 a[148:151], a[12:15], a[76:79], a[148:151]\n"
  "s_add_u32 m0, s83, 0x9000\n"
  "v_mfma_f32_16x16x32_bf16 a[144:147], a[12:15], a[68:71], a[144:147]\n"
  "buffer_load_dwordx4 v252, s[52:55], s68 offen sc1 lds\n"
  "v_mfma_f32_16x16x32_bf16 a[144:147], a[8:11], a[64:67], a[144:147]\n"
  "v_add_u32 v230, s94, v236\n"
  "v_mfma_f32_16x16x32_bf16 a[160:163], a[16:19], a[64:67], a[160:163]\n"
  "v_mfma_f32_16x16x32_bf16 a[160:163], a[20:23], a[68:71], a[160:163]\n"
  "v_add_u32 v231, s94, v237\n"
  "v_mfma_f32_16x16x32_bf16 a[164:167], a[20:23], a[76:79], a[164:167]\n"
  "buffer_load_dwordx4 v253, s[52:55], s69 offen offset:1024 sc1 lds\n"
  "v_mfma_f32_16x16x32_bf16 a[164:167], a[16:19], a[72:75], a[164:167]\n"
  "v_mfma_f32_16x16x32_bf16 a[168:171], a[16:19], a[80:83], a[168:171]\n"
  "v_add_u32 v232, s94, v238\n"
  "v_mfma_f32_16x16x32_bf16 a[168:171], a[20:23], a[84:87], a[168:171]\n"
  "v_add_u32 v233, s94, v239\n"
  "v_mfma_f32_16x16x32_bf16 a[172:175], a[20:23], a[92:95], a[172:175]\n"
  "buffer_load_dwordx4 v252, s[52:55], s70 offen offset:2048 sc1 lds\n"
  "v_mfma_f32_16x16x32_bf16 a[172:175], a[16:19], a[88:91], a[172:175]\n"
  "v_mfma_f32_16x16x32_bf16 a[188:191], a[24:27], a[88:91], a[188:191]\n"
  "v_mfma_f32_16x16x32_bf16 a[188:191], a[28:31], a[92:95], a[188:191]\n"
  "v_mfma_f32_16x16x32_bf16 a[184:187], a[28:31], a[84:87], a[184:187]\n"
  "buffer_load_dwordx4 v253, s[52:55], s71 offen offset:3072 sc1 lds\n"
  "v_mfma_f32_16x16x32_bf16 a[184:187], a[24:27], a[80:83], a[184:187]\n"
  "v_mfma_f32_16x16x32_bf16 a[180:183], a[24:27], a[72:75], a[180:183]\n"
  "v_mfma_f32_16x16x32_bf16 a[180:183], a[28:31], a[76:79], a[180:183]\n"
  "s_add_u32 m0, s84, s28\n"
  "v_mfma_f32_16x16x32_bf16 a[176:179], a[28:31], a[68:71], a[176:179]\n"
  "buffer_load_dwordx4 v250, s[56:59], s72 offen lds\n"
  "v_mfma_f32_16x16x32_bf16 a[176:179], a[24:27], a[64:67], a[176:179]\n"
  "v_mfma_f32_16x16x32_bf16 a[192:195], a[32:35], a[64:67], a[192:195]\n"
  "v_mfma_f32_16x16x32_bf16 a[192:195], a[36:39], a[68:71], a[192:195]\n"
  "v_mfma_f32_16x16x32_bf16 a[196:199], a[36:39], a[76:79], a[196:199]\n"
  "buffer_load_dwordx4 v251, s[56:59], s73 offen offset:1024 lds\n"
  "v_mfma_f32_16x16x32_bf16 a[196:199], a[32:35], a[72:75], a[196:199]\n"
  "v_mfma_f32_16x16x32_bf16 a[200:203], a[32:35], a[80:83], a[200:203]\n"
  "v_mfma_f32_16x16x32_bf16 a[200:203], a[36:39], a[84:87], a[200:203]\n"
  "v_mfma_f32_16x16x32_bf16 a[204:207], a[36:39], a[92:95], a[204:207]\n"
  "buffer_load_dwordx4 v250, s[56:59], s74 offen offset:2048 lds\n"
  "v_mfma_f32_16x16x32_bf16 a[204:207], a[32:35], a[88:91], a[204:207]\n"
  "v_mfma_f32_16x16x32_bf16 a[220:223], a[40:43], a[88:91], a[220:223]\n"
  "v_mfma_f32_16x16x32_bf16 a[220:223], a[44:47], a[92:95], a[220:223]\n"
  "v_mfma_f32_16x16x32_bf16 a[216:219], a[44:47], a[84:87], a[216:219]\n"
  "buffer_load_dwordx4 v251, s[56:59], s75 offen offset:3072 lds\n"
  "v_mfma_f32_16x16x32_bf16 a[216:219], a[40:43], a[80:83], a[216:219]\n"
  "v_mfma_f32_16x16x32_bf16 a[212:215], a[40:43], a[72:75], a[212:215]\n"
  "v_mfma_f32_16x16x32_bf16 a[212:215], a[44:47], a[76:79], a[212:215]\n"
  "s_add_u32 m0, m0, s29\n"
  "v_mfma_f32_16x16x32_bf16 a[208:211], a[44:47], a[68:71], a[208:211]\n"
  "buffer_load_dwordx4 v250, s[56:59], s76 offen lds\n"
  "v_mfma_f32_16x16x32_bf16 a[208:211], a[40:43], a[64:67], a[208:211]\n"
  "v_mfma_f32_16x16x32_bf16 a[224:227], a[48:51], a[64:67], a[224:227]\n"
  "v_mfma_f32_16x16x32_bf16 a[224:227], a[52:55], a[68:71], a[224:227]\n"
  "v_mfma_f32_16x16x32_bf16 a[228:231], a[52:55], a[76:79], a[228:231]\n"
  "buffer_load_dwordx4 v251, s[56:59], s77 offen offset:1024 lds\n"
  "v_mfma_f32_16x16x32_bf16 a[228:231], a[48:51], a[72:75], a[228:231]\n"
  "v_mfma_f32_16x16x32_bf16 a[232:235], a[48:51], a[80:83], a[232:235]\n"
  "v_mfma_f32_16x16x32_bf16 a[232:235], a[52:55], a[84:87], a[232:235]\n"
  "v_mfma_f32_16x16x32_bf16 a[236:239], a[52:55], a[92:95], a[236:239]\n"
  "buffer_load_dwordx4 v250, s[56:59], s78 offen offset:2048 lds\n"
  "v_mfma_f32_16x16x32_bf16 a[236:239], a[48:51], a[88:91], a[236:239]\n"
  "v_mfma_f32_16x16x32_bf16 a[252:255], a[56:59], a[88:91], a[252:255]\n"
  "v_mfma_f32_16x16x32_bf16 a[252:255], a[60:63], a[92:95], a[252:255]\n"
  "v_mfma_f32_16x16x32_bf16 a[248:251], a[60:63], a[84:87], a[248:251]\n"
  "buffer_load_dwordx4 v251, s[56:59], s79 offen offset:3072 lds\n"
  "v_mfma_f32_16x16x32_bf16 a[248:251], a[56:59], a[80:83], a[248:251]\n"
  "v_mfma_f32_16x16x32_bf16 a[244:247], a[56:59], a[72:75], a[244:247]\n"
  "v_mfma_f32_16x16x32_bf16 a[244:247], a[60:63], a[76:79], a[244:247]\n"
  "v_mfma_f32_16x16x32_bf16 a[240:243], a[60:63], a[68:71], a[240:243]\n"
  "s_cmp_lg_u32 s86, 0\n"
  "v_mfma_f32_16x16x32_bf16 a[240:243], a[56:59], a[64:67], a[240:243]\n"
  "s_waitcnt vmcnt(24)\n"
  "s_barrier\n"
  "v_mfma_f32_16x16x32_bf16 v[64:67], a[32:35], a[96:99], v[64:67]\n"
  "s_cbranch_scc1 L_hk_gs%=\n"
  "L_hkr_gs%=:\n"
  "v_mfma_f32_16x16x32_bf16 v[64:67], a[36:39], a[100:103], v[64:67]\n"
  "ds_read_b128 a[0:3], v228\n"
  "v_mfma_f32_16x16x32_bf16 v[68:71], a[36:39], a[108:111], v[68:71]\n"
  "ds_read_b128 a[4:7], v229\n"
  "v_mfma_f32_16x16x32_bf16 v[68:71], a[32:35], a[104:107], v[68:71]\n"
  "ds_read_b64_tr_b16 a[64:65], v230\n"
  "v_mfma_f32_16x16x32_bf16 v[72:75], a[32:35], a[112:115], v[72:75]\n"
  "ds_read_b64_tr_b16 a[66:67], v230 offset:512\n"
  "v_mfma_f32_16x16x32_bf16 v[72:75], a[36:39], a[116:119], v[72:75]\n"
  "ds_read_b64_tr_b16 a[68:69], v230 offset:4096\n"
  "v_mfma_f32_16x16x32_bf16 v[76:79], a[36:39], a[124:127], v[76:79]\n"
  "ds_read_b64_tr_b16 a[70:71], v230 offset:4608\n"
  "v_mfma_f32_16x16x32_bf16 v[76:79], a[32:35], a[120:123], v[76:79]\n"
  "ds_read_b64_tr_b16 a[72:73], v231\n"
  "v_mfma_f32_16x16x32_bf16 v[92:95], a[40:43], a[120:123], v[92:95]\n"
  "ds_read_b64_tr_b16 a[74:75], v231 offset:512\n"
  "v_mfma_f32_16x16x32_bf16 v[92:95], a[44:47], a[124:127], v[92:95]\n"
  "ds_read_b64_tr_b16 a[76:77], v231 offset:4096\n"
  "v_mfma_f32_16x16x32_bf16 v[88:91], a[44:47], a[116:119], v[88:91]\n"
  "ds_read_b64_tr_b16 a[78:79], v231 offset:4608\n"
  "v_mfma_f32_16x16x32_bf16 v[88:91], a[40:43], a[112:115], v[88:91]\n"
  "ds_read_b64_tr_b16 a[80:81], v232\n"
  "v_mfma_f32_16x16x32_bf16 v[84:87], a[40:43], a[104:107], v[84:87]\n"
  "ds_read_b64_tr_b16 a[82:83], v232 offset:512\n"
  "v_mfma_f32_16x16x32_bf16 v[84:87], a[44:47], a[108:111], v[84:87]\n"
  "ds_read_b64_tr_b16 a[84:85], v232 offset:4096\n"
  "v_mfma_f32_16x16x32_bf16 v[80:83], a[44:47], a[100:103], v[80:83]\n"
  "ds_read_b64_tr_b16 a[86:87], v232 offset:4608\n"
  "v_mfma_f32_16x16x32_bf16 v[80:83], a[40:43], a[96:99], v[80:83]\n"
  "ds_read_b64_tr_b16 a[88:89], v233\n"
  "v_mfma_f32_16x16x32_bf16 v[96:99], a[48:51], a[96:99], v[96:99]\n"
  "ds_read_b64_tr_b16 a[90:91], v233 offset:512\n"
  "v_mfma_f32_16x16x32_bf16 v[96:99], a[52:55], a[100:103], v[96:99]\n"
  "ds_read_b64_tr_b16 a[92:93], v233 offset:4096\n"
  "v_mfma_f32_16x16x32_bf16 v[100:103], a[52:55], a[108:111], v[100:103]\n"
  "ds_read_b64_tr_b16 a[94:95], v233 offset:4608\n"
  "v_mfma_f32_16x16x32_bf16 v[100:103], a[48:51], a[104:107], v[100:103]\n"
  "ds_read_b128 a[8:11], v228 offset:2048\n"
  "v_mfma_f32_16x16x32_bf16 v[104:107], a[48:51], a[112:115], v[104:107]\n"
  "ds_read_b128 a[12:15], v229 offset:2048\n"
  "v_mfma_f32_16x16x32_bf16 v[104:107], a[52:55], a[116:119], v[104:107]\n"
  "ds_read_b128 a[16:19], v228 offset:4096\n"
  "v_mfma_f32_16x16x32_bf16 v[108:111], a[52:55], a[124:127], v[108:111]\n"
  "ds_read_b128 a[20:23], v229 offset:4096\n"
  "v_mfma_f32_16x16x32_bf16 v[108:111], a[48:51], a[120:123], v[108:111]\n"
  "ds_read_b128 a[24:27], v228 offset:6144\n"
  "v_mfma_f32_16x16x32_bf16 v[124:127], a[56:59], a[120:123], v[124:127]\n"
  "ds_read_b128 a[28:31], v229 offset:6144\n"
  "v_mfma_f32_16x16x32_bf16 v[124:127], a[60:63], a[124:127], v[124:127]\n"
  "v_mfma_f32_16x16x32_bf16 v[120:123], a[60:63], a[116:119], v[120:123]\n"
  "v_mfma_f32_16x16x32_bf16 v[120:123], a[56:59], a[112:115], v[120:123]\n"
  "v_mfma_f32_16x16x32_bf16 v[116:119], a[56:59], a[104:107], v[116:119]\n"
  "v_mfma_f32_16x16x32_bf16 v[116:119], a[60:63], a[108:111], v[116:119]\n"
  "v_mfma_f32_16x16x32_bf16 v[112:115], a[60:63], a[100:103], v[112:115]\n"
  "v_mfma_f32_16x16x32_bf16 v[112:115], a[56:59], a[96:99], v[112:115]\n"
  "s_waitcnt lgkmcnt(0)\n"
  "s_sub_u32 s82, s82, 1\n"
  "s_cmp_lg_u32 s82, 0\n"
  "s_cbranch_scc1 L_gs_loop%=\n"
  "L_gs_last%=:\n"
  "v_mfma_f32_16x16x32_bf16 v[0:3], a[0:3], a[96:99], v[0:3]\n"
  "ds_read_b64_tr_b16 a[64:65], v230\n"
  "v_mfma_f32_16x16x32_bf16 v[0:3], a[4:7], a[100:103], v[0:3]\n"
  "ds_read_b64_tr_b16 a[66:67], v230 offset:512\n"
  "v_mfma_f32_16x16x32_bf16 v[4:7], a[4:7], a[108:111], v[4:7]\n"
  "ds_read_b64_tr_b16 a[68:69], v230 offset:4096\n"
  "v_mfma_f32_16x16x32_bf16 v[4:7], a[0:3], a[104:107], v[4:7]\n"
  "ds_read_b64_tr_b16 a[70:71], v230 offset:4608\n"
  "v_mfma_f32_16x16x32_bf16 v[8:11], a[0:3], a[112:115], v[8:11]\n"
  "ds_read_b64_tr_b16 a[72:73], v231\n"
  "v_mfma_f32_16x16x32_bf16 v[8:11], a[4:7], a[116:119], v[8:11]\n"
  "ds_read_b64_tr_b16 a[74:75], v231 offset:512\n"
  "v_mfma_f32_16x16x32_bf16 v[12:15], a[4:7], a[124:127], v[12:15]\n"
  "ds_read_b64_tr_b16 a[76:77], v231 offset:4096\n"
  "v_mfma_f32_16x16x32_bf16 v[12:15], a[0:3], a[120:123], v[12:15]\n"
  "ds_read_b64_tr_b16 a[78:79], v231 offset:4608\n"
  "v_mfma_f32_16x16x32_bf16 v[28:31], a[8:11], a[120:123], v[28:31]\n"
  "ds_read_b64_tr_b16 a[80:81], v232\n"
  "v_mfma_f32_16x16x32_bf16 v[28:31], a[12:15], a[124:127], v[28:31]\n"
  "ds_read_b64_tr_b16 a[82:83], v232 offset:512\n"
  "v_mfma_f32_16x16x32_bf16 v[24:27], a[12:15], a[116:119], v[24:27]\n"
  "ds_read_b64_tr_b16 a[84:85], v232 offset:4096\n"
  "v_mfma_f32_16x16x32_bf16 v[24:27], a[8:11], a[112:115], v[24:27]\n"
  "ds_read_b64_tr_b16 a[86:87], v232 offset:4608\n"
  "v_mfma_f32_16x16x32_bf16 v[20:23], a[8:11], a[104:107], v[20:23]\n"
  "ds_read_b64_tr_b16 a[88:89], v233\n"
  "v_mfma_f32_16x16x32_bf16 v[20:23], a[12:15], a[108:111], v[20:23]\n"
  "ds_read_b64_tr_b16 a[90:91], v233 offset:512\n"
  "v_mfma_f32_16x16x32_bf16 v[16:19], a[12:15], a[100:103], v[16:19]\n"
  "ds_read_b64_tr_b16 a[92:93], v233 offset:4096\n"
  "v_mfma_f32_16x16x32_bf16 v[16:19], a[8:11], a[96:99], v[16:19]\n"
  "ds_read_b64_tr_b16 a[94:95], v233 offset:4608\n"
  "v_mfma_f32_16x16x32_bf16 v[32:35], a[16:19], a[96:99], v[32:35]\n"
  "ds_read_b128 a[32:35], v234 offset:8192\n"
  "v_mfma_f32_16x16x32_bf16 v[32:35], a[20:23], a[100:103], v[32:35]\n"
  "ds_read_b128 a[36:39], v235 offset:8192\n"
  "v_mfma_f32_16x16x32_bf16 v[36:39], a[20:23], a[108:111], v[36:39]\n"
  "ds_read_b128 a[40:43], v234 offset:10240\n"
  "v_mfma_f32_16x16x32_bf16 v[36:39], a[16:19], a[104:107], v[36:39]\n"
  "ds_read_b128 a[44:47], v235 offset:10240\n"
  "v_mfma_f32_16x16x32_bf16 v[40:43], a[16:19], a[112:115], v[40:43]\n"
  "ds_read_b128 a[48:51], v234 offset:12288\n"
  "v_mfma_f32_16x16x32_bf16 v[40:43], a[20:23], a[116:119], v[40:43]\n"
  "ds_read_b128 a[52:55], v235 offset:12288\n"
  "v_mfma_f32_16x16x32_bf16 v[44:47], a[20:23], a[124:127], v[44:47]\n"
  "ds_read_b128 a[56:59], v234 offset:14336\n"
  "v_mfma_f32_16x16x32_bf16 v[44:47], a[16:19], a[120:123], v[44:47]\n"
  "ds_read_b128 a[60:63], v235 offset:14336\n"
  "v_mfma_f32_16x16x32_bf16 v[60:63], a[24:27], a[120:123], v[60:63]\n"
  "s_add_u32 s52, s52, s81\n"
  "s_addc_u32 s53, s53, 0\n"
  "v_mfma_f32_16x16x32_bf16 v[60:63], a[28:31], a[124:127], v[60:63]\n"
  "s_add_u32 s56, s56, s80\n"
  "s_addc_u32 s57, s57, 0\n"
  "v_mfma_f32_16x16x32_bf16 v[56:59], a[28:31], a[116:119], v[56:59]\n"
  "v_mfma_f32_16x16x32_bf16 v[56:59], a[24:27], a[112:115], v[56:59]\n"
  "s_cmp_lt_u32 s44, 4\n"
  "v_mfma_f32_16x16x32_bf16 v[52:55], a[24:27], a[104:107], v[52:55]\n"
  "s_waitcnt lgkmcnt(0)\n"
  "s_barrier\n"
  "v_mfma_f32_16x16x32_bf16 v[52:55], a[28:31], a[108:111], v[52:55]\n"
  "s_cbranch_scc1 L_kl_v%=\n"
  "s_getpc_b64 s[96:97]\n"
  "s_add_u32 s96, s96, 12\n"
  "s_addc_u32 s97, s97, 0\n"
  "s_branch L_sregsw%=\n"
  "L_kl_v%=:\n"
  "s_add_u32 m0, s83, 0x8000\n"
  "v_mfma_f32_16x16x32_bf16 v[48:51], a[28:31], a[100:103], v[48:51]\n"
  "buffer_load_dwordx4 v252, s[52:55], s64 offen sc1 lds\n"
  "v_mfma_f32_16x16x32_bf16 v[48:51], a[24:27], a[96:99], v[48:51]\n"
  "s_and_b32 s87, s34, 1\n"
  "s_mul_i32 s87, s87, 0x108000\n"
  "v_mfma_f32_16x16x32_bf16 a[128:131], a[0:3], a[64:67], a[128:131]\n"
  "s_lshr_b32 s90, s34, 1\n"
  "s_lshl_b32 s90, s90, 16\n"
  "v_mfma_f32_16x16x32_bf16 a[128:131], a[4:7], a[68:71], a[128:131]\n"
  "s_add_u32 s87, s87, s90\n"
  "s_add_u32 s87, s87, s39\n"
  "v_mfma_f32_16x16x32_bf16 a[132:135], a[4:7], a[76:79], a[132:135]\n"
  "buffer_load_dwordx4 v253, s[52:55], s65 offen offset:1024 sc1 lds\n"
  "s_add_u32 s87, s87, 0x8000\n"
  "v_mfma_f32_16x16x32_bf16 a[132:135], a[0:3], a[72:75], a[132:135]\n"
  "s_add_u32 s60, s8, s87\n"
  "s_addc_u32 s61, s9, 0\n"
  "v_mfma_f32_16x16x32_bf16 a[136:139], a[0:3], a[80:83], a[136:139]\n"
  "s_sub_u32 s62, 0x5b00000, s87\n"
  "s_mov_b32 s63, 0x20000\n"
  "v_mfma_f32_16x16x32_bf16 a[136:139], a[4:7], a[84:87], a[136:139]\n"
  "s_mov_b32 s90, 0x1000\n"
  "v_cvt_pk_bf16_f32 v136, v0, v1\n"
  "v_mfma_f32_16x16x32_bf16 a[140:143], a[4:7], a[92:95], a[140:143]\n"
  "buffer_load_dwordx4 v252, s[52:55], s66 offen offset:2048 sc1 lds\n"
  "v_cvt_pk_bf16_f32 v137, v2, v3\n"
  "v_mfma_f32_16x16x32_bf16 a[140:143], a[0:3], a[88:91], a[140:143]\n"
  "v_cvt_pk_bf16_f32 v138, v16, v17\n"
  "v_cvt_pk_bf16_f32 v139, v18, v19\n"
  "v_mfma_f32_16x16x32_bf16 a[156:159], a[8:11], a[88:91], a[156:159]\n"
  "buffer_store_dwordx4 v[136:139], v246, s[60:63], s90 offen sc1\n"
  "s_and_b32 s28, s94, s95\n"
  "v_mfma_f32_16x16x32_bf16 a[156:159], a[12:15], a[92:95], a[156:159]\n"
  "s_add_u32 s94, s94, 0x8000\n"
  "v_cvt_pk_bf16_f32 v148, v4, v5\n"
  "v_mfma_f32_16x16x32_bf16 a[152:155], a[12:15], a[84:87], a[152:155]\n"
  "buffer_load_dwordx4 v253, s[52:55], s67 offen offset:3072 sc1 lds\n"
  "v_cvt_pk_bf16_f32 v149, v6, v7\n"
  "v_mfma_f32_16x16x32_bf16 a[152:155], a[8:11], a[80:83], a[152:155]\n"
  "s_cmp_eq_u32 s94, 0x18000\n"
  "s_cselect_b32 s94, 0, s94\n"
  "v_mfma_f32_16x16x32_bf16 a[148:151], a[8:11], a[72:75], a[148:151]\n"
  "v_cvt_pk_bf16_f32 v150, v20, v21\n"
  "v_cvt_pk_bf16_f32 v151, v22, v23\n"
  "v_mfma_f32_16x16x32_bf16 a[148:151], a[12:15], a[76:79], a[148:151]\n"
  "s_add_u32 m0, s83, 0x9000\n"
  "v_cvt_pk_bf16_f32 v160, v8, v9\n"
  "v_mfma_f32_16x16x32_bf16 a[144:147], a[12:15], a[68:71], a[144:147]\n"
  "buffer_load_dwordx4 v252, s[52:55], s68 offen sc1 lds\n"
  "v_cvt_pk_bf16_f32 v161, v10, v11\n"
  "v_mfma_f32_16x16x32_bf16 a[144:147], a[8:11], a[64:67], a[144:147]\n"
  "v_add_u32 v230, s94, v236\n"
  "v_cvt_pk_bf16_f32 v162, v24, v25\n"
  "v_mfma_f32_16x16x32_bf16 a[160:163], a[16:19], a[64:67], a[160:163]\n"
  "buffer_store_dwordx4 v[148:151], v246, s[60:63], s90 offen offset:256 sc1\n"
  "v_cvt_pk_bf16_f32 v163, v26, v27\n"
  "v_mfma_f32_16x16x32_bf16 a[160:163], a[20:23], a[68:71], a[160:163]\n"
  "v_add_u32 v231, s94, v237\n"
  "v_cvt_pk_bf16_f32 v172, v12, v13\n"
  "v_mfma_f32_16x16x32_bf16 a[164:167], a[20:23], a[76:79], a[164:167]\n"
  "buffer_load_dwordx4 v253, s[52:55], s69 offen offset:1024 sc1 lds\n"
  "v_cvt_pk_bf16_f32 v173, v14, v15\n"
  "v_mfma_f32_16x16x32_bf16 a[164:167], a[16:19], a[72:75], a[164:167]\n"
  "v_cvt_pk_bf16_f32 v174, v28, v29\n"
  "v_cvt_pk_bf16_f32 v175, v30, v31\n"
  "v_mfma_f32_16x16x32_bf16 a[168:171], a[16:19], a[80:83], a[168:171]\n"
  "v_add_u32 v232, s94, v238\n"
  "v_mfma_f32_16x16x32_bf16 a[168:171], a[20:23], a[84:87], a[168:171]\n"
  "v_add_u32 v233, s94, v239\n"
  "v_mfma_f32_16x16x32_bf16 a[172:175], a[20:23], a[92:95], a[172:175]\n"
  "buffer_load_dwordx4 v252, s[52:55], s70 offen offset:2048 sc1 lds\n"
  "v_mfma_f32_16x16x32_bf16 a[172:175], a[16:19], a[88:91], a[172:175]\n"
  "v_mfma_f32_16x16x32_bf16 a[188:191], a[24:27], a[88:91], a[188:191]\n"
  "buffer_store_dwordx4 v[160:163], v246, s[60:63], s90 offen offset:512 sc1\n"
  "v_mfma_f32_16x16x32_bf16 a[188:191], a[28:31], a[92:95], a[188:191]\n"
  "v_mfma_f32_16x16x32_bf16 a[184:187], a[28:31], a[84:87], a[184:187]\n"
  "buffer_load_dwordx4 v253, s[52:55], s71 offen offset:3072 sc1 lds\n"
  "v_mfma_f32_16x16x32_bf16 a[184:187], a[24:27], a[80:83], a[184:187]\n"
  "v_mfma_f32_16x16x32_bf16 a[180:183], a[24:27], a[72:75], a[180:183]\n"
  "v_mfma_f32_16x16x32_bf16 a[180:183], a[28:31], a[76:79], a[180:183]\n"
  "s_add_u32 m0, s84, s28\n"
  "v_mfma_f32_16x16x32_bf16 a[176:179], a[28:31], a[68:71], a[176:179]\n"
  "buffer_load_dwordx4 v250, s[56:59], s72 offen lds\n"
  "v_mfma_f32_16x16x32_bf16 a[176:179], a[24:27], a[64:67], a[176:179]\n"
  "v_mfma_f32_16x16x32_bf16 a[192:195], a[32:35], a[64:67], a[192:195]\n"
  "buffer_store_dwordx4 v[172:175], v246, s[60:63], s90 offen offset:768 sc1\n"
  "s_mov_b32 s90, 0x3000\n"
  "v_mfma_f32_16x16x32_bf16 a[192:195], a[36:39], a[68:71], a[192:195]\n"
  "v_cvt_pk_bf16_f32 v136, v32, v33\n"
  "v_cvt_pk_bf16_f32 v137, v34, v35\n"
  "v_mfma_f32_16x16x32_bf16 a[196:199], a[36:39], a[76:79], a[196:199]\n"
  "buffer_load_dwordx4 v251, s[56:59], s73 offen offset:1024 lds\n"
  "v_cvt_pk_bf16_f32 v138, v48, v49\n"
  "v_mfma_f32_16x16x32_bf16 a[196:199], a[32:35], a[72:75], a[196:199]\n"
  "v_cvt_pk_bf16_f32 v139, v50, v51\n"
  "v_cvt_pk_bf16_f32 v148, v36, v37\n"
  "v_mfma_f32_16x16x32_bf16 a[200:203], a[32:35], a[80:83], a[200:203]\n"
  "v_cvt_pk_bf16_f32 v149, v38, v39\n"
  "v_cvt_pk_bf16_f32 v150, v52, v53\n"
  "v_mfma_f32_16x16x32_bf16 a[200:203], a[36:39], a[84:87], a[200:203]\n"
  "v_cvt_pk_bf16_f32 v151, v54, v55\n"
  "v_cvt_pk_bf16_f32 v160, v40, v41\n"
  "v_mfma_f32_16x16x32_bf16 a[204:207], a[36:39], a[92:95], a[204:207]\n"
  "buffer_load_dwordx4 v250, s[56:59], s74 offen offset:2048 lds\n"
  "v_cvt_pk_bf16_f32 v161, v42, v43\n"
  "v_mfma_f32_16x16x32_bf16 a[204:207], a[32:35], a[88:91], a[204:207]\n"
  "v_cvt_pk_bf16_f32 v162, v56, v57\n"
  "v_cvt_pk_bf16_f32 v163, v58, v59\n"
  "v_mfma_f32_16x16x32_bf16 a[220:223], a[40:43], a[88:91], a[220:223]\n"
  "buffer_store_dwordx4 v[136:139], v246, s[60:63], s90 offen sc1\n"
  "v_cvt_pk_bf16_f32 v172, v44, v45\n"
  "v_mfma_f32_16x16x32_bf16 a[220:223], a[44:47], a[92:95], a[220:223]\n"
  "v_cvt_pk_bf16_f32 v173, v46, v47\n"
  "v_cvt_pk_bf16_f32 v174, v60, v61\n"
  "v_mfma_f32_16x16x32_bf16 a[216:219], a[44:47], a[84:87], a[216:219]\n"
  "buffer_load_dwordx4 v251, s[56:59], s75 offen offset:3072 lds\n"
  "v_cvt_pk_bf16_f32 v175, v62, v63\n"
  "v_mfma_f32_16x16x32_bf16 a[216:219], a[40:43], a[80:83], a[216:219]\n"
  "v_mfma_f32_16x16x32_bf16 a[212:215], a[40:43], a[72:75], a[212:215]\n"
  "v_mfma_f32_16x16x32_bf16 a[212:215], a[44:47], a[76:79], a[212:215]\n"
  "s_add_u32 m0, m0, s29\n"
  "v_mfma_f32_16x16x32_bf16 a[208:211], a[44:47], a[68:71], a[208:211]\n"
  "buffer_load_dwordx4 v250, s[56:59], s76 offen lds\n"
  "v_mfma_f32_16x16x32_bf16 a[208:211], a[40:43], a[64:67], a[208:211]\n"
  "v_mfma_f32_16x16x32_bf16 a[224:227], a[48:51], a[64:67], a[224:227]\n"
  "buffer_store_dwordx4 v[148:151], v246, s[60:63], s90 offen offset:256 sc1\n"
  "v_mfma_f32_16x16x32_bf16 a[224:227], a[52:55], a[68:71], a[224:227]\n"
  "v_mfma_f32_16x16x32_bf16 a[228:231], a[52:55], a[76:79], a[228:231]\n"
  "buffer_load_dwordx4 v251, s[56:59], s77 offen offset:1024 lds\n"
  "v_mfma_f32_16x16x32_bf16 a[228:231], a[48:51], a[72:75], a[228:231]\n"
  "v_mfma_f32_16x16x32_bf16 a[232:235], a[48:51], a[80:83], a[232:235]\n"
  "v_mfma_f32_16x16x32_bf16 a[232:235], a[52:55], a[84:87], a[232:235]\n"
  "v_mfma_f32_16x16x32_bf16 a[236:239], a[52:55], a[92:95], a[236:239]\n"
  "buffer_load_dwordx4 v250, s[56:59], s78 offen offset:2048 lds\n"
  "v_mfma_f32_16x16x32_bf16 a[236:239], a[48:51], a[88:91], a[236:239]\n"
  "v_mfma_f32_16x16x32_bf16 a[252:255], a[56:59], a[88:91], a[252:255]\n"
  "buffer_store_dwordx4 v[160:163], v246, s[60:63], s90 offen offset:512 sc1\n"
  "v_mfma_f32_16x16x32_bf16 a[252:255], a[60:63], a[92:95], a[252:255]\n"
  "v_mfma_f32_16x16x32_bf16 a[248:251], a[60:63], a[84:87], a[248:251]\n"
  "buffer_load_dwordx4 v251, s[56:59], s79 offen offset:3072 lds\n"
  "v_mfma_f32_16x16x32_bf16 a[248:251], a[56:59], a[80:83], a[248:251]\n"
  "v_mfma_f32_16x16x32_bf16 a[244:247], a[56:59], a[72:75], a[244:247]\n"
  "v_mfma_f32_16x16x32_bf16 a[244:247], a[60:63], a[76:79], a[244:247]\n"
  "v_mfma_f32_16x16x32_bf16 a[240:243], a[60:63], a[68:71], a[240:243]\n"
  "v_mfma_f32_16x16x32_bf16 a[240:243], a[56:59], a[64:67], a[240:243]\n"
  "s_waitcnt vmcnt(31)\n"
  "s_barrier\n"
  "v_mfma_f32_16x16x32_bf16 v[64:67], a[32:35], a[96:99], v[64:67]\n"
  "buffer_store_dwordx4 v[172:175], v246, s[60:63], s90 offen offset:768 sc1\n"
  "s_mov_b32 s90, 0\n"
  "v_mfma_f32_16x16x32_bf16 v[64:67], a[36:39], a[100:103], v[64:67]\n"
  "ds_read_b128 a[0:3], v228\n"
  "v_accvgpr_read_b32 v128, a128\n"
  "v_mfma_f32_16x16x32_bf16 v[68:71], a[36:39], a[108:111], v[68:71]\n"
  "ds_read_b128 a[4:7], v229\n"
  "v_accvgpr_read_b32 v129, a129\n"
  "v_mfma_f32_16x16x32_bf16 v[68:71], a[32:35], a[104:107], v[68:71]\n"
  "ds_read_b64_tr_b16 a[64:65], v230\n"
  "v_accvgpr_read_b32 v130, a130\n"
  "v_mfma_f32_16x16x32_bf16 v[72:75], a[32:35], a[112:115], v[72:75]\n"
  "ds_read_b64_tr_b16 a[66:67], v230 offset:512\n"
  "v_accvgpr_read_b32 v131, a131\n"
  "v_mfma_f32_16x16x32_bf16 v[72:75], a[36:39], a[116:119], v[72:75]\n"
  "ds_read_b64_tr_b16 a[68:69], v230 offset:4096\n"
  "v_accvgpr_read_b32 v132, a144\n"
  "v_mfma_f32_16x16x32_bf16 v[76:79], a[36:39], a[124:127], v[76:79]\n"
  "ds_read_b64_tr_b16 a[70:71], v230 offset:4608\n"
  "v_accvgpr_read_b32 v133, a145\n"
  "v_mfma_f32_16x16x32_bf16 v[76:79], a[32:35], a[120:123], v[76:79]\n"
  "ds_read_b64_tr_b16 a[72:73], v231\n"
  "v_accvgpr_read_b32 v134, a146\n"
  "v_mfma_f32_16x16x32_bf16 v[92:95], a[40:43], a[120:123], v[92:95]\n"
  "ds_read_b64_tr_b16 a[74:75], v231 offset:512\n"
  "v_accvgpr_read_b32 v135, a147\n"
  "v_mfma_f32_16x16x32_bf16 v[92:95], a[44:47], a[124:127], v[92:95]\n"
  "ds_read_b64_tr_b16 a[76:77], v231 offset:4096\n"
  "v_cvt_pk_bf16_f32 v136, v128, v129\n"
  "v_mfma_f32_16x16x32_bf16 v[88:91], a[44:47], a[116:119], v[88:91]\n"
  "ds_read_b64_tr_b16 a[78:79], v231 offset:4608\n"
  "v_cvt_pk_bf16_f32 v137, v130, v131\n"
  "v_mfma_f32_16x16x32_bf16 v[88:91], a[40:43], a[112:115], v[88:91]\n"
  "ds_read_b64_tr_b16 a[80:81], v232\n"
  "v_cvt_pk_bf16_f32 v138, v132, v133\n"
  "v_mfma_f32_16x16x32_bf16 v[84:87], a[40:43], a[104:107], v[84:87]\n"
  "ds_read_b64_tr_b16 a[82:83], v232 offset:512\n"
  "v_cvt_pk_bf16_f32 v139, v134, v135\n"
  "v_mfma_f32_16x16x32_bf16 v[84:87], a[44:47], a[108:111], v[84:87]\n"
  "buffer_store_dwordx4 v[136:139], v246, s[60:63], s90 offen sc1\n"
  "ds_read_b64_tr_b16 a[84:85], v232 offset:4096\n"
  "v_mfma_f32_16x16x32_bf16 v[80:83], a[44:47], a[100:103], v[80:83]\n"
  "ds_read_b64_tr_b16 a[86:87], v232 offset:4608\n"
  "v_accvgpr_read_b32 v140, a132\n"
  "v_mfma_f32_16x16x32_bf16 v[80:83], a[40:43], a[96:99], v[80:83]\n"
  "ds_read_b64_tr_b16 a[88:89], v233\n"
  "v_accvgpr_read_b32 v141, a133\n"
  "v_mfma_f32_16x16x32_bf16 v[96:99], a[48:51], a[96:99], v[96:99]\n"
  "ds_read_b64_tr_b16 a[90:91], v233 offset:512\n"
  "v_accvgpr_read_b32 v142, a134\n"
  "v_mfma_f32_16x16x32_bf16 v[96:99], a[52:55], a[100:103], v[96:99]\n"
  "ds_read_b64_tr_b16 a[92:93], v233 offset:4096\n"
  "v_accvgpr_read_b32 v143, a135\n"
  "v_mfma_f32_16x16x32_bf16 v[100:103], a[52:55], a[108:111], v[100:103]\n"
  "ds_read_b64_tr_b16 a[94:95], v233 offset:4608\n"
  "v_accvgpr_read_b32 v144, a148\n"
  "v_mfma_f32_16x16x32_bf16 v[100:103], a[48:51], a[104:107], v[100:103]\n"
  "ds_read_b128 a[8:11], v228 offset:2048\n"
  "v_accvgpr_read_b32 v145, a149\n"
  "v_mfma_f32_16x16x32_bf16 v[104:107], a[48:51], a[112:115], v[104:107]\n"
  "ds_read_b128 a[12:15], v229 offset:2048\n"
  "v_accvgpr_read_b32 v146, a150\n"
  "v_mfma_f32_16x16x32_bf16 v[104:107], a[52:55], a[116:119], v[104:107]\n"
  "ds_read_b128 a[16:19], v228 offset:4096\n"
  "v_accvgpr_read_b32 v147, a151\n"
  "v_mfma_f32_16x16x32_bf16 v[108:111], a[52:55], a[124:127], v[108:111]\n"
  "ds_read_b128 a[20:23], v229 offset:4096\n"
  "v_cvt_pk_bf16_f32 v148, v140, v141\n"
  "v_mfma_f32_16x16x32_bf16 v[108:111], a[48:51], a[120:123], v[108:111]\n"
  "ds_read_b128 a[24:27], v228 offset:6144\n"
  "v_cvt_pk_bf16_f32 v149, v142, v143\n"
  "v_mfma_f32_16x16x32_bf16 v[124:127], a[56:59], a[120:123], v[124:127]\n"
  "ds_read_b128 a[28:31], v229 offset:6144\n"
  "v_cvt_pk_bf16_f32 v150, v144, v145\n"
  "v_mfma_f32_16x16x32_bf16 v[124:127], a[60:63], a[124:127], v[124:127]\n"
  "v_cvt_pk_bf16_f32 v151, v146, v147\n"
  "v_mfma_f32_16x16x32_bf16 v[120:123], a[60:63], a[116:119], v[120:123]\n"
  "buffer_store_dwordx4 v[148:151], v246, s[60:63], s90 offen offset:256 sc1\n"
  "v_mfma_f32_16x16x32_bf16 v[120:123], a[56:59], a[112:115], v[120:123]\n"
  "v_mfma_f32_16x16x32_bf16 v[116:119], a[56:59], a[104:107], v[116:119]\n"
  "v_mfma_f32_16x16x32_bf16 v[116:119], a[60:63], a[108:111], v[116:119]\n"
  "v_mfma_f32_16x16x32_bf16 v[112:115], a[60:63], a[100:103], v[112:115]\n"
  "v_mfma_f32_16x16x32_bf16 v[112:115], a[56:59], a[96:99], v[112:115]\n"
  "s_waitcnt lgkmcnt(0)\n"
  "s_cmp_lt_u32 s44, 4\n"
  "s_cbranch_scc1 L_al_gs%=\n"
  "s_add_u32 s52, s52, s81\n"
  "s_addc_u32 s53, s53, 0\n"
  "s_add_u32 s56, s56, s80\n"
  "s_addc_u32 s57, s57, 0\n"
  "s_add_u32 s56, s56, s80\n"
  "s_addc_u32 s57, s57, 0\n"
  "s_add_u32 m0, s84, 0x8000\n"
  "s_nop 0\n"
  "buffer_load_dwordx4 v250, s[56:59], s72 offen lds\n"
  "buffer_load_dwordx4 v251, s[56:59], s73 offen offset:1024 lds\n"
  "buffer_load_dwordx4 v250, s[56:59], s74 offen offset:2048 lds\n"
  "buffer_load_dwordx4 v251, s[56:59], s75 offen offset:3072 lds\n"
  "s_add_u32 m0, s83, 0x10000\n"
  "s_nop 0\n"
  "buffer_load_dwordx4 v252, s[52:55], s64 offen sc1 lds\n"
  "buffer_load_dwordx4 v253, s[52:55], s65 offen offset:1024 sc1 lds\n"
  "buffer_load_dwordx4 v252, s[52:55], s66 offen offset:2048 sc1 lds\n"
  "buffer_load_dwordx4 v253, s[52:55], s67 offen offset:3072 sc1 lds\n"
  "L_al_gs%=:\n"
  "s_mov_b32 s86, s43\n"
  "v_accvgpr_read_b32 v152, a136\n"
  "v_accvgpr_read_b32 v153, a137\n"
  "v_accvgpr_read_b32 v154, a138\n"
  "v_accvgpr_read_b32 v155, a139\n"
  "v_accvgpr_read_b32 v156, a152\n"
  "v_accvgpr_read_b32 v157, a153\n"
  "v_accvgpr_read_b32 v158, a154\n"
  "v_accvgpr_read_b32 v159, a155\n"
  "v_cvt_pk_bf16_f32 v160, v152, v153\n"
  "v_cvt_pk_bf16_f32 v161, v154, v155\n"
  "v_cvt_pk_bf16_f32 v162, v156, v157\n"
  "v_cvt_pk_bf16_f32 v163, v158, v159\n"
  "buffer_store_dwordx4 v[160:163], v246, s[60:63], s90 offen offset:512 sc1\n"
  "v_accvgpr_read_b32 v164, a140\n"
  "v_accvgpr_read_b32 v165, a141\n"
  "v_accvgpr_read_b32 v166, a142\n"
  "v_accvgpr_read_b32 v167, a143\n"
  "v_accvgpr_read_b32 v168, a156\n"
  "v_accvgpr_read_b32 v169, a157\n"
  "v_accvgpr_read_b32 v170, a158\n"
  "v_accvgpr_read_b32 v171, a159\n"
  "v_cvt_pk_bf16_f32 v172, v164, v165\n"
  "v_cvt_pk_bf16_f32 v173, v166, v167\n"
  "v_cvt_pk_bf16_f32 v174, v168, v169\n"
  "v_cvt_pk_bf16_f32 v175, v170, v171\n"
  "buffer_store_dwordx4 v[172:175], v246, s[60:63], s90 offen offset:768 sc1\n"
  "s_mov_b32 s90, 0x2000\n"
  "v_accvgpr_read_b32 v128, a160\n"
  "v_accvgpr_read_b32 v129, a161\n"
  "v_accvgpr_read_b32 v130, a162\n"
  "v_accvgpr_read_b32 v131, a163\n"
  "v_accvgpr_read_b32 v132, a176\n"
  "v_accvgpr_read_b32 v133, a177\n"
  "v_accvgpr_read_b32 v134, a178\n"
  "v_accvgpr_read_b32 v135, a179\n"
  "v_cvt_pk_bf16_f32 v136, v128, v129\n"
  "v_cvt_pk_bf16_f32 v137, v130, v131\n"
  "v_cvt_pk_bf16_f32 v138, v132, v133\n"
  "v_cvt_pk_bf16_f32 v139, v134, v135\n"
  "buffer_store_dwordx4 v[136:139], v246, s[60:63], s90 offen sc1\n"
  "v_accvgpr_read_b32 v140, a164\n"
  "v_accvgpr_read_b32 v141, a165\n"
  "v_accvgpr_read_b32 v142, a166\n"
  "v_accvgpr_read_b32 v143, a167\n"
  "v_accvgpr_read_b32 v144, a180\n"
  "v_accvgpr_read_b32 v145, a181\n"
  "v_accvgpr_read_b32 v146, a182\n"
  "v_accvgpr_read_b32 v147, a183\n"
  "v_cvt_pk_bf16_f32 v148, v140, v141\n"
  "v_cvt_pk_bf16_f32 v149, v142, v143\n"
  "v_cvt_pk_bf16_f32 v150, v144, v145\n"
  "v_cvt_pk_bf16_f32 v151, v146, v147\n"
  "buffer_store_dwordx4 v[148:151], v246, s[60:63], s90 offen offset:256 sc1\n"
  "v_accvgpr_read_b32 v152, a168\n"
  "v_accvgpr_read_b32 v153, a169\n"
  "v_accvgpr_read_b32 v154, a170\n"
  "v_accvgpr_read_b32 v155, a171\n"
  "v_accvgpr_read_b32 v156, a184\n"
  "v_accvgpr_read_b32 v157, a185\n"
  "v_accvgpr_read_b32 v158, a186\n"
  "v_accvgpr_read_b32 v159, a187\n"
  "v_cvt_pk_bf16_f32 v160, v152, v153\n"
  "v_cvt_pk_bf16_f32 v161, v154, v155\n"
  "v_cvt_pk_bf16_f32 v162, v156, v157\n"
  "v_cvt_pk_bf16_f32 v163, v158, v159\n"
  "buffer_store_dwordx4 v[160:163], v246, s[60:63], s90 offen offset:512 sc1\n"
  "v_accvgpr_read_b32 v164, a172\n"
  "v_accvgpr_read_b32 v165, a173\n"
  "v_accvgpr_read_b32 v166, a174\n"
  "v_accvgpr_read_b32 v167, a175\n"
  "v_accvgpr_read_b32 v168, a188\n"
  "v_accvgpr_read_b32 v169, a189\n"
  "v_accvgpr_read_b32 v170, a190\n"
  "v_accvgpr_read_b32 v171, a191\n"
  "v_cvt_pk_bf16_f32 v172, v164, v165\n"
  "v_cvt_pk_bf16_f32 v173, v166, v167\n"
  "v_cvt_pk_bf16_f32 v174, v168, v169\n"
  "v_cvt_pk_bf16_f32 v175, v170, v171\n"
  "buffer_store_dwordx4 v[172:175], v246, s[60:63], s90 offen offset:768 sc1\n"
  "s_mov_b32 s90, 0x8000\n"
  "v_accvgpr_read_b32 v128, a192\n"
  "v_accvgpr_read_b32 v129, a193\n"
  "v_accvgpr_read_b32 v130, a194\n"
  "v_accvgpr_read_b32 v131, a195\n"
  "v_accvgpr_read_b32 v132, a208\n"
  "v_accvgpr_read_b32 v133, a209\n"
  "v_accvgpr_read_b32 v134, a210\n"
  "v_accvgpr_read_b32 v135, a211\n"
  "v_cvt_pk_bf16_f32 v136, v128, v129\n"
  "v_cvt_pk_bf16_f32 v137, v130, v131\n"
  "v_cvt_pk_bf16_f32 v138, v132, v133\n"
  "v_cvt_pk_bf16_f32 v139, v134, v135\n"
  "buffer_store_dwordx4 v[136:139], v246, s[60:63], s90 offen sc1\n"
  "v_accvgpr_read_b32 v140, a196\n"
  "v_accvgpr_read_b32 v141, a197\n"
  "v_accvgpr_read_b32 v142, a198\n"
  "v_accvgpr_read_b32 v143, a199\n"
  "v_accvgpr_read_b32 v144, a212\n"
  "v_accvgpr_read_b32 v145, a213\n"
  "v_accvgpr_read_b32 v146, a214\n"
  "v_accvgpr_read_b32 v147, a215\n"
  "v_cvt_pk_bf16_f32 v148, v140, v141\n"
  "v_cvt_pk_bf16_f32 v149, v142, v143\n"
  "v_cvt_pk_bf16_f32 v150, v144, v145\n"
  "v_cvt_pk_bf16_f32 v151, v146, v147\n"
  "buffer_store_dwordx4 v[148:151], v246, s[60:63], s90 offen offset:256 sc1\n"
  "v_accvgpr_read_b32 v152, a200\n"
  "v_accvgpr_read_b32 v153, a201\n"
  "v_accvgpr_read_b32 v154, a202\n"
  "v_accvgpr_read_b32 v155, a203\n"
  "v_accvgpr_read_b32 v156, a216\n"
  "v_accvgpr_read_b32 v157, a217\n"
  "v_accvgpr_read_b32 v158, a218\n"
  "v_accvgpr_read_b32 v159, a219\n"
  "v_cvt_pk_bf16_f32 v160, v152, v153\n"
  "v_cvt_pk_bf16_f32 v161, v154, v155\n"
  "v_cvt_pk_bf16_f32 v162, v156, v157\n"
  "v_cvt_pk_bf16_f32 v163, v158, v159\n"
  "buffer_store_dwordx4 v[160:163], v246, s[60:63], s90 offen offset:512 sc1\n"
  "v_accvgpr_read_b32 v164, a204\n"
  "v_accvgpr_read_b32 v165, a205\n"
  "v_accvgpr_read_b32 v166, a206\n"
  "v_accvgpr_read_b32 v167, a207\n"
  "v_accvgpr_read_b32 v168, a220\n"
  "v_accvgpr_read_b32 v169, a221\n"
  "v_accvgpr_read_b32 v170, a222\n"
  "v_accvgpr_read_b32 v171, a223\n"
  "v_cvt_pk_bf16_f32 v172, v164, v165\n"
  "v_cvt_pk_bf16_f32 v173, v166, v167\n"
  "v_cvt_pk_bf16_f32 v174, v168, v169\n"
  "v_cvt_pk_bf16_f32 v175, v170, v171\n"
  "buffer_store_dwordx4 v[172:175], v246, s[60:63], s90 offen offset:768 sc1\n"
  "s_mov_b32 s90, 0xa000\n"
  "v_accvgpr_read_b32 v128, a224\n"
  "v_accvgpr_read_b32 v129, a225\n"
  "v_accvgpr_read_b32 v130, a226\n"
  "v_accvgpr_read_b32 v131, a227\n"
  "v_accvgpr_read_b32 v132, a240\n"
  "v_accvgpr_read_b32 v133, a241\n"
  "v_accvgpr_read_b32 v134, a242\n"
  "v_accvgpr_read_b32 v135, a243\n"
  "v_cvt_pk_bf16_f32 v136, v128, v129\n"
  "v_cvt_pk_bf16_f32 v137, v130, v131\n"
  "v_cvt_pk_bf16_f32 v138, v132, v133\n"
  "v_cvt_pk_bf16_f32 v139, v134, v135\n"
  "buffer_store_dwordx4 v[136:139], v246, s[60:63], s90 offen sc1\n"
  "v_accvgpr_read_b32 v140, a228\n"
  "v_accvgpr_read_b32 v141, a229\n"
  "v_accvgpr_read_b32 v142, a230\n"
  "v_accvgpr_read_b32 v143, a231\n"
  "v_accvgpr_read_b32 v144, a244\n"
  "v_accvgpr_read_b32 v145, a245\n"
  "v_accvgpr_read_b32 v146, a246\n"
  "v_accvgpr_read_b32 v147, a247\n"
  "v_cvt_pk_bf16_f32 v148, v140, v141\n"
  "v_cvt_pk_bf16_f32 v149, v142, v143\n"
  "v_cvt_pk_bf16_f32 v150, v144, v145\n"
  "v_cvt_pk_bf16_f32 v151, v146, v147\n"
  "buffer_store_dwordx4 v[148:151], v246, s[60:63], s90 offen offset:256 sc1\n"
  "v_accvgpr_read_b32 v152, a232\n"
  "v_accvgpr_read_b32 v153, a233\n"
  "v_accvgpr_read_b32 v154, a234\n"
  "v_accvgpr_read_b32 v155, a235\n"
  "v_accvgpr_read_b32 v156, a248\n"
  "v_accvgpr_read_b32 v157, a249\n"
  "v_accvgpr_read_b32 v158, a250\n"
  "v_accvgpr_read_b32 v159, a251\n"
  "v_cvt_pk_bf16_f32 v160, v152, v153\n"
  "v_cvt_pk_bf16_f32 v161, v154, v155\n"
  "v_cvt_pk_bf16_f32 v162, v156, v157\n"
  "v_cvt_pk_bf16_f32 v163, v158, v159\n"
  "buffer_store_dwordx4 v[160:163], v246, s[60:63], s90 offen offset:512 sc1\n"
  "v_accvgpr_read_b32 v164, a236\n"
  "v_accvgpr_read_b32 v165, a237\n"
  "v_accvgpr_read_b32 v166, a238\n"
  "v_accvgpr_read_b32 v167, a239\n"
  "v_accvgpr_read_b32 v168, a252\n"
  "v_accvgpr_read_b32 v169, a253\n"
  "v_accvgpr_read_b32 v170, a254\n"
  "v_accvgpr_read_b32 v171, a255\n"
  "v_cvt_pk_bf16_f32 v172, v164, v165\n"
  "v_cvt_pk_bf16_f32 v173, v166, v167\n"
  "v_cvt_pk_bf16_f32 v174, v168, v169\n"
  "v_cvt_pk_bf16_f32 v175, v170, v171\n"
  "buffer_store_dwordx4 v[172:175], v246, s[60:63], s90 offen offset:768 sc1\n"
  "s_mov_b32 s90, 0x9000\n"
  "v_cvt_pk_bf16_f32 v136, v64, v65\n"
  "v_cvt_pk_bf16_f32 v137, v66, v67\n"
  "v_cvt_pk_bf16_f32 v138, v80, v81\n"
  "v_cvt_pk_bf16_f32 v139, v82, v83\n"
  "buffer_store_dwordx4 v[136:139], v246, s[60:63], s90 offen sc1\n"
  "v_cvt_pk_bf16_f32 v148, v68, v69\n"
  "v_cvt_pk_bf16_f32 v149, v70, v71\n"
  "v_cvt_pk_bf16_f32 v150, v84, v85\n"
  "v_cvt_pk_bf16_f32 v151, v86, v87\n"
  "buffer_store_dwordx4 v[148:151], v246, s[60:63], s90 offen offset:256 sc1\n"
  "v_cvt_pk_bf16_f32 v160, v72, v73\n"
  "v_cvt_pk_bf16_f32 v161, v74, v75\n"
  "v_cvt_pk_bf16_f32 v162, v88, v89\n"
  "v_cvt_pk_bf16_f32 v163, v90, v91\n"
  "buffer_store_dwordx4 v[160:163], v246, s[60:63], s90 offen offset:512 sc1\n"
  "v_cvt_pk_bf16_f32 v172, v76, v77\n"
  "v_cvt_pk_bf16_f32 v173, v78, v79\n"
  "v_cvt_pk_bf16_f32 v174, v92, v93\n"
  "v_cvt_pk_bf16_f32 v175, v94, v95\n"
  "buffer_store_dwordx4 v[172:175], v246, s[60:63], s90 offen offset:768 sc1\n"
  "s_mov_b32 s90, 0xb000\n"
  "v_cvt_pk_bf16_f32 v136, v96, v97\n"
  "v_cvt_pk_bf16_f32 v137, v98, v99\n"
  "v_cvt_pk_bf16_f32 v138, v112, v113\n"
  "v_cvt_pk_bf16_f32 v139, v114, v115\n"
  "buffer_store_dwordx4 v[136:139], v246, s[60:63], s90 offen sc1\n"
  "v_cvt_pk_bf16_f32 v148, v100, v101\n"
  "v_cvt_pk_bf16_f32 v149, v102, v103\n"
  "v_cvt_pk_bf16_f32 v150, v116, v117\n"
  "v_cvt_pk_bf16_f32 v151, v118, v119\n"
  "buffer_store_dwordx4 v[148:151], v246, s[60:63], s90 offen offset:256 sc1\n"
  "v_cvt_pk_bf16_f32 v160, v104, v105\n"
  "v_cvt_pk_bf16_f32 v161, v106, v107\n"
  "v_cvt_pk_bf16_f32 v162, v120, v121\n"
  "v_cvt_pk_bf16_f32 v163, v122, v123\n"
  "buffer_store_dwordx4 v[160:163], v246, s[60:63], s90 offen offset:512 sc1\n"
  "v_cvt_pk_bf16_f32 v172, v108, v109\n"
  "v_cvt_pk_bf16_f32 v173, v110, v111\n"
  "v_cvt_pk_bf16_f32 v174, v124, v125\n"
  "v_cvt_pk_bf16_f32 v175, v126, v127\n"
  "buffer_store_dwordx4 v[172:175], v246, s[60:63], s90 offen offset:768 sc1\n"
  "s_waitcnt vmcnt(22)\n"
  "s_branch L_next%=\n"
  "L_u_at%=:\n"
  "s_lshl_b32 s87, s34, 12\n"
  "s_mov_b32 s90, 0\n"
  "v_and_b32 v128, 15, v255\n"
  "v_lshrrev_b32 v129, 4, v255\n"
  "v_bfe_u32 v130, v255, 1, 3\n"
  "v_xor_b32 v130, v130, v129\n"
  "v_lshlrev_b32 v130, 4, v130\n"
  "v_lshl_add_u32 v130, v128, 7, v130\n"
  "v_add_u32 v130, s87, v130\n"
  "v_bfe_u32 v131, v255, 2, 2\n"
  "v_and_b32 v132, 3, v255\n"
  "v_lshlrev_b32 v132, 3, v132\n"
  "v_lshl_add_u32 v132, v131, 7, v132\n"
  "v_lshl_add_u32 v132, v129, 10, v132\n"
  "v_bfe_u32 v133, v255, 3, 2\n"
  "v_lshl_add_u32 v132, v133, 5, v132\n"
  "v_add_u32 v132, s90, v132\n"
  "v_add_u32 v228, 0, v130\n"
  "v_xor_b32 v229, 64, v228\n"
  "v_add_u32 v230, 0x18000, v132\n"
  "v_xor_b32 v231, 32, v230\n"
  "v_xor_b32 v232, 64, v230\n"
  "v_xor_b32 v233, 0x60, v230\n"
  "v_add_u32 v234, 0x8000, v130\n"
  "v_xor_b32 v235, 64, v234\n"
  "v_add_u32 v236, 0x1c000, v132\n"
  "v_xor_b32 v237, 32, v236\n"
  "v_xor_b32 v238, 64, v236\n"
  "v_xor_b32 v239, 0x60, v236\n"
  "v_add_u32 v240, 0x10000, v130\n"
  "v_xor_b32 v241, 64, v240\n"
  "v_add_u32 v242, 0x20000, v132\n"
  "v_xor_b32 v243, 32, v242\n"
  "v_xor_b32 v244, 64, v242\n"
  "v_xor_b32 v245, 0x60, v242\n"
  "s_mov_b32 s82, 5\n"
  "s_mov_b32 s94, 38\n"
  "s_cmp_eq_u32 s101, 2\n"
  "s_cbranch_scc1 L_ew_q%=\n"
  "s_cmp_eq_u32 s101, 3\n"
  "s_cbranch_scc1 L_ewb_q%=\n"
  "s_waitcnt vmcnt(20)\n"
  "L_ewb_q%=:\n"
  "s_barrier\n"
  "L_ew_q%=:\n"
  "ds_read_b128 v[0:3], v228\n"
  "ds_read_b128 v[4:7], v229\n"
  "ds_read_b128 v[8:11], v228 offset:2048\n"
  "ds_read_b128 v[12:15], v229 offset:2048\n"
  "ds_read_b128 v[16:19], v228 offset:16384\n"
  "ds_read_b128 v[20:23], v229 offset:16384\n"
  "ds_read_b128 v[24:27], v228 offset:18432\n"
  "ds_read_b128 v[28:31], v229 offset:18432\n"
  "ds_read_b64_tr_b16 a[144:145], v230\n"
  "ds_read_b64_tr_b16 a[146:147], v230 offset:512\n"
  "ds_read_b64_tr_b16 a[148:149], v230 offset:4096\n"
  "ds_read_b64_tr_b16 a[150:151], v230 offset:4608\n"
  "ds_read_b64_tr_b16 a[152:153], v231\n"
  "ds_read_b64_tr_b16 a[154:155], v231 offset:512\n"
  "ds_read_b64_tr_b16 a[156:157], v231 offset:4096\n"
  "ds_read_b64_tr_b16 a[158:159], v231 offset:4608\n"
  "ds_read_b64_tr_b16 a[160:161], v232\n"
  "ds_read_b64_tr_b16 a[162:163], v232 offset:512\n"
  "ds_read_b64_tr_b16 a[164:165], v232 offset:4096\n"
  "ds_read_b64_tr_b16 a[166:167], v232 offset:4608\n"
  "ds_read_b64_tr_b16 a[168:169], v233\n"
  "ds_read_b64_tr_b16 a[170:171], v233 offset:512\n"
  "ds_read_b64_tr_b16 a[172:173], v233 offset:4096\n"
  "ds_read_b64_tr_b16 a[174:175], v233 offset:4608\n"
  "s_waitcnt lgkmcnt(0)\n"
  "v_mfma_f32_16x16x32_bf16 v[64:67], a[144:147], v[0:3], 0\n"
  "ds_read_b64_tr_b16 a[176:177], v230 offset:8192\n"
  "s_add_u32 m0, s83, 0x11000\n"
  "v_mfma_f32_16x16x32_bf16 v[64:67], a[148:151], v[4:7], v[64:67]\n"
  "buffer_load_dwordx4 v252, s[52:55], s68 offen sc1 lds\n"
  "ds_read_b64_tr_b16 a[178:179], v230 offset:8704\n"
  "v_mfma_f32_16x16x32_bf16 v[96:99], a[148:151], v[12:15], 0\n"
  "ds_read_b64_tr_b16 a[180:181], v230 offset:12288\n"
  "v_accvgpr_write_b32 a0, 0\n"
  "v_mfma_f32_16x16x32_bf16 v[96:99], a[144:147], v[8:11], v[96:99]\n"
  "ds_read_b64_tr_b16 a[182:183], v230 offset:12800\n"
  "v_accvgpr_write_b32 a1, 0\n"
  "v_mfma_f32_16x16x32_bf16 v[128:131], a[144:147], v[16:19], 0\n"
  "ds_read_b64_tr_b16 a[184:185], v231 offset:8192\n"
  "v_accvgpr_write_b32 a2, 0\n"
  "v_mfma_f32_16x16x32_bf16 v[128:131], a[148:151], v[20:23], v[128:131]\n"
  "ds_read_b64_tr_b16 a[186:187], v231 offset:8704\n"
  "v_accvgpr_write_b32 a3, 0\n"
  "v_mfma_f32_16x16x32_bf16 v[160:163], a[148:151], v[28:31], 0\n"
  "buffer_load_dwordx4 v253, s[52:55], s69 offen offset:1024 sc1 lds\n"
  "ds_read_b64_tr_b16 a[188:189], v231 offset:12288\n"
  "v_mfma_f32_16x16x32_bf16 v[160:163], a[144:147], v[24:27], v[160:163]\n"
  "ds_read_b64_tr_b16 a[190:191], v231 offset:12800\n"
  "v_accvgpr_write_b32 a4, 0\n"
  "v_mfma_f32_16x16x32_bf16 v[164:167], a[152:155], v[24:27], 0\n"
  "ds_read_b64_tr_b16 a[192:193], v232 offset:8192\n"
  "v_accvgpr_write_b32 a5, 0\n"
  "v_mfma_f32_16x16x32_bf16 v[164:167], a[156:159], v[28:31], v[164:167]\n"
  "ds_read_b64_tr_b16 a[194:195], v232 offset:8704\n"
  "v_accvgpr_write_b32 a6, 0\n"
  "v_mfma_f32_16x16x32_bf16 v[132:135], a[156:159], v[20:23], 0\n"
  "ds_read_b64_tr_b16 a[196:197], v232 offset:12288\n"
  "v_accvgpr_write_b32 a7, 0\n"
  "v_mfma_f32_16x16x32_bf16 v[132:135], a[152:155], v[16:19], v[132:135]\n"
  "buffer_load_dwordx4 v252, s[52:55], s70 offen offset:2048 sc1 lds\n"
  "ds_read_b64_tr_b16 a[198:199], v232 offset:12800\n"
  "v_mfma_f32_16x16x32_bf16 v[100:103], a[152:155], v[8:11], 0\n"
  "ds_read_b64_tr_b16 a[200:201], v233 offset:8192\n"
  "v_accvgpr_write_b32 a8, 0\n"
  "v_mfma_f32_16x16x32_bf16 v[100:103], a[156:159], v[12:15], v[100:103]\n"
  "ds_read_b64_tr_b16 a[202:203], v233 offset:8704\n"
  "v_accvgpr_write_b32 a9, 0\n"
  "v_mfma_f32_16x16x32_bf16 v[68:71], a[156:159], v[4:7], 0\n"
  "ds_read_b64_tr_b16 a[204:205], v233 offset:12288\n"
  "v_accvgpr_write_b32 a10, 0\n"
  "v_mfma_f32_16x16x32_bf16 v[68:71], a[152:155], v[0:3], v[68:71]\n"
  "ds_read_b64_tr_b16 a[206:207], v233 offset:12800\n"
  "v_accvgpr_write_b32 a11, 0\n"
  "v_mfma_f32_16x16x32_bf16 v[72:75], a[160:163], v[0:3], 0\n"
  "buffer_load_dwordx4 v253, s[52:55], s71 offen offset:3072 sc1 lds\n"
  "v_mfma_f32_16x16x32_bf16 v[72:75], a[164:167], v[4:7], v[72:75]\n"
  "v_accvgpr_write_b32 a12, 0\n"
  "v_accvgpr_write_b32 a47, 0\n"
  "v_mfma_f32_16x16x32_bf16 v[104:107], a[164:167], v[12:15], 0\n"
  "s_add_u32 s52, s52, s81\n"
  "s_addc_u32 s53, s53, 0\n"
  "v_mfma_f32_16x16x32_bf16 v[104:107], a[160:163], v[8:11], v[104:107]\n"
  "s_add_u32 s56, s56, s80\n"
  "s_addc_u32 s57, s57, 0\n"
  "v_mfma_f32_16x16x32_bf16 v[136:139], a[160:163], v[16:19], 0\n"
  "v_accvgpr_write_b32 a13, 0\n"
  "v_mfma_f32_16x16x32_bf16 v[136:139], a[164:167], v[20:23], v[136:139]\n"
  "v_accvgpr_write_b32 a14, 0\n"
  "v_mfma_f32_16x16x32_bf16 v[168:171], a[164:167], v[28:31], 0\n"
  "s_cmp_eq_u32 s101, 3\n"
  "s_cbranch_scc1 L_pw_qp%=\n"
  "s_waitcnt vmcnt(12)\n"
  "L_pw_qp%=:\n"
  "s_waitcnt lgkmcnt(0)\n"
  "s_barrier\n"
  "v_mfma_f32_16x16x32_bf16 v[168:171], a[160:163], v[24:27], v[168:171]\n"
  "s_add_u32 m0, s84, 0\n"
  "v_accvgpr_write_b32 a15, 0\n"
  "v_mfma_f32_16x16x32_bf16 v[172:175], a[168:171], v[24:27], 0\n"
  "buffer_load_dwordx4 v250, s[56:59], s72 offen lds\n"
  "v_mfma_f32_16x16x32_bf16 v[172:175], a[172:175], v[28:31], v[172:175]\n"
  "ds_read_b128 v[32:35], v234\n"
  "v_accvgpr_write_b32 a16, 0\n"
  "v_mfma_f32_16x16x32_bf16 v[140:143], a[172:175], v[20:23], 0\n"
  "ds_read_b128 v[36:39], v235\n"
  "v_accvgpr_write_b32 a17, 0\n"
  "v_mfma_f32_16x16x32_bf16 v[140:143], a[168:171], v[16:19], v[140:143]\n"
  "ds_read_b128 v[40:43], v234 offset:2048\n"
  "v_accvgpr_write_b32 a18, 0\n"
  "v_mfma_f32_16x16x32_bf16 v[108:111], a[168:171], v[8:11], 0\n"
  "ds_read_b128 v[44:47], v235 offset:2048\n"
  "v_accvgpr_write_b32 a19, 0\n"
  "v_mfma_f32_16x16x32_bf16 v[108:111], a[172:175], v[12:15], v[108:111]\n"
  "buffer_load_dwordx4 v251, s[56:59], s73 offen offset:1024 lds\n"
  "v_mfma_f32_16x16x32_bf16 v[76:79], a[172:175], v[4:7], 0\n"
  "ds_read_b128 v[48:51], v234 offset:16384\n"
  "v_accvgpr_write_b32 a20, 0\n"
  "v_mfma_f32_16x16x32_bf16 v[76:79], a[168:171], v[0:3], v[76:79]\n"
  "ds_read_b128 v[52:55], v235 offset:16384\n"
  "v_accvgpr_write_b32 a21, 0\n"
  "v_mfma_f32_16x16x32_bf16 v[80:83], a[176:179], v[0:3], 0\n"
  "ds_read_b128 v[56:59], v234 offset:18432\n"
  "v_accvgpr_write_b32 a22, 0\n"
  "v_mfma_f32_16x16x32_bf16 v[80:83], a[180:183], v[4:7], v[80:83]\n"
  "ds_read_b128 v[60:63], v235 offset:18432\n"
  "v_accvgpr_write_b32 a23, 0\n"
  "v_mfma_f32_16x16x32_bf16 v[112:115], a[180:183], v[12:15], 0\n"
  "buffer_load_dwordx4 v250, s[56:59], s74 offen offset:2048 lds\n"
  "v_mfma_f32_16x16x32_bf16 v[112:115], a[176:179], v[8:11], v[112:115]\n"
  "ds_read_b64_tr_b16 a[144:145], v236\n"
  "v_accvgpr_write_b32 a24, 0\n"
  "v_mfma_f32_16x16x32_bf16 v[144:147], a[176:179], v[16:19], 0\n"
  "ds_read_b64_tr_b16 a[146:147], v236 offset:512\n"
  "v_accvgpr_write_b32 a25, 0\n"
  "v_mfma_f32_16x16x32_bf16 v[144:147], a[180:183], v[20:23], v[144:147]\n"
  "ds_read_b64_tr_b16 a[148:149], v236 offset:4096\n"
  "v_accvgpr_write_b32 a26, 0\n"
  "v_mfma_f32_16x16x32_bf16 v[176:179], a[180:183], v[28:31], 0\n"
  "ds_read_b64_tr_b16 a[150:151], v236 offset:4608\n"
  "v_accvgpr_write_b32 a27, 0\n"
  "v_mfma_f32_16x16x32_bf16 v[176:179], a[176:179], v[24:27], v[176:179]\n"
  "buffer_load_dwordx4 v251, s[56:59], s75 offen offset:3072 lds\n"
  "v_mfma_f32_16x16x32_bf16 v[180:183], a[184:187], v[24:27], 0\n"
  "ds_read_b64_tr_b16 a[152:153], v237\n"
  "v_accvgpr_write_b32 a28, 0\n"
  "v_mfma_f32_16x16x32_bf16 v[180:183], a[188:191], v[28:31], v[180:183]\n"
  "ds_read_b64_tr_b16 a[154:155], v237 offset:512\n"
  "v_accvgpr_write_b32 a29, 0\n"
  "v_mfma_f32_16x16x32_bf16 v[148:151], a[188:191], v[20:23], 0\n"
  "ds_read_b64_tr_b16 a[156:157], v237 offset:4096\n"
  "v_accvgpr_write_b32 a30, 0\n"
  "v_mfma_f32_16x16x32_bf16 v[148:151], a[184:187], v[16:19], v[148:151]\n"
  "ds_read_b64_tr_b16 a[158:159], v237 offset:4608\n"
  "s_add_u32 m0, s83, 0\n"
  "v_mfma_f32_16x16x32_bf16 v[116:119], a[184:187], v[8:11], 0\n"
  "buffer_load_dwordx4 v252, s[52:55], s64 offen sc1 lds\n"
  "v_mfma_f32_16x16x32_bf16 v[116:119], a[188:191], v[12:15], v[116:119]\n"
  "ds_read_b64_tr_b16 a[160:161], v238\n"
  "v_accvgpr_write_b32 a31, 0\n"
  "v_mfma_f32_16x16x32_bf16 v[84:87], a[188:191], v[4:7], 0\n"
  "ds_read_b64_tr_b16 a[162:163], v238 offset:512\n"
  "v_accvgpr_write_b32 a32, 0\n"
  "v_mfma_f32_16x16x32_bf16 v[84:87], a[184:187], v[0:3], v[84:87]\n"
  "ds_read_b64_tr_b16 a[164:165], v238 offset:4096\n"
  "v_accvgpr_write_b32 a33, 0\n"
  "v_mfma_f32_16x16x32_bf16 v[88:91], a[192:195], v[0:3], 0\n"
  "ds_read_b64_tr_b16 a[166:167], v238 offset:4608\n"
  "v_accvgpr_write_b32 a34, 0\n"
  "v_mfma_f32_16x16x32_bf16 v[88:91], a[196:199], v[4:7], v[88:91]\n"
  "buffer_load_dwordx4 v253, s[52:55], s65 offen offset:1024 sc1 lds\n"
  "v_mfma_f32_16x16x32_bf16 v[120:123], a[196:199], v[12:15], 0\n"
  "ds_read_b64_tr_b16 a[168:169], v239\n"
  "v_accvgpr_write_b32 a35, 0\n"
  "v_mfma_f32_16x16x32_bf16 v[120:123], a[192:195], v[8:11], v[120:123]\n"
  "ds_read_b64_tr_b16 a[170:171], v239 offset:512\n"
  "v_accvgpr_write_b32 a36, 0\n"
  "v_mfma_f32_16x16x32_bf16 v[152:155], a[192:195], v[16:19], 0\n"
  "ds_read_b64_tr_b16 a[172:173], v239 offset:4096\n"
  "v_accvgpr_write_b32 a37, 0\n"
  "v_mfma_f32_16x16x32_bf16 v[152:155], a[196:199], v[20:23], v[152:155]\n"
  "ds_read_b64_tr_b16 a[174:175], v239 offset:4608\n"
  "v_accvgpr_write_b32 a38, 0\n"
  "v_mfma_f32_16x16x32_bf16 v[184:187], a[196:199], v[28:31], 0\n"
  "buffer_load_dwordx4 v252, s[52:55], s66 offen offset:2048 sc1 lds\n"
  "v_mfma_f32_16x16x32_bf16 v[184:187], a[192:195], v[24:27], v[184:187]\n"
  "v_accvgpr_write_b32 a39, 0\n"
  "v_mfma_f32_16x16x32_bf16 v[188:191], a[200:203], v[24:27], 0\n"
  "v_accvgpr_write_b32 a40, 0\n"
  "v_mfma_f32_16x16x32_bf16 v[188:191], a[204:207], v[28:31], v[188:191]\n"
  "v_accvgpr_write_b32 a41, 0\n"
  "v_mfma_f32_16x16x32_bf16 v[156:159], a[204:207], v[20:23], 0\n"
  "v_accvgpr_write_b32 a42, 0\n"
  "v_mfma_f32_16x16x32_bf16 v[156:159], a[200:203], v[16:19], v[156:159]\n"
  "buffer_load_dwordx4 v253, s[52:55], s67 offen offset:3072 sc1 lds\n"
  "v_mfma_f32_16x16x32_bf16 v[124:127], a[200:203], v[8:11], 0\n"
  "v_accvgpr_write_b32 a43, 0\n"
  "v_mfma_f32_16x16x32_bf16 v[124:127], a[204:207], v[12:15], v[124:127]\n"
  "v_accvgpr_write_b32 a44, 0\n"
  "v_mfma_f32_16x16x32_bf16 v[92:95], a[204:207], v[4:7], 0\n"
  "v_accvgpr_write_b32 a45, 0\n"
  "v_mfma_f32_16x16x32_bf16 v[92:95], a[200:203], v[0:3], v[92:95]\n"
  "s_waitcnt lgkmcnt(0)\n"
  "v_accvgpr_write_b32 a46, 0\n"
  "s_branch L_q_mid%=\n"
  "L_q_loop%=:\n"
  "v_mfma_f32_16x16x32_bf16 v[64:67], a[144:147], v[0:3], v[64:67]\n"
  "ds_read_b64_tr_b16 a[176:177], v230 offset:8192\n"
  "s_add_u32 m0, s83, 0x11000\n"
  "v_mfma_f32_16x16x32_bf16 v[64:67], a[148:151], v[4:7], v[64:67]\n"
  "buffer_load_dwordx4 v252, s[52:55], s68 offen sc1 lds\n"
  "ds_read_b64_tr_b16 a[178:179], v230 offset:8704\n"
  "v_mfma_f32_16x16x32_bf16 v[96:99], a[148:151], v[12:15], v[96:99]\n"
  "ds_read_b64_tr_b16 a[180:181], v230 offset:12288\n"
  "v_mfma_f32_16x16x32_bf16 v[96:99], a[144:147], v[8:11], v[96:99]\n"
  "ds_read_b64_tr_b16 a[182:183], v230 offset:12800\n"
  "v_mfma_f32_16x16x32_bf16 v[128:131], a[144:147], v[16:19], v[128:131]\n"
  "ds_read_b64_tr_b16 a[184:185], v231 offset:8192\n"
  "v_mfma_f32_16x16x32_bf16 v[128:131], a[148:151], v[20:23], v[128:131]\n"
  "ds_read_b64_tr_b16 a[186:187], v231 offset:8704\n"
  "v_mfma_f32_16x16x32_bf16 v[160:163], a[148:151], v[28:31], v[160:163]\n"
  "buffer_load_dwordx4 v253, s[52:55], s69 offen offset:1024 sc1 lds\n"
  "ds_read_b64_tr_b16 a[188:189], v231 offset:12288\n"
  "v_mfma_f32_16x16x32_bf16 v[160:163], a[144:147], v[24:27], v[160:163]\n"
  "ds_read_b64_tr_b16 a[190:191], v231 offset:12800\n"
  "v_mfma_f32_16x16x32_bf16 v[164:167], a[152:155], v[24:27], v[164:167]\n"
  "ds_read_b64_tr_b16 a[192:193], v232 offset:8192\n"
  "v_mfma_f32_16x16x32_bf16 v[164:167], a[156:159], v[28:31], v[164:167]\n"
  "ds_read_b64_tr_b16 a[194:195], v232 offset:8704\n"
  "v_mfma_f32_16x16x32_bf16 v[132:135], a[156:159], v[20:23], v[132:135]\n"
  "ds_read_b64_tr_b16 a[196:197], v232 offset:12288\n"
  "v_mfma_f32_16x16x32_bf16 v[132:135], a[152:155], v[16:19], v[132:135]\n"
  "buffer_load_dwordx4 v252, s[52:55], s70 offen offset:2048 sc1 lds\n"
  "ds_read_b64_tr_b16 a[198:199], v232 offset:12800\n"
  "v_mfma_f32_16x16x32_bf16 v[100:103], a[152:155], v[8:11], v[100:103]\n"
  "ds_read_b64_tr_b16 a[200:201], v233 offset:8192\n"
  "v_mfma_f32_16x16x32_bf16 v[100:103], a[156:159], v[12:15], v[100:103]\n"
  "ds_read_b64_tr_b16 a[202:203], v233 offset:8704\n"
  "v_mfma_f32_16x16x32_bf16 v[68:71], a[156:159], v[4:7], v[68:71]\n"
  "ds_read_b64_tr_b16 a[204:205], v233 offset:12288\n"
  "v_mfma_f32_16x16x32_bf16 v[68:71], a[152:155], v[0:3], v[68:71]\n"
  "ds_read_b64_tr_b16 a[206:207], v233 offset:12800\n"
  "v_mfma_f32_16x16x32_bf16 v[72:75], a[160:163], v[0:3], v[72:75]\n"
  "buffer_load_dwordx4 v253, s[52:55], s71 offen offset:3072 sc1 lds\n"
  "v_mfma_f32_16x16x32_bf16 v[72:75], a[164:167], v[4:7], v[72:75]\n"
  "v_mfma_f32_16x16x32_bf16 v[104:107], a[164:167], v[12:15], v[104:107]\n"
  "s_add_u32 s52, s52, s81\n"
  "s_addc_u32 s53, s53, 0\n"
  "v_mfma_f32_16x16x32_bf16 v[104:107], a[160:163], v[8:11], v[104:107]\n"
  "s_add_u32 s56, s56, s80\n"
  "s_addc_u32 s57, s57, 0\n"
  "v_mfma_f32_16x16x32_bf16 v[136:139], a[160:163], v[16:19], v[136:139]\n"
  "v_mfma_f32_16x16x32_bf16 v[136:139], a[164:167], v[20:23], v[136:139]\n"
  "v_mfma_f32_16x16x32_bf16 v[168:171], a[164:167], v[28:31], v[168:171]\n"
  "s_waitcnt vmcnt(12) lgkmcnt(0)\n"
  "s_barrier\n"
  "v_mfma_f32_16x16x32_bf16 v[168:171], a[160:163], v[24:27], v[168:171]\n"
  "s_add_u32 m0, s84, 0\n"
  "v_mfma_f32_16x16x32_bf16 v[172:175], a[168:171], v[24:27], v[172:175]\n"
  "buffer_load_dwordx4 v250, s[56:59], s72 offen lds\n"
  "v_mfma_f32_16x16x32_bf16 v[172:175], a[172:175], v[28:31], v[172:175]\n"
  "ds_read_b128 v[32:35], v234\n"
  "v_mfma_f32_16x16x32_bf16 v[140:143], a[172:175], v[20:23], v[140:143]\n"
  "ds_read_b128 v[36:39], v235\n"
  "v_mfma_f32_16x16x32_bf16 v[140:143], a[168:171], v[16:19], v[140:143]\n"
  "ds_read_b128 v[40:43], v234 offset:2048\n"
  "v_mfma_f32_16x16x32_bf16 v[108:111], a[168:171], v[8:11], v[108:111]\n"
  "ds_read_b128 v[44:47], v235 offset:2048\n"
  "v_mfma_f32_16x16x32_bf16 v[108:111], a[172:175], v[12:15], v[108:111]\n"
  "buffer_load_dwordx4 v251, s[56:59], s73 offen offset:1024 lds\n"
  "v_mfma_f32_16x16x32_bf16 v[76:79], a[172:175], v[4:7], v[76:79]\n"
  "ds_read_b128 v[48:51], v234 offset:16384\n"
  "v_mfma_f32_16x16x32_bf16 v[76:79], a[168:171], v[0:3], v[76:79]\n"
  "ds_read_b128 v[52:55], v235 offset:16384\n"
  "v_mfma_f32_16x16x32_bf16 v[80:83], a[176:179], v[0:3], v[80:83]\n"
  "ds_read_b128 v[56:59], v234 offset:18432\n"
  "v_mfma_f32_16x16x32_bf16 v[80:83], a[180:183], v[4:7], v[80:83]\n"
  "ds_read_b128 v[60:63], v235 offset:18432\n"
  "v_mfma_f32_16x16x32_bf16 v[112:115], a[180:183], v[12:15], v[112:115]\n"
  "buffer_load_dwordx4 v250, s[56:59], s74 offen offset:2048 lds\n"
  "v_mfma_f32_16x16x32_bf16 v[112:115], a[176:179], v[8:11], v[112:115]\n"
  "ds_read_b64_tr_b16 a[144:145], v236\n"
  "v_mfma_f32_16x16x32_bf16 v[144:147], a[176:179], v[16:19], v[144:147]\n"
  "ds_read_b64_tr_b16 a[146:147], v236 offset:512\n"
  "v_mfma_f32_16x16x32_bf16 v[144:147], a[180:183], v[20:23], v[144:147]\n"
  "ds_read_b64_tr_b16 a[148:149], v236 offset:4096\n"
  "v_mfma_f32_16x16x32_bf16 v[176:179], a[180:183], v[28:31], v[176:179]\n"
  "ds_read_b64_tr_b16 a[150:151], v236 offset:4608\n"
  "v_mfma_f32_16x16x32_bf16 v[176:179], a[176:179], v[24:27], v[176:179]\n"
  "buffer_load_dwordx4 v251, s[56:59], s75 offen offset:3072 lds\n"
  "v_mfma_f32_16x16x32_bf16 v[180:183], a[184:187], v[24:27], v[180:183]\n"
  "ds_read_b64_tr_b16 a[152:153], v237\n"
  "v_mfma_f32_16x16x32_bf16 v[180:183], a[188:191], v[28:31], v[180:183]\n"
  "ds_read_b64_tr_b16 a[154:155], v237 offset:512\n"
  "v_mfma_f32_16x16x32_bf16 v[148:151], a[188:191], v[20:23], v[148:151]\n"
  "ds_read_b64_tr_b16 a[156:157], v237 offset:4096\n"
  "v_mfma_f32_16x16x32_bf16 v[148:151], a[184:187], v[16:19], v[148:151]\n"
  "ds_read_b64_tr_b16 a[158:159], v237 offset:4608\n"
  "s_add_u32 m0, s83, 0\n"
  "v_mfma_f32_16x16x32_bf16 v[116:119], a[184:187], v[8:11], v[116:119]\n"
  "buffer_load_dwordx4 v252, s[52:55], s64 offen sc1 lds\n"
  "v_mfma_f32_16x16x32_bf16 v[116:119], a[188:191], v[12:15], v[116:119]\n"
  "ds_read_b64_tr_b16 a[160:161], v238\n"
  "v_mfma_f32_16x16x32_bf16 v[84:87], a[188:191], v[4:7], v[84:87]\n"
  "ds_read_b64_tr_b16 a[162:163], v238 offset:512\n"
  "v_mfma_f32_16x16x32_bf16 v[84:87], a[184:187], v[0:3], v[84:87]\n"
  "ds_read_b64_tr_b16 a[164:165], v238 offset:4096\n"
  "v_mfma_f32_16x16x32_bf16 v[88:91], a[192:195], v[0:3], v[88:91]\n"
  "ds_read_b64_tr_b16 a[166:167], v238 offset:4608\n"
  "v_mfma_f32_16x16x32_bf16 v[88:91], a[196:199], v[4:7], v[88:91]\n"
  "buffer_load_dwordx4 v253, s[52:55], s65 offen offset:1024 sc1 lds\n"
  "v_mfma_f32_16x16x32_bf16 v[120:123], a[196:199], v[12:15], v[120:123]\n"
  "ds_read_b64_tr_b16 a[168:169], v239\n"
  "v_mfma_f32_16x16x32_bf16 v[120:123], a[192:195], v[8:11], v[120:123]\n"
  "ds_read_b64_tr_b16 a[170:171], v239 offset:512\n"
  "v_mfma_f32_16x16x32_bf16 v[152:155], a[192:195], v[16:19], v[152:155]\n"
  "ds_read_b64_tr_b16 a[172:173], v239 offset:4096\n"
  "v_mfma_f32_16x16x32_bf16 v[152:155], a[196:199], v[20:23], v[152:155]\n"
  "ds_read_b64_tr_b16 a[174:175], v239 offset:4608\n"
  "v_mfma_f32_16x16x32_bf16 v[184:187], a[196:199], v[28:31], v[184:187]\n"
  "buffer_load_dwordx4 v252, s[52:55], s66 offen offset:2048 sc1 lds\n"
  "v_mfma_f32_16x16x32_bf16 v[184:187], a[192:195], v[24:27], v[184:187]\n"
  "v_mfma_f32_16x16x32_bf16 v[188:191], a[200:203], v[24:27], v[188:191]\n"
  "v_mfma_f32_16x16x32_bf16 v[188:191], a[204:207], v[28:31], v[188:191]\n"
  "v_mfma_f32_16x16x32_bf16 v[156:159], a[204:207], v[20:23], v[156:159]\n"
  "v_mfma_f32_16x16x32_bf16 v[156:159], a[200:203], v[16:19], v[156:159]\n"
  "buffer_load_dwordx4 v253, s[52:55], s67 offen offset:3072 sc1 lds\n"
  "v_mfma_f32_16x16x32_bf16 v[124:127], a[200:203], v[8:11], v[124:127]\n"
  "v_mfma_f32_16x16x32_bf16 v[124:127], a[204:207], v[12:15], v[124:127]\n"
  "v_mfma_f32_16x16x32_bf16 v[92:95], a[204:207], v[4:7], v[92:95]\n"
  "v_mfma_f32_16x16x32_bf16 v[92:95], a[200:203], v[0:3], v[92:95]\n"
  "s_waitcnt lgkmcnt(0)\n"
  "L_q_mid%=:\n"
  "s_cmp_eq_u32 s82, 0\n"
  "s_cbranch_scc1 L_q_done%=\n"
  "v_mfma_f32_16x16x32_bf16 v[64:67], a[144:147], v[32:35], v[64:67]\n"
  "ds_read_b64_tr_b16 a[176:177], v236 offset:8192\n"
  "s_add_u32 m0, s83, 0x1000\n"
  "v_mfma_f32_16x16x32_bf16 v[64:67], a[148:151], v[36:39], v[64:67]\n"
  "buffer_load_dwordx4 v252, s[52:55], s68 offen sc1 lds\n"
  "ds_read_b64_tr_b16 a[178:179], v236 offset:8704\n"
  "v_mfma_f32_16x16x32_bf16 v[96:99], a[148:151], v[44:47], v[96:99]\n"
  "ds_read_b64_tr_b16 a[180:181], v236 offset:12288\n"
  "v_accvgpr_write_b32 a48, 0\n"
  "v_mfma_f32_16x16x32_bf16 v[96:99], a[144:147], v[40:43], v[96:99]\n"
  "ds_read_b64_tr_b16 a[182:183], v236 offset:12800\n"
  "v_accvgpr_write_b32 a49, 0\n"
  "v_mfma_f32_16x16x32_bf16 v[128:131], a[144:147], v[48:51], v[128:131]\n"
  "ds_read_b64_tr_b16 a[184:185], v237 offset:8192\n"
  "v_accvgpr_write_b32 a50, 0\n"
  "v_mfma_f32_16x16x32_bf16 v[128:131], a[148:151], v[52:55], v[128:131]\n"
  "ds_read_b64_tr_b16 a[186:187], v237 offset:8704\n"
  "v_accvgpr_write_b32 a51, 0\n"
  "v_mfma_f32_16x16x32_bf16 v[160:163], a[148:151], v[60:63], v[160:163]\n"
  "buffer_load_dwordx4 v253, s[52:55], s69 offen offset:1024 sc1 lds\n"
  "ds_read_b64_tr_b16 a[188:189], v237 offset:12288\n"
  "v_mfma_f32_16x16x32_bf16 v[160:163], a[144:147], v[56:59], v[160:163]\n"
  "ds_read_b64_tr_b16 a[190:191], v237 offset:12800\n"
  "v_accvgpr_write_b32 a52, 0\n"
  "v_mfma_f32_16x16x32_bf16 v[164:167], a[152:155], v[56:59], v[164:167]\n"
  "ds_read_b64_tr_b16 a[192:193], v238 offset:8192\n"
  "v_accvgpr_write_b32 a53, 0\n"
  "v_mfma_f32_16x16x32_bf16 v[164:167], a[156:159], v[60:63], v[164:167]\n"
  "ds_read_b64_tr_b16 a[194:195], v238 offset:8704\n"
  "v_accvgpr_write_b32 a54, 0\n"
  "v_mfma_f32_16x16x32_bf16 v[132:135], a[156:159], v[52:55], v[132:135]\n"
  "ds_read_b64_tr_b16 a[196:197], v238 offset:12288\n"
  "v_accvgpr_write_b32 a55, 0\n"
  "v_mfma_f32_16x16x32_bf16 v[132:135], a[152:155], v[48:51], v[132:135]\n"
  "buffer_load_dwordx4 v252, s[52:55], s70 offen offset:2048 sc1 lds\n"
  "ds_read_b64_tr_b16 a[198:199], v238 offset:12800\n"
  "v_mfma_f32_16x16x32_bf16 v[100:103], a[152:155], v[40:43], v[100:103]\n"
  "ds_read_b64_tr_b16 a[200:201], v239 offset:8192\n"
  "v_accvgpr_write_b32 a56, 0\n"
  "v_mfma_f32_16x16x32_bf16 v[100:103], a[156:159], v[44:47], v[100:103]\n"
  "ds_read_b64_tr_b16 a[202:203], v239 offset:8704\n"
  "v_accvgpr_write_b32 a57, 0\n"
  "v_mfma_f32_16x16x32_bf16 v[68:71], a[156:159], v[36:39], v[68:71]\n"
  "ds_read_b64_tr_b16 a[204:205], v239 offset:12288\n"
  "v_accvgpr_write_b32 a58, 0\n"
  "v_mfma_f32_16x16x32_bf16 v[68:71], a[152:155], v[32:35], v[68:71]\n"
  "ds_read_b64_tr_b16 a[206:207], v239 offset:12800\n"
  "v_accvgpr_write_b32 a59, 0\n"
  "v_mfma_f32_16x16x32_bf16 v[72:75], a[160:163], v[32:35], v[72:75]\n"
  "buffer_load_dwordx4 v253, s[52:55], s71 offen offset:3072 sc1 lds\n"
  "v_mfma_f32_16x16x32_bf16 v[72:75], a[164:167], v[36:39], v[72:75]\n"
  "v_accvgpr_write_b32 a60, 0\n"
  "v_accvgpr_write_b32 a94, 0\n"
  "v_mfma_f32_16x16x32_bf16 v[104:107], a[164:167], v[44:47], v[104:107]\n"
  "s_add_u32 s52, s52, s81\n"
  "s_addc_u32 s53, s53, 0\n"
  "v_mfma_f32_16x16x32_bf16 v[104:107], a[160:163], v[40:43], v[104:107]\n"
  "s_add_u32 s56, s56, s80\n"
  "s_addc_u32 s57, s57, 0\n"
  "v_mfma_f32_16x16x32_bf16 v[136:139], a[160:163], v[48:51], v[136:139]\n"
  "v_accvgpr_write_b32 a61, 0\n"
  "v_accvgpr_write_b32 a95, 0\n"
  "v_mfma_f32_16x16x32_bf16 v[136:139], a[164:167], v[52:55], v[136:139]\n"
  "s_bitcmp1_b32 s94, s82\n"
  "v_accvgpr_write_b32 a62, 0\n"
  "v_mfma_f32_16x16x32_bf16 v[168:171], a[164:167], v[60:63], v[168:171]\n"
  "s_waitcnt vmcnt(12) lgkmcnt(0)\n"
  "s_barrier\n"
  "v_mfma_f32_16x16x32_bf16 v[168:171], a[160:163], v[56:59], v[168:171]\n"
  "s_cbranch_scc1 L_qev%=\n"
  "L_qsw_ret%=:\n"
  "s_add_u32 m0, s84, 0x4000\n"
  "v_mfma_f32_16x16x32_bf16 v[172:175], a[168:171], v[56:59], v[172:175]\n"
  "buffer_load_dwordx4 v250, s[56:59], s72 offen lds\n"
  "v_mfma_f32_16x16x32_bf16 v[172:175], a[172:175], v[60:63], v[172:175]\n"
  "ds_read_b128 a[208:211], v240\n"
  "v_accvgpr_write_b32 a63, 0\n"
  "v_mfma_f32_16x16x32_bf16 v[140:143], a[172:175], v[52:55], v[140:143]\n"
  "ds_read_b128 a[212:215], v241\n"
  "v_accvgpr_write_b32 a64, 0\n"
  "v_mfma_f32_16x16x32_bf16 v[140:143], a[168:171], v[48:51], v[140:143]\n"
  "ds_read_b128 a[216:219], v240 offset:2048\n"
  "v_accvgpr_write_b32 a65, 0\n"
  "v_mfma_f32_16x16x32_bf16 v[108:111], a[168:171], v[40:43], v[108:111]\n"
  "ds_read_b128 a[220:223], v241 offset:2048\n"
  "v_accvgpr_write_b32 a66, 0\n"
  "v_mfma_f32_16x16x32_bf16 v[108:111], a[172:175], v[44:47], v[108:111]\n"
  "buffer_load_dwordx4 v251, s[56:59], s73 offen offset:1024 lds\n"
  "v_mfma_f32_16x16x32_bf16 v[76:79], a[172:175], v[36:39], v[76:79]\n"
  "ds_read_b128 a[224:227], v240 offset:16384\n"
  "v_accvgpr_write_b32 a67, 0\n"
  "v_mfma_f32_16x16x32_bf16 v[76:79], a[168:171], v[32:35], v[76:79]\n"
  "ds_read_b128 a[228:231], v241 offset:16384\n"
  "v_accvgpr_write_b32 a68, 0\n"
  "v_mfma_f32_16x16x32_bf16 v[80:83], a[176:179], v[32:35], v[80:83]\n"
  "ds_read_b128 a[232:235], v240 offset:18432\n"
  "v_accvgpr_write_b32 a69, 0\n"
  "v_mfma_f32_16x16x32_bf16 v[80:83], a[180:183], v[36:39], v[80:83]\n"
  "ds_read_b128 a[236:239], v241 offset:18432\n"
  "v_accvgpr_write_b32 a70, 0\n"
  "v_mfma_f32_16x16x32_bf16 v[112:115], a[180:183], v[44:47], v[112:115]\n"
  "buffer_load_dwordx4 v250, s[56:59], s74 offen offset:2048 lds\n"
  "v_mfma_f32_16x16x32_bf16 v[112:115], a[176:179], v[40:43], v[112:115]\n"
  "ds_read_b64_tr_b16 a[144:145], v242\n"
  "v_accvgpr_write_b32 a71, 0\n"
  "v_mfma_f32_16x16x32_bf16 v[144:147], a[176:179], v[48:51], v[144:147]\n"
  "ds_read_b64_tr_b16 a[146:147], v242 offset:512\n"
  "v_accvgpr_write_b32 a72, 0\n"
  "v_mfma_f32_16x16x32_bf16 v[144:147], a[180:183], v[52:55], v[144:147]\n"
  "ds_read_b64_tr_b16 a[148:149], v242 offset:4096\n"
  "v_accvgpr_write_b32 a73, 0\n"
  "v_mfma_f32_16x16x32_bf16 v[176:179], a[180:183], v[60:63], v[176:179]\n"
  "ds_read_b64_tr_b16 a[150:151], v242 offset:4608\n"
  "v_accvgpr_write_b32 a74, 0\n"
  "v_mfma_f32_16x16x32_bf16 v[176:179], a[176:179], v[56:59], v[176:179]\n"
  "buffer_load_dwordx4 v251, s[56:59], s75 offen offset:3072 lds\n"
  "v_mfma_f32_16x16x32_bf16 v[180:183], a[184:187], v[56:59], v[180:183]\n"
  "ds_read_b64_tr_b16 a[152:153], v243\n"
  "v_accvgpr_write_b32 a75, 0\n"
  "v_mfma_f32_16x16x32_bf16 v[180:183], a[188:191], v[60:63], v[180:183]\n"
  "ds_read_b64_tr_b16 a[154:155], v243 offset:512\n"
  "v_accvgpr_write_b32 a76, 0\n"
  "v_mfma_f32_16x16x32_bf16 v[148:151], a[188:191], v[52:55], v[148:151]\n"
  "ds_read_b64_tr_b16 a[156:157], v243 offset:4096\n"
  "v_accvgpr_write_b32 a77, 0\n"
  "v_mfma_f32_16x16x32_bf16 v[148:151], a[184:187], v[48:51], v[148:151]\n"
  "ds_read_b64_tr_b16 a[158:159], v243 offset:4608\n"
  "s_add_u32 m0, s83, 0x8000\n"
  "v_mfma_f32_16x16x32_bf16 v[116:119], a[184:187], v[40:43], v[116:119]\n"
  "buffer_load_dwordx4 v252, s[52:55], s64 offen sc1 lds\n"
  "v_mfma_f32_16x16x32_bf16 v[116:119], a[188:191], v[44:47], v[116:119]\n"
  "ds_read_b64_tr_b16 a[160:161], v244\n"
  "v_accvgpr_write_b32 a78, 0\n"
  "v_mfma_f32_16x16x32_bf16 v[84:87], a[188:191], v[36:39], v[84:87]\n"
  "ds_read_b64_tr_b16 a[162:163], v244 offset:512\n"
  "v_accvgpr_write_b32 a79, 0\n"
  "v_mfma_f32_16x16x32_bf16 v[84:87], a[184:187], v[32:35], v[84:87]\n"
  "ds_read_b64_tr_b16 a[164:165], v244 offset:4096\n"
  "v_accvgpr_write_b32 a80, 0\n"
  "v_mfma_f32_16x16x32_bf16 v[88:91], a[192:195], v[32:35], v[88:91]\n"
  "ds_read_b64_tr_b16 a[166:167], v244 offset:4608\n"
  "v_accvgpr_write_b32 a81, 0\n"
  "v_mfma_f32_16x16x32_bf16 v[88:91], a[196:199], v[36:39], v[88:91]\n"
  "buffer_load_dwordx4 v253, s[52:55], s65 offen offset:1024 sc1 lds\n"
  "v_mfma_f32_16x16x32_bf16 v[120:123], a[196:199], v[44:47], v[120:123]\n"
  "ds_read_b64_tr_b16 a[168:169], v245\n"
  "v_accvgpr_write_b32 a82, 0\n"
  "v_mfma_f32_16x16x32_bf16 v[120:123], a[192:195], v[40:43], v[120:123]\n"
  "ds_read_b64_tr_b16 a[170:171], v245 offset:512\n"
  "v_accvgpr_write_b32 a83, 0\n"
  "v_mfma_f32_16x16x32_bf16 v[152:155], a[192:195], v[48:51], v[152:155]\n"
  "ds_read_b64_tr_b16 a[172:173], v245 offset:4096\n"
  "v_accvgpr_write_b32 a84, 0\n"
  "v_mfma_f32_16x16x32_bf16 v[152:155], a[196:199], v[52:55], v[152:155]\n"
  "ds_read_b64_tr_b16 a[174:175], v245 offset:4608\n"
  "v_accvgpr_write_b32 a85, 0\n"
  "v_mfma_f32_16x16x32_bf16 v[184:187], a[196:199], v[60:63], v[184:187]\n"
  "buffer_load_dwordx4 v252, s[52:55], s66 offen offset:2048 sc1 lds\n"
  "v_mfma_f32_16x16x32_bf16 v[184:187], a[192:195], v[56:59], v[184:187]\n"
  "v_accvgpr_write_b32 a86, 0\n"
  "v_mfma_f32_16x16x32_bf16 v[188:191], a[200:203], v[56:59], v[188:191]\n"
  "v_accvgpr_write_b32 a87, 0\n"
  "v_mfma_f32_16x16x32_bf16 v[188:191], a[204:207], v[60:63], v[188:191]\n"
  "v_accvgpr_write_b32 a88, 0\n"
  "v_mfma_f32_16x16x32_bf16 v[156:159], a[204:207], v[52:55], v[156:159]\n"
  "v_accvgpr_write_b32 a89, 0\n"
  "v_mfma_f32_16x16x32_bf16 v[156:159], a[200:203], v[48:51], v[156:159]\n"
  "buffer_load_dwordx4 v253, s[52:55], s67 offen offset:3072 sc1 lds\n"
  "v_mfma_f32_16x16x32_bf16 v[124:127], a[200:203], v[40:43], v[124:127]\n"
  "v_accvgpr_write_b32 a90, 0\n"
  "v_mfma_f32_16x16x32_bf16 v[124:127], a[204:207], v[44:47], v[124:127]\n"
  "v_accvgpr_write_b32 a91, 0\n"
  "v_mfma_f32_16x16x32_bf16 v[92:95], a[204:207], v[36:39], v[92:95]\n"
  "v_accvgpr_write_b32 a92, 0\n"
  "v_mfma_f32_16x16x32_bf16 v[92:95], a[200:203], v[32:35], v[92:95]\n"
  "s_waitcnt lgkmcnt(0)\n"
  "v_accvgpr_write_b32 a93, 0\n"
  "v_mfma_f32_16x16x32_bf16 v[64:67], a[144:147], a[208:211], v[64:67]\n"
  "ds_read_b64_tr_b16 a[176:177], v242 offset:8192\n"
  "s_add_u32 m0, s83, 0x9000\n"
  "v_mfma_f32_16x16x32_bf16 v[64:67], a[148:151], a[212:215], v[64:67]\n"
  "buffer_load_dwordx4 v252, s[52:55], s68 offen sc1 lds\n"
  "ds_read_b64_tr_b16 a[178:179], v242 offset:8704\n"
  "v_mfma_f32_16x16x32_bf16 v[96:99], a[148:151], a[220:223], v[96:99]\n"
  "ds_read_b64_tr_b16 a[180:181], v242 offset:12288\n"
  "v_accvgpr_write_b32 a96, 0\n"
  "v_mfma_f32_16x16x32_bf16 v[96:99], a[144:147], a[216:219], v[96:99]\n"
  "ds_read_b64_tr_b16 a[182:183], v242 offset:12800\n"
  "v_accvgpr_write_b32 a97, 0\n"
  "v_mfma_f32_16x16x32_bf16 v[128:131], a[144:147], a[224:227], v[128:131]\n"
  "ds_read_b64_tr_b16 a[184:185], v243 offset:8192\n"
  "v_accvgpr_write_b32 a98, 0\n"
  "v_mfma_f32_16x16x32_bf16 v[128:131], a[148:151], a[228:231], v[128:131]\n"
  "ds_read_b64_tr_b16 a[186:187], v243 offset:8704\n"
  "v_accvgpr_write_b32 a99, 0\n"
  "v_mfma_f32_16x16x32_bf16 v[160:163], a[148:151], a[236:239], v[160:163]\n"
  "buffer_load_dwordx4 v253, s[52:55], s69 offen offset:1024 sc1 lds\n"
  "ds_read_b64_tr_b16 a[188:189], v243 offset:12288\n"
  "v_mfma_f32_16x16x32_bf16 v[160:163], a[144:147], a[232:235], v[160:163]\n"
  "ds_read_b64_tr_b16 a[190:191], v243 offset:12800\n"
  "v_accvgpr_write_b32 a100, 0\n"
  "v_mfma_f32_16x16x32_bf16 v[164:167], a[152:155], a[232:235], v[164:167]\n"
  "ds_read_b64_tr_b16 a[192:193], v244 offset:8192\n"
  "v_accvgpr_write_b32 a101, 0\n"
  "v_mfma_f32_16x16x32_bf16 v[164:167], a[156:159], a[236:239], v[164:167]\n"
  "ds_read_b64_tr_b16 a[194:195], v244 offset:8704\n"
  "v_accvgpr_write_b32 a102, 0\n"
  "v_mfma_f32_16x16x32_bf16 v[132:135], a[156:159], a[228:231], v[132:135]\n"
  "ds_read_b64_tr_b16 a[196:197], v244 offset:12288\n"
  "v_accvgpr_write_b32 a103, 0\n"
  "v_mfma_f32_16x16x32_bf16 v[132:135], a[152:155], a[224:227], v[132:135]\n"
  "buffer_load_dwordx4 v252, s[52:55], s70 offen offset:2048 sc1 lds\n"
  "ds_read_b64_tr_b16 a[198:199], v244 offset:12800\n"
  "v_mfma_f32_16x16x32_bf16 v[100:103], a[152:155], a[216:219], v[100:103]\n"
  "ds_read_b64_tr_b16 a[200:201], v245 offset:8192\n"
  "v_accvgpr_write_b32 a104, 0\n"
  "v_mfma_f32_16x16x32_bf16 v[100:103], a[156:159], a[220:223], v[100:103]\n"
  "ds_read_b64_tr_b16 a[202:203], v245 offset:8704\n"
  "v_accvgpr_write_b32 a105, 0\n"
  "v_mfma_f32_16x16x32_bf16 v[68:71], a[156:159], a[212:215], v[68:71]\n"
  "ds_read_b64_tr_b16 a[204:205], v245 offset:12288\n"
  "v_accvgpr_write_b32 a106, 0\n"
  "v_mfma_f32_16x16x32_bf16 v[68:71], a[152:155], a[208:211], v[68:71]\n"
  "ds_read_b64_tr_b16 a[206:207], v245 offset:12800\n"
  "v_accvgpr_write_b32 a107, 0\n"
  "v_mfma_f32_16x16x32_bf16 v[72:75], a[160:163], a[208:211], v[72:75]\n"
  "buffer_load_dwordx4 v253, s[52:55], s71 offen offset:3072 sc1 lds\n"
  "v_mfma_f32_16x16x32_bf16 v[72:75], a[164:167], a[212:215], v[72:75]\n"
  "v_accvgpr_write_b32 a108, 0\n"
  "v_accvgpr_write_b32 a143, 0\n"
  "v_mfma_f32_16x16x32_bf16 v[104:107], a[164:167], a[220:223], v[104:107]\n"
  "s_add_u32 s52, s52, s81\n"
  "s_addc_u32 s53, s53, 0\n"
  "v_mfma_f32_16x16x32_bf16 v[104:107], a[160:163], a[216:219], v[104:107]\n"
  "s_add_u32 s56, s56, s80\n"
  "s_addc_u32 s57, s57, 0\n"
  "v_mfma_f32_16x16x32_bf16 v[136:139], a[160:163], a[224:227], v[136:139]\n"
  "v_accvgpr_write_b32 a109, 0\n"
  "v_mfma_f32_16x16x32_bf16 v[136:139], a[164:167], a[228:231], v[136:139]\n"
  "v_accvgpr_write_b32 a110, 0\n"
  "v_mfma_f32_16x16x32_bf16 v[168:171], a[164:167], a[236:239], v[168:171]\n"
  "s_waitcnt vmcnt(12) lgkmcnt(0)\n"
  "s_barrier\n"
  "v_mfma_f32_16x16x32_bf16 v[168:171], a[160:163], a[232:235], v[168:171]\n"
  "s_add_u32 m0, s84, 0x8000\n"
  "v_accvgpr_write_b32 a111, 0\n"
  "v_mfma_f32_16x16x32_bf16 v[172:175], a[168:171], a[232:235], v[172:175]\n"
  "buffer_load_dwordx4 v250, s[56:59], s72 offen lds\n"
  "v_mfma_f32_16x16x32_bf16 v[172:175], a[172:175], a[236:239], v[172:175]\n"
  "ds_read_b128 v[0:3], v228\n"
  "v_accvgpr_write_b32 a112, 0\n"
  "v_mfma_f32_16x16x32_bf16 v[140:143], a[172:175], a[228:231], v[140:143]\n"
  "ds_read_b128 v[4:7], v229\n"
  "v_accvgpr_write_b32 a113, 0\n"
  "v_mfma_f32_16x16x32_bf16 v[140:143], a[168:171], a[224:227], v[140:143]\n"
  "ds_read_b128 v[8:11], v228 offset:2048\n"
  "v_accvgpr_write_b32 a114, 0\n"
  "v_mfma_f32_16x16x32_bf16 v[108:111], a[168:171], a[216:219], v[108:111]\n"
  "ds_read_b128 v[12:15], v229 offset:2048\n"
  "v_accvgpr_write_b32 a115, 0\n"
  "v_mfma_f32_16x16x32_bf16 v[108:111], a[172:175], a[220:223], v[108:111]\n"
  "buffer_load_dwordx4 v251, s[56:59], s73 offen offset:1024 lds\n"
  "v_mfma_f32_16x16x32_bf16 v[76:79], a[172:175], a[212:215], v[76:79]\n"
  "ds_read_b128 v[16:19], v228 offset:16384\n"
  "v_accvgpr_write_b32 a116, 0\n"
  "v_mfma_f32_16x16x32_bf16 v[76:79], a[168:171], a[208:211], v[76:79]\n"
  "ds_read_b128 v[20:23], v229 offset:16384\n"
  "v_accvgpr_write_b32 a117, 0\n"
  "v_mfma_f32_16x16x32_bf16 v[80:83], a[176:179], a[208:211], v[80:83]\n"
  "ds_read_b128 v[24:27], v228 offset:18432\n"
  "v_accvgpr_write_b32 a118, 0\n"
  "v_mfma_f32_16x16x32_bf16 v[80:83], a[180:183], a[212:215], v[80:83]\n"
  "ds_read_b128 v[28:31], v229 offset:18432\n"
  "v_accvgpr_write_b32 a119, 0\n"
  "v_mfma_f32_16x16x32_bf16 v[112:115], a[180:183], a[220:223], v[112:115]\n"
  "buffer_load_dwordx4 v250, s[56:59], s74 offen offset:2048 lds\n"
  "v_mfma_f32_16x16x32_bf16 v[112:115], a[176:179], a[216:219], v[112:115]\n"
  "ds_read_b64_tr_b16 a[144:145], v230\n"
  "v_accvgpr_write_b32 a120, 0\n"
  "v_mfma_f32_16x16x32_bf16 v[144:147], a[176:179], a[224:227], v[144:147]\n"
  "ds_read_b64_tr_b16 a[146:147], v230 offset:512\n"
  "v_accvgpr_write_b32 a121, 0\n"
  "v_mfma_f32_16x16x32_bf16 v[144:147], a[180:183], a[228:231], v[144:147]\n"
  "ds_read_b64_tr_b16 a[148:149], v230 offset:4096\n"
  "v_accvgpr_write_b32 a122, 0\n"
  "v_mfma_f32_16x16x32_bf16 v[176:179], a[180:183], a[236:239], v[176:179]\n"
  "ds_read_b64_tr_b16 a[150:151], v230 offset:4608\n"
  "v_accvgpr_write_b32 a123, 0\n"
  "v_mfma_f32_16x16x32_bf16 v[176:179], a[176:179], a[232:235], v[176:179]\n"
  "buffer_load_dwordx4 v251, s[56:59], s75 offen offset:3072 lds\n"
  "v_mfma_f32_16x16x32_bf16 v[180:183], a[184:187], a[232:235], v[180:183]\n"
  "ds_read_b64_tr_b16 a[152:153], v231\n"
  "v_accvgpr_write_b32 a124, 0\n"
  "v_mfma_f32_16x16x32_bf16 v[180:183], a[188:191], a[236:239], v[180:183]\n"
  "ds_read_b64_tr_b16 a[154:155], v231 offset:512\n"
  "v_accvgpr_write_b32 a125, 0\n"
  "v_mfma_f32_16x16x32_bf16 v[148:151], a[188:191], a[228:231], v[148:151]\n"
  "ds_read_b64_tr_b16 a[156:157], v231 offset:4096\n"
  "v_accvgpr_write_b32 a126, 0\n"
  "v_mfma_f32_16x16x32_bf16 v[148:151], a[184:187], a[224:227], v[148:151]\n"
  "ds_read_b64_tr_b16 a[158:159], v231 offset:4608\n"
  "s_add_u32 m0, s83, 0x10000\n"
  "v_mfma_f32_16x16x32_bf16 v[116:119], a[184:187], a[216:219], v[116:119]\n"
  "buffer_load_dwordx4 v252, s[52:55], s64 offen sc1 lds\n"
  "v_mfma_f32_16x16x32_bf16 v[116:119], a[188:191], a[220:223], v[116:119]\n"
  "ds_read_b64_tr_b16 a[160:161], v232\n"
  "v_accvgpr_write_b32 a127, 0\n"
  "v_mfma_f32_16x16x32_bf16 v[84:87], a[188:191], a[212:215], v[84:87]\n"
  "ds_read_b64_tr_b16 a[162:163], v232 offset:512\n"
  "v_accvgpr_write_b32 a128, 0\n"
  "v_mfma_f32_16x16x32_bf16 v[84:87], a[184:187], a[208:211], v[84:87]\n"
  "ds_read_b64_tr_b16 a[164:165], v232 offset:4096\n"
  "v_accvgpr_write_b32 a129, 0\n"
  "v_mfma_f32_16x16x32_bf16 v[88:91], a[192:195], a[208:211], v[88:91]\n"
  "ds_read_b64_tr_b16 a[166:167], v232 offset:4608\n"
  "v_accvgpr_write_b32 a130, 0\n"
  "v_mfma_f32_16x16x32_bf16 v[88:91], a[196:199], a[212:215], v[88:91]\n"
  "buffer_load_dwordx4 v253, s[52:55], s65 offen offset:1024 sc1 lds\n"
  "v_mfma_f32_16x16x32_bf16 v[120:123], a[196:199], a[220:223], v[120:123]\n"
  "ds_read_b64_tr_b16 a[168:169], v233\n"
  "v_accvgpr_write_b32 a131, 0\n"
  "v_mfma_f32_16x16x32_bf16 v[120:123], a[192:195], a[216:219], v[120:123]\n"
  "ds_read_b64_tr_b16 a[170:171], v233 offset:512\n"
  "v_accvgpr_write_b32 a132, 0\n"
  "v_mfma_f32_16x16x32_bf16 v[152:155], a[192:195], a[224:227], v[152:155]\n"
  "ds_read_b64_tr_b16 a[172:173], v233 offset:4096\n"
  "v_accvgpr_write_b32 a133, 0\n"
  "v_mfma_f32_16x16x32_bf16 v[152:155], a[196:199], a[228:231], v[152:155]\n"
  "ds_read_b64_tr_b16 a[174:175], v233 offset:4608\n"
  "v_accvgpr_write_b32 a134, 0\n"
  "v_mfma_f32_16x16x32_bf16 v[184:187], a[196:199], a[236:239], v[184:187]\n"
  "buffer_load_dwordx4 v252, s[52:55], s66 offen offset:2048 sc1 lds\n"
  "v_mfma_f32_16x16x32_bf16 v[184:187], a[192:195], a[232:235], v[184:187]\n"
  "v_accvgpr_write_b32 a135, 0\n"
  "v_mfma_f32_16x16x32_bf16 v[188:191], a[200:203], a[232:235], v[188:191]\n"
  "v_accvgpr_write_b32 a136, 0\n"
  "v_mfma_f32_16x16x32_bf16 v[188:191], a[204:207], a[236:239], v[188:191]\n"
  "v_accvgpr_write_b32 a137, 0\n"
  "v_mfma_f32_16x16x32_bf16 v[156:159], a[204:207], a[228:231], v[156:159]\n"
  "v_accvgpr_write_b32 a138, 0\n"
  "v_mfma_f32_16x16x32_bf16 v[156:159], a[200:203], a[224:227], v[156:159]\n"
  "buffer_load_dwordx4 v253, s[52:55], s67 offen offset:3072 sc1 lds\n"
  "v_mfma_f32_16x16x32_bf16 v[124:127], a[200:203], a[216:219], v[124:127]\n"
  "v_accvgpr_write_b32 a139, 0\n"
  "v_mfma_f32_16x16x32_bf16 v[124:127], a[204:207], a[220:223], v[124:127]\n"
  "v_accvgpr_write_b32 a140, 0\n"
  "v_mfma_f32_16x16x32_bf16 v[92:95], a[204:207], a[212:215], v[92:95]\n"
  "v_accvgpr_write_b32 a141, 0\n"
  "v_mfma_f32_16x16x32_bf16 v[92:95], a[200:203], a[208:211], v[92:95]\n"
  "s_waitcnt lgkmcnt(0)\n"
  "v_accvgpr_write_b32 a142, 0\n"
  "s_sub_u32 s82, s82, 1\n"
  "s_branch L_q_loop%=\n"
  "L_qev%=:\n"
  "s_cmp_eq_u32 s82, 1\n"
  "s_cbranch_scc1 L_qsw%=\n"
  "s_cmp_eq_u32 s82, 2\n"
  "s_cbranch_scc1 L_qpre%=\n"
  "s_cmp_eq_u32 s86, 0\n"
  "s_cbranch_scc1 L_sg_q%=\n"
  "s_lshl2_add_u32 s86, s34, s86\n"
  "v_mov_b32 v220, s86\n"
  "v_mov_b32 v221, s35\n"
  "global_store_dword v220, v221, s[8:9] sc1\n"
  "s_mov_b32 s86, 0\n"
  "L_sg_q%=:\n"
  "s_branch L_qsw_ret%=\n"
  "L_qpre%=:\n"
  "v_bfe_u32 v218, v255, 2, 3\n"
  "v_min_u32 v218, s42, v218\n"
  "v_lshrrev_b32 v220, 5, v255\n"
  "v_lshl_add_u32 v218, v220, 3, v218\n"
  "s_lshr_b32 s87, s38, 9\n"
  "s_lshl_b32 s87, s87, 11\n"
  "s_add_u32 s87, s87, 0x5a10000\n"
  "v_lshl_add_u32 v218, v218, 7, s87\n"
  "v_and_b32 v220, 3, v255\n"
  "v_lshl_add_u32 v218, v220, 2, v218\n"
  "global_load_dword v219, v218, s[8:9] sc1\n"
  "s_lshl_b32 s87, s34, 8\n"
  "s_add_u32 s87, s87, s40\n"
  "s_cmp_eq_u32 s44, 6\n"
  "s_cselect_b32 s90, s46, 0\n"
  "s_add_u32 s90, s90, 0x5a00804\n"
  "v_mov_b32 v212, s90\n"
  "global_load_dword v213, v212, s[8:9] sc1\n"
  "v_and_b32 v214, 15, v255\n"
  "v_lshl_add_u32 v214, v214, 3, s87\n"
  "v_add_u32 v214, 0x4000000, v214\n"
  "global_load_dwordx2 v[192:193], v214, s[8:9] offset:0 sc1\n"
  "global_load_dwordx2 v[194:195], v214, s[8:9] offset:128 sc1\n"
  "global_load_dwordx2 v[196:197], v214, s[8:9] offset:1024 sc1\n"
  "global_load_dwordx2 v[198:199], v214, s[8:9] offset:1152 sc1\n"
  "s_branch L_qsw_ret%=\n"
  "; (covered by loop waits)\n"
  "L_qsw%=:\n"
  "s_mov_b32 s95, 0xff\n"
  "s_mov_b32 s99, 0x4000\n"
  "L_fc_q%=:\n"
  "v_cmp_ne_u32 vcc, s35, v219\n"
  "s_nop 4\n"
  "s_or_b32 s91, vcc_lo, vcc_hi\n"
  "s_and_b32 s91, s91, s95\n"
  "s_cbranch_scc0 L_fd_q%=\n"
  "s_sleep 2\n"
  "global_load_dword v219, v218, s[8:9] sc1\n"
  "s_waitcnt vmcnt(0)\n"
  "s_sub_u32 s99, s99, 1\n"
  "s_cbranch_scc0 L_fc_q%=\n"
  "L_fd_q%=:\n"
  "global_load_dword v219, v218, s[8:9] sc1\n"
  "s_mov_b32 s99, 0x4000\n"
  "L_gc_at%=:\n"
  "v_xor_b32 v215, s35, v193\n"
  "v_xor_b32 v195, s35, v195\n"
  "v_or_b32 v215, v215, v195\n"
  "v_xor_b32 v197, s35, v197\n"
  "v_or_b32 v215, v215, v197\n"
  "v_xor_b32 v199, s35, v199\n"
  "v_or_b32 v215, v215, v199\n"
  "v_cmp_ne_u32 vcc, 0, v215\n"
  "s_nop 4\n"
  "s_cbranch_vccz L_gd_at%=\n"
  "s_sleep 2\n"
  "global_load_dwordx2 v[192:193], v214, s[8:9] offset:0 sc1\n"
  "global_load_dwordx2 v[194:195], v214, s[8:9] offset:128 sc1\n"
  "global_load_dwordx2 v[196:197], v214, s[8:9] offset:1024 sc1\n"
  "global_load_dwordx2 v[198:199], v214, s[8:9] offset:1152 sc1\n"
  "s_waitcnt vmcnt(0)\n"
  "s_sub_u32 s99, s99, 1\n"
  "s_cbranch_scc0 L_gc_at%=\n"
  "L_gd_at%=:\n"
  "s_mov_b32 s99, 0x4000\n"
  "L_qt_c%=:\n"
  "v_cmp_ne_u32 vcc, s35, v213\n"
  "s_nop 4\n"
  "s_cbranch_vccz L_qt_d%=\n"
  "s_sleep 2\n"
  "global_load_dword v213, v212, s[8:9] sc1\n"
  "s_waitcnt vmcnt(0)\n"
  "s_sub_u32 s99, s99, 1\n"
  "s_cbranch_scc0 L_qt_c%=\n"
  "L_qt_d%=:\n"
  "v_mov_b32 v224, v192\n"
  "v_mov_b32 v225, v194\n"
  "v_mov_b32 v226, v196\n"
  "v_mov_b32 v227, v198\n"
  "s_lshl_b32 s87, s34, 13\n"
  "s_add_u32 s87, s87, s41\n"
  "s_add_u32 s52, s8, s87\n"
  "s_addc_u32 s53, s9, 0\n"
  "s_mov_b32 s54, 0x2000\n"
  "s_mov_b32 s58, 0\n"
  "v_lshlrev_b32 v252, 4, v255\n"
  "v_lshlrev_b32 v253, 4, v255\n"
  "s_mov_b32 s64, 0\n"
  "s_mov_b32 s65, 0\n"
  "s_mov_b32 s66, 0\n"
  "s_mov_b32 s67, 0\n"
  "s_mov_b32 s68, 0x1000\n"
  "s_mov_b32 s69, 0x1000\n"
  "s_mov_b32 s70, 0x1000\n"
  "s_mov_b32 s71, 0x1000\n"
  "s_mov_b32 s81, 0x8000\n"
  "s_lshl_b32 s87, s42, 2\n"
  "s_lshr_b32 s90, s34, 1\n"
  "s_add_u32 s85, s87, 2\n"
  "s_sub_u32 s85, s85, s90\n"
  "s_branch L_qsw_ret%=\n"
  "L_q_done%=:\n"
  "s_add_u32 m0, s83, 0x1000\n"
  "v_add_u32 v228, 0x8000, v246\n"
  "buffer_load_dwordx4 v252, s[52:55], s68 offen sc1 lds\n"
  "s_nop 0\n"
  "s_nop 0\n"
  "s_nop 0\n"
  "s_lshl_b32 s82, s42, 2\n"
  "s_lshr_b32 s90, s34, 1\n"
  "s_add_u32 s82, s82, s90\n"
  "v_cvt_pk_bf16_f32 v0, v64, v65\n"
  "v_cvt_pk_bf16_f32 v1, v66, v67\n"
  "v_cvt_pk_bf16_f32 v2, v68, v69\n"
  "v_cvt_pk_bf16_f32 v3, v70, v71\n"
  "v_cvt_pk_bf16_f32 v4, v72, v73\n"
  "v_cvt_pk_bf16_f32 v5, v74, v75\n"
  "v_cvt_pk_bf16_f32 v6, v76, v77\n"
  "v_cvt_pk_bf16_f32 v7, v78, v79\n"
  "buffer_load_dwordx4 v253, s[52:55], s69 offen offset:1024 sc1 lds\n"
  "v_cvt_pk_bf16_f32 v8, v80, v81\n"
  "v_cvt_pk_bf16_f32 v9, v82, v83\n"
  "v_cvt_pk_bf16_f32 v10, v84, v85\n"
  "v_cvt_pk_bf16_f32 v11, v86, v87\n"
  "v_cvt_pk_bf16_f32 v12, v88, v89\n"
  "v_cvt_pk_bf16_f32 v13, v90, v91\n"
  "v_cvt_pk_bf16_f32 v14, v92, v93\n"
  "v_cvt_pk_bf16_f32 v15, v94, v95\n"
  "buffer_load_dwordx4 v252, s[52:55], s70 offen offset:2048 sc1 lds\n"
  "v_cvt_pk_bf16_f32 v16, v96, v97\n"
  "v_cvt_pk_bf16_f32 v17, v98, v99\n"
  "v_cvt_pk_bf16_f32 v18, v100, v101\n"
  "v_cvt_pk_bf16_f32 v19, v102, v103\n"
  "v_cvt_pk_bf16_f32 v20, v104, v105\n"
  "v_cvt_pk_bf16_f32 v21, v106, v107\n"
  "v_cvt_pk_bf16_f32 v22, v108, v109\n"
  "v_cvt_pk_bf16_f32 v23, v110, v111\n"
  "buffer_load_dwordx4 v253, s[52:55], s71 offen offset:3072 sc1 lds\n"
  "v_cvt_pk_bf16_f32 v24, v112, v113\n"
  "v_cvt_pk_bf16_f32 v25, v114, v115\n"
  "v_cvt_pk_bf16_f32 v26, v116, v117\n"
  "v_cvt_pk_bf16_f32 v27, v118, v119\n"
  "v_cvt_pk_bf16_f32 v28, v120, v121\n"
  "v_cvt_pk_bf16_f32 v29, v122, v123\n"
  "v_cvt_pk_bf16_f32 v30, v124, v125\n"
  "v_cvt_pk_bf16_f32 v31, v126, v127\n"
  "s_waitcnt vmcnt(24)\n"
  "s_barrier\n"
  "ds_read_b128 v[76:79], v228 offset:28672\n"
  "ds_read_b128 v[72:75], v228 offset:24576\n"
  "ds_read_b128 v[68:71], v228 offset:20480\n"
  "ds_read_b128 v[64:67], v228 offset:16384\n"
  "ds_read_b128 v[92:95], v228 offset:28928\n"
  "ds_read_b128 v[88:91], v228 offset:24832\n"
  "ds_read_b128 v[84:87], v228 offset:20736\n"
  "ds_read_b128 v[80:83], v228 offset:16640\n"
  "ds_read_b128 v[108:111], v228 offset:29184\n"
  "ds_read_b128 v[104:107], v228 offset:25088\n"
  "ds_read_b128 v[100:103], v228 offset:20992\n"
  "ds_read_b128 v[96:99], v228 offset:16896\n"
  "ds_read_b128 v[124:127], v228 offset:29440\n"
  "ds_read_b128 v[120:123], v228 offset:25344\n"
  "ds_read_b128 v[116:119], v228 offset:21248\n"
  "ds_read_b128 v[112:115], v228 offset:17152\n"
  "v_cvt_pk_bf16_f32 v32, v128, v129\n"
  "v_cvt_pk_bf16_f32 v33, v130, v131\n"
  "v_cvt_pk_bf16_f32 v34, v132, v133\n"
  "v_cvt_pk_bf16_f32 v35, v134, v135\n"
  "v_cvt_pk_bf16_f32 v36, v136, v137\n"
  "v_cvt_pk_bf16_f32 v37, v138, v139\n"
  "v_cvt_pk_bf16_f32 v38, v140, v141\n"
  "v_cvt_pk_bf16_f32 v39, v142, v143\n"
  "v_cvt_pk_bf16_f32 v40, v144, v145\n"
  "v_cvt_pk_bf16_f32 v41, v146, v147\n"
  "v_cvt_pk_bf16_f32 v42, v148, v149\n"
  "v_cvt_pk_bf16_f32 v43, v150, v151\n"
  "v_cvt_pk_bf16_f32 v44, v152, v153\n"
  "v_cvt_pk_bf16_f32 v45, v154, v155\n"
  "v_cvt_pk_bf16_f32 v46, v156, v157\n"
  "v_cvt_pk_bf16_f32 v47, v158, v159\n"
  "s_cmp_lg_u32 s82, 0\n"
  "s_cbranch_scc1 L_at_t0%=\n"
  "v_cvt_pk_bf16_f32 v48, v160, v161\n"
  "v_cvt_pk_bf16_f32 v49, v162, v163\n"
  "v_cvt_pk_bf16_f32 v50, v164, v165\n"
  "v_cvt_pk_bf16_f32 v51, v166, v167\n"
  "v_cvt_pk_bf16_f32 v52, v168, v169\n"
  "v_cvt_pk_bf16_f32 v53, v170, v171\n"
  "v_cvt_pk_bf16_f32 v54, v172, v173\n"
  "v_cvt_pk_bf16_f32 v55, v174, v175\n"
  "v_cvt_pk_bf16_f32 v56, v176, v177\n"
  "v_cvt_pk_bf16_f32 v57, v178, v179\n"
  "v_cvt_pk_bf16_f32 v58, v180, v181\n"
  "v_cvt_pk_bf16_f32 v59, v182, v183\n"
  "v_cvt_pk_bf16_f32 v60, v184, v185\n"
  "v_cvt_pk_bf16_f32 v61, v186, v187\n"
  "v_cvt_pk_bf16_f32 v62, v188, v189\n"
  "v_cvt_pk_bf16_f32 v63, v190, v191\n"
  "s_mov_b32 s100, 0xff800000\n"
  "s_and_b32 s87, s42, 1\n"
  "s_lshl_b32 s87, s87, 5\n"
  "s_lshr_b32 s90, s38, 8\n"
  "s_add_u32 s87, s87, s90\n"
  "s_cmp_ge_u32 s42, 6\n"
  "s_cselect_b64 s[56:57], s[26:27], s[20:21]\n"
  "s_cselect_b32 s90, 14, 16\n"
  "s_lshl_b32 s90, s90, 13\n"
  "s_mul_i32 s87, s87, s90\n"
  "s_sub_u32 s90, s90, 0x2000\n"
  "s_cmp_ge_u32 s42, 4\n"
  "s_cselect_b32 s80, 0x2000, 0\n"
  "s_cselect_b32 s91, 15, 0\n"
  "s_lshl_b32 s99, s34, 11\n"
  "s_add_u32 s87, s87, s99\n"
  "v_and_b32 v233, s91, v255\n"
  "v_lshl_add_u32 v233, v233, 7, s87\n"
  "v_add_u32 v234, s90, v233\n"
  "s_mov_b32 s94, 4\n"
  "v_mov_b32 v230, v218\n"
  "v_mov_b32 v231, v219\n"
  "v_mov_b32 v208, 0x3f803f80\n"
  "v_mov_b32 v209, 0x3f803f80\n"
  "v_mov_b32 v210, 0x3f803f80\n"
  "v_mov_b32 v211, 0x3f803f80\n"
  "v_mov_b32 v220, 0\n"
  "v_mov_b32 v221, s100\n"
  "v_and_b32 v222, 15, v255\n"
  "v_lshrrev_b32 v215, 4, v255\n"
  "v_lshlrev_b32 v223, 12, v215\n"
  "v_lshl_add_u32 v223, v222, 4, v223\n"
  "v_lshlrev_b32 v215, 2, v215\n"
  "v_sub_u32 v222, v222, v215\n"
  "s_and_b32 s87, s34, 1\n"
  "s_lshl_b32 s87, s87, 5\n"
  "v_add_u32 v222, s87, v222\n"
  "s_lshl_b32 s87, s34, 9\n"
  "s_add_u32 s87, s87, s39\n"
  "s_add_u32 s60, s8, s87\n"
  "s_addc_u32 s61, s9, 0\n"
  "s_sub_u32 s62, 0x5b00000, s87\n"
  "s_mov_b32 s63, 0x20000\n"
  "s_mov_b32 s86, s43\n"
  "s_mov_b32 s88, 0x4000\n"
  "s_mov_b32 s89, 0x8000\n"
  "s_mov_b32 s100, 0xc000\n"
  "s_mov_b32 s39, 0xff800000\n"
  "s_mov_b32 s36, 0x8000\n"
  "s_mov_b32 s37, 0xff800000\n"
  "s_mov_b32 s38, 0xff800000\n"
  "v_mov_b32 v160, 0\n"
  "v_mov_b32 v161, 0\n"
  "v_mov_b32 v162, 0\n"
  "v_mov_b32 v163, 0\n"
  "v_mov_b32 v164, 0\n"
  "v_mov_b32 v165, 0\n"
  "v_mov_b32 v166, 0\n"
  "v_mov_b32 v167, 0\n"
  "v_mov_b32 v168, 0\n"
  "v_mov_b32 v169, 0\n"
  "v_mov_b32 v170, 0\n"
  "v_mov_b32 v171, 0\n"
  "v_mov_b32 v172, 0\n"
  "v_mov_b32 v173, 0\n"
  "v_mov_b32 v174, 0\n"
  "v_mov_b32 v175, 0\n"
  "v_mov_b32 v176, 0\n"
  "v_mov_b32 v177, 0\n"
  "v_mov_b32 v178, 0\n"
  "v_mov_b32 v179, 0\n"
  "v_mov_b32 v180, 0\n"
  "v_mov_b32 v181, 0\n"
  "v_mov_b32 v182, 0\n"
  "v_mov_b32 v183, 0\n"
  "v_mov_b32 v184, s39\n"
  "v_mov_b32 v185, s39\n"
  "v_mov_b32 v186, s39\n"
  "v_mov_b32 v187, s39\n"
  "v_mov_b32 v188, s39\n"
  "v_mov_b32 v189, s39\n"
  "v_mov_b32 v190, s39\n"
  "v_mov_b32 v191, s39\n"
  "v_mov_b32 v192, 0\n"
  "v_mov_b32 v193, 0\n"
  "v_mov_b32 v194, 0\n"
  "v_mov_b32 v195, 0\n"
  "v_mov_b32 v196, 0\n"
  "v_mov_b32 v197, 0\n"
  "v_mov_b32 v198, 0\n"
  "v_mov_b32 v199, 0\n"
  "v_mov_b32 v200, 0\n"
  "v_mov_b32 v201, 0\n"
  "v_mov_b32 v202, 0\n"
  "v_mov_b32 v203, 0\n"
  "v_mov_b32 v204, 0\n"
  "v_mov_b32 v205, 0\n"
  "v_mov_b32 v206, 0\n"
  "v_mov_b32 v207, 0\n"
  "s_branch L_at_m%=\n"
  "L_at_t0%=:\n"
  "s_waitcnt vmcnt(9) lgkmcnt(0)\n"
  "s_barrier\n"
  "v_mfma_f32_16x16x32_bf16 v[128:131], v[64:67], v[0:3], 0\n"
  "s_mov_b32 s100, 0xff800000\n"
  "s_and_b32 s87, s42, 1\n"
  "v_mfma_f32_16x16x32_bf16 v[128:131], v[68:71], v[4:7], v[128:131]\n"
  "s_lshl_b32 s87, s87, 5\n"
  "s_lshr_b32 s90, s38, 8\n"
  "s_add_u32 s87, s87, s90\n"
  "v_mfma_f32_16x16x32_bf16 v[128:131], v[72:75], v[8:11], v[128:131]\n"
  "s_cmp_ge_u32 s42, 6\n"
  "s_cselect_b64 s[56:57], s[26:27], s[20:21]\n"
  "s_cselect_b32 s90, 14, 16\n"
  "v_mfma_f32_16x16x32_bf16 v[128:131], v[76:79], v[12:15], v[128:131]\n"
  "s_lshl_b32 s90, s90, 13\n"
  "s_mul_i32 s87, s87, s90\n"
  "s_sub_u32 s90, s90, 0x2000\n"
  "v_mfma_f32_16x16x32_bf16 v[144:147], v[76:79], v[28:31], 0\n"
  "s_cmp_ge_u32 s42, 4\n"
  "s_cselect_b32 s80, 0x2000, 0\n"
  "s_cselect_b32 s91, 15, 0\n"
  "v_mfma_f32_16x16x32_bf16 v[144:147], v[72:75], v[24:27], v[144:147]\n"
  "s_lshl_b32 s99, s34, 11\n"
  "s_add_u32 s87, s87, s99\n"
  "v_and_b32 v233, s91, v255\n"
  "v_mfma_f32_16x16x32_bf16 v[144:147], v[68:71], v[20:23], v[144:147]\n"
  "v_lshl_add_u32 v233, v233, 7, s87\n"
  "v_add_u32 v234, s90, v233\n"
  "s_mov_b32 s94, 4\n"
  "v_mfma_f32_16x16x32_bf16 v[144:147], v[64:67], v[16:19], v[144:147]\n"
  "v_mov_b32 v230, v218\n"
  "v_mov_b32 v231, v219\n"
  "v_mov_b32 v208, 0x3f803f80\n"
  "v_mfma_f32_16x16x32_bf16 v[132:135], v[80:83], v[0:3], 0\n"
  "v_mov_b32 v209, 0x3f803f80\n"
  "v_mov_b32 v210, 0x3f803f80\n"
  "v_mfma_f32_16x16x32_bf16 v[132:135], v[84:87], v[4:7], v[132:135]\n"
  "v_mov_b32 v211, 0x3f803f80\n"
  "v_mov_b32 v220, 0\n"
  "v_mov_b32 v221, s100\n"
  "v_mfma_f32_16x16x32_bf16 v[132:135], v[88:91], v[8:11], v[132:135]\n"
  "v_and_b32 v222, 15, v255\n"
  "v_lshrrev_b32 v215, 4, v255\n"
  "v_lshlrev_b32 v223, 12, v215\n"
  "v_mfma_f32_16x16x32_bf16 v[132:135], v[92:95], v[12:15], v[132:135]\n"
  "v_lshl_add_u32 v223, v222, 4, v223\n"
  "v_lshlrev_b32 v215, 2, v215\n"
  "v_sub_u32 v222, v222, v215\n"
  "v_mfma_f32_16x16x32_bf16 v[148:151], v[92:95], v[28:31], 0\n"
  "s_and_b32 s87, s34, 1\n"
  "s_lshl_b32 s87, s87, 5\n"
  "v_add_u32 v222, s87, v222\n"
  "v_mfma_f32_16x16x32_bf16 v[148:151], v[88:91], v[24:27], v[148:151]\n"
  "s_lshl_b32 s87, s34, 9\n"
  "s_add_u32 s87, s87, s39\n"
  "s_add_u32 s60, s8, s87\n"
  "v_mfma_f32_16x16x32_bf16 v[148:151], v[84:87], v[20:23], v[148:151]\n"
  "s_addc_u32 s61, s9, 0\n"
  "s_sub_u32 s62, 0x5b00000, s87\n"
  "s_mov_b32 s63, 0x20000\n"
  "v_mfma_f32_16x16x32_bf16 v[148:151], v[80:83], v[16:19], v[148:151]\n"
  "s_mov_b32 s86, s43\n"
  "s_mov_b32 s88, 0x4000\n"
  "s_mov_b32 s89, 0x8000\n"
  "v_mfma_f32_16x16x32_bf16 v[136:139], v[96:99], v[0:3], 0\n"
  "s_mov_b32 s100, 0xc000\n"
  "s_mov_b32 s39, 0xff800000\n"
  "v_mfma_f32_16x16x32_bf16 v[136:139], v[100:103], v[4:7], v[136:139]\n"
  "s_mov_b32 s36, 0x8000\n"
  "s_mov_b32 s37, 0x41800000\n"
  "s_mov_b32 s38, 0x41800000\n"
  "v_mfma_f32_16x16x32_bf16 v[136:139], v[104:107], v[8:11], v[136:139]\n"
  "s_sub_u32 s42, s82, 1\n"
  "s_add_u32 s52, s52, s81\n"
  "s_addc_u32 s53, s53, 0\n"
  "v_mfma_f32_16x16x32_bf16 v[136:139], v[108:111], v[12:15], v[136:139]\n"
  "s_sub_u32 s85, s85, 1\n"
  "s_cselect_b32 s54, 0, s54\n"
  "s_add_u32 m0, s83, s36\n"
  "v_mfma_f32_16x16x32_bf16 v[152:155], v[108:111], v[28:31], 0\n"
  "s_nop 0\n"
  "buffer_load_dwordx4 v252, s[52:55], s64 offen sc1 lds\n"
  "v_cvt_pk_bf16_f32 v48, v160, v161\n"
  "v_mfma_f32_16x16x32_bf16 v[152:155], v[104:107], v[24:27], v[152:155]\n"
  "v_cvt_pk_bf16_f32 v49, v162, v163\n"
  "buffer_load_dwordx4 v252, s[52:55], s65 offen offset:1024 sc1 lds\n"
  "v_cvt_pk_bf16_f32 v50, v164, v165\n"
  "v_mfma_f32_16x16x32_bf16 v[152:155], v[100:103], v[20:23], v[152:155]\n"
  "v_cvt_pk_bf16_f32 v51, v166, v167\n"
  "buffer_load_dwordx4 v252, s[52:55], s66 offen offset:2048 sc1 lds\n"
  "v_cvt_pk_bf16_f32 v52, v168, v169\n"
  "v_mfma_f32_16x16x32_bf16 v[152:155], v[96:99], v[16:19], v[152:155]\n"
  "v_cvt_pk_bf16_f32 v53, v170, v171\n"
  "buffer_load_dwordx4 v252, s[52:55], s67 offen offset:3072 sc1 lds\n"
  "v_cvt_pk_bf16_f32 v54, v172, v173\n"
  "v_mfma_f32_16x16x32_bf16 v[140:143], v[112:115], v[0:3], 0\n"
  "v_cvt_pk_bf16_f32 v55, v174, v175\n"
  "s_add_u32 m0, m0, 0x1000\n"
  "v_mfma_f32_16x16x32_bf16 v[140:143], v[116:119], v[4:7], v[140:143]\n"
  "s_nop 0\n"
  "buffer_load_dwordx4 v252, s[52:55], s68 offen sc1 lds\n"
  "v_cvt_pk_bf16_f32 v56, v176, v177\n"
  "v_mfma_f32_16x16x32_bf16 v[140:143], v[120:123], v[8:11], v[140:143]\n"
  "v_cvt_pk_bf16_f32 v57, v178, v179\n"
  "buffer_load_dwordx4 v252, s[52:55], s69 offen offset:1024 sc1 lds\n"
  "v_cvt_pk_bf16_f32 v58, v180, v181\n"
  "v_mfma_f32_16x16x32_bf16 v[140:143], v[124:127], v[12:15], v[140:143]\n"
  "v_cvt_pk_bf16_f32 v59, v182, v183\n"
  "buffer_load_dwordx4 v252, s[52:55], s70 offen offset:2048 sc1 lds\n"
  "v_cvt_pk_bf16_f32 v60, v184, v185\n"
  "v_mfma_f32_16x16x32_bf16 v[156:159], v[124:127], v[28:31], 0\n"
  "v_cvt_pk_bf16_f32 v61, v186, v187\n"
  "buffer_load_dwordx4 v252, s[52:55], s71 offen offset:3072 sc1 lds\n"
  "v_cvt_pk_bf16_f32 v62, v188, v189\n"
  "v_mfma_f32_16x16x32_bf16 v[156:159], v[120:123], v[24:27], v[156:159]\n"
  "v_cvt_pk_bf16_f32 v63, v190, v191\n"
  "global_load_dword v232, v233, s[56:57]\n"
  "v_add_u32 v233, s80, v233\n"
  "v_mfma_f32_16x16x32_bf16 v[156:159], v[116:119], v[20:23], v[156:159]\n"
  "v_min_u32 v233, v234, v233\n"
  "s_add_u32 s36, s36, 0x8000\n"
  "s_cmp_eq_u32 s36, 0x18000\n"
  "v_mfma_f32_16x16x32_bf16 v[156:159], v[112:115], v[16:19], v[156:159]\n"
  "s_cselect_b32 s36, 0, s36\n"
  "v_add_u32 v228, s36, v246\n"
  "s_nop 1\n"
  "v_mfma_f32_16x16x32_bf16 v[160:163], v[64:67], v[32:35], 0\n"
  "ds_read_b128 a[144:147], v228\n"
  "v_max3_f32 v214, v128, v129, v130\n"
  "v_max3_f32 v215, v144, v145, v146\n"
  "v_mfma_f32_16x16x32_bf16 v[160:163], v[68:71], v[36:39], v[160:163]\n"
  "ds_read_b128 a[148:151], v228 offset:8192\n"
  "v_max3_f32 v214, v214, v131, v132\n"
  "v_max3_f32 v215, v215, v147, v148\n"
  "v_max3_f32 v214, v214, v133, v134\n"
  "v_mfma_f32_16x16x32_bf16 v[160:163], v[72:75], v[40:43], v[160:163]\n"
  "ds_read_b128 a[152:155], v228 offset:256\n"
  "v_max3_f32 v215, v215, v149, v150\n"
  "v_max3_f32 v214, v214, v135, v136\n"
  "v_mfma_f32_16x16x32_bf16 v[160:163], v[76:79], v[44:47], v[160:163]\n"
  "ds_read_b128 a[156:159], v228 offset:8448\n"
  "v_max3_f32 v215, v215, v151, v152\n"
  "v_max3_f32 v214, v214, v137, v138\n"
  "v_max3_f32 v215, v215, v153, v154\n"
  "v_mfma_f32_16x16x32_bf16 v[176:179], v[76:79], v[60:63], 0\n"
  "ds_read_b128 v[76:79], v228 offset:28672\n"
  "v_max3_f32 v214, v214, v139, v140\n"
  "v_max3_f32 v215, v215, v155, v156\n"
  "v_mfma_f32_16x16x32_bf16 v[176:179], v[72:75], v[56:59], v[176:179]\n"
  "ds_read_b128 v[72:75], v228 offset:24576\n"
  "v_max3_f32 v214, v214, v141, v142\n"
  "v_max3_f32 v215, v215, v157, v158\n"
  "v_max_f32 v214, v214, v143\n"
  "v_mfma_f32_16x16x32_bf16 v[176:179], v[68:71], v[52:55], v[176:179]\n"
  "ds_read_b128 v[68:71], v228 offset:20480\n"
  "v_max_f32 v215, v215, v159\n"
  "v_mov_b32 v216, v214\n"
  "v_mfma_f32_16x16x32_bf16 v[176:179], v[64:67], v[48:51], v[176:179]\n"
  "ds_read_b128 v[64:67], v228 offset:16384\n"
  "v_mov_b32 v217, v215\n"
  "s_nop 1\n"
  "v_permlane32_swap_b32 v214, v216\n"
  "v_mfma_f32_16x16x32_bf16 v[164:167], v[80:83], v[32:35], 0\n"
  "ds_read_b128 a[160:163], v228 offset:512\n"
  "v_permlane32_swap_b32 v215, v217\n"
  "s_nop 0\n"
  "v_mfma_f32_16x16x32_bf16 v[164:167], v[84:87], v[36:39], v[164:167]\n"
  "ds_read_b128 a[164:167], v228 offset:8704\n"
  "v_max_f32 v214, v214, v216\n"
  "v_max_f32 v215, v215, v217\n"
  "v_mov_b32 v216, v214\n"
  "v_mfma_f32_16x16x32_bf16 v[164:167], v[88:91], v[40:43], v[164:167]\n"
  "ds_read_b128 a[168:171], v228 offset:768\n"
  "v_mov_b32 v217, v215\n"
  "s_nop 1\n"
  "v_mfma_f32_16x16x32_bf16 v[164:167], v[92:95], v[44:47], v[164:167]\n"
  "ds_read_b128 a[172:175], v228 offset:8960\n"
  "v_permlane16_swap_b32 v214, v216\n"
  "v_permlane16_swap_b32 v215, v217\n"
  "s_nop 0\n"
  "v_mfma_f32_16x16x32_bf16 v[180:183], v[92:95], v[60:63], 0\n"
  "ds_read_b128 v[92:95], v228 offset:28928\n"
  "v_max_f32 v214, v214, v216\n"
  "v_max_f32 v215, v215, v217\n"
  "v_mfma_f32_16x16x32_bf16 v[180:183], v[88:91], v[56:59], v[180:183]\n"
  "ds_read_b128 v[88:91], v228 offset:24832\n"
  "v_sub_f32 v128, v128, v214\n"
  "v_sub_f32 v129, v129, v214\n"
  "v_sub_f32 v130, v130, v214\n"
  "v_mfma_f32_16x16x32_bf16 v[180:183], v[84:87], v[52:55], v[180:183]\n"
  "ds_read_b128 v[84:87], v228 offset:20736\n"
  "v_sub_f32 v131, v131, v214\n"
  "v_sub_f32 v132, v132, v214\n"
  "v_mfma_f32_16x16x32_bf16 v[180:183], v[80:83], v[48:51], v[180:183]\n"
  "ds_read_b128 v[80:83], v228 offset:16640\n"
  "v_sub_f32 v133, v133, v214\n"
  "v_sub_f32 v134, v134, v214\n"
  "v_sub_f32 v135, v135, v214\n"
  "v_mfma_f32_16x16x32_bf16 v[168:171], v[96:99], v[32:35], 0\n"
  "ds_read_b128 a[176:179], v228 offset:4096\n"
  "v_sub_f32 v136, v136, v214\n"
  "v_sub_f32 v137, v137, v214\n"
  "v_mfma_f32_16x16x32_bf16 v[168:171], v[100:103], v[36:39], v[168:171]\n"
  "ds_read_b128 a[180:183], v228 offset:12288\n"
  "v_sub_f32 v138, v138, v214\n"
  "v_sub_f32 v139, v139, v214\n"
  "v_sub_f32 v140, v140, v214\n"
  "v_mfma_f32_16x16x32_bf16 v[168:171], v[104:107], v[40:43], v[168:171]\n"
  "ds_read_b128 a[184:187], v228 offset:4352\n"
  "v_sub_f32 v141, v141, v214\n"
  "v_sub_f32 v142, v142, v214\n"
  "v_mfma_f32_16x16x32_bf16 v[168:171], v[108:111], v[44:47], v[168:171]\n"
  "ds_read_b128 a[188:191], v228 offset:12544\n"
  "v_sub_f32 v143, v143, v214\n"
  "v_sub_f32 v144, v144, v215\n"
  "v_sub_f32 v145, v145, v215\n"
  "v_mfma_f32_16x16x32_bf16 v[184:187], v[108:111], v[60:63], 0\n"
  "ds_read_b128 v[108:111], v228 offset:29184\n"
  "v_sub_f32 v146, v146, v215\n"
  "v_sub_f32 v147, v147, v215\n"
  "v_mfma_f32_16x16x32_bf16 v[184:187], v[104:107], v[56:59], v[184:187]\n"
  "ds_read_b128 v[104:107], v228 offset:25088\n"
  "v_sub_f32 v148, v148, v215\n"
  "v_sub_f32 v149, v149, v215\n"
  "v_sub_f32 v150, v150, v215\n"
  "v_mfma_f32_16x16x32_bf16 v[184:187], v[100:103], v[52:55], v[184:187]\n"
  "ds_read_b128 v[100:103], v228 offset:20992\n"
  "v_sub_f32 v151, v151, v215\n"
  "v_sub_f32 v152, v152, v215\n"
  "v_mfma_f32_16x16x32_bf16 v[184:187], v[96:99], v[48:51], v[184:187]\n"
  "ds_read_b128 v[96:99], v228 offset:16896\n"
  "v_sub_f32 v153, v153, v215\n"
  "v_sub_f32 v154, v154, v215\n"
  "v_sub_f32 v155, v155, v215\n"
  "v_mfma_f32_16x16x32_bf16 v[172:175], v[112:115], v[32:35], 0\n"
  "ds_read_b128 a[192:195], v228 offset:4608\n"
  "v_sub_f32 v156, v156, v215\n"
  "v_sub_f32 v157, v157, v215\n"
  "v_mfma_f32_16x16x32_bf16 v[172:175], v[116:119], v[36:39], v[172:175]\n"
  "ds_read_b128 a[196:199], v228 offset:12800\n"
  "v_sub_f32 v158, v158, v215\n"
  "v_sub_f32 v159, v159, v215\n"
  "v_sub_f32 v192, 0, v214\n"
  "v_mfma_f32_16x16x32_bf16 v[172:175], v[120:123], v[40:43], v[172:175]\n"
  "ds_read_b128 a[200:203], v228 offset:4864\n"
  "v_sub_f32 v193, 0, v214\n"
  "v_sub_f32 v194, 0, v214\n"
  "v_mfma_f32_16x16x32_bf16 v[172:175], v[124:127], v[44:47], v[172:175]\n"
  "ds_read_b128 a[204:207], v228 offset:13056\n"
  "v_sub_f32 v195, 0, v214\n"
  "v_sub_f32 v196, 0, v215\n"
  "v_sub_f32 v197, 0, v215\n"
  "v_mfma_f32_16x16x32_bf16 v[188:191], v[124:127], v[60:63], 0\n"
  "ds_read_b128 v[124:127], v228 offset:29440\n"
  "v_sub_f32 v198, 0, v215\n"
  "v_sub_f32 v199, 0, v215\n"
  "v_mfma_f32_16x16x32_bf16 v[188:191], v[120:123], v[56:59], v[188:191]\n"
  "ds_read_b128 v[120:123], v228 offset:25344\n"
  "v_exp_f32 v128, v128\n"
  "v_exp_f32 v129, v129\n"
  "v_exp_f32 v130, v130\n"
  "v_mfma_f32_16x16x32_bf16 v[188:191], v[116:119], v[52:55], v[188:191]\n"
  "ds_read_b128 v[116:119], v228 offset:21248\n"
  "v_cvt_pk_bf16_f32 v128, v128, v129\n"
  "v_exp_f32 v131, v131\n"
  "v_mfma_f32_16x16x32_bf16 v[188:191], v[112:115], v[48:51], v[188:191]\n"
  "ds_read_b128 v[112:115], v228 offset:17152\n"
  "v_exp_f32 v132, v132\n"
  "v_cvt_pk_bf16_f32 v129, v130, v131\n"
  "v_exp_f32 v133, v133\n"
  "v_exp_f32 v134, v134\n"
  "v_cvt_pk_bf16_f32 v130, v132, v133\n"
  "v_exp_f32 v135, v135\n"
  "v_exp_f32 v136, v136\n"
  "v_cvt_pk_bf16_f32 v131, v134, v135\n"
  "v_exp_f32 v137, v137\n"
  "v_exp_f32 v138, v138\n"
  "v_cvt_pk_bf16_f32 v136, v136, v137\n"
  "v_exp_f32 v139, v139\n"
  "v_exp_f32 v140, v140\n"
  "v_cvt_pk_bf16_f32 v137, v138, v139\n"
  "v_exp_f32 v141, v141\n"
  "v_exp_f32 v142, v142\n"
  "v_cvt_pk_bf16_f32 v138, v140, v141\n"
  "v_exp_f32 v143, v143\n"
  "s_nop 0\n"
  "v_cvt_pk_bf16_f32 v139, v142, v143\n"
  "s_nop 1\n"
  "s_waitcnt lgkmcnt(12)\n"
  "v_mfma_f32_16x16x32_bf16 a[0:3], a[144:147], v[128:131], a[0:3]\n"
  "v_exp_f32 v144, v144\n"
  "v_mfma_f32_16x16x32_bf16 a[0:3], a[148:151], v[136:139], a[0:3]\n"
  "v_exp_f32 v145, v145\n"
  "v_mfma_f32_16x16x32_bf16 a[16:19], a[156:159], v[136:139], a[16:19]\n"
  "v_exp_f32 v146, v146\n"
  "v_cvt_pk_bf16_f32 v144, v144, v145\n"
  "v_mfma_f32_16x16x32_bf16 a[16:19], a[152:155], v[128:131], a[16:19]\n"
  "v_exp_f32 v147, v147\n"
  "v_mfma_f32_16x16x32_bf16 a[32:35], a[160:163], v[128:131], a[32:35]\n"
  "v_exp_f32 v148, v148\n"
  "v_cvt_pk_bf16_f32 v145, v146, v147\n"
  "v_mfma_f32_16x16x32_bf16 a[32:35], a[164:167], v[136:139], a[32:35]\n"
  "v_exp_f32 v149, v149\n"
  "v_mfma_f32_16x16x32_bf16 a[48:51], a[172:175], v[136:139], a[48:51]\n"
  "v_exp_f32 v150, v150\n"
  "v_cvt_pk_bf16_f32 v146, v148, v149\n"
  "v_mfma_f32_16x16x32_bf16 a[48:51], a[168:171], v[128:131], a[48:51]\n"
  "v_exp_f32 v151, v151\n"
  "v_mfma_f32_16x16x32_bf16 a[64:67], a[176:179], v[128:131], a[64:67]\n"
  "v_exp_f32 v152, v152\n"
  "v_cvt_pk_bf16_f32 v147, v150, v151\n"
  "v_mfma_f32_16x16x32_bf16 a[64:67], a[180:183], v[136:139], a[64:67]\n"
  "v_exp_f32 v153, v153\n"
  "v_mfma_f32_16x16x32_bf16 a[80:83], a[188:191], v[136:139], a[80:83]\n"
  "v_exp_f32 v154, v154\n"
  "v_mfma_f32_16x16x32_bf16 a[80:83], a[184:187], v[128:131], a[80:83]\n"
  "v_cvt_pk_bf16_f32 v152, v152, v153\n"
  "v_exp_f32 v155, v155\n"
  "s_waitcnt lgkmcnt(7)\n"
  "v_mfma_f32_16x16x32_bf16 a[96:99], a[192:195], v[128:131], a[96:99]\n"
  "v_exp_f32 v156, v156\n"
  "s_waitcnt lgkmcnt(6)\n"
  "v_mfma_f32_16x16x32_bf16 a[96:99], a[196:199], v[136:139], a[96:99]\n"
  "v_cvt_pk_bf16_f32 v153, v154, v155\n"
  "v_exp_f32 v157, v157\n"
  "s_waitcnt lgkmcnt(4)\n"
  "v_mfma_f32_16x16x32_bf16 a[112:115], a[204:207], v[136:139], a[112:115]\n"
  "v_exp_f32 v158, v158\n"
  "v_mfma_f32_16x16x32_bf16 a[112:115], a[200:203], v[128:131], a[112:115]\n"
  "v_cvt_pk_bf16_f32 v154, v156, v157\n"
  "v_exp_f32 v159, v159\n"
  "v_mfma_f32_16x16x32_bf16 a[128:131], v[208:211], v[128:131], a[128:131]\n"
  "s_nop 0\n"
  "v_mfma_f32_16x16x32_bf16 a[128:131], v[208:211], v[136:139], a[128:131]\n"
  "v_cvt_pk_bf16_f32 v155, v158, v159\n"
  "s_nop 1\n"
  "v_mfma_f32_16x16x32_bf16 a[4:7], a[144:147], v[144:147], a[4:7]\n"
  "v_max3_f32 v214, v160, v161, v162\n"
  "v_max3_f32 v215, v176, v177, v178\n"
  "v_max3_f32 v214, v214, v163, v164\n"
  "v_max3_f32 v215, v215, v179, v180\n"
  "v_mfma_f32_16x16x32_bf16 a[4:7], a[148:151], v[152:155], a[4:7]\n"
  "v_max3_f32 v214, v214, v165, v166\n"
  "v_max3_f32 v215, v215, v181, v182\n"
  "v_max3_f32 v214, v214, v167, v168\n"
  "v_max3_f32 v215, v215, v183, v184\n"
  "v_mfma_f32_16x16x32_bf16 a[20:23], a[156:159], v[152:155], a[20:23]\n"
  "v_max3_f32 v214, v214, v169, v170\n"
  "v_max3_f32 v215, v215, v185, v186\n"
  "v_max3_f32 v214, v214, v171, v172\n"
  "v_max3_f32 v215, v215, v187, v188\n"
  "v_mfma_f32_16x16x32_bf16 a[20:23], a[152:155], v[144:147], a[20:23]\n"
  "v_max3_f32 v214, v214, v173, v174\n"
  "v_max3_f32 v215, v215, v189, v190\n"
  "v_max_f32 v214, v214, v175\n"
  "v_max_f32 v215, v215, v191\n"
  "v_mfma_f32_16x16x32_bf16 a[36:39], a[160:163], v[144:147], a[36:39]\n"
  "v_mov_b32 v216, v214\n"
  "v_mov_b32 v217, v215\n"
  "s_nop 1\n"
  "v_permlane32_swap_b32 v214, v216\n"
  "v_mfma_f32_16x16x32_bf16 a[36:39], a[164:167], v[152:155], a[36:39]\n"
  "v_permlane32_swap_b32 v215, v217\n"
  "s_nop 0\n"
  "v_max_f32 v214, v214, v216\n"
  "v_max_f32 v215, v215, v217\n"
  "v_mfma_f32_16x16x32_bf16 a[52:55], a[172:175], v[152:155], a[52:55]\n"
  "v_mov_b32 v216, v214\n"
  "v_mov_b32 v217, v215\n"
  "s_nop 1\n"
  "v_permlane16_swap_b32 v214, v216\n"
  "v_mfma_f32_16x16x32_bf16 a[52:55], a[168:171], v[144:147], a[52:55]\n"
  "v_permlane16_swap_b32 v215, v217\n"
  "s_nop 0\n"
  "v_max_f32 v214, v214, v216\n"
  "v_max_f32 v215, v215, v217\n"
  "v_mfma_f32_16x16x32_bf16 a[68:71], a[176:179], v[144:147], a[68:71]\n"
  "v_sub_f32 v160, v160, v214\n"
  "v_sub_f32 v161, v161, v214\n"
  "v_sub_f32 v162, v162, v214\n"
  "v_sub_f32 v163, v163, v214\n"
  "v_mfma_f32_16x16x32_bf16 a[68:71], a[180:183], v[152:155], a[68:71]\n"
  "v_sub_f32 v164, v164, v214\n"
  "v_sub_f32 v165, v165, v214\n"
  "v_sub_f32 v166, v166, v214\n"
  "v_sub_f32 v167, v167, v214\n"
  "v_mfma_f32_16x16x32_bf16 a[84:87], a[188:191], v[152:155], a[84:87]\n"
  "v_sub_f32 v168, v168, v214\n"
  "v_sub_f32 v169, v169, v214\n"
  "v_sub_f32 v170, v170, v214\n"
  "v_sub_f32 v171, v171, v214\n"
  "v_mfma_f32_16x16x32_bf16 a[84:87], a[184:187], v[144:147], a[84:87]\n"
  "v_sub_f32 v172, v172, v214\n"
  "v_sub_f32 v173, v173, v214\n"
  "v_sub_f32 v174, v174, v214\n"
  "v_sub_f32 v175, v175, v214\n"
  "v_mfma_f32_16x16x32_bf16 a[100:103], a[192:195], v[144:147], a[100:103]\n"
  "v_sub_f32 v176, v176, v215\n"
  "v_sub_f32 v177, v177, v215\n"
  "v_sub_f32 v178, v178, v215\n"
  "v_sub_f32 v179, v179, v215\n"
  "v_mfma_f32_16x16x32_bf16 a[100:103], a[196:199], v[152:155], a[100:103]\n"
  "v_sub_f32 v180, v180, v215\n"
  "v_sub_f32 v181, v181, v215\n"
  "v_sub_f32 v182, v182, v215\n"
  "v_sub_f32 v183, v183, v215\n"
  "v_mfma_f32_16x16x32_bf16 a[116:119], a[204:207], v[152:155], a[116:119]\n"
  "v_sub_f32 v184, v184, v215\n"
  "v_sub_f32 v185, v185, v215\n"
  "v_sub_f32 v186, v186, v215\n"
  "v_sub_f32 v187, v187, v215\n"
  "v_mfma_f32_16x16x32_bf16 a[116:119], a[200:203], v[144:147], a[116:119]\n"
  "v_sub_f32 v188, v188, v215\n"
  "v_sub_f32 v189, v189, v215\n"
  "v_sub_f32 v190, v190, v215\n"
  "v_sub_f32 v191, v191, v215\n"
  "v_mfma_f32_16x16x32_bf16 a[132:135], v[208:211], v[144:147], a[132:135]\n"
  "v_sub_f32 v200, 0, v214\n"
  "v_sub_f32 v201, 0, v214\n"
  "v_sub_f32 v202, 0, v214\n"
  "v_sub_f32 v203, 0, v214\n"
  "v_mfma_f32_16x16x32_bf16 a[132:135], v[208:211], v[152:155], a[132:135]\n"
  "v_sub_f32 v204, 0, v215\n"
  "v_sub_f32 v205, 0, v215\n"
  "v_sub_f32 v206, 0, v215\n"
  "v_sub_f32 v207, 0, v215\n"
  "v_exp_f32 v160, v160\n"
  "v_exp_f32 v161, v161\n"
  "v_exp_f32 v162, v162\n"
  "v_cvt_pk_bf16_f32 v160, v160, v161\n"
  "v_exp_f32 v163, v163\n"
  "v_exp_f32 v164, v164\n"
  "v_exp_f32 v165, v165\n"
  "v_exp_f32 v166, v166\n"
  "v_exp_f32 v167, v167\n"
  "v_exp_f32 v168, v168\n"
  "v_exp_f32 v169, v169\n"
  "v_exp_f32 v170, v170\n"
  "v_exp_f32 v171, v171\n"
  "v_exp_f32 v172, v172\n"
  "v_exp_f32 v173, v173\n"
  "v_exp_f32 v174, v174\n"
  "v_exp_f32 v175, v175\n"
  "v_exp_f32 v176, v176\n"
  "v_exp_f32 v177, v177\n"
  "v_exp_f32 v178, v178\n"
  "v_exp_f32 v179, v179\n"
  "v_exp_f32 v180, v180\n"
  "v_exp_f32 v181, v181\n"
  "v_exp_f32 v182, v182\n"
  "v_exp_f32 v183, v183\n"
  "s_cmp_eq_u32 s42, 0\n"
  "s_cbranch_scc1 L_at_m%=\n"
  "L_kevr_l%=:\n"
  "s_min_u32 s82, s42, s94\n"
  "s_sub_u32 s42, s42, s82\n"
  "s_sub_u32 s82, s82, 1\n"
  "L_at_loop%=:\n"
  "s_waitcnt vmcnt(9) lgkmcnt(0)\n"
  "s_barrier\n"
  "v_mfma_f32_16x16x32_bf16 v[128:131], v[64:67], v[0:3], v[192:195]\n"
  "s_add_u32 s52, s52, s81\n"
  "s_addc_u32 s53, s53, 0\n"
  "v_mfma_f32_16x16x32_bf16 v[128:131], v[68:71], v[4:7], v[128:131]\n"
  "s_sub_u32 s85, s85, 1\n"
  "s_cselect_b32 s54, 0, s54\n"
  "v_mfma_f32_16x16x32_bf16 v[128:131], v[72:75], v[8:11], v[128:131]\n"
  "s_add_u32 m0, s83, s36\n"
  "v_cvt_pk_bf16_f32 v161, v162, v163\n"
  "v_mfma_f32_16x16x32_bf16 v[128:131], v[76:79], v[12:15], v[128:131]\n"
  "buffer_load_dwordx4 v252, s[52:55], s64 offen sc1 lds\n"
  "v_cvt_pk_bf16_f32 v162, v164, v165\n"
  "v_mfma_f32_16x16x32_bf16 v[144:147], v[76:79], v[28:31], v[196:199]\n"
  "v_exp_f32 v184, v184\n"
  "v_mfma_f32_16x16x32_bf16 v[144:147], v[72:75], v[24:27], v[144:147]\n"
  "v_exp_f32 v185, v185\n"
  "v_mfma_f32_16x16x32_bf16 v[144:147], v[68:71], v[20:23], v[144:147]\n"
  "v_exp_f32 v186, v186\n"
  "v_mfma_f32_16x16x32_bf16 v[144:147], v[64:67], v[16:19], v[144:147]\n"
  "buffer_load_dwordx4 v252, s[52:55], s65 offen offset:1024 sc1 lds\n"
  "v_cvt_pk_bf16_f32 v163, v166, v167\n"
  "v_mfma_f32_16x16x32_bf16 v[132:135], v[80:83], v[0:3], v[192:195]\n"
  "v_exp_f32 v187, v187\n"
  "v_mfma_f32_16x16x32_bf16 v[132:135], v[84:87], v[4:7], v[132:135]\n"
  "v_exp_f32 v188, v188\n"
  "v_mfma_f32_16x16x32_bf16 v[132:135], v[88:91], v[8:11], v[132:135]\n"
  "v_exp_f32 v189, v189\n"
  "v_mfma_f32_16x16x32_bf16 v[132:135], v[92:95], v[12:15], v[132:135]\n"
  "buffer_load_dwordx4 v252, s[52:55], s66 offen offset:2048 sc1 lds\n"
  "v_cvt_pk_bf16_f32 v168, v168, v169\n"
  "v_mfma_f32_16x16x32_bf16 v[148:151], v[92:95], v[28:31], v[196:199]\n"
  "v_exp_f32 v190, v190\n"
  "v_mfma_f32_16x16x32_bf16 v[148:151], v[88:91], v[24:27], v[148:151]\n"
  "v_exp_f32 v191, v191\n"
  "v_mfma_f32_16x16x32_bf16 v[148:151], v[84:87], v[20:23], v[148:151]\n"
  "v_max3_f32 v212, v128, v129, v130\n"
  "v_max3_f32 v212, v212, v131, v144\n"
  "v_mfma_f32_16x16x32_bf16 v[148:151], v[80:83], v[16:19], v[148:151]\n"
  "buffer_load_dwordx4 v252, s[52:55], s67 offen offset:3072 sc1 lds\n"
  "v_max3_f32 v212, v212, v145, v146\n"
  "v_mfma_f32_16x16x32_bf16 v[136:139], v[96:99], v[0:3], v[192:195]\n"
  "s_add_u32 m0, m0, 0x1000\n"
  "v_cvt_pk_bf16_f32 v169, v170, v171\n"
  "v_mfma_f32_16x16x32_bf16 v[136:139], v[100:103], v[4:7], v[136:139]\n"
  "v_cvt_pk_bf16_f32 v170, v172, v173\n"
  "v_cvt_pk_bf16_f32 v171, v174, v175\n"
  "v_mfma_f32_16x16x32_bf16 v[136:139], v[104:107], v[8:11], v[136:139]\n"
  "v_cvt_pk_bf16_f32 v176, v176, v177\n"
  "v_cvt_pk_bf16_f32 v177, v178, v179\n"
  "v_mfma_f32_16x16x32_bf16 v[136:139], v[108:111], v[12:15], v[136:139]\n"
  "buffer_load_dwordx4 v252, s[52:55], s68 offen sc1 lds\n"
  "v_cvt_pk_bf16_f32 v178, v180, v181\n"
  "v_mfma_f32_16x16x32_bf16 v[152:155], v[108:111], v[28:31], v[196:199]\n"
  "v_cvt_pk_bf16_f32 v179, v182, v183\n"
  "v_cvt_pk_bf16_f32 v184, v184, v185\n"
  "v_mfma_f32_16x16x32_bf16 v[152:155], v[104:107], v[24:27], v[152:155]\n"
  "v_cvt_pk_bf16_f32 v185, v186, v187\n"
  "v_cvt_pk_bf16_f32 v186, v188, v189\n"
  "v_mfma_f32_16x16x32_bf16 v[152:155], v[100:103], v[20:23], v[152:155]\n"
  "v_max3_f32 v212, v212, v147, v132\n"
  "v_max3_f32 v212, v212, v133, v134\n"
  "v_mfma_f32_16x16x32_bf16 v[152:155], v[96:99], v[16:19], v[152:155]\n"
  "buffer_load_dwordx4 v252, s[52:55], s69 offen offset:1024 sc1 lds\n"
  "v_max3_f32 v212, v212, v135, v148\n"
  "v_mfma_f32_16x16x32_bf16 v[140:143], v[112:115], v[0:3], v[192:195]\n"
  "v_max3_f32 v212, v212, v149, v150\n"
  "v_cvt_pk_bf16_f32 v187, v190, v191\n"
  "v_mfma_f32_16x16x32_bf16 v[140:143], v[116:119], v[4:7], v[140:143]\n"
  "v_mfma_f32_16x16x32_bf16 v[140:143], v[120:123], v[8:11], v[140:143]\n"
  "v_mfma_f32_16x16x32_bf16 v[140:143], v[124:127], v[12:15], v[140:143]\n"
  "buffer_load_dwordx4 v252, s[52:55], s70 offen offset:2048 sc1 lds\n"
  "v_mfma_f32_16x16x32_bf16 v[156:159], v[124:127], v[28:31], v[196:199]\n"
  "v_mfma_f32_16x16x32_bf16 v[156:159], v[120:123], v[24:27], v[156:159]\n"
  "v_mfma_f32_16x16x32_bf16 v[156:159], v[116:119], v[20:23], v[156:159]\n"
  "v_max3_f32 v212, v212, v151, v136\n"
  "v_max3_f32 v212, v212, v137, v138\n"
  "v_mfma_f32_16x16x32_bf16 v[156:159], v[112:115], v[16:19], v[156:159]\n"
  "buffer_load_dwordx4 v252, s[52:55], s71 offen offset:3072 sc1 lds\n"
  "v_max3_f32 v212, v212, v139, v152\n"
  "v_mfma_f32_16x16x32_bf16 a[8:11], a[144:147], v[160:163], a[8:11]\n"
  "v_max3_f32 v212, v212, v153, v154\n"
  "s_add_u32 s36, s36, 0x8000\n"
  "v_mfma_f32_16x16x32_bf16 a[8:11], a[148:151], v[168:171], a[8:11]\n"
  "s_cmp_eq_u32 s36, 0x18000\n"
  "s_cselect_b32 s36, 0, s36\n"
  "v_mfma_f32_16x16x32_bf16 a[24:27], a[156:159], v[168:171], a[24:27]\n"
  "global_load_dword v232, v233, s[56:57]\n"
  "v_add_u32 v228, s36, v246\n"
  "v_mfma_f32_16x16x32_bf16 a[24:27], a[152:155], v[160:163], a[24:27]\n"
  "v_add_u32 v233, s80, v233\n"
  "v_min_u32 v233, v234, v233\n"
  "v_mfma_f32_16x16x32_bf16 a[40:43], a[160:163], v[160:163], a[40:43]\n"
  "v_mfma_f32_16x16x32_bf16 a[40:43], a[164:167], v[168:171], a[40:43]\n"
  "v_mfma_f32_16x16x32_bf16 a[56:59], a[172:175], v[168:171], a[56:59]\n"
  "v_max3_f32 v212, v212, v155, v140\n"
  "v_max3_f32 v212, v212, v141, v142\n"
  "v_mfma_f32_16x16x32_bf16 a[56:59], a[168:171], v[160:163], a[56:59]\n"
  "v_max3_f32 v212, v212, v143, v156\n"
  "v_max3_f32 v212, v212, v157, v158\n"
  "v_mfma_f32_16x16x32_bf16 a[72:75], a[176:179], v[160:163], a[72:75]\n"
  "v_max_f32 v212, v212, v159\n"
  "v_cmp_lt_f32 vcc, s37, v212\n"
  "v_mfma_f32_16x16x32_bf16 a[72:75], a[180:183], v[168:171], a[72:75]\n"
  "v_mfma_f32_16x16x32_bf16 a[88:91], a[188:191], v[168:171], a[88:91]\n"
  "s_cbranch_vccnz L_tr1%=\n"
  "L_bk1%=:\n"
  "v_mfma_f32_16x16x32_bf16 a[88:91], a[184:187], v[160:163], a[88:91]\n"
  "v_exp_f32 v128, v128\n"
  "v_mfma_f32_16x16x32_bf16 a[104:107], a[192:195], v[160:163], a[104:107]\n"
  "v_exp_f32 v129, v129\n"
  "v_mfma_f32_16x16x32_bf16 a[104:107], a[196:199], v[168:171], a[104:107]\n"
  "v_exp_f32 v130, v130\n"
  "v_mfma_f32_16x16x32_bf16 a[120:123], a[204:207], v[168:171], a[120:123]\n"
  "v_exp_f32 v131, v131\n"
  "v_mfma_f32_16x16x32_bf16 a[120:123], a[200:203], v[160:163], a[120:123]\n"
  "v_exp_f32 v132, v132\n"
  "v_mfma_f32_16x16x32_bf16 a[136:139], v[208:211], v[160:163], a[136:139]\n"
  "v_exp_f32 v133, v133\n"
  "v_mfma_f32_16x16x32_bf16 a[136:139], v[208:211], v[168:171], a[136:139]\n"
  "v_exp_f32 v134, v134\n"
  "v_mfma_f32_16x16x32_bf16 a[12:15], a[144:147], v[176:179], a[12:15]\n"
  "v_exp_f32 v135, v135\n"
  "v_mfma_f32_16x16x32_bf16 a[12:15], a[148:151], v[184:187], a[12:15]\n"
  "ds_read_b128 a[144:147], v228\n"
  "v_cvt_pk_bf16_f32 v128, v128, v129\n"
  "v_mfma_f32_16x16x32_bf16 a[28:31], a[156:159], v[184:187], a[28:31]\n"
  "ds_read_b128 a[148:151], v228 offset:8192\n"
  "v_cvt_pk_bf16_f32 v129, v130, v131\n"
  "v_mfma_f32_16x16x32_bf16 a[28:31], a[152:155], v[176:179], a[28:31]\n"
  "ds_read_b128 a[152:155], v228 offset:256\n"
  "v_cvt_pk_bf16_f32 v130, v132, v133\n"
  "v_mfma_f32_16x16x32_bf16 a[44:47], a[160:163], v[176:179], a[44:47]\n"
  "ds_read_b128 a[156:159], v228 offset:8448\n"
  "v_cvt_pk_bf16_f32 v131, v134, v135\n"
  "v_mfma_f32_16x16x32_bf16 a[44:47], a[164:167], v[184:187], a[44:47]\n"
  "v_exp_f32 v136, v136\n"
  "v_mfma_f32_16x16x32_bf16 a[60:63], a[172:175], v[184:187], a[60:63]\n"
  "v_exp_f32 v137, v137\n"
  "v_mfma_f32_16x16x32_bf16 a[60:63], a[168:171], v[176:179], a[60:63]\n"
  "ds_read_b128 a[160:163], v228 offset:512\n"
  "v_cvt_pk_bf16_f32 v136, v136, v137\n"
  "v_mfma_f32_16x16x32_bf16 a[76:79], a[176:179], v[176:179], a[76:79]\n"
  "v_exp_f32 v138, v138\n"
  "v_mfma_f32_16x16x32_bf16 a[76:79], a[180:183], v[184:187], a[76:79]\n"
  "v_exp_f32 v139, v139\n"
  "v_mfma_f32_16x16x32_bf16 a[92:95], a[188:191], v[184:187], a[92:95]\n"
  "ds_read_b128 a[164:167], v228 offset:8704\n"
  "v_cvt_pk_bf16_f32 v137, v138, v139\n"
  "v_mfma_f32_16x16x32_bf16 a[92:95], a[184:187], v[176:179], a[92:95]\n"
  "v_exp_f32 v140, v140\n"
  "v_mfma_f32_16x16x32_bf16 a[108:111], a[192:195], v[176:179], a[108:111]\n"
  "v_exp_f32 v141, v141\n"
  "v_mfma_f32_16x16x32_bf16 a[108:111], a[196:199], v[184:187], a[108:111]\n"
  "ds_read_b128 a[168:171], v228 offset:768\n"
  "v_cvt_pk_bf16_f32 v138, v140, v141\n"
  "v_mfma_f32_16x16x32_bf16 a[124:127], a[204:207], v[184:187], a[124:127]\n"
  "v_exp_f32 v142, v142\n"
  "v_mfma_f32_16x16x32_bf16 a[124:127], a[200:203], v[176:179], a[124:127]\n"
  "v_exp_f32 v143, v143\n"
  "v_mfma_f32_16x16x32_bf16 a[140:143], v[208:211], v[176:179], a[140:143]\n"
  "ds_read_b128 a[172:175], v228 offset:8960\n"
  "v_cvt_pk_bf16_f32 v139, v142, v143\n"
  "v_mfma_f32_16x16x32_bf16 a[140:143], v[208:211], v[184:187], a[140:143]\n"
  "v_exp_f32 v144, v144\n"
  "v_mfma_f32_16x16x32_bf16 v[160:163], v[64:67], v[32:35], v[200:203]\n"
  "v_exp_f32 v145, v145\n"
  "v_mfma_f32_16x16x32_bf16 v[160:163], v[68:71], v[36:39], v[160:163]\n"
  "ds_read_b128 a[176:179], v228 offset:4096\n"
  "v_cvt_pk_bf16_f32 v144, v144, v145\n"
  "v_mfma_f32_16x16x32_bf16 v[160:163], v[72:75], v[40:43], v[160:163]\n"
  "v_exp_f32 v146, v146\n"
  "v_mfma_f32_16x16x32_bf16 v[160:163], v[76:79], v[44:47], v[160:163]\n"
  "v_exp_f32 v147, v147\n"
  "v_mfma_f32_16x16x32_bf16 v[176:179], v[76:79], v[60:63], v[204:207]\n"
  "ds_read_b128 a[180:183], v228 offset:12288\n"
  "v_cvt_pk_bf16_f32 v145, v146, v147\n"
  "v_mfma_f32_16x16x32_bf16 v[176:179], v[72:75], v[56:59], v[176:179]\n"
  "v_exp_f32 v148, v148\n"
  "v_mfma_f32_16x16x32_bf16 v[176:179], v[68:71], v[52:55], v[176:179]\n"
  "v_exp_f32 v149, v149\n"
  "v_mfma_f32_16x16x32_bf16 v[176:179], v[64:67], v[48:51], v[176:179]\n"
  "ds_read_b128 a[184:187], v228 offset:4352\n"
  "v_cvt_pk_bf16_f32 v146, v148, v149\n"
  "v_mfma_f32_16x16x32_bf16 v[164:167], v[80:83], v[32:35], v[200:203]\n"
  "v_exp_f32 v150, v150\n"
  "v_mfma_f32_16x16x32_bf16 v[164:167], v[84:87], v[36:39], v[164:167]\n"
  "v_exp_f32 v151, v151\n"
  "v_mfma_f32_16x16x32_bf16 v[164:167], v[88:91], v[40:43], v[164:167]\n"
  "ds_read_b128 a[188:191], v228 offset:12544\n"
  "v_cvt_pk_bf16_f32 v147, v150, v151\n"
  "v_mfma_f32_16x16x32_bf16 v[164:167], v[92:95], v[44:47], v[164:167]\n"
  "v_exp_f32 v152, v152\n"
  "v_mfma_f32_16x16x32_bf16 v[180:183], v[92:95], v[60:63], v[204:207]\n"
  "v_exp_f32 v153, v153\n"
  "v_mfma_f32_16x16x32_bf16 v[180:183], v[88:91], v[56:59], v[180:183]\n"
  "ds_read_b128 a[192:195], v228 offset:4608\n"
  "v_cvt_pk_bf16_f32 v152, v152, v153\n"
  "v_mfma_f32_16x16x32_bf16 v[180:183], v[84:87], v[52:55], v[180:183]\n"
  "ds_read_b128 a[196:199], v228 offset:12800\n"
  "v_max3_f32 v213, v160, v161, v162\n"
  "v_mfma_f32_16x16x32_bf16 v[180:183], v[80:83], v[48:51], v[180:183]\n"
  "ds_read_b128 a[200:203], v228 offset:4864\n"
  "v_max3_f32 v213, v213, v163, v176\n"
  "v_mfma_f32_16x16x32_bf16 v[168:171], v[96:99], v[32:35], v[200:203]\n"
  "ds_read_b128 a[204:207], v228 offset:13056\n"
  "v_max3_f32 v213, v213, v177, v178\n"
  "v_mfma_f32_16x16x32_bf16 v[168:171], v[100:103], v[36:39], v[168:171]\n"
  "v_exp_f32 v154, v154\n"
  "v_mfma_f32_16x16x32_bf16 v[168:171], v[104:107], v[40:43], v[168:171]\n"
  "v_exp_f32 v155, v155\n"
  "v_mfma_f32_16x16x32_bf16 v[168:171], v[108:111], v[44:47], v[168:171]\n"
  "ds_read_b128 v[76:79], v228 offset:28672\n"
  "v_cvt_pk_bf16_f32 v153, v154, v155\n"
  "v_mfma_f32_16x16x32_bf16 v[184:187], v[108:111], v[60:63], v[204:207]\n"
  "v_exp_f32 v156, v156\n"
  "v_mfma_f32_16x16x32_bf16 v[184:187], v[104:107], v[56:59], v[184:187]\n"
  "v_exp_f32 v157, v157\n"
  "v_mfma_f32_16x16x32_bf16 v[184:187], v[100:103], v[52:55], v[184:187]\n"
  "ds_read_b128 v[72:75], v228 offset:24576\n"
  "v_max3_f32 v213, v213, v179, v164\n"
  "v_mfma_f32_16x16x32_bf16 v[184:187], v[96:99], v[48:51], v[184:187]\n"
  "ds_read_b128 v[68:71], v228 offset:20480\n"
  "v_max3_f32 v213, v213, v165, v166\n"
  "v_mfma_f32_16x16x32_bf16 v[172:175], v[112:115], v[32:35], v[200:203]\n"
  "ds_read_b128 v[64:67], v228 offset:16384\n"
  "v_max3_f32 v213, v213, v167, v180\n"
  "v_mfma_f32_16x16x32_bf16 v[172:175], v[116:119], v[36:39], v[172:175]\n"
  "ds_read_b128 v[92:95], v228 offset:28928\n"
  "v_max3_f32 v213, v213, v181, v182\n"
  "v_mfma_f32_16x16x32_bf16 v[172:175], v[120:123], v[40:43], v[172:175]\n"
  "ds_read_b128 v[88:91], v228 offset:24832\n"
  "v_cvt_pk_bf16_f32 v154, v156, v157\n"
  "v_mfma_f32_16x16x32_bf16 v[172:175], v[124:127], v[44:47], v[172:175]\n"
  "v_exp_f32 v158, v158\n"
  "v_mfma_f32_16x16x32_bf16 v[188:191], v[124:127], v[60:63], v[204:207]\n"
  "v_exp_f32 v159, v159\n"
  "v_mfma_f32_16x16x32_bf16 v[188:191], v[120:123], v[56:59], v[188:191]\n"
  "ds_read_b128 v[84:87], v228 offset:20736\n"
  "v_cvt_pk_bf16_f32 v155, v158, v159\n"
  "v_mfma_f32_16x16x32_bf16 v[188:191], v[116:119], v[52:55], v[188:191]\n"
  "ds_read_b128 v[80:83], v228 offset:16640\n"
  "v_max3_f32 v213, v213, v183, v168\n"
  "v_mfma_f32_16x16x32_bf16 v[188:191], v[112:115], v[48:51], v[188:191]\n"
  "ds_read_b128 v[108:111], v228 offset:29184\n"
  "v_max3_f32 v213, v213, v169, v170\n"
  "s_waitcnt lgkmcnt(12)\n"
  "v_mfma_f32_16x16x32_bf16 a[0:3], a[144:147], v[128:131], a[0:3]\n"
  "ds_read_b128 v[104:107], v228 offset:25088\n"
  "v_max3_f32 v213, v213, v171, v184\n"
  "v_mfma_f32_16x16x32_bf16 a[0:3], a[148:151], v[136:139], a[0:3]\n"
  "ds_read_b128 v[100:103], v228 offset:20992\n"
  "v_max3_f32 v213, v213, v185, v186\n"
  "v_mfma_f32_16x16x32_bf16 a[16:19], a[156:159], v[136:139], a[16:19]\n"
  "ds_read_b128 v[96:99], v228 offset:16896\n"
  "v_mfma_f32_16x16x32_bf16 a[16:19], a[152:155], v[128:131], a[16:19]\n"
  "ds_read_b128 v[124:127], v228 offset:29440\n"
  "v_mfma_f32_16x16x32_bf16 a[32:35], a[160:163], v[128:131], a[32:35]\n"
  "ds_read_b128 v[120:123], v228 offset:25344\n"
  "v_mfma_f32_16x16x32_bf16 a[32:35], a[164:167], v[136:139], a[32:35]\n"
  "ds_read_b128 v[116:119], v228 offset:21248\n"
  "v_mfma_f32_16x16x32_bf16 a[48:51], a[172:175], v[136:139], a[48:51]\n"
  "v_max3_f32 v213, v213, v187, v172\n"
  "v_max3_f32 v213, v213, v173, v174\n"
  "v_mfma_f32_16x16x32_bf16 a[48:51], a[168:171], v[128:131], a[48:51]\n"
  "v_max3_f32 v213, v213, v175, v188\n"
  "v_max3_f32 v213, v213, v189, v190\n"
  "v_mfma_f32_16x16x32_bf16 a[64:67], a[176:179], v[128:131], a[64:67]\n"
  "v_max_f32 v213, v213, v191\n"
  "v_cmp_lt_f32 vcc, s38, v213\n"
  "v_mfma_f32_16x16x32_bf16 a[64:67], a[180:183], v[136:139], a[64:67]\n"
  "ds_read_b128 v[112:115], v228 offset:17152\n"
  "v_mfma_f32_16x16x32_bf16 a[80:83], a[188:191], v[136:139], a[80:83]\n"
  "s_cbranch_vccnz L_tr0%=\n"
  "L_bk0%=:\n"
  "v_mfma_f32_16x16x32_bf16 a[80:83], a[184:187], v[128:131], a[80:83]\n"
  "v_exp_f32 v160, v160\n"
  "v_mfma_f32_16x16x32_bf16 a[96:99], a[192:195], v[128:131], a[96:99]\n"
  "v_exp_f32 v161, v161\n"
  "s_waitcnt lgkmcnt(12)\n"
  "v_mfma_f32_16x16x32_bf16 a[96:99], a[196:199], v[136:139], a[96:99]\n"
  "v_exp_f32 v162, v162\n"
  "v_mfma_f32_16x16x32_bf16 a[112:115], a[204:207], v[136:139], a[112:115]\n"
  "v_exp_f32 v163, v163\n"
  "v_mfma_f32_16x16x32_bf16 a[112:115], a[200:203], v[128:131], a[112:115]\n"
  "v_exp_f32 v164, v164\n"
  "v_mfma_f32_16x16x32_bf16 a[128:131], v[208:211], v[128:131], a[128:131]\n"
  "v_exp_f32 v165, v165\n"
  "v_mfma_f32_16x16x32_bf16 a[128:131], v[208:211], v[136:139], a[128:131]\n"
  "v_exp_f32 v166, v166\n"
  "v_mfma_f32_16x16x32_bf16 a[4:7], a[144:147], v[144:147], a[4:7]\n"
  "v_exp_f32 v167, v167\n"
  "v_mfma_f32_16x16x32_bf16 a[4:7], a[148:151], v[152:155], a[4:7]\n"
  "v_exp_f32 v168, v168\n"
  "v_mfma_f32_16x16x32_bf16 a[20:23], a[156:159], v[152:155], a[20:23]\n"
  "v_exp_f32 v169, v169\n"
  "v_mfma_f32_16x16x32_bf16 a[20:23], a[152:155], v[144:147], a[20:23]\n"
  "v_exp_f32 v170, v170\n"
  "v_mfma_f32_16x16x32_bf16 a[36:39], a[160:163], v[144:147], a[36:39]\n"
  "v_exp_f32 v171, v171\n"
  "v_mfma_f32_16x16x32_bf16 a[36:39], a[164:167], v[152:155], a[36:39]\n"
  "v_exp_f32 v172, v172\n"
  "v_mfma_f32_16x16x32_bf16 a[52:55], a[172:175], v[152:155], a[52:55]\n"
  "v_exp_f32 v173, v173\n"
  "v_mfma_f32_16x16x32_bf16 a[52:55], a[168:171], v[144:147], a[52:55]\n"
  "v_exp_f32 v174, v174\n"
  "v_mfma_f32_16x16x32_bf16 a[68:71], a[176:179], v[144:147], a[68:71]\n"
  "v_exp_f32 v175, v175\n"
  "v_mfma_f32_16x16x32_bf16 a[68:71], a[180:183], v[152:155], a[68:71]\n"
  "v_exp_f32 v176, v176\n"
  "v_mfma_f32_16x16x32_bf16 a[84:87], a[188:191], v[152:155], a[84:87]\n"
  "v_exp_f32 v177, v177\n"
  "v_mfma_f32_16x16x32_bf16 a[84:87], a[184:187], v[144:147], a[84:87]\n"
  "v_exp_f32 v178, v178\n"
  "v_mfma_f32_16x16x32_bf16 a[100:103], a[192:195], v[144:147], a[100:103]\n"
  "v_exp_f32 v179, v179\n"
  "v_mfma_f32_16x16x32_bf16 a[100:103], a[196:199], v[152:155], a[100:103]\n"
  "v_exp_f32 v180, v180\n"
  "v_mfma_f32_16x16x32_bf16 a[116:119], a[204:207], v[152:155], a[116:119]\n"
  "s_sub_u32 s82, s82, 1\n"
  "v_cvt_pk_bf16_f32 v160, v160, v161\n"
  "v_mfma_f32_16x16x32_bf16 a[116:119], a[200:203], v[144:147], a[116:119]\n"
  "v_exp_f32 v181, v181\n"
  "v_mfma_f32_16x16x32_bf16 a[132:135], v[208:211], v[144:147], a[132:135]\n"
  "v_exp_f32 v182, v182\n"
  "v_mfma_f32_16x16x32_bf16 a[132:135], v[208:211], v[152:155], a[132:135]\n"
  "v_exp_f32 v183, v183\n"
  "s_cbranch_scc0 L_at_loop%=\n"
  "s_cmp_lg_u32 s42, 0\n"
  "s_cbranch_scc1 L_kev_l%=\n"
  "L_at_m%=:\n"
  "s_xor_b32 s87, s44, 6\n"
  "s_or_b32 s87, s87, s34\n"
  "s_cbranch_scc1 L_at_nt%=\n"
  "s_add_u32 s87, s46, 0x5a00800\n"
  "v_mov_b32 v235, s87\n"
  "v_mov_b32 v238, 1\n"
  "s_mov_b64 exec, 1\n"
  "global_atomic_add v236, v235, v238, s[8:9] sc0 sc1\n"
  "s_mov_b64 exec, -1\n"
  "v_and_b32 v237, 15, v255\n"
  "v_lshl_add_u32 v237, v237, 7, s45\n"
  "global_load_dword v232, v237, s[32:33]\n"
  "L_at_nt%=:\n"
  "s_waitcnt vmcnt(9) lgkmcnt(0)\n"
  "s_barrier\n"
  "v_mfma_f32_16x16x32_bf16 v[128:131], v[64:67], v[0:3], v[192:195]\n"
  "s_add_u32 s52, s52, s81\n"
  "s_addc_u32 s53, s53, 0\n"
  "v_mfma_f32_16x16x32_bf16 v[128:131], v[68:71], v[4:7], v[128:131]\n"
  "s_sub_u32 s85, s85, 1\n"
  "s_cselect_b32 s54, 0, s54\n"
  "v_mfma_f32_16x16x32_bf16 v[128:131], v[72:75], v[8:11], v[128:131]\n"
  "s_add_u32 m0, s83, s36\n"
  "v_cvt_pk_bf16_f32 v161, v162, v163\n"
  "v_mfma_f32_16x16x32_bf16 v[128:131], v[76:79], v[12:15], v[128:131]\n"
  "buffer_load_dwordx4 v252, s[52:55], s64 offen sc1 lds\n"
  "v_cvt_pk_bf16_f32 v162, v164, v165\n"
  "v_mfma_f32_16x16x32_bf16 v[144:147], v[76:79], v[28:31], v[196:199]\n"
  "v_exp_f32 v184, v184\n"
  "v_mfma_f32_16x16x32_bf16 v[144:147], v[72:75], v[24:27], v[144:147]\n"
  "v_exp_f32 v185, v185\n"
  "v_mfma_f32_16x16x32_bf16 v[144:147], v[68:71], v[20:23], v[144:147]\n"
  "v_exp_f32 v186, v186\n"
  "v_mfma_f32_16x16x32_bf16 v[144:147], v[64:67], v[16:19], v[144:147]\n"
  "buffer_load_dwordx4 v252, s[52:55], s65 offen offset:1024 sc1 lds\n"
  "v_cvt_pk_bf16_f32 v163, v166, v167\n"
  "v_mfma_f32_16x16x32_bf16 v[132:135], v[80:83], v[0:3], v[192:195]\n"
  "v_exp_f32 v187, v187\n"
  "v_mfma_f32_16x16x32_bf16 v[132:135], v[84:87], v[4:7], v[132:135]\n"
  "v_exp_f32 v188, v188\n"
  "v_mfma_f32_16x16x32_bf16 v[132:135], v[88:91], v[8:11], v[132:135]\n"
  "v_exp_f32 v189, v189\n"
  "v_mfma_f32_16x16x32_bf16 v[132:135], v[92:95], v[12:15], v[132:135]\n"
  "buffer_load_dwordx4 v252, s[52:55], s66 offen offset:2048 sc1 lds\n"
  "v_cvt_pk_bf16_f32 v168, v168, v169\n"
  "v_mfma_f32_16x16x32_bf16 v[148:151], v[92:95], v[28:31], v[196:199]\n"
  "v_exp_f32 v190, v190\n"
  "v_mfma_f32_16x16x32_bf16 v[148:151], v[88:91], v[24:27], v[148:151]\n"
  "v_exp_f32 v191, v191\n"
  "v_mfma_f32_16x16x32_bf16 v[148:151], v[84:87], v[20:23], v[148:151]\n"
  "v_cmp_gt_i32 vcc, 0, v222\n"
  "v_cmp_gt_i32 s[90:91], 1, v222\n"
  "v_cmp_gt_i32 s[92:93], 2, v222\n"
  "v_cndmask_b32 v128, v128, v221, vcc\n"
  "v_cmp_gt_i32 vcc, 3, v222\n"
  "v_cndmask_b32 v129, v129, v221, s[90:91]\n"
  "v_cndmask_b32 v130, v130, v221, s[92:93]\n"
  "v_cndmask_b32 v131, v131, v221, vcc\n"
  "v_max3_f32 v212, v128, v129, v130\n"
  "v_max3_f32 v212, v212, v131, v144\n"
  "v_mfma_f32_16x16x32_bf16 v[148:151], v[80:83], v[16:19], v[148:151]\n"
  "buffer_load_dwordx4 v252, s[52:55], s67 offen offset:3072 sc1 lds\n"
  "v_max3_f32 v212, v212, v145, v146\n"
  "v_mfma_f32_16x16x32_bf16 v[136:139], v[96:99], v[0:3], v[192:195]\n"
  "s_add_u32 m0, m0, 0x1000\n"
  "v_cvt_pk_bf16_f32 v169, v170, v171\n"
  "v_mfma_f32_16x16x32_bf16 v[136:139], v[100:103], v[4:7], v[136:139]\n"
  "v_cvt_pk_bf16_f32 v170, v172, v173\n"
  "v_cvt_pk_bf16_f32 v171, v174, v175\n"
  "v_mfma_f32_16x16x32_bf16 v[136:139], v[104:107], v[8:11], v[136:139]\n"
  "v_cvt_pk_bf16_f32 v176, v176, v177\n"
  "v_cvt_pk_bf16_f32 v177, v178, v179\n"
  "v_mfma_f32_16x16x32_bf16 v[136:139], v[108:111], v[12:15], v[136:139]\n"
  "buffer_load_dwordx4 v252, s[52:55], s68 offen sc1 lds\n"
  "v_cvt_pk_bf16_f32 v178, v180, v181\n"
  "v_mfma_f32_16x16x32_bf16 v[152:155], v[108:111], v[28:31], v[196:199]\n"
  "v_cvt_pk_bf16_f32 v179, v182, v183\n"
  "v_cvt_pk_bf16_f32 v184, v184, v185\n"
  "v_mfma_f32_16x16x32_bf16 v[152:155], v[104:107], v[24:27], v[152:155]\n"
  "v_cvt_pk_bf16_f32 v185, v186, v187\n"
  "v_cvt_pk_bf16_f32 v186, v188, v189\n"
  "v_mfma_f32_16x16x32_bf16 v[152:155], v[100:103], v[20:23], v[152:155]\n"
  "v_cmp_gt_i32 vcc, 16, v222\n"
  "v_cmp_gt_i32 s[90:91], 17, v222\n"
  "v_cmp_gt_i32 s[92:93], 18, v222\n"
  "v_cndmask_b32 v132, v132, v221, vcc\n"
  "v_cmp_gt_i32 vcc, 19, v222\n"
  "v_cndmask_b32 v133, v133, v221, s[90:91]\n"
  "v_cndmask_b32 v134, v134, v221, s[92:93]\n"
  "v_cndmask_b32 v135, v135, v221, vcc\n"
  "v_cmp_gt_i32 vcc, 0, v222\n"
  "v_cmp_gt_i32 s[90:91], 1, v222\n"
  "v_cmp_gt_i32 s[92:93], 2, v222\n"
  "v_cndmask_b32 v148, v148, v221, vcc\n"
  "v_cmp_gt_i32 vcc, 3, v222\n"
  "v_cndmask_b32 v149, v149, v221, s[90:91]\n"
  "v_cndmask_b32 v150, v150, v221, s[92:93]\n"
  "v_cndmask_b32 v151, v151, v221, vcc\n"
  "v_max3_f32 v212, v212, v147, v132\n"
  "v_max3_f32 v212, v212, v133, v134\n"
  "v_mfma_f32_16x16x32_bf16 v[152:155], v[96:99], v[16:19], v[152:155]\n"
  "buffer_load_dwordx4 v252, s[52:55], s69 offen offset:1024 sc1 lds\n"
  "v_max3_f32 v212, v212, v135, v148\n"
  "v_mfma_f32_16x16x32_bf16 v[140:143], v[112:115], v[0:3], v[192:195]\n"
  "v_max3_f32 v212, v212, v149, v150\n"
  "v_cvt_pk_bf16_f32 v187, v190, v191\n"
  "v_mfma_f32_16x16x32_bf16 v[140:143], v[116:119], v[4:7], v[140:143]\n"
  "v_mfma_f32_16x16x32_bf16 v[140:143], v[120:123], v[8:11], v[140:143]\n"
  "v_mfma_f32_16x16x32_bf16 v[140:143], v[124:127], v[12:15], v[140:143]\n"
  "buffer_load_dwordx4 v252, s[52:55], s70 offen offset:2048 sc1 lds\n"
  "v_mfma_f32_16x16x32_bf16 v[156:159], v[124:127], v[28:31], v[196:199]\n"
  "v_mfma_f32_16x16x32_bf16 v[156:159], v[120:123], v[24:27], v[156:159]\n"
  "v_mfma_f32_16x16x32_bf16 v[156:159], v[116:119], v[20:23], v[156:159]\n"
  "v_cmp_gt_i32 vcc, 32, v222\n"
  "v_cmp_gt_i32 s[90:91], 33, v222\n"
  "v_cmp_gt_i32 s[92:93], 34, v222\n"
  "v_cndmask_b32 v136, v136, v221, vcc\n"
  "v_cmp_gt_i32 vcc, 35, v222\n"
  "v_cndmask_b32 v137, v137, v221, s[90:91]\n"
  "v_cndmask_b32 v138, v138, v221, s[92:93]\n"
  "v_cndmask_b32 v139, v139, v221, vcc\n"
  "v_cmp_gt_i32 vcc, 16, v222\n"
  "v_cmp_gt_i32 s[90:91], 17, v222\n"
  "v_cmp_gt_i32 s[92:93], 18, v222\n"
  "v_cndmask_b32 v152, v152, v221, vcc\n"
  "v_cmp_gt_i32 vcc, 19, v222\n"
  "v_cndmask_b32 v153, v153, v221, s[90:91]\n"
  "v_cndmask_b32 v154, v154, v221, s[92:93]\n"
  "v_cndmask_b32 v155, v155, v221, vcc\n"
  "v_max3_f32 v212, v212, v151, v136\n"
  "v_max3_f32 v212, v212, v137, v138\n"
  "v_mfma_f32_16x16x32_bf16 v[156:159], v[112:115], v[16:19], v[156:159]\n"
  "buffer_load_dwordx4 v252, s[52:55], s71 offen offset:3072 sc1 lds\n"
  "v_max3_f32 v212, v212, v139, v152\n"
  "v_mfma_f32_16x16x32_bf16 a[8:11], a[144:147], v[160:163], a[8:11]\n"
  "v_max3_f32 v212, v212, v153, v154\n"
  "s_add_u32 s36, s36, 0x8000\n"
  "v_mfma_f32_16x16x32_bf16 a[8:11], a[148:151], v[168:171], a[8:11]\n"
  "s_cmp_eq_u32 s36, 0x18000\n"
  "s_cselect_b32 s36, 0, s36\n"
  "v_mfma_f32_16x16x32_bf16 a[24:27], a[156:159], v[168:171], a[24:27]\n"
  "global_load_dword v232, v233, s[56:57]\n"
  "v_add_u32 v228, s36, v246\n"
  "v_mfma_f32_16x16x32_bf16 a[24:27], a[152:155], v[160:163], a[24:27]\n"
  "v_add_u32 v233, s80, v233\n"
  "v_min_u32 v233, v234, v233\n"
  "v_mfma_f32_16x16x32_bf16 a[40:43], a[160:163], v[160:163], a[40:43]\n"
  "v_mfma_f32_16x16x32_bf16 a[40:43], a[164:167], v[168:171], a[40:43]\n"
  "v_mfma_f32_16x16x32_bf16 a[56:59], a[172:175], v[168:171], a[56:59]\n"
  "v_mov_b32 v140, s39\n"
  "v_mov_b32 v141, s39\n"
  "v_mov_b32 v142, s39\n"
  "v_mov_b32 v143, s39\n"
  "v_cmp_gt_i32 vcc, 32, v222\n"
  "v_cmp_gt_i32 s[90:91], 33, v222\n"
  "v_cmp_gt_i32 s[92:93], 34, v222\n"
  "v_cndmask_b32 v156, v156, v221, vcc\n"
  "v_cmp_gt_i32 vcc, 35, v222\n"
  "v_cndmask_b32 v157, v157, v221, s[90:91]\n"
  "v_cndmask_b32 v158, v158, v221, s[92:93]\n"
  "v_cndmask_b32 v159, v159, v221, vcc\n"
  "v_max3_f32 v212, v212, v155, v140\n"
  "v_max3_f32 v212, v212, v141, v142\n"
  "v_mfma_f32_16x16x32_bf16 a[56:59], a[168:171], v[160:163], a[56:59]\n"
  "v_max3_f32 v212, v212, v143, v156\n"
  "v_max3_f32 v212, v212, v157, v158\n"
  "v_mfma_f32_16x16x32_bf16 a[72:75], a[176:179], v[160:163], a[72:75]\n"
  "v_max_f32 v212, v212, v159\n"
  "v_cmp_lt_f32 vcc, s37, v212\n"
  "v_mfma_f32_16x16x32_bf16 a[72:75], a[180:183], v[168:171], a[72:75]\n"
  "v_mfma_f32_16x16x32_bf16 a[88:91], a[188:191], v[168:171], a[88:91]\n"
  "s_cbranch_vccnz L_tr3%=\n"
  "L_bk3%=:\n"
  "v_mfma_f32_16x16x32_bf16 a[88:91], a[184:187], v[160:163], a[88:91]\n"
  "v_exp_f32 v128, v128\n"
  "v_mfma_f32_16x16x32_bf16 a[104:107], a[192:195], v[160:163], a[104:107]\n"
  "v_exp_f32 v129, v129\n"
  "v_mfma_f32_16x16x32_bf16 a[104:107], a[196:199], v[168:171], a[104:107]\n"
  "v_exp_f32 v130, v130\n"
  "v_mfma_f32_16x16x32_bf16 a[120:123], a[204:207], v[168:171], a[120:123]\n"
  "v_exp_f32 v131, v131\n"
  "v_mfma_f32_16x16x32_bf16 a[120:123], a[200:203], v[160:163], a[120:123]\n"
  "v_exp_f32 v132, v132\n"
  "v_mfma_f32_16x16x32_bf16 a[136:139], v[208:211], v[160:163], a[136:139]\n"
  "v_exp_f32 v133, v133\n"
  "v_mfma_f32_16x16x32_bf16 a[136:139], v[208:211], v[168:171], a[136:139]\n"
  "v_exp_f32 v134, v134\n"
  "v_mfma_f32_16x16x32_bf16 a[12:15], a[144:147], v[176:179], a[12:15]\n"
  "v_exp_f32 v135, v135\n"
  "v_mfma_f32_16x16x32_bf16 a[12:15], a[148:151], v[184:187], a[12:15]\n"
  "ds_read_b128 a[144:147], v228\n"
  "v_cvt_pk_bf16_f32 v128, v128, v129\n"
  "v_mfma_f32_16x16x32_bf16 a[28:31], a[156:159], v[184:187], a[28:31]\n"
  "ds_read_b128 a[148:151], v228 offset:8192\n"
  "v_cvt_pk_bf16_f32 v129, v130, v131\n"
  "v_mfma_f32_16x16x32_bf16 a[28:31], a[152:155], v[176:179], a[28:31]\n"
  "ds_read_b128 a[152:155], v228 offset:256\n"
  "v_cvt_pk_bf16_f32 v130, v132, v133\n"
  "v_mfma_f32_16x16x32_bf16 a[44:47], a[160:163], v[176:179], a[44:47]\n"
  "ds_read_b128 a[156:159], v228 offset:8448\n"
  "v_cvt_pk_bf16_f32 v131, v134, v135\n"
  "v_mfma_f32_16x16x32_bf16 a[44:47], a[164:167], v[184:187], a[44:47]\n"
  "v_exp_f32 v136, v136\n"
  "v_mfma_f32_16x16x32_bf16 a[60:63], a[172:175], v[184:187], a[60:63]\n"
  "v_exp_f32 v137, v137\n"
  "v_mfma_f32_16x16x32_bf16 a[60:63], a[168:171], v[176:179], a[60:63]\n"
  "ds_read_b128 a[160:163], v228 offset:512\n"
  "v_cvt_pk_bf16_f32 v136, v136, v137\n"
  "v_mfma_f32_16x16x32_bf16 a[76:79], a[176:179], v[176:179], a[76:79]\n"
  "v_exp_f32 v138, v138\n"
  "v_mfma_f32_16x16x32_bf16 a[76:79], a[180:183], v[184:187], a[76:79]\n"
  "v_exp_f32 v139, v139\n"
  "v_mfma_f32_16x16x32_bf16 a[92:95], a[188:191], v[184:187], a[92:95]\n"
  "ds_read_b128 a[164:167], v228 offset:8704\n"
  "v_cvt_pk_bf16_f32 v137, v138, v139\n"
  "v_mfma_f32_16x16x32_bf16 a[92:95], a[184:187], v[176:179], a[92:95]\n"
  "v_exp_f32 v140, v140\n"
  "v_mfma_f32_16x16x32_bf16 a[108:111], a[192:195], v[176:179], a[108:111]\n"
  "v_exp_f32 v141, v141\n"
  "v_mfma_f32_16x16x32_bf16 a[108:111], a[196:199], v[184:187], a[108:111]\n"
  "ds_read_b128 a[168:171], v228 offset:768\n"
  "v_cvt_pk_bf16_f32 v138, v140, v141\n"
  "v_mfma_f32_16x16x32_bf16 a[124:127], a[204:207], v[184:187], a[124:127]\n"
  "v_exp_f32 v142, v142\n"
  "v_mfma_f32_16x16x32_bf16 a[124:127], a[200:203], v[176:179], a[124:127]\n"
  "v_exp_f32 v143, v143\n"
  "v_mfma_f32_16x16x32_bf16 a[140:143], v[208:211], v[176:179], a[140:143]\n"
  "ds_read_b128 a[172:175], v228 offset:8960\n"
  "v_cvt_pk_bf16_f32 v139, v142, v143\n"
  "v_mfma_f32_16x16x32_bf16 a[140:143], v[208:211], v[184:187], a[140:143]\n"
  "v_exp_f32 v144, v144\n"
  "v_mfma_f32_16x16x32_bf16 v[160:163], v[64:67], v[32:35], v[200:203]\n"
  "v_exp_f32 v145, v145\n"
  "v_mfma_f32_16x16x32_bf16 v[160:163], v[68:71], v[36:39], v[160:163]\n"
  "ds_read_b128 a[176:179], v228 offset:4096\n"
  "v_cvt_pk_bf16_f32 v144, v144, v145\n"
  "v_mfma_f32_16x16x32_bf16 v[160:163], v[72:75], v[40:43], v[160:163]\n"
  "v_exp_f32 v146, v146\n"
  "v_mfma_f32_16x16x32_bf16 v[160:163], v[76:79], v[44:47], v[160:163]\n"
  "v_exp_f32 v147, v147\n"
  "v_mfma_f32_16x16x32_bf16 v[176:179], v[76:79], v[60:63], v[204:207]\n"
  "ds_read_b128 a[180:183], v228 offset:12288\n"
  "v_cvt_pk_bf16_f32 v145, v146, v147\n"
  "v_mfma_f32_16x16x32_bf16 v[176:179], v[72:75], v[56:59], v[176:179]\n"
  "v_exp_f32 v148, v148\n"
  "v_mfma_f32_16x16x32_bf16 v[176:179], v[68:71], v[52:55], v[176:179]\n"
  "v_exp_f32 v149, v149\n"
  "v_mfma_f32_16x16x32_bf16 v[176:179], v[64:67], v[48:51], v[176:179]\n"
  "ds_read_b128 a[184:187], v228 offset:4352\n"
  "v_cvt_pk_bf16_f32 v146, v148, v149\n"
  "v_mfma_f32_16x16x32_bf16 v[164:167], v[80:83], v[32:35], v[200:203]\n"
  "v_exp_f32 v150, v150\n"
  "v_mfma_f32_16x16x32_bf16 v[164:167], v[84:87], v[36:39], v[164:167]\n"
  "v_exp_f32 v151, v151\n"
  "v_mfma_f32_16x16x32_bf16 v[164:167], v[88:91], v[40:43], v[164:167]\n"
  "ds_read_b128 a[188:191], v228 offset:12544\n"
  "v_cvt_pk_bf16_f32 v147, v150, v151\n"
  "v_mfma_f32_16x16x32_bf16 v[164:167], v[92:95], v[44:47], v[164:167]\n"
  "v_exp_f32 v152, v152\n"
  "v_mfma_f32_16x16x32_bf16 v[180:183], v[92:95], v[60:63], v[204:207]\n"
  "v_exp_f32 v153, v153\n"
  "v_mfma_f32_16x16x32_bf16 v[180:183], v[88:91], v[56:59], v[180:183]\n"
  "ds_read_b128 a[192:195], v228 offset:4608\n"
  "v_cvt_pk_bf16_f32 v152, v152, v153\n"
  "v_mfma_f32_16x16x32_bf16 v[180:183], v[84:87], v[52:55], v[180:183]\n"
  "ds_read_b128 a[196:199], v228 offset:12800\n"
  "v_max3_f32 v213, v160, v161, v162\n"
  "v_mfma_f32_16x16x32_bf16 v[180:183], v[80:83], v[48:51], v[180:183]\n"
  "ds_read_b128 a[200:203], v228 offset:4864\n"
  "v_max3_f32 v213, v213, v163, v176\n"
  "v_mfma_f32_16x16x32_bf16 v[168:171], v[96:99], v[32:35], v[200:203]\n"
  "ds_read_b128 a[204:207], v228 offset:13056\n"
  "v_max3_f32 v213, v213, v177, v178\n"
  "v_mfma_f32_16x16x32_bf16 v[168:171], v[100:103], v[36:39], v[168:171]\n"
  "v_exp_f32 v154, v154\n"
  "v_mfma_f32_16x16x32_bf16 v[168:171], v[104:107], v[40:43], v[168:171]\n"
  "v_exp_f32 v155, v155\n"
  "v_mfma_f32_16x16x32_bf16 v[168:171], v[108:111], v[44:47], v[168:171]\n"
  "ds_read_b128 v[76:79], v228 offset:28672\n"
  "v_cvt_pk_bf16_f32 v153, v154, v155\n"
  "v_mfma_f32_16x16x32_bf16 v[184:187], v[108:111], v[60:63], v[204:207]\n"
  "v_exp_f32 v156, v156\n"
  "v_mfma_f32_16x16x32_bf16 v[184:187], v[104:107], v[56:59], v[184:187]\n"
  "v_exp_f32 v157, v157\n"
  "v_mfma_f32_16x16x32_bf16 v[184:187], v[100:103], v[52:55], v[184:187]\n"
  "ds_read_b128 v[72:75], v228 offset:24576\n"
  "v_max3_f32 v213, v213, v179, v164\n"
  "v_mfma_f32_16x16x32_bf16 v[184:187], v[96:99], v[48:51], v[184:187]\n"
  "ds_read_b128 v[68:71], v228 offset:20480\n"
  "v_max3_f32 v213, v213, v165, v166\n"
  "v_mfma_f32_16x16x32_bf16 v[172:175], v[112:115], v[32:35], v[200:203]\n"
  "ds_read_b128 v[64:67], v228 offset:16384\n"
  "v_max3_f32 v213, v213, v167, v180\n"
  "v_mfma_f32_16x16x32_bf16 v[172:175], v[116:119], v[36:39], v[172:175]\n"
  "ds_read_b128 v[92:95], v228 offset:28928\n"
  "v_max3_f32 v213, v213, v181, v182\n"
  "v_mfma_f32_16x16x32_bf16 v[172:175], v[120:123], v[40:43], v[172:175]\n"
  "ds_read_b128 v[88:91], v228 offset:24832\n"
  "v_cvt_pk_bf16_f32 v154, v156, v157\n"
  "v_mfma_f32_16x16x32_bf16 v[172:175], v[124:127], v[44:47], v[172:175]\n"
  "v_exp_f32 v158, v158\n"
  "v_mfma_f32_16x16x32_bf16 v[188:191], v[124:127], v[60:63], v[204:207]\n"
  "v_exp_f32 v159, v159\n"
  "v_mfma_f32_16x16x32_bf16 v[188:191], v[120:123], v[56:59], v[188:191]\n"
  "ds_read_b128 v[84:87], v228 offset:20736\n"
  "v_cvt_pk_bf16_f32 v155, v158, v159\n"
  "v_mfma_f32_16x16x32_bf16 v[188:191], v[116:119], v[52:55], v[188:191]\n"
  "ds_read_b128 v[80:83], v228 offset:16640\n"
  "v_max3_f32 v213, v213, v183, v168\n"
  "v_mfma_f32_16x16x32_bf16 v[188:191], v[112:115], v[48:51], v[188:191]\n"
  "ds_read_b128 v[108:111], v228 offset:29184\n"
  "v_max3_f32 v213, v213, v169, v170\n"
  "s_waitcnt lgkmcnt(12)\n"
  "v_mfma_f32_16x16x32_bf16 a[0:3], a[144:147], v[128:131], a[0:3]\n"
  "ds_read_b128 v[104:107], v228 offset:25088\n"
  "v_max3_f32 v213, v213, v171, v184\n"
  "v_mfma_f32_16x16x32_bf16 a[0:3], a[148:151], v[136:139], a[0:3]\n"
  "ds_read_b128 v[100:103], v228 offset:20992\n"
  "v_max3_f32 v213, v213, v185, v186\n"
  "v_mfma_f32_16x16x32_bf16 a[16:19], a[156:159], v[136:139], a[16:19]\n"
  "ds_read_b128 v[96:99], v228 offset:16896\n"
  "v_mfma_f32_16x16x32_bf16 a[16:19], a[152:155], v[128:131], a[16:19]\n"
  "ds_read_b128 v[124:127], v228 offset:29440\n"
  "v_mfma_f32_16x16x32_bf16 a[32:35], a[160:163], v[128:131], a[32:35]\n"
  "ds_read_b128 v[120:123], v228 offset:25344\n"
  "v_mfma_f32_16x16x32_bf16 a[32:35], a[164:167], v[136:139], a[32:35]\n"
  "ds_read_b128 v[116:119], v228 offset:21248\n"
  "v_mfma_f32_16x16x32_bf16 a[48:51], a[172:175], v[136:139], a[48:51]\n"
  "v_max3_f32 v213, v213, v187, v172\n"
  "v_max3_f32 v213, v213, v173, v174\n"
  "v_mfma_f32_16x16x32_bf16 a[48:51], a[168:171], v[128:131], a[48:51]\n"
  "v_max3_f32 v213, v213, v175, v188\n"
  "v_max3_f32 v213, v213, v189, v190\n"
  "v_mfma_f32_16x16x32_bf16 a[64:67], a[176:179], v[128:131], a[64:67]\n"
  "v_max_f32 v213, v213, v191\n"
  "v_cmp_lt_f32 vcc, s38, v213\n"
  "v_mfma_f32_16x16x32_bf16 a[64:67], a[180:183], v[136:139], a[64:67]\n"
  "ds_read_b128 v[112:115], v228 offset:17152\n"
  "v_mfma_f32_16x16x32_bf16 a[80:83], a[188:191], v[136:139], a[80:83]\n"
  "s_cbranch_vccnz L_tr2%=\n"
  "L_bk2%=:\n"
  "v_mfma_f32_16x16x32_bf16 a[80:83], a[184:187], v[128:131], a[80:83]\n"
  "v_exp_f32 v160, v160\n"
  "v_mfma_f32_16x16x32_bf16 a[96:99], a[192:195], v[128:131], a[96:99]\n"
  "v_exp_f32 v161, v161\n"
  "s_waitcnt lgkmcnt(12)\n"
  "v_mfma_f32_16x16x32_bf16 a[96:99], a[196:199], v[136:139], a[96:99]\n"
  "v_exp_f32 v162, v162\n"
  "v_mfma_f32_16x16x32_bf16 a[112:115], a[204:207], v[136:139], a[112:115]\n"
  "v_exp_f32 v163, v163\n"
  "s_cmp_eq_u32 s44, 6\n"
  "s_cbranch_scc1 L_kev_m%=\n"
  "L_kevr_m%=:\n"
  "v_mfma_f32_16x16x32_bf16 a[112:115], a[200:203], v[128:131], a[112:115]\n"
  "v_exp_f32 v164, v164\n"
  "v_mfma_f32_16x16x32_bf16 a[128:131], v[208:211], v[128:131], a[128:131]\n"
  "v_exp_f32 v165, v165\n"
  "v_mfma_f32_16x16x32_bf16 a[128:131], v[208:211], v[136:139], a[128:131]\n"
  "v_exp_f32 v166, v166\n"
  "v_mfma_f32_16x16x32_bf16 a[4:7], a[144:147], v[144:147], a[4:7]\n"
  "v_exp_f32 v167, v167\n"
  "v_mfma_f32_16x16x32_bf16 a[4:7], a[148:151], v[152:155], a[4:7]\n"
  "v_exp_f32 v168, v168\n"
  "v_mfma_f32_16x16x32_bf16 a[20:23], a[156:159], v[152:155], a[20:23]\n"
  "v_exp_f32 v169, v169\n"
  "v_mfma_f32_16x16x32_bf16 a[20:23], a[152:155], v[144:147], a[20:23]\n"
  "v_exp_f32 v170, v170\n"
  "v_mfma_f32_16x16x32_bf16 a[36:39], a[160:163], v[144:147], a[36:39]\n"
  "v_exp_f32 v171, v171\n"
  "v_mfma_f32_16x16x32_bf16 a[36:39], a[164:167], v[152:155], a[36:39]\n"
  "v_exp_f32 v172, v172\n"
  "v_mfma_f32_16x16x32_bf16 a[52:55], a[172:175], v[152:155], a[52:55]\n"
  "v_exp_f32 v173, v173\n"
  "v_mfma_f32_16x16x32_bf16 a[52:55], a[168:171], v[144:147], a[52:55]\n"
  "v_exp_f32 v174, v174\n"
  "v_mfma_f32_16x16x32_bf16 a[68:71], a[176:179], v[144:147], a[68:71]\n"
  "v_exp_f32 v175, v175\n"
  "v_mfma_f32_16x16x32_bf16 a[68:71], a[180:183], v[152:155], a[68:71]\n"
  "v_exp_f32 v176, v176\n"
  "v_mfma_f32_16x16x32_bf16 a[84:87], a[188:191], v[152:155], a[84:87]\n"
  "v_exp_f32 v177, v177\n"
  "v_mfma_f32_16x16x32_bf16 a[84:87], a[184:187], v[144:147], a[84:87]\n"
  "v_exp_f32 v178, v178\n"
  "v_mfma_f32_16x16x32_bf16 a[100:103], a[192:195], v[144:147], a[100:103]\n"
  "v_exp_f32 v179, v179\n"
  "v_mfma_f32_16x16x32_bf16 a[100:103], a[196:199], v[152:155], a[100:103]\n"
  "v_exp_f32 v180, v180\n"
  "v_mfma_f32_16x16x32_bf16 a[116:119], a[204:207], v[152:155], a[116:119]\n"
  "s_sub_u32 s82, s82, 1\n"
  "v_cvt_pk_bf16_f32 v160, v160, v161\n"
  "v_mfma_f32_16x16x32_bf16 a[116:119], a[200:203], v[144:147], a[116:119]\n"
  "v_exp_f32 v181, v181\n"
  "v_mfma_f32_16x16x32_bf16 a[132:135], v[208:211], v[144:147], a[132:135]\n"
  "v_exp_f32 v182, v182\n"
  "v_mfma_f32_16x16x32_bf16 a[132:135], v[208:211], v[152:155], a[132:135]\n"
  "v_exp_f32 v183, v183\n"
  "s_waitcnt vmcnt(9) lgkmcnt(0)\n"
  "s_xor_b32 s87, s44, 6\n"
  "s_or_b32 s87, s87, s34\n"
  "s_cbranch_scc1 L_tk_n%=\n"
  "L_tk_ok%=:\n"
  "v_readfirstlane_b32 s87, v236\n"
  "s_nop 3\n"
  "s_min_u32 s87, s87, 63\n"
  "v_mov_b32 v236, s87\n"
  "v_mov_b32 v235, 0x27f00\n"
  "ds_write_b32 v235, v236\n"
  "s_waitcnt lgkmcnt(0)\n"
  "L_tk_n%=:\n"
  "s_barrier\n"
  "s_cmp_lg_u32 s44, 6\n"
  "s_cbranch_scc1 L_tk_f%=\n"
  "v_mov_b32 v235, 0x27f00\n"
  "ds_read_b32 v236, v235\n"
  "s_waitcnt lgkmcnt(0)\n"
  "v_readfirstlane_b32 s87, v236\n"
  "s_nop 3\n"
  "s_lshl_b32 s87, s87, 5\n"
  "s_add_u32 s87, s87, s45\n"
  "s_load_dwordx8 s[44:51], s[32:33], s87\n"
  "s_waitcnt lgkmcnt(0)\n"
  "L_tk_f%=:\n"
  "s_bfe_u32 s87, s48, 0x5000e\n"
  "s_bfe_u32 s91, s48, 0x40013\n"
  "s_lshr_b32 s90, s51, 3\n"
  "s_lshl_b32 s90, s90, 5\n"
  "s_add_u32 s87, s87, s90\n"
  "s_bitcmp0_b32 s48, 2\n"
  "s_cselect_b32 s99, 0, 0x100\n"
  "s_add_u32 s87, s87, s99\n"
  "s_lshl_b32 s87, s87, 7\n"
  "s_add_u32 s87, s87, 0x5a18000\n"
  "v_lshrrev_b32 v230, 2, v255\n"
  "v_min_u32 v230, s91, v230\n"
  "v_lshl_add_u32 v230, v230, 7, s87\n"
  "v_and_b32 v231, 3, v255\n"
  "v_lshl_add_u32 v230, v231, 2, v230\n"
  "global_load_dword v231, v230, s[8:9] sc1\n"
  "s_and_b32 s91, s34, 1\n"
  "s_lshl_b32 s91, s91, 16\n"
  "s_lshr_b32 s99, s34, 1\n"
  "s_lshl_b32 s99, s99, 7\n"
  "s_add_u32 s91, s91, s99\n"
  "s_bitcmp1_b32 s48, 2\n"
  "s_cselect_b64 s[56:57], s[20:21], s[26:27]\n"
  "s_add_u32 s90, s91, s46\n"
  "s_add_u32 s56, s56, s90\n"
  "s_addc_u32 s57, s57, 0\n"
  "s_mov_b32 s58, 0x2f880\n"
  "v_and_b32 v250, 32, v255\n"
  "v_lshlrev_b32 v250, 1, v250\n"
  "v_xor_b32 v250, v250, v247\n"
  "v_lshl_add_u32 v250, v249, 11, v250\n"
  "v_mov_b32 v251, v250\n"
  "s_mov_b32 s72, 0\n"
  "s_mov_b32 s73, 0x3c00\n"
  "s_mov_b32 s74, 0x7800\n"
  "s_mov_b32 s75, 0xb400\n"
  "s_mov_b32 s80, 0x20000\n"
  "v_mfma_f32_16x16x32_bf16 v[128:131], v[64:67], v[32:35], v[200:203]\n"
  "s_add_u32 s52, s52, s81\n"
  "s_addc_u32 s53, s53, 0\n"
  "v_mfma_f32_16x16x32_bf16 v[128:131], v[68:71], v[36:39], v[128:131]\n"
  "s_sub_u32 s85, s85, 1\n"
  "s_cselect_b32 s54, 0, s54\n"
  "v_mfma_f32_16x16x32_bf16 v[128:131], v[72:75], v[40:43], v[128:131]\n"
  "s_add_u32 m0, s83, s36\n"
  "v_cvt_pk_bf16_f32 v161, v162, v163\n"
  "v_mfma_f32_16x16x32_bf16 v[128:131], v[76:79], v[44:47], v[128:131]\n"
  "buffer_load_dwordx4 v252, s[52:55], s64 offen sc1 lds\n"
  "v_cvt_pk_bf16_f32 v162, v164, v165\n"
  "v_mfma_f32_16x16x32_bf16 v[144:147], v[76:79], v[60:63], v[204:207]\n"
  "v_exp_f32 v184, v184\n"
  "v_mfma_f32_16x16x32_bf16 v[144:147], v[72:75], v[56:59], v[144:147]\n"
  "v_exp_f32 v185, v185\n"
  "v_mfma_f32_16x16x32_bf16 v[144:147], v[68:71], v[52:55], v[144:147]\n"
  "v_exp_f32 v186, v186\n"
  "v_mfma_f32_16x16x32_bf16 v[144:147], v[64:67], v[48:51], v[144:147]\n"
  "buffer_load_dwordx4 v252, s[52:55], s65 offen offset:1024 sc1 lds\n"
  "v_cvt_pk_bf16_f32 v163, v166, v167\n"
  "v_mfma_f32_16x16x32_bf16 v[132:135], v[80:83], v[32:35], v[200:203]\n"
  "v_exp_f32 v187, v187\n"
  "v_mfma_f32_16x16x32_bf16 v[132:135], v[84:87], v[36:39], v[132:135]\n"
  "v_exp_f32 v188, v188\n"
  "v_mfma_f32_16x16x32_bf16 v[132:135], v[88:91], v[40:43], v[132:135]\n"
  "v_exp_f32 v189, v189\n"
  "v_mfma_f32_16x16x32_bf16 v[132:135], v[92:95], v[44:47], v[132:135]\n"
  "buffer_load_dwordx4 v252, s[52:55], s66 offen offset:2048 sc1 lds\n"
  "v_cvt_pk_bf16_f32 v168, v168, v169\n"
  "v_mfma_f32_16x16x32_bf16 v[148:151], v[92:95], v[60:63], v[204:207]\n"
  "v_exp_f32 v190, v190\n"
  "v_mfma_f32_16x16x32_bf16 v[148:151], v[88:91], v[56:59], v[148:151]\n"
  "v_exp_f32 v191, v191\n"
  "v_mfma_f32_16x16x32_bf16 v[148:151], v[84:87], v[52:55], v[148:151]\n"
  "v_cvt_pk_bf16_f32 v169, v170, v171\n"
  "v_cvt_pk_bf16_f32 v170, v172, v173\n"
  "v_mfma_f32_16x16x32_bf16 v[148:151], v[80:83], v[48:51], v[148:151]\n"
  "buffer_load_dwordx4 v252, s[52:55], s67 offen offset:3072 sc1 lds\n"
  "s_add_u32 m0, m0, 0x1000\n"
  "v_mfma_f32_16x16x32_bf16 v[136:139], v[96:99], v[32:35], v[200:203]\n"
  "v_cvt_pk_bf16_f32 v171, v174, v175\n"
  "v_cvt_pk_bf16_f32 v176, v176, v177\n"
  "v_mfma_f32_16x16x32_bf16 v[136:139], v[100:103], v[36:39], v[136:139]\n"
  "v_cvt_pk_bf16_f32 v177, v178, v179\n"
  "v_cvt_pk_bf16_f32 v178, v180, v181\n"
  "v_mfma_f32_16x16x32_bf16 v[136:139], v[104:107], v[40:43], v[136:139]\n"
  "v_cvt_pk_bf16_f32 v179, v182, v183\n"
  "v_cvt_pk_bf16_f32 v184, v184, v185\n"
  "v_mfma_f32_16x16x32_bf16 v[136:139], v[108:111], v[44:47], v[136:139]\n"
  "buffer_load_dwordx4 v252, s[52:55], s68 offen sc1 lds\n"
  "v_cvt_pk_bf16_f32 v185, v186, v187\n"
  "v_mfma_f32_16x16x32_bf16 v[152:155], v[108:111], v[60:63], v[204:207]\n"
  "v_cvt_pk_bf16_f32 v186, v188, v189\n"
  "v_cvt_pk_bf16_f32 v187, v190, v191\n"
  "v_mfma_f32_16x16x32_bf16 v[152:155], v[104:107], v[56:59], v[152:155]\n"
  "v_max3_f32 v212, v128, v129, v130\n"
  "v_max3_f32 v212, v212, v131, v144\n"
  "v_mfma_f32_16x16x32_bf16 v[152:155], v[100:103], v[52:55], v[152:155]\n"
  "v_max3_f32 v212, v212, v145, v146\n"
  "v_max3_f32 v212, v212, v147, v132\n"
  "v_mfma_f32_16x16x32_bf16 v[152:155], v[96:99], v[48:51], v[152:155]\n"
  "buffer_load_dwordx4 v252, s[52:55], s69 offen offset:1024 sc1 lds\n"
  "v_max3_f32 v212, v212, v133, v134\n"
  "v_mfma_f32_16x16x32_bf16 v[140:143], v[112:115], v[32:35], v[200:203]\n"
  "v_max3_f32 v212, v212, v135, v148\n"
  "v_max3_f32 v212, v212, v149, v150\n"
  "v_mfma_f32_16x16x32_bf16 v[140:143], v[116:119], v[36:39], v[140:143]\n"
  "v_accvgpr_read_b32 v0, a128\n"
  "v_accvgpr_read_b32 v2, a132\n"
  "v_mfma_f32_16x16x32_bf16 v[140:143], v[120:123], v[40:43], v[140:143]\n"
  "v_rcp_f32 v0, v0\n"
  "v_mfma_f32_16x16x32_bf16 v[140:143], v[124:127], v[44:47], v[140:143]\n"
  "buffer_load_dwordx4 v252, s[52:55], s70 offen offset:2048 sc1 lds\n"
  "v_mfma_f32_16x16x32_bf16 v[156:159], v[124:127], v[60:63], v[204:207]\n"
  "v_rcp_f32 v2, v2\n"
  "v_mfma_f32_16x16x32_bf16 v[156:159], v[120:123], v[56:59], v[156:159]\n"
  "s_nop 0\n"
  "v_mul_f32 v1, v0, v224\n"
  "v_mfma_f32_16x16x32_bf16 v[156:159], v[116:119], v[52:55], v[156:159]\n"
  "v_max3_f32 v212, v212, v151, v136\n"
  "v_max3_f32 v212, v212, v137, v138\n"
  "v_mfma_f32_16x16x32_bf16 v[156:159], v[112:115], v[48:51], v[156:159]\n"
  "buffer_load_dwordx4 v252, s[52:55], s71 offen offset:3072 sc1 lds\n"
  "s_add_u32 s36, s36, 0x8000\n"
  "v_mfma_f32_16x16x32_bf16 a[8:11], a[144:147], v[160:163], a[8:11]\n"
  "s_cmp_eq_u32 s36, 0x18000\n"
  "s_cselect_b32 s36, 0, s36\n"
  "v_mfma_f32_16x16x32_bf16 a[8:11], a[148:151], v[168:171], a[8:11]\n"
  "v_add_u32 v228, s36, v246\n"
  "v_max3_f32 v212, v212, v139, v152\n"
  "v_mfma_f32_16x16x32_bf16 a[24:27], a[156:159], v[168:171], a[24:27]\n"
  "s_add_u32 m0, s84, 0\n"
  "v_max3_f32 v212, v212, v153, v154\n"
  "v_mfma_f32_16x16x32_bf16 a[24:27], a[152:155], v[160:163], a[24:27]\n"
  "buffer_load_dwordx4 v250, s[56:59], s72 offen lds\n"
  "v_mul_f32 v0, v0, v224\n"
  "v_mfma_f32_16x16x32_bf16 a[40:43], a[160:163], v[160:163], a[40:43]\n"
  "ds_read_b128 v[76:79], v228 offset:28672\n"
  "v_mul_f32 v3, v2, v225\n"
  "v_mfma_f32_16x16x32_bf16 a[40:43], a[164:167], v[168:171], a[40:43]\n"
  "ds_read_b128 v[72:75], v228 offset:24576\n"
  "v_mul_f32 v2, v2, v225\n"
  "v_mfma_f32_16x16x32_bf16 a[56:59], a[172:175], v[168:171], a[56:59]\n"
  "ds_read_b128 v[68:71], v228 offset:20480\n"
  "v_max3_f32 v212, v212, v155, v140\n"
  "v_mfma_f32_16x16x32_bf16 a[56:59], a[168:171], v[160:163], a[56:59]\n"
  "buffer_load_dwordx4 v251, s[56:59], s73 offen offset:1024 lds\n"
  "v_max3_f32 v212, v212, v141, v142\n"
  "v_mfma_f32_16x16x32_bf16 a[72:75], a[176:179], v[160:163], a[72:75]\n"
  "ds_read_b128 v[64:67], v228 offset:16384\n"
  "v_max3_f32 v212, v212, v143, v156\n"
  "v_mfma_f32_16x16x32_bf16 a[72:75], a[180:183], v[168:171], a[72:75]\n"
  "ds_read_b128 v[92:95], v228 offset:28928\n"
  "v_max3_f32 v212, v212, v157, v158\n"
  "v_mfma_f32_16x16x32_bf16 a[88:91], a[188:191], v[168:171], a[88:91]\n"
  "ds_read_b128 v[88:91], v228 offset:24832\n"
  "v_max_f32 v212, v212, v159\n"
  "v_mfma_f32_16x16x32_bf16 a[88:91], a[184:187], v[160:163], a[88:91]\n"
  "buffer_load_dwordx4 v250, s[56:59], s74 offen offset:2048 lds\n"
  "v_accvgpr_read_b32 v4, a0\n"
  "v_mfma_f32_16x16x32_bf16 a[104:107], a[192:195], v[160:163], a[104:107]\n"
  "ds_read_b128 v[84:87], v228 offset:20736\n"
  "v_accvgpr_read_b32 v5, a1\n"
  "v_mfma_f32_16x16x32_bf16 a[104:107], a[196:199], v[168:171], a[104:107]\n"
  "ds_read_b128 v[80:83], v228 offset:16640\n"
  "v_accvgpr_read_b32 v6, a2\n"
  "v_mfma_f32_16x16x32_bf16 a[120:123], a[204:207], v[168:171], a[120:123]\n"
  "ds_read_b128 v[108:111], v228 offset:29184\n"
  "v_accvgpr_read_b32 v7, a3\n"
  "v_mfma_f32_16x16x32_bf16 a[120:123], a[200:203], v[160:163], a[120:123]\n"
  "buffer_load_dwordx4 v251, s[56:59], s75 offen offset:3072 lds\n"
  "v_accvgpr_read_b32 v8, a16\n"
  "v_mfma_f32_16x16x32_bf16 a[136:139], v[208:211], v[160:163], a[136:139]\n"
  "ds_read_b128 v[104:107], v228 offset:25088\n"
  "v_accvgpr_read_b32 v9, a17\n"
  "v_mfma_f32_16x16x32_bf16 a[136:139], v[208:211], v[168:171], a[136:139]\n"
  "s_add_u32 s56, s56, s80\n"
  "s_addc_u32 s57, s57, 0\n"
  "v_mfma_f32_16x16x32_bf16 a[12:15], a[144:147], v[176:179], a[12:15]\n"
  "ds_read_b128 v[100:103], v228 offset:20992\n"
  "s_add_u32 m0, s84, 0x4000\n"
  "v_mfma_f32_16x16x32_bf16 a[12:15], a[148:151], v[184:187], a[12:15]\n"
  "ds_read_b128 v[96:99], v228 offset:16896\n"
  "v_accvgpr_read_b32 v10, a18\n"
  "v_mfma_f32_16x16x32_bf16 a[28:31], a[156:159], v[184:187], a[28:31]\n"
  "ds_read_b128 v[124:127], v228 offset:29440\n"
  "v_accvgpr_read_b32 v11, a19\n"
  "v_mfma_f32_16x16x32_bf16 a[28:31], a[152:155], v[176:179], a[28:31]\n"
  "ds_read_b128 v[120:123], v228 offset:25344\n"
  "v_mul_f32 v4, v4, v0\n"
  "v_mfma_f32_16x16x32_bf16 a[44:47], a[160:163], v[176:179], a[44:47]\n"
  "buffer_load_dwordx4 v250, s[56:59], s72 offen lds\n"
  "v_mul_f32 v5, v5, v0\n"
  "v_mfma_f32_16x16x32_bf16 a[44:47], a[164:167], v[184:187], a[44:47]\n"
  "ds_read_b128 v[116:119], v228 offset:21248\n"
  "v_mul_f32 v6, v6, v0\n"
  "v_mfma_f32_16x16x32_bf16 a[60:63], a[172:175], v[184:187], a[60:63]\n"
  "ds_read_b128 v[112:115], v228 offset:17152\n"
  "v_mul_f32 v7, v7, v0\n"
  "v_mfma_f32_16x16x32_bf16 a[60:63], a[168:171], v[176:179], a[60:63]\n"
  "ds_read_b128 a[144:147], v228\n"
  "v_mul_f32 v8, v8, v0\n"
  "v_mfma_f32_16x16x32_bf16 a[76:79], a[176:179], v[176:179], a[76:79]\n"
  "buffer_load_dwordx4 v251, s[56:59], s73 offen offset:1024 lds\n"
  "v_mul_f32 v9, v9, v0\n"
  "v_mfma_f32_16x16x32_bf16 a[76:79], a[180:183], v[184:187], a[76:79]\n"
  "ds_read_b128 a[148:151], v228 offset:8192\n"
  "v_mul_f32 v10, v10, v0\n"
  "v_mfma_f32_16x16x32_bf16 a[92:95], a[188:191], v[184:187], a[92:95]\n"
  "ds_read_b128 a[152:155], v228 offset:256\n"
  "v_mul_f32 v11, v11, v0\n"
  "v_mfma_f32_16x16x32_bf16 a[92:95], a[184:187], v[176:179], a[92:95]\n"
  "ds_read_b128 a[156:159], v228 offset:8448\n"
  "v_cvt_pk_bf16_f32 v12, v4, v5\n"
  "v_mfma_f32_16x16x32_bf16 a[108:111], a[192:195], v[176:179], a[108:111]\n"
  "buffer_load_dwordx4 v250, s[56:59], s74 offen offset:2048 lds\n"
  "v_cvt_pk_bf16_f32 v13, v6, v7\n"
  "v_mfma_f32_16x16x32_bf16 a[108:111], a[196:199], v[184:187], a[108:111]\n"
  "ds_read_b128 a[160:163], v228 offset:512\n"
  "v_cvt_pk_bf16_f32 v14, v8, v9\n"
  "v_mfma_f32_16x16x32_bf16 a[124:127], a[204:207], v[184:187], a[124:127]\n"
  "ds_read_b128 a[164:167], v228 offset:8704\n"
  "v_cvt_pk_bf16_f32 v15, v10, v11\n"
  "v_mfma_f32_16x16x32_bf16 a[124:127], a[200:203], v[176:179], a[124:127]\n"
  "ds_read_b128 a[168:171], v228 offset:768\n"
  "v_cmp_lt_f32 vcc, s38, v212\n"
  "v_mfma_f32_16x16x32_bf16 a[140:143], v[208:211], v[176:179], a[140:143]\n"
  "buffer_load_dwordx4 v251, s[56:59], s75 offen offset:3072 lds\n"
  "v_mfma_f32_16x16x32_bf16 a[140:143], v[208:211], v[184:187], a[140:143]\n"
  "ds_read_b128 a[172:175], v228 offset:8960\n"
  "s_cbranch_vccnz L_tr4%=\n"
  "L_bk4%=:\n"
  "s_waitcnt lgkmcnt(12)\n"
  "v_mfma_f32_16x16x32_bf16 v[160:163], v[64:67], v[32:35], v[200:203]\n"
  "v_exp_f32 v128, v128\n"
  "v_mfma_f32_16x16x32_bf16 v[160:163], v[68:71], v[36:39], v[160:163]\n"
  "v_exp_f32 v129, v129\n"
  "v_mfma_f32_16x16x32_bf16 v[160:163], v[72:75], v[40:43], v[160:163]\n"
  "ds_read_b128 a[176:179], v228 offset:4096\n"
  "v_cvt_pk_bf16_f32 v128, v128, v129\n"
  "v_mfma_f32_16x16x32_bf16 v[160:163], v[76:79], v[44:47], v[160:163]\n"
  "v_exp_f32 v130, v130\n"
  "v_mfma_f32_16x16x32_bf16 v[176:179], v[76:79], v[60:63], v[204:207]\n"
  "v_exp_f32 v131, v131\n"
  "v_mfma_f32_16x16x32_bf16 v[176:179], v[72:75], v[56:59], v[176:179]\n"
  "ds_read_b128 a[180:183], v228 offset:12288\n"
  "v_cvt_pk_bf16_f32 v129, v130, v131\n"
  "v_mfma_f32_16x16x32_bf16 v[176:179], v[68:71], v[52:55], v[176:179]\n"
  "v_exp_f32 v132, v132\n"
  "v_mfma_f32_16x16x32_bf16 v[176:179], v[64:67], v[48:51], v[176:179]\n"
  "v_exp_f32 v133, v133\n"
  "v_mfma_f32_16x16x32_bf16 v[164:167], v[80:83], v[32:35], v[200:203]\n"
  "ds_read_b128 a[184:187], v228 offset:4352\n"
  "v_cvt_pk_bf16_f32 v130, v132, v133\n"
  "v_mfma_f32_16x16x32_bf16 v[164:167], v[84:87], v[36:39], v[164:167]\n"
  "v_exp_f32 v134, v134\n"
  "v_mfma_f32_16x16x32_bf16 v[164:167], v[88:91], v[40:43], v[164:167]\n"
  "v_exp_f32 v135, v135\n"
  "v_mfma_f32_16x16x32_bf16 v[164:167], v[92:95], v[44:47], v[164:167]\n"
  "ds_read_b128 a[188:191], v228 offset:12544\n"
  "v_cvt_pk_bf16_f32 v131, v134, v135\n"
  "v_mfma_f32_16x16x32_bf16 v[180:183], v[92:95], v[60:63], v[204:207]\n"
  "v_exp_f32 v136, v136\n"
  "v_mfma_f32_16x16x32_bf16 v[180:183], v[88:91], v[56:59], v[180:183]\n"
  "v_exp_f32 v137, v137\n"
  "v_mfma_f32_16x16x32_bf16 v[180:183], v[84:87], v[52:55], v[180:183]\n"
  "ds_read_b128 a[192:195], v228 offset:4608\n"
  "v_cvt_pk_bf16_f32 v136, v136, v137\n"
  "v_mfma_f32_16x16x32_bf16 v[180:183], v[80:83], v[48:51], v[180:183]\n"
  "ds_read_b128 a[196:199], v228 offset:12800\n"
  "v_cmp_gt_i32 vcc, 0, v222\n"
  "v_mfma_f32_16x16x32_bf16 v[168:171], v[96:99], v[32:35], v[200:203]\n"
  "v_exp_f32 v138, v138\n"
  "v_mfma_f32_16x16x32_bf16 v[168:171], v[100:103], v[36:39], v[168:171]\n"
  "v_exp_f32 v139, v139\n"
  "v_mfma_f32_16x16x32_bf16 v[168:171], v[104:107], v[40:43], v[168:171]\n"
  "ds_read_b128 a[200:203], v228 offset:4864\n"
  "v_cvt_pk_bf16_f32 v137, v138, v139\n"
  "v_mfma_f32_16x16x32_bf16 v[168:171], v[108:111], v[44:47], v[168:171]\n"
  "v_exp_f32 v140, v140\n"
  "v_mfma_f32_16x16x32_bf16 v[184:187], v[108:111], v[60:63], v[204:207]\n"
  "v_exp_f32 v141, v141\n"
  "v_mfma_f32_16x16x32_bf16 v[184:187], v[104:107], v[56:59], v[184:187]\n"
  "ds_read_b128 a[204:207], v228 offset:13056\n"
  "v_cvt_pk_bf16_f32 v138, v140, v141\n"
  "v_mfma_f32_16x16x32_bf16 v[184:187], v[100:103], v[52:55], v[184:187]\n"
  "v_exp_f32 v142, v142\n"
  "v_mfma_f32_16x16x32_bf16 v[184:187], v[96:99], v[48:51], v[184:187]\n"
  "v_exp_f32 v143, v143\n"
  "s_waitcnt lgkmcnt(12)\n"
  "v_mfma_f32_16x16x32_bf16 v[172:175], v[112:115], v[32:35], v[200:203]\n"
  "v_exp_f32 v144, v144\n"
  "v_mfma_f32_16x16x32_bf16 v[172:175], v[116:119], v[36:39], v[172:175]\n"
  "v_exp_f32 v145, v145\n"
  "v_mfma_f32_16x16x32_bf16 v[172:175], v[120:123], v[40:43], v[172:175]\n"
  "v_exp_f32 v146, v146\n"
  "v_mfma_f32_16x16x32_bf16 v[172:175], v[124:127], v[44:47], v[172:175]\n"
  "v_cvt_pk_bf16_f32 v139, v142, v143\n"
  "s_add_u32 s36, s36, 0x8000\n"
  "v_mfma_f32_16x16x32_bf16 v[188:191], v[124:127], v[60:63], v[204:207]\n"
  "v_exp_f32 v147, v147\n"
  "v_mfma_f32_16x16x32_bf16 v[188:191], v[120:123], v[56:59], v[188:191]\n"
  "s_cmp_eq_u32 s36, 0x18000\n"
  "s_cselect_b32 s36, 0, s36\n"
  "v_mfma_f32_16x16x32_bf16 v[188:191], v[116:119], v[52:55], v[188:191]\n"
  "v_add_u32 v228, s36, v246\n"
  "v_cvt_pk_bf16_f32 v144, v144, v145\n"
  "v_mfma_f32_16x16x32_bf16 v[188:191], v[112:115], v[48:51], v[188:191]\n"
  "s_waitcnt vmcnt(17) lgkmcnt(0)\n"
  "s_barrier\n"
  "v_mfma_f32_16x16x32_bf16 a[8:11], a[144:147], v[128:131], a[8:11]\n"
  "v_exp_f32 v148, v148\n"
  "v_mfma_f32_16x16x32_bf16 a[8:11], a[148:151], v[136:139], a[8:11]\n"
  "v_exp_f32 v149, v149\n"
  "v_mfma_f32_16x16x32_bf16 a[24:27], a[156:159], v[136:139], a[24:27]\n"
  "v_exp_f32 v150, v150\n"
  "v_mfma_f32_16x16x32_bf16 a[24:27], a[152:155], v[128:131], a[24:27]\n"
  "v_exp_f32 v151, v151\n"
  "v_mfma_f32_16x16x32_bf16 a[40:43], a[160:163], v[128:131], a[40:43]\n"
  "v_exp_f32 v152, v152\n"
  "v_mfma_f32_16x16x32_bf16 a[40:43], a[164:167], v[136:139], a[40:43]\n"
  "v_exp_f32 v153, v153\n"
  "v_mfma_f32_16x16x32_bf16 a[56:59], a[172:175], v[136:139], a[56:59]\n"
  "v_exp_f32 v154, v154\n"
  "v_mfma_f32_16x16x32_bf16 a[56:59], a[168:171], v[128:131], a[56:59]\n"
  "v_exp_f32 v155, v155\n"
  "v_mfma_f32_16x16x32_bf16 a[72:75], a[176:179], v[128:131], a[72:75]\n"
  "v_exp_f32 v156, v156\n"
  "v_mfma_f32_16x16x32_bf16 a[72:75], a[180:183], v[136:139], a[72:75]\n"
  "v_exp_f32 v157, v157\n"
  "v_mfma_f32_16x16x32_bf16 a[88:91], a[188:191], v[136:139], a[88:91]\n"
  "v_exp_f32 v158, v158\n"
  "v_mfma_f32_16x16x32_bf16 a[88:91], a[184:187], v[128:131], a[88:91]\n"
  "v_cvt_pk_bf16_f32 v145, v146, v147\n"
  "v_cvt_pk_bf16_f32 v146, v148, v149\n"
  "v_mfma_f32_16x16x32_bf16 a[104:107], a[192:195], v[128:131], a[104:107]\n"
  "v_exp_f32 v159, v159\n"
  "v_mfma_f32_16x16x32_bf16 a[104:107], a[196:199], v[136:139], a[104:107]\n"
  "v_cvt_pk_bf16_f32 v147, v150, v151\n"
  "v_cvt_pk_bf16_f32 v152, v152, v153\n"
  "v_mfma_f32_16x16x32_bf16 a[120:123], a[204:207], v[136:139], a[120:123]\n"
  "v_cvt_pk_bf16_f32 v153, v154, v155\n"
  "v_cvt_pk_bf16_f32 v154, v156, v157\n"
  "v_mfma_f32_16x16x32_bf16 a[120:123], a[200:203], v[128:131], a[120:123]\n"
  "s_add_u32 s56, s56, s80\n"
  "s_addc_u32 s57, s57, 0\n"
  "v_mfma_f32_16x16x32_bf16 a[136:139], v[208:211], v[128:131], a[136:139]\n"
  "v_cvt_pk_bf16_f32 v155, v158, v159\n"
  "s_add_u32 m0, s84, 0x8000\n"
  "v_mfma_f32_16x16x32_bf16 a[136:139], v[208:211], v[136:139], a[136:139]\n"
  "buffer_load_dwordx4 v250, s[56:59], s72 offen lds\n"
  "v_cmp_gt_i32 s[90:91], 1, v222\n"
  "v_mfma_f32_16x16x32_bf16 a[12:15], a[144:147], v[144:147], a[12:15]\n"
  "buffer_store_dwordx4 v[12:15], v223, s[60:63], 0 offen sc1\n"
  "v_cmp_gt_i32 s[92:93], 2, v222\n"
  "v_mfma_f32_16x16x32_bf16 a[12:15], a[148:151], v[152:155], a[12:15]\n"
  "ds_read_b128 a[144:147], v228\n"
  "v_cndmask_b32 v160, v160, v221, vcc\n"
  "v_mfma_f32_16x16x32_bf16 a[28:31], a[156:159], v[152:155], a[28:31]\n"
  "ds_read_b128 a[148:151], v228 offset:8192\n"
  "v_cmp_gt_i32 vcc, 3, v222\n"
  "v_mfma_f32_16x16x32_bf16 a[28:31], a[152:155], v[144:147], a[28:31]\n"
  "buffer_load_dwordx4 v251, s[56:59], s73 offen offset:1024 lds\n"
  "v_cndmask_b32 v161, v161, v221, s[90:91]\n"
  "v_mfma_f32_16x16x32_bf16 a[44:47], a[160:163], v[144:147], a[44:47]\n"
  "ds_read_b128 a[152:155], v228 offset:256\n"
  "v_cndmask_b32 v162, v162, v221, s[92:93]\n"
  "v_mfma_f32_16x16x32_bf16 a[44:47], a[164:167], v[152:155], a[44:47]\n"
  "ds_read_b128 a[156:159], v228 offset:8448\n"
  "v_cndmask_b32 v163, v163, v221, vcc\n"
  "v_mfma_f32_16x16x32_bf16 a[60:63], a[172:175], v[152:155], a[60:63]\n"
  "ds_read_b128 a[160:163], v228 offset:512\n"
  "v_max3_f32 v213, v160, v161, v162\n"
  "v_mfma_f32_16x16x32_bf16 a[60:63], a[168:171], v[144:147], a[60:63]\n"
  "buffer_load_dwordx4 v250, s[56:59], s74 offen offset:2048 lds\n"
  "v_max3_f32 v213, v213, v163, v176\n"
  "v_mfma_f32_16x16x32_bf16 a[76:79], a[176:179], v[144:147], a[76:79]\n"
  "v_max3_f32 v213, v213, v177, v178\n"
  "v_cmp_gt_i32 vcc, 16, v222\n"
  "v_mfma_f32_16x16x32_bf16 a[76:79], a[180:183], v[152:155], a[76:79]\n"
  "v_cmp_gt_i32 s[90:91], 17, v222\n"
  "v_cmp_gt_i32 s[92:93], 18, v222\n"
  "v_mfma_f32_16x16x32_bf16 a[92:95], a[188:191], v[152:155], a[92:95]\n"
  "v_cndmask_b32 v164, v164, v221, vcc\n"
  "v_cmp_gt_i32 vcc, 19, v222\n"
  "v_mfma_f32_16x16x32_bf16 a[92:95], a[184:187], v[144:147], a[92:95]\n"
  "buffer_load_dwordx4 v251, s[56:59], s75 offen offset:3072 lds\n"
  "v_cndmask_b32 v165, v165, v221, s[90:91]\n"
  "v_mfma_f32_16x16x32_bf16 a[108:111], a[192:195], v[144:147], a[108:111]\n"
  "v_cndmask_b32 v166, v166, v221, s[92:93]\n"
  "v_cndmask_b32 v167, v167, v221, vcc\n"
  "v_mfma_f32_16x16x32_bf16 a[108:111], a[196:199], v[152:155], a[108:111]\n"
  "v_cmp_gt_i32 vcc, 0, v222\n"
  "v_cmp_gt_i32 s[90:91], 1, v222\n"
  "v_mfma_f32_16x16x32_bf16 a[124:127], a[204:207], v[152:155], a[124:127]\n"
  "v_cmp_gt_i32 s[92:93], 2, v222\n"
  "v_cndmask_b32 v180, v180, v221, vcc\n"
  "v_mfma_f32_16x16x32_bf16 a[124:127], a[200:203], v[144:147], a[124:127]\n"
  "v_cmp_gt_i32 vcc, 3, v222\n"
  "v_cndmask_b32 v181, v181, v221, s[90:91]\n"
  "v_mfma_f32_16x16x32_bf16 a[140:143], v[208:211], v[144:147], a[140:143]\n"
  "v_cndmask_b32 v182, v182, v221, s[92:93]\n"
  "v_cndmask_b32 v183, v183, v221, vcc\n"
  "v_mfma_f32_16x16x32_bf16 a[140:143], v[208:211], v[152:155], a[140:143]\n"
  "v_max3_f32 v213, v213, v179, v164\n"
  "v_max3_f32 v213, v213, v165, v166\n"
  "ds_read_b128 a[164:167], v228 offset:8704\n"
  "ds_read_b128 a[168:171], v228 offset:768\n"
  "ds_read_b128 a[172:175], v228 offset:8960\n"
  "ds_read_b128 a[176:179], v228 offset:4096\n"
  "ds_read_b128 a[180:183], v228 offset:12288\n"
  "ds_read_b128 a[184:187], v228 offset:4352\n"
  "ds_read_b128 a[188:191], v228 offset:12544\n"
  "ds_read_b128 a[192:195], v228 offset:4608\n"
  "ds_read_b128 a[196:199], v228 offset:12800\n"
  "ds_read_b128 a[200:203], v228 offset:4864\n"
  "ds_read_b128 a[204:207], v228 offset:13056\n"
  "v_max3_f32 v213, v213, v167, v180\n"
  "v_max3_f32 v213, v213, v181, v182\n"
  "v_cmp_gt_i32 vcc, 32, v222\n"
  "v_cmp_gt_i32 s[90:91], 33, v222\n"
  "v_cmp_gt_i32 s[92:93], 34, v222\n"
  "v_cndmask_b32 v168, v168, v221, vcc\n"
  "v_cmp_gt_i32 vcc, 35, v222\n"
  "v_cndmask_b32 v169, v169, v221, s[90:91]\n"
  "v_cndmask_b32 v170, v170, v221, s[92:93]\n"
  "v_cndmask_b32 v171, v171, v221, vcc\n"
  "v_cmp_gt_i32 vcc, 16, v222\n"
  "v_cmp_gt_i32 s[90:91], 17, v222\n"
  "v_cmp_gt_i32 s[92:93], 18, v222\n"
  "v_cndmask_b32 v184, v184, v221, vcc\n"
  "v_cmp_gt_i32 vcc, 19, v222\n"
  "v_cndmask_b32 v185, v185, v221, s[90:91]\n"
  "v_cndmask_b32 v186, v186, v221, s[92:93]\n"
  "v_cndmask_b32 v187, v187, v221, vcc\n"
  "v_max3_f32 v213, v213, v183, v168\n"
  "v_max3_f32 v213, v213, v169, v170\n"
  "v_max3_f32 v213, v213, v171, v184\n"
  "v_max3_f32 v213, v213, v185, v186\n"
  "v_mov_b32 v172, s39\n"
  "v_mov_b32 v173, s39\n"
  "v_mov_b32 v174, s39\n"
  "v_mov_b32 v175, s39\n"
  "v_cmp_gt_i32 vcc, 32, v222\n"
  "v_cmp_gt_i32 s[90:91], 33, v222\n"
  "v_cmp_gt_i32 s[92:93], 34, v222\n"
  "v_cndmask_b32 v188, v188, v221, vcc\n"
  "v_cmp_gt_i32 vcc, 35, v222\n"
  "v_cndmask_b32 v189, v189, v221, s[90:91]\n"
  "v_cndmask_b32 v190, v190, v221, s[92:93]\n"
  "v_cndmask_b32 v191, v191, v221, vcc\n"
  "v_max3_f32 v213, v213, v187, v172\n"
  "v_max3_f32 v213, v213, v173, v174\n"
  "v_max3_f32 v213, v213, v175, v188\n"
  "v_max3_f32 v213, v213, v189, v190\n"
  "v_max_f32 v213, v213, v191\n"
  "v_cmp_lt_f32 vcc, s38, v213\n"
  "s_nop 1\n"
  "s_cbranch_vccnz L_tr5%=\n"
  "L_bk5%=:\n"
  "v_exp_f32 v160, v160\n"
  "v_exp_f32 v161, v161\n"
  "v_exp_f32 v162, v162\n"
  "v_exp_f32 v163, v163\n"
  "v_cvt_pk_bf16_f32 v160, v160, v161\n"
  "v_exp_f32 v164, v164\n"
  "v_exp_f32 v165, v165\n"
  "v_cvt_pk_bf16_f32 v161, v162, v163\n"
  "v_exp_f32 v166, v166\n"
  "v_exp_f32 v167, v167\n"
  "v_cvt_pk_bf16_f32 v162, v164, v165\n"
  "v_exp_f32 v168, v168\n"
  "v_exp_f32 v169, v169\n"
  "v_cvt_pk_bf16_f32 v163, v166, v167\n"
  "v_exp_f32 v170, v170\n"
  "v_exp_f32 v171, v171\n"
  "v_cvt_pk_bf16_f32 v168, v168, v169\n"
  "v_exp_f32 v172, v172\n"
  "v_exp_f32 v173, v173\n"
  "v_cvt_pk_bf16_f32 v169, v170, v171\n"
  "v_exp_f32 v174, v174\n"
  "v_exp_f32 v175, v175\n"
  "v_cvt_pk_bf16_f32 v170, v172, v173\n"
  "v_exp_f32 v176, v176\n"
  "v_exp_f32 v177, v177\n"
  "v_cvt_pk_bf16_f32 v171, v174, v175\n"
  "v_exp_f32 v178, v178\n"
  "v_exp_f32 v179, v179\n"
  "v_cvt_pk_bf16_f32 v176, v176, v177\n"
  "v_exp_f32 v180, v180\n"
  "v_exp_f32 v181, v181\n"
  "v_cvt_pk_bf16_f32 v177, v178, v179\n"
  "v_exp_f32 v182, v182\n"
  "v_exp_f32 v183, v183\n"
  "v_cvt_pk_bf16_f32 v178, v180, v181\n"
  "s_waitcnt vmcnt(13) lgkmcnt(0)\n"
  "s_mov_b32 s82, -1\n"
  "s_cmp_ge_u32 s34, 2\n"
  "s_cbranch_scc1 L_at_t6%=\n"
  "s_barrier\n"
  "s_add_u32 s82, s36, 0x8000\n"
  "s_cmp_eq_u32 s82, 0x18000\n"
  "s_cselect_b32 s82, 0, s82\n"
  "L_at_t6%=:\n"
  "v_cmp_ne_u32 vcc, s35, v231\n"
  "s_lshl_b32 s87, s34, 13\n"
  "s_add_u32 s90, s87, s45\n"
  "s_add_u32 s52, s8, s90\n"
  "s_addc_u32 s53, s9, 0\n"
  "s_mov_b32 s54, 0x2000\n"
  "s_mov_b32 s101, 1\n"
  "s_cbranch_vccz L_at_ok%=\n"
  "s_mov_b32 s101, 0\n"
  "s_mov_b32 s54, 0\n"
  "s_mov_b32 s83, 0x28000\n"
  "L_at_ok%=:\n"
  "s_add_u32 s87, s83, 0\n"
  "s_cmp_eq_u32 s82, 0\n"
  "s_cselect_b32 s87, 0x30000, s87\n"
  "s_add_u32 s90, s83, 0x8000\n"
  "s_cmp_eq_u32 s82, 0x8000\n"
  "s_cselect_b32 s90, 0x30000, s90\n"
  "s_add_u32 s91, s83, 0x10000\n"
  "s_cmp_eq_u32 s82, 0x10000\n"
  "s_cselect_b32 s91, 0x30000, s91\n"
  "v_mfma_f32_16x16x32_bf16 a[8:11], a[144:147], v[160:163], a[8:11]\n"
  "s_mov_b32 m0, s87\n"
  "v_cvt_pk_bf16_f32 v179, v182, v183\n"
  "v_mfma_f32_16x16x32_bf16 a[8:11], a[148:151], v[168:171], a[8:11]\n"
  "buffer_load_dwordx4 v252, s[52:55], s64 offen sc1 lds\n"
  "v_accvgpr_read_b32 v16, a32\n"
  "v_mfma_f32_16x16x32_bf16 a[24:27], a[156:159], v[168:171], a[24:27]\n"
  "v_exp_f32 v184, v184\n"
  "v_mfma_f32_16x16x32_bf16 a[24:27], a[152:155], v[160:163], a[24:27]\n"
  "v_exp_f32 v185, v185\n"
  "v_mfma_f32_16x16x32_bf16 a[40:43], a[160:163], v[160:163], a[40:43]\n"
  "v_exp_f32 v186, v186\n"
  "v_mfma_f32_16x16x32_bf16 a[40:43], a[164:167], v[168:171], a[40:43]\n"
  "buffer_load_dwordx4 v253, s[52:55], s65 offen offset:1024 sc1 lds\n"
  "v_cvt_pk_bf16_f32 v184, v184, v185\n"
  "v_mfma_f32_16x16x32_bf16 a[56:59], a[172:175], v[168:171], a[56:59]\n"
  "v_exp_f32 v187, v187\n"
  "v_mfma_f32_16x16x32_bf16 a[56:59], a[168:171], v[160:163], a[56:59]\n"
  "v_exp_f32 v188, v188\n"
  "v_mfma_f32_16x16x32_bf16 a[72:75], a[176:179], v[160:163], a[72:75]\n"
  "v_exp_f32 v189, v189\n"
  "v_mfma_f32_16x16x32_bf16 a[72:75], a[180:183], v[168:171], a[72:75]\n"
  "buffer_load_dwordx4 v252, s[52:55], s66 offen offset:2048 sc1 lds\n"
  "v_cvt_pk_bf16_f32 v185, v186, v187\n"
  "v_mfma_f32_16x16x32_bf16 a[88:91], a[188:191], v[168:171], a[88:91]\n"
  "v_exp_f32 v190, v190\n"
  "v_mfma_f32_16x16x32_bf16 a[88:91], a[184:187], v[160:163], a[88:91]\n"
  "v_exp_f32 v191, v191\n"
  "v_mfma_f32_16x16x32_bf16 a[104:107], a[192:195], v[160:163], a[104:107]\n"
  "v_cvt_pk_bf16_f32 v186, v188, v189\n"
  "v_cvt_pk_bf16_f32 v187, v190, v191\n"
  "v_mfma_f32_16x16x32_bf16 a[104:107], a[196:199], v[168:171], a[104:107]\n"
  "buffer_load_dwordx4 v253, s[52:55], s67 offen offset:3072 sc1 lds\n"
  "s_add_u32 m0, m0, 0x1000\n"
  "v_mfma_f32_16x16x32_bf16 a[120:123], a[204:207], v[168:171], a[120:123]\n"
  "buffer_load_dwordx4 v252, s[52:55], s68 offen sc1 lds\n"
  "v_accvgpr_read_b32 v17, a33\n"
  "v_mfma_f32_16x16x32_bf16 a[120:123], a[200:203], v[160:163], a[120:123]\n"
  "v_accvgpr_read_b32 v18, a34\n"
  "v_accvgpr_read_b32 v19, a35\n"
  "v_mfma_f32_16x16x32_bf16 a[136:139], v[208:211], v[160:163], a[136:139]\n"
  "v_accvgpr_read_b32 v20, a48\n"
  "v_accvgpr_read_b32 v21, a49\n"
  "v_mfma_f32_16x16x32_bf16 a[136:139], v[208:211], v[168:171], a[136:139]\n"
  "v_accvgpr_read_b32 v22, a50\n"
  "v_accvgpr_read_b32 v23, a51\n"
  "v_mfma_f32_16x16x32_bf16 a[12:15], a[144:147], v[176:179], a[12:15]\n"
  "buffer_load_dwordx4 v253, s[52:55], s69 offen offset:1024 sc1 lds\n"
  "v_mul_f32 v16, v16, v0\n"
  "v_mfma_f32_16x16x32_bf16 a[12:15], a[148:151], v[184:187], a[12:15]\n"
  "v_mul_f32 v17, v17, v0\n"
  "v_mul_f32 v18, v18, v0\n"
  "v_mfma_f32_16x16x32_bf16 a[28:31], a[156:159], v[184:187], a[28:31]\n"
  "v_mul_f32 v19, v19, v0\n"
  "v_mul_f32 v20, v20, v0\n"
  "v_mfma_f32_16x16x32_bf16 a[28:31], a[152:155], v[176:179], a[28:31]\n"
  "v_mul_f32 v21, v21, v0\n"
  "v_mul_f32 v22, v22, v0\n"
  "v_mfma_f32_16x16x32_bf16 a[44:47], a[160:163], v[176:179], a[44:47]\n"
  "buffer_load_dwordx4 v252, s[52:55], s70 offen offset:2048 sc1 lds\n"
  "v_mul_f32 v23, v23, v0\n"
  "v_mfma_f32_16x16x32_bf16 a[44:47], a[164:167], v[184:187], a[44:47]\n"
  "v_cvt_pk_bf16_f32 v24, v16, v17\n"
  "v_cvt_pk_bf16_f32 v25, v18, v19\n"
  "v_mfma_f32_16x16x32_bf16 a[60:63], a[172:175], v[184:187], a[60:63]\n"
  "v_cvt_pk_bf16_f32 v26, v20, v21\n"
  "v_cvt_pk_bf16_f32 v27, v22, v23\n"
  "v_mfma_f32_16x16x32_bf16 a[60:63], a[168:171], v[176:179], a[60:63]\n"
  "buffer_store_dwordx4 v[24:27], v223, s[60:63], s88 offen sc1\n"
  "v_accvgpr_read_b32 v4, a64\n"
  "v_mfma_f32_16x16x32_bf16 a[76:79], a[176:179], v[176:179], a[76:79]\n"
  "buffer_load_dwordx4 v253, s[52:55], s71 offen offset:3072 sc1 lds\n"
  "v_accvgpr_read_b32 v5, a65\n"
  "v_mfma_f32_16x16x32_bf16 a[76:79], a[180:183], v[184:187], a[76:79]\n"
  "s_add_u32 s52, s52, s81\n"
  "s_addc_u32 s53, s53, 0\n"
  "v_mfma_f32_16x16x32_bf16 a[92:95], a[188:191], v[184:187], a[92:95]\n"
  "s_mov_b32 m0, s90\n"
  "v_accvgpr_read_b32 v6, a66\n"
  "v_mfma_f32_16x16x32_bf16 a[92:95], a[184:187], v[176:179], a[92:95]\n"
  "buffer_load_dwordx4 v252, s[52:55], s64 offen sc1 lds\n"
  "v_accvgpr_read_b32 v7, a67\n"
  "v_mfma_f32_16x16x32_bf16 a[108:111], a[192:195], v[176:179], a[108:111]\n"
  "v_accvgpr_read_b32 v8, a80\n"
  "v_accvgpr_read_b32 v9, a81\n"
  "v_mfma_f32_16x16x32_bf16 a[108:111], a[196:199], v[184:187], a[108:111]\n"
  "v_accvgpr_read_b32 v10, a82\n"
  "v_accvgpr_read_b32 v11, a83\n"
  "v_mfma_f32_16x16x32_bf16 a[124:127], a[204:207], v[184:187], a[124:127]\n"
  "v_mul_f32 v4, v4, v0\n"
  "v_mul_f32 v5, v5, v0\n"
  "v_mfma_f32_16x16x32_bf16 a[124:127], a[200:203], v[176:179], a[124:127]\n"
  "buffer_load_dwordx4 v253, s[52:55], s65 offen offset:1024 sc1 lds\n"
  "v_mul_f32 v6, v6, v0\n"
  "v_mfma_f32_16x16x32_bf16 a[140:143], v[208:211], v[176:179], a[140:143]\n"
  "v_mul_f32 v7, v7, v0\n"
  "v_mul_f32 v8, v8, v0\n"
  "v_mfma_f32_16x16x32_bf16 a[140:143], v[208:211], v[184:187], a[140:143]\n"
  "v_mul_f32 v9, v9, v0\n"
  "v_mul_f32 v10, v10, v0\n"
  "s_cmp_lt_u32 s34, 2\n"
  "s_cbranch_scc1 L_at_nb%=\n"
  "s_barrier\n"
  "L_at_nb%=:\n"
  "v_mul_f32 v11, v11, v0\n"
  "v_cvt_pk_bf16_f32 v12, v4, v5\n"
  "v_cvt_pk_bf16_f32 v13, v6, v7\n"
  "v_cvt_pk_bf16_f32 v14, v8, v9\n"
  "v_cvt_pk_bf16_f32 v15, v10, v11\n"
  "buffer_store_dwordx4 v[12:15], v223, s[60:63], s89 offen sc1\n"
  "v_accvgpr_read_b32 v16, a96\n"
  "v_accvgpr_read_b32 v17, a97\n"
  "v_accvgpr_read_b32 v18, a98\n"
  "v_accvgpr_read_b32 v19, a99\n"
  "v_accvgpr_read_b32 v20, a112\n"
  "v_accvgpr_read_b32 v21, a113\n"
  "v_accvgpr_read_b32 v22, a114\n"
  "v_accvgpr_read_b32 v23, a115\n"
  "buffer_load_dwordx4 v252, s[52:55], s66 offen offset:2048 sc1 lds\n"
  "v_pk_mul_f32 v[16:17], v[16:17], v[0:1]\n"
  "v_pk_mul_f32 v[18:19], v[18:19], v[0:1]\n"
  "v_pk_mul_f32 v[20:21], v[20:21], v[0:1]\n"
  "v_pk_mul_f32 v[22:23], v[22:23], v[0:1]\n"
  "v_cvt_pk_bf16_f32 v24, v16, v17\n"
  "v_cvt_pk_bf16_f32 v25, v18, v19\n"
  "v_cvt_pk_bf16_f32 v26, v20, v21\n"
  "v_cvt_pk_bf16_f32 v27, v22, v23\n"
  "buffer_store_dwordx4 v[24:27], v223, s[60:63], s100 offen sc1\n"
  "v_accvgpr_read_b32 v4, a4\n"
  "v_accvgpr_read_b32 v5, a5\n"
  "v_accvgpr_read_b32 v6, a6\n"
  "v_accvgpr_read_b32 v7, a7\n"
  "v_accvgpr_read_b32 v8, a20\n"
  "buffer_load_dwordx4 v253, s[52:55], s67 offen offset:3072 sc1 lds\n"
  "v_accvgpr_read_b32 v9, a21\n"
  "v_accvgpr_read_b32 v10, a22\n"
  "v_accvgpr_read_b32 v11, a23\n"
  "v_pk_mul_f32 v[4:5], v[4:5], v[2:3]\n"
  "v_pk_mul_f32 v[6:7], v[6:7], v[2:3]\n"
  "v_pk_mul_f32 v[8:9], v[8:9], v[2:3]\n"
  "v_pk_mul_f32 v[10:11], v[10:11], v[2:3]\n"
  "v_cvt_pk_bf16_f32 v12, v4, v5\n"
  "v_cvt_pk_bf16_f32 v13, v6, v7\n"
  "v_cvt_pk_bf16_f32 v14, v8, v9\n"
  "v_cvt_pk_bf16_f32 v15, v10, v11\n"
  "buffer_store_dwordx4 v[12:15], v223, s[60:63], 0 offen offset:256 sc1\n"
  "v_accvgpr_read_b32 v16, a36\n"
  "s_add_u32 m0, m0, 0x1000\n"
  "v_accvgpr_read_b32 v17, a37\n"
  "buffer_load_dwordx4 v252, s[52:55], s68 offen sc1 lds\n"
  "v_accvgpr_read_b32 v18, a38\n"
  "v_accvgpr_read_b32 v19, a39\n"
  "v_accvgpr_read_b32 v20, a52\n"
  "v_accvgpr_read_b32 v21, a53\n"
  "v_accvgpr_read_b32 v22, a54\n"
  "v_accvgpr_read_b32 v23, a55\n"
  "v_pk_mul_f32 v[16:17], v[16:17], v[2:3]\n"
  "v_pk_mul_f32 v[18:19], v[18:19], v[2:3]\n"
  "v_pk_mul_f32 v[20:21], v[20:21], v[2:3]\n"
  "v_pk_mul_f32 v[22:23], v[22:23], v[2:3]\n"
  "v_cvt_pk_bf16_f32 v24, v16, v17\n"
  "v_cvt_pk_bf16_f32 v25, v18, v19\n"
  "v_cvt_pk_bf16_f32 v26, v20, v21\n"
  "v_cvt_pk_bf16_f32 v27, v22, v23\n"
  "buffer_load_dwordx4 v253, s[52:55], s69 offen offset:1024 sc1 lds\n"
  "buffer_store_dwordx4 v[24:27], v223, s[60:63], s88 offen offset:256 sc1\n"
  "v_accvgpr_read_b32 v4, a68\n"
  "v_accvgpr_read_b32 v5, a69\n"
  "v_accvgpr_read_b32 v6, a70\n"
  "v_accvgpr_read_b32 v7, a71\n"
  "v_accvgpr_read_b32 v8, a84\n"
  "v_accvgpr_read_b32 v9, a85\n"
  "v_accvgpr_read_b32 v10, a86\n"
  "v_accvgpr_read_b32 v11, a87\n"
  "v_pk_mul_f32 v[4:5], v[4:5], v[2:3]\n"
  "v_pk_mul_f32 v[6:7], v[6:7], v[2:3]\n"
  "v_pk_mul_f32 v[8:9], v[8:9], v[2:3]\n"
  "v_pk_mul_f32 v[10:11], v[10:11], v[2:3]\n"
  "v_cvt_pk_bf16_f32 v12, v4, v5\n"
  "buffer_load_dwordx4 v252, s[52:55], s70 offen offset:2048 sc1 lds\n"
  "v_cvt_pk_bf16_f32 v13, v6, v7\n"
  "v_cvt_pk_bf16_f32 v14, v8, v9\n"
  "v_cvt_pk_bf16_f32 v15, v10, v11\n"
  "buffer_store_dwordx4 v[12:15], v223, s[60:63], s89 offen offset:256 sc1\n"
  "v_accvgpr_read_b32 v16, a100\n"
  "v_accvgpr_read_b32 v17, a101\n"
  "v_accvgpr_read_b32 v18, a102\n"
  "v_accvgpr_read_b32 v19, a103\n"
  "v_accvgpr_read_b32 v20, a116\n"
  "v_accvgpr_read_b32 v21, a117\n"
  "v_accvgpr_read_b32 v22, a118\n"
  "v_accvgpr_read_b32 v23, a119\n"
  "v_pk_mul_f32 v[16:17], v[16:17], v[2:3]\n"
  "v_pk_mul_f32 v[18:19], v[18:19], v[2:3]\n"
  "buffer_load_dwordx4 v253, s[52:55], s71 offen offset:3072 sc1 lds\n"
  "v_pk_mul_f32 v[20:21], v[20:21], v[2:3]\n"
  "v_pk_mul_f32 v[22:23], v[22:23], v[2:3]\n"
  "v_cvt_pk_bf16_f32 v24, v16, v17\n"
  "v_cvt_pk_bf16_f32 v25, v18, v19\n"
  "v_cvt_pk_bf16_f32 v26, v20, v21\n"
  "v_cvt_pk_bf16_f32 v27, v22, v23\n"
  "buffer_store_dwordx4 v[24:27], v223, s[60:63], s100 offen offset:256 sc1\n"
  "v_accvgpr_read_b32 v64, a136\n"
  "v_accvgpr_read_b32 v66, a140\n"
  "v_rcp_f32 v64, v64\n"
  "v_rcp_f32 v66, v66\n"
  "s_nop 0\n"
  "v_mul_f32 v65, v64, v226\n"
  "s_add_u32 s52, s52, s81\n"
  "s_addc_u32 s53, s53, 0\n"
  "s_mov_b32 m0, s91\n"
  "v_mul_f32 v64, v64, v226\n"
  "buffer_load_dwordx4 v252, s[52:55], s64 offen sc1 lds\n"
  "v_mul_f32 v67, v66, v227\n"
  "v_mul_f32 v66, v66, v227\n"
  "v_accvgpr_read_b32 v68, a8\n"
  "v_accvgpr_read_b32 v69, a9\n"
  "v_accvgpr_read_b32 v70, a10\n"
  "v_accvgpr_read_b32 v71, a11\n"
  "v_accvgpr_read_b32 v72, a24\n"
  "v_accvgpr_read_b32 v73, a25\n"
  "v_accvgpr_read_b32 v74, a26\n"
  "v_accvgpr_read_b32 v75, a27\n"
  "v_pk_mul_f32 v[68:69], v[68:69], v[64:65]\n"
  "v_pk_mul_f32 v[70:71], v[70:71], v[64:65]\n"
  "v_pk_mul_f32 v[72:73], v[72:73], v[64:65]\n"
  "v_pk_mul_f32 v[74:75], v[74:75], v[64:65]\n"
  "buffer_load_dwordx4 v253, s[52:55], s65 offen offset:1024 sc1 lds\n"
  "v_cvt_pk_bf16_f32 v76, v68, v69\n"
  "v_cvt_pk_bf16_f32 v77, v70, v71\n"
  "v_cvt_pk_bf16_f32 v78, v72, v73\n"
  "v_cvt_pk_bf16_f32 v79, v74, v75\n"
  "buffer_store_dwordx4 v[76:79], v223, s[60:63], 0 offen offset:2048 sc1\n"
  "v_accvgpr_read_b32 v80, a40\n"
  "v_accvgpr_read_b32 v81, a41\n"
  "v_accvgpr_read_b32 v82, a42\n"
  "v_accvgpr_read_b32 v83, a43\n"
  "v_accvgpr_read_b32 v84, a56\n"
  "v_accvgpr_read_b32 v85, a57\n"
  "v_accvgpr_read_b32 v86, a58\n"
  "v_accvgpr_read_b32 v87, a59\n"
  "v_pk_mul_f32 v[80:81], v[80:81], v[64:65]\n"
  "buffer_load_dwordx4 v252, s[52:55], s66 offen offset:2048 sc1 lds\n"
  "v_pk_mul_f32 v[82:83], v[82:83], v[64:65]\n"
  "v_pk_mul_f32 v[84:85], v[84:85], v[64:65]\n"
  "v_pk_mul_f32 v[86:87], v[86:87], v[64:65]\n"
  "v_cvt_pk_bf16_f32 v88, v80, v81\n"
  "v_cvt_pk_bf16_f32 v89, v82, v83\n"
  "v_cvt_pk_bf16_f32 v90, v84, v85\n"
  "v_cvt_pk_bf16_f32 v91, v86, v87\n"
  "buffer_store_dwordx4 v[88:91], v223, s[60:63], s88 offen offset:2048 sc1\n"
  "v_accvgpr_read_b32 v92, a72\n"
  "v_accvgpr_read_b32 v93, a73\n"
  "v_accvgpr_read_b32 v94, a74\n"
  "v_accvgpr_read_b32 v95, a75\n"
  "v_accvgpr_read_b32 v96, a88\n"
  "v_accvgpr_read_b32 v97, a89\n"
  "buffer_load_dwordx4 v253, s[52:55], s67 offen offset:3072 sc1 lds\n"
  "v_accvgpr_read_b32 v98, a90\n"
  "v_accvgpr_read_b32 v99, a91\n"
  "v_pk_mul_f32 v[92:93], v[92:93], v[64:65]\n"
  "v_pk_mul_f32 v[94:95], v[94:95], v[64:65]\n"
  "v_pk_mul_f32 v[96:97], v[96:97], v[64:65]\n"
  "v_pk_mul_f32 v[98:99], v[98:99], v[64:65]\n"
  "v_cvt_pk_bf16_f32 v100, v92, v93\n"
  "v_cvt_pk_bf16_f32 v101, v94, v95\n"
  "v_cvt_pk_bf16_f32 v102, v96, v97\n"
  "v_cvt_pk_bf16_f32 v103, v98, v99\n"
  "buffer_store_dwordx4 v[100:103], v223, s[60:63], s89 offen offset:2048 sc1\n"
  "v_accvgpr_read_b32 v104, a104\n"
  "v_accvgpr_read_b32 v105, a105\n"
  "v_accvgpr_read_b32 v106, a106\n"
  "v_accvgpr_read_b32 v107, a107\n"
  "v_accvgpr_read_b32 v108, a120\n"
  "v_accvgpr_read_b32 v109, a121\n"
  "v_accvgpr_read_b32 v110, a122\n"
  "v_accvgpr_read_b32 v111, a123\n"
  "v_pk_mul_f32 v[104:105], v[104:105], v[64:65]\n"
  "v_pk_mul_f32 v[106:107], v[106:107], v[64:65]\n"
  "v_pk_mul_f32 v[108:109], v[108:109], v[64:65]\n"
  "v_pk_mul_f32 v[110:111], v[110:111], v[64:65]\n"
  "v_cvt_pk_bf16_f32 v112, v104, v105\n"
  "v_cvt_pk_bf16_f32 v113, v106, v107\n"
  "v_cvt_pk_bf16_f32 v114, v108, v109\n"
  "v_cvt_pk_bf16_f32 v115, v110, v111\n"
  "buffer_store_dwordx4 v[112:115], v223, s[60:63], s100 offen offset:2048 sc1\n"
  "v_accvgpr_read_b32 v68, a12\n"
  "v_accvgpr_read_b32 v69, a13\n"
  "v_accvgpr_read_b32 v70, a14\n"
  "v_accvgpr_read_b32 v71, a15\n"
  "v_accvgpr_read_b32 v72, a28\n"
  "v_accvgpr_read_b32 v73, a29\n"
  "v_accvgpr_read_b32 v74, a30\n"
  "v_accvgpr_read_b32 v75, a31\n"
  "v_pk_mul_f32 v[68:69], v[68:69], v[66:67]\n"
  "v_pk_mul_f32 v[70:71], v[70:71], v[66:67]\n"
  "v_pk_mul_f32 v[72:73], v[72:73], v[66:67]\n"
  "v_pk_mul_f32 v[74:75], v[74:75], v[66:67]\n"
  "v_cvt_pk_bf16_f32 v76, v68, v69\n"
  "v_cvt_pk_bf16_f32 v77, v70, v71\n"
  "v_cvt_pk_bf16_f32 v78, v72, v73\n"
  "v_cvt_pk_bf16_f32 v79, v74, v75\n"
  "buffer_store_dwordx4 v[76:79], v223, s[60:63], 0 offen offset:2304 sc1\n"
  "v_accvgpr_read_b32 v80, a44\n"
  "v_accvgpr_read_b32 v81, a45\n"
  "v_accvgpr_read_b32 v82, a46\n"
  "v_accvgpr_read_b32 v83, a47\n"
  "v_accvgpr_read_b32 v84, a60\n"
  "v_accvgpr_read_b32 v85, a61\n"
  "v_accvgpr_read_b32 v86, a62\n"
  "v_accvgpr_read_b32 v87, a63\n"
  "v_pk_mul_f32 v[80:81], v[80:81], v[66:67]\n"
  "v_pk_mul_f32 v[82:83], v[82:83], v[66:67]\n"
  "v_pk_mul_f32 v[84:85], v[84:85], v[66:67]\n"
  "v_pk_mul_f32 v[86:87], v[86:87], v[66:67]\n"
  "v_cvt_pk_bf16_f32 v88, v80, v81\n"
  "v_cvt_pk_bf16_f32 v89, v82, v83\n"
  "v_cvt_pk_bf16_f32 v90, v84, v85\n"
  "v_cvt_pk_bf16_f32 v91, v86, v87\n"
  "buffer_store_dwordx4 v[88:91], v223, s[60:63], s88 offen offset:2304 sc1\n"
  "v_accvgpr_read_b32 v92, a76\n"
  "v_accvgpr_read_b32 v93, a77\n"
  "v_accvgpr_read_b32 v94, a78\n"
  "v_accvgpr_read_b32 v95, a79\n"
  "v_accvgpr_read_b32 v96, a92\n"
  "v_accvgpr_read_b32 v97, a93\n"
  "v_accvgpr_read_b32 v98, a94\n"
  "v_accvgpr_read_b32 v99, a95\n"
  "v_pk_mul_f32 v[92:93], v[92:93], v[66:67]\n"
  "v_pk_mul_f32 v[94:95], v[94:95], v[66:67]\n"
  "v_pk_mul_f32 v[96:97], v[96:97], v[66:67]\n"
  "v_pk_mul_f32 v[98:99], v[98:99], v[66:67]\n"
  "v_cvt_pk_bf16_f32 v100, v92, v93\n"
  "v_cvt_pk_bf16_f32 v101, v94, v95\n"
  "v_cvt_pk_bf16_f32 v102, v96, v97\n"
  "v_cvt_pk_bf16_f32 v103, v98, v99\n"
  "buffer_store_dwordx4 v[100:103], v223, s[60:63], s89 offen offset:2304 sc1\n"
  "v_accvgpr_read_b32 v104, a108\n"
  "v_accvgpr_read_b32 v105, a109\n"
  "v_accvgpr_read_b32 v106, a110\n"
  "v_accvgpr_read_b32 v107, a111\n"
  "v_accvgpr_read_b32 v108, a124\n"
  "v_accvgpr_read_b32 v109, a125\n"
  "v_accvgpr_read_b32 v110, a126\n"
  "v_accvgpr_read_b32 v111, a127\n"
  "v_pk_mul_f32 v[104:105], v[104:105], v[66:67]\n"
  "v_pk_mul_f32 v[106:107], v[106:107], v[66:67]\n"
  "v_pk_mul_f32 v[108:109], v[108:109], v[66:67]\n"
  "v_pk_mul_f32 v[110:111], v[110:111], v[66:67]\n"
  "v_cvt_pk_bf16_f32 v112, v104, v105\n"
  "v_cvt_pk_bf16_f32 v113, v106, v107\n"
  "v_cvt_pk_bf16_f32 v114, v108, v109\n"
  "v_cvt_pk_bf16_f32 v115, v110, v111\n"
  "buffer_store_dwordx4 v[112:115], v223, s[60:63], s100 offen offset:2304 sc1\n"
  "s_waitcnt vmcnt(13)\n"
  "s_cmp_ge_u32 s34, 2\n"
  "s_cbranch_scc1 L_next%=\n"
  "s_barrier\n"
  "s_lshr_b32 s87, s82, 15\n"
  "s_sub_u32 s87, 2, s87\n"
  "s_mul_i32 s87, s87, s81\n"
  "s_sub_u32 s52, s52, s87\n"
  "s_subb_u32 s53, s53, 0\n"
  "s_add_u32 m0, s83, s82\n"
  "s_nop 0\n"
  "buffer_load_dwordx4 v252, s[52:55], s64 offen sc1 lds\n"
  "buffer_load_dwordx4 v253, s[52:55], s65 offen offset:1024 sc1 lds\n"
  "buffer_load_dwordx4 v252, s[52:55], s66 offen offset:2048 sc1 lds\n"
  "buffer_load_dwordx4 v253, s[52:55], s67 offen offset:3072 sc1 lds\n"
  "s_add_u32 m0, m0, 0x1000\n"
  "s_nop 0\n"
  "buffer_load_dwordx4 v252, s[52:55], s68 offen sc1 lds\n"
  "buffer_load_dwordx4 v253, s[52:55], s69 offen offset:1024 sc1 lds\n"
  "buffer_load_dwordx4 v252, s[52:55], s70 offen offset:2048 sc1 lds\n"
  "buffer_load_dwordx4 v253, s[52:55], s71 offen offset:3072 sc1 lds\n"
  "s_add_u32 s52, s52, s87\n"
  "s_addc_u32 s53, s53, 0\n"
  "s_waitcnt vmcnt(8)\n"
  "s_cmp_eq_u32 s86, 0\n"
  "s_cbranch_scc1 L_sg_at%=\n"
  "s_lshl2_add_u32 s86, s34, s86\n"
  "v_mov_b32 v220, s86\n"
  "v_mov_b32 v221, s35\n"
  "global_store_dword v220, v221, s[8:9] sc1\n"
  "s_mov_b32 s86, 0\n"
  "L_sg_at%=:\n"
  "s_or_b32 s87, s98, s34\n"
  "s_cbranch_scc1 L_eb_done%=\n"
  "v_lshlrev_b32 v80, 4, v255\n"
  "v_add_u32 v80, 0x4000004, v80\n"
  "v_add_u32 v81, 0x1000, v80\n"
  "v_add_u32 v82, 0x2000, v80\n"
  "v_add_u32 v83, 0x3000, v80\n"
  "s_mov_b32 s99, 0x4000\n"
  "L_eb_p%=:\n"
  "global_load_dword v64, v80, s[8:9] offset:0 sc1\n"
  "global_load_dword v65, v80, s[8:9] offset:1024 sc1\n"
  "global_load_dword v66, v80, s[8:9] offset:2048 sc1\n"
  "global_load_dword v67, v80, s[8:9] offset:3072 sc1\n"
  "global_load_dword v68, v81, s[8:9] offset:0 sc1\n"
  "global_load_dword v69, v81, s[8:9] offset:1024 sc1\n"
  "global_load_dword v70, v81, s[8:9] offset:2048 sc1\n"
  "global_load_dword v71, v81, s[8:9] offset:3072 sc1\n"
  "global_load_dword v72, v82, s[8:9] offset:0 sc1\n"
  "global_load_dword v73, v82, s[8:9] offset:1024 sc1\n"
  "global_load_dword v74, v82, s[8:9] offset:2048 sc1\n"
  "global_load_dword v75, v82, s[8:9] offset:3072 sc1\n"
  "global_load_dword v76, v83, s[8:9] offset:0 sc1\n"
  "global_load_dword v77, v83, s[8:9] offset:1024 sc1\n"
  "global_load_dword v78, v83, s[8:9] offset:2048 sc1\n"
  "global_load_dword v79, v83, s[8:9] offset:3072 sc1\n"
  "s_waitcnt vmcnt(0)\n"
  "v_xor_b32 v64, s35, v64\n"
  "v_xor_b32 v65, s35, v65\n"
  "v_xor_b32 v66, s35, v66\n"
  "v_xor_b32 v67, s35, v67\n"
  "v_xor_b32 v68, s35, v68\n"
  "v_xor_b32 v69, s35, v69\n"
  "v_xor_b32 v70, s35, v70\n"
  "v_xor_b32 v71, s35, v71\n"
  "v_xor_b32 v72, s35, v72\n"
  "v_xor_b32 v73, s35, v73\n"
  "v_xor_b32 v74, s35, v74\n"
  "v_xor_b32 v75, s35, v75\n"
  "v_xor_b32 v76, s35, v76\n"
  "v_xor_b32 v77, s35, v77\n"
  "v_xor_b32 v78, s35, v78\n"
  "v_xor_b32 v79, s35, v79\n"
  "v_or_b32 v64, v64, v65\n"
  "v_or_b32 v64, v64, v66\n"
  "v_or_b32 v64, v64, v67\n"
  "v_or_b32 v64, v64, v68\n"
  "v_or_b32 v64, v64, v69\n"
  "v_or_b32 v64, v64, v70\n"
  "v_or_b32 v64, v64, v71\n"
  "v_or_b32 v64, v64, v72\n"
  "v_or_b32 v64, v64, v73\n"
  "v_or_b32 v64, v64, v74\n"
  "v_or_b32 v64, v64, v75\n"
  "v_or_b32 v64, v64, v76\n"
  "v_or_b32 v64, v64, v77\n"
  "v_or_b32 v64, v64, v78\n"
  "v_or_b32 v64, v64, v79\n"
  "v_cmp_ne_u32 vcc, 0, v64\n"
  "s_nop 4\n"
  "s_cbranch_vccz L_eb_ok%=\n"
  "s_sleep 2\n"
  "s_sub_u32 s99, s99, 1\n"
  "s_cbranch_scc0 L_eb_p%=\n"
  "L_eb_ok%=:\n"
  "v_mov_b32 v64, 0x5a00000\n"
  "v_mov_b32 v65, s35\n"
  "global_store_dword v64, v65, s[8:9] sc1\n"
  "L_eb_done%=:\n"
  "s_waitcnt vmcnt(1)\n"
  "s_branch L_next%=\n"
  "L_tr0%=:\n"
  "s_getpc_b64 s[40:41]\n"
  "s_add_u32 s40, s40, 12\n"
  "s_addc_u32 s41, s41, 0\n"
  "s_branch L_rare11%=\n"
  "s_branch L_bk0%=\n"
  "L_tr1%=:\n"
  "s_getpc_b64 s[40:41]\n"
  "s_add_u32 s40, s40, 12\n"
  "s_addc_u32 s41, s41, 0\n"
  "s_branch L_rare00%=\n"
  "s_branch L_bk1%=\n"
  "L_tr2%=:\n"
  "s_getpc_b64 s[40:41]\n"
  "s_add_u32 s40, s40, 12\n"
  "s_addc_u32 s41, s41, 0\n"
  "s_branch L_rare11%=\n"
  "s_branch L_bk2%=\n"
  "L_tr3%=:\n"
  "s_getpc_b64 s[40:41]\n"
  "s_add_u32 s40, s40, 12\n"
  "s_addc_u32 s41, s41, 0\n"
  "s_branch L_rare00%=\n"
  "s_branch L_bk3%=\n"
  "L_tr4%=:\n"
  "s_getpc_b64 s[40:41]\n"
  "s_add_u32 s40, s40, 12\n"
  "s_addc_u32 s41, s41, 0\n"
  "s_branch L_rare10%=\n"
  "s_branch L_bk4%=\n"
  "L_tr5%=:\n"
  "s_getpc_b64 s[40:41]\n"
  "s_add_u32 s40, s40, 12\n"
  "s_addc_u32 s41, s41, 0\n"
  "s_branch L_rare11%=\n"
  "s_branch L_bk5%=\n"
  "L_rare00%=:\n"
  "s_nop 9\n"
  "v_max3_f32 v214, v128, v129, v130\n"
  "v_max3_f32 v214, v214, v131, v132\n"
  "v_max3_f32 v214, v214, v133, v134\n"
  "v_max3_f32 v214, v214, v135, v136\n"
  "v_max3_f32 v214, v214, v137, v138\n"
  "v_max3_f32 v214, v214, v139, v140\n"
  "v_max3_f32 v214, v214, v141, v142\n"
  "v_max_f32 v214, v214, v143\n"
  "v_max3_f32 v215, v144, v145, v146\n"
  "v_max3_f32 v215, v215, v147, v148\n"
  "v_max3_f32 v215, v215, v149, v150\n"
  "v_max3_f32 v215, v215, v151, v152\n"
  "v_max3_f32 v215, v215, v153, v154\n"
  "v_max3_f32 v215, v215, v155, v156\n"
  "v_max3_f32 v215, v215, v157, v158\n"
  "v_max_f32 v215, v215, v159\n"
  "v_mov_b32 v216, v214\n"
  "v_mov_b32 v217, v215\n"
  "s_nop 1\n"
  "v_permlane32_swap_b32 v214, v216\n"
  "v_permlane32_swap_b32 v215, v217\n"
  "s_nop 1\n"
  "v_max_f32 v214, v214, v216\n"
  "v_max_f32 v215, v215, v217\n"
  "v_mov_b32 v216, v214\n"
  "v_mov_b32 v217, v215\n"
  "s_nop 1\n"
  "v_permlane16_swap_b32 v214, v216\n"
  "v_permlane16_swap_b32 v215, v217\n"
  "s_nop 1\n"
  "v_max_f32 v214, v214, v216\n"
  "v_max_f32 v215, v215, v217\n"
  "v_min_f32 v216, s37, v220\n"
  "v_max_f32 v214, v214, v216\n"
  "v_max_f32 v215, v215, v216\n"
  "v_sub_f32 v128, v128, v214\n"
  "v_sub_f32 v129, v129, v214\n"
  "v_sub_f32 v130, v130, v214\n"
  "v_sub_f32 v131, v131, v214\n"
  "v_sub_f32 v132, v132, v214\n"
  "v_sub_f32 v133, v133, v214\n"
  "v_sub_f32 v134, v134, v214\n"
  "v_sub_f32 v135, v135, v214\n"
  "v_sub_f32 v136, v136, v214\n"
  "v_sub_f32 v137, v137, v214\n"
  "v_sub_f32 v138, v138, v214\n"
  "v_sub_f32 v139, v139, v214\n"
  "v_sub_f32 v140, v140, v214\n"
  "v_sub_f32 v141, v141, v214\n"
  "v_sub_f32 v142, v142, v214\n"
  "v_sub_f32 v143, v143, v214\n"
  "v_sub_f32 v192, v192, v214\n"
  "v_sub_f32 v193, v193, v214\n"
  "v_sub_f32 v194, v194, v214\n"
  "v_sub_f32 v195, v195, v214\n"
  "v_sub_f32 v144, v144, v215\n"
  "v_sub_f32 v145, v145, v215\n"
  "v_sub_f32 v146, v146, v215\n"
  "v_sub_f32 v147, v147, v215\n"
  "v_sub_f32 v148, v148, v215\n"
  "v_sub_f32 v149, v149, v215\n"
  "v_sub_f32 v150, v150, v215\n"
  "v_sub_f32 v151, v151, v215\n"
  "v_sub_f32 v152, v152, v215\n"
  "v_sub_f32 v153, v153, v215\n"
  "v_sub_f32 v154, v154, v215\n"
  "v_sub_f32 v155, v155, v215\n"
  "v_sub_f32 v156, v156, v215\n"
  "v_sub_f32 v157, v157, v215\n"
  "v_sub_f32 v158, v158, v215\n"
  "v_sub_f32 v159, v159, v215\n"
  "v_sub_f32 v196, v196, v215\n"
  "v_sub_f32 v197, v197, v215\n"
  "v_sub_f32 v198, v198, v215\n"
  "v_sub_f32 v199, v199, v215\n"
  "s_cmp_eq_u32 s37, s39\n"
  "s_mov_b32 s37, 0x41800000\n"
  "s_cbranch_scc0 L_resc0%=\n"
  "s_nop 1\n"
  "s_setpc_b64 s[40:41]\n"
  "L_rare10%=:\n"
  "s_nop 9\n"
  "v_max3_f32 v214, v128, v129, v130\n"
  "v_max3_f32 v214, v214, v131, v132\n"
  "v_max3_f32 v214, v214, v133, v134\n"
  "v_max3_f32 v214, v214, v135, v136\n"
  "v_max3_f32 v214, v214, v137, v138\n"
  "v_max3_f32 v214, v214, v139, v140\n"
  "v_max3_f32 v214, v214, v141, v142\n"
  "v_max_f32 v214, v214, v143\n"
  "v_max3_f32 v215, v144, v145, v146\n"
  "v_max3_f32 v215, v215, v147, v148\n"
  "v_max3_f32 v215, v215, v149, v150\n"
  "v_max3_f32 v215, v215, v151, v152\n"
  "v_max3_f32 v215, v215, v153, v154\n"
  "v_max3_f32 v215, v215, v155, v156\n"
  "v_max3_f32 v215, v215, v157, v158\n"
  "v_max_f32 v215, v215, v159\n"
  "v_mov_b32 v216, v214\n"
  "v_mov_b32 v217, v215\n"
  "s_nop 1\n"
  "v_permlane32_swap_b32 v214, v216\n"
  "v_permlane32_swap_b32 v215, v217\n"
  "s_nop 1\n"
  "v_max_f32 v214, v214, v216\n"
  "v_max_f32 v215, v215, v217\n"
  "v_mov_b32 v216, v214\n"
  "v_mov_b32 v217, v215\n"
  "s_nop 1\n"
  "v_permlane16_swap_b32 v214, v216\n"
  "v_permlane16_swap_b32 v215, v217\n"
  "s_nop 1\n"
  "v_max_f32 v214, v214, v216\n"
  "v_max_f32 v215, v215, v217\n"
  "v_min_f32 v216, s38, v220\n"
  "v_max_f32 v214, v214, v216\n"
  "v_max_f32 v215, v215, v216\n"
  "v_sub_f32 v128, v128, v214\n"
  "v_sub_f32 v129, v129, v214\n"
  "v_sub_f32 v130, v130, v214\n"
  "v_sub_f32 v131, v131, v214\n"
  "v_sub_f32 v132, v132, v214\n"
  "v_sub_f32 v133, v133, v214\n"
  "v_sub_f32 v134, v134, v214\n"
  "v_sub_f32 v135, v135, v214\n"
  "v_sub_f32 v136, v136, v214\n"
  "v_sub_f32 v137, v137, v214\n"
  "v_sub_f32 v138, v138, v214\n"
  "v_sub_f32 v139, v139, v214\n"
  "v_sub_f32 v140, v140, v214\n"
  "v_sub_f32 v141, v141, v214\n"
  "v_sub_f32 v142, v142, v214\n"
  "v_sub_f32 v143, v143, v214\n"
  "v_sub_f32 v200, v200, v214\n"
  "v_sub_f32 v201, v201, v214\n"
  "v_sub_f32 v202, v202, v214\n"
  "v_sub_f32 v203, v203, v214\n"
  "v_sub_f32 v144, v144, v215\n"
  "v_sub_f32 v145, v145, v215\n"
  "v_sub_f32 v146, v146, v215\n"
  "v_sub_f32 v147, v147, v215\n"
  "v_sub_f32 v148, v148, v215\n"
  "v_sub_f32 v149, v149, v215\n"
  "v_sub_f32 v150, v150, v215\n"
  "v_sub_f32 v151, v151, v215\n"
  "v_sub_f32 v152, v152, v215\n"
  "v_sub_f32 v153, v153, v215\n"
  "v_sub_f32 v154, v154, v215\n"
  "v_sub_f32 v155, v155, v215\n"
  "v_sub_f32 v156, v156, v215\n"
  "v_sub_f32 v157, v157, v215\n"
  "v_sub_f32 v158, v158, v215\n"
  "v_sub_f32 v159, v159, v215\n"
  "v_sub_f32 v204, v204, v215\n"
  "v_sub_f32 v205, v205, v215\n"
  "v_sub_f32 v206, v206, v215\n"
  "v_sub_f32 v207, v207, v215\n"
  "s_cmp_eq_u32 s38, s39\n"
  "s_mov_b32 s38, 0x41800000\n"
  "s_cbranch_scc0 L_resc1%=\n"
  "s_nop 1\n"
  "s_setpc_b64 s[40:41]\n"
  "L_rare11%=:\n"
  "s_nop 9\n"
  "v_max3_f32 v214, v160, v161, v162\n"
  "v_max3_f32 v214, v214, v163, v164\n"
  "v_max3_f32 v214, v214, v165, v166\n"
  "v_max3_f32 v214, v214, v167, v168\n"
  "v_max3_f32 v214, v214, v169, v170\n"
  "v_max3_f32 v214, v214, v171, v172\n"
  "v_max3_f32 v214, v214, v173, v174\n"
  "v_max_f32 v214, v214, v175\n"
  "v_max3_f32 v215, v176, v177, v178\n"
  "v_max3_f32 v215, v215, v179, v180\n"
  "v_max3_f32 v215, v215, v181, v182\n"
  "v_max3_f32 v215, v215, v183, v184\n"
  "v_max3_f32 v215, v215, v185, v186\n"
  "v_max3_f32 v215, v215, v187, v188\n"
  "v_max3_f32 v215, v215, v189, v190\n"
  "v_max_f32 v215, v215, v191\n"
  "v_mov_b32 v216, v214\n"
  "v_mov_b32 v217, v215\n"
  "s_nop 1\n"
  "v_permlane32_swap_b32 v214, v216\n"
  "v_permlane32_swap_b32 v215, v217\n"
  "s_nop 1\n"
  "v_max_f32 v214, v214, v216\n"
  "v_max_f32 v215, v215, v217\n"
  "v_mov_b32 v216, v214\n"
  "v_mov_b32 v217, v215\n"
  "s_nop 1\n"
  "v_permlane16_swap_b32 v214, v216\n"
  "v_permlane16_swap_b32 v215, v217\n"
  "s_nop 1\n"
  "v_max_f32 v214, v214, v216\n"
  "v_max_f32 v215, v215, v217\n"
  "v_min_f32 v216, s38, v220\n"
  "v_max_f32 v214, v214, v216\n"
  "v_max_f32 v215, v215, v216\n"
  "v_sub_f32 v160, v160, v214\n"
  "v_sub_f32 v161, v161, v214\n"
  "v_sub_f32 v162, v162, v214\n"
  "v_sub_f32 v163, v163, v214\n"
  "v_sub_f32 v164, v164, v214\n"
  "v_sub_f32 v165, v165, v214\n"
  "v_sub_f32 v166, v166, v214\n"
  "v_sub_f32 v167, v167, v214\n"
  "v_sub_f32 v168, v168, v214\n"
  "v_sub_f32 v169, v169, v214\n"
  "v_sub_f32 v170, v170, v214\n"
  "v_sub_f32 v171, v171, v214\n"
  "v_sub_f32 v172, v172, v214\n"
  "v_sub_f32 v173, v173, v214\n"
  "v_sub_f32 v174, v174, v214\n"
  "v_sub_f32 v175, v175, v214\n"
  "v_sub_f32 v200, v200, v214\n"
  "v_sub_f32 v201, v201, v214\n"
  "v_sub_f32 v202, v202, v214\n"
  "v_sub_f32 v203, v203, v214\n"
  "v_sub_f32 v176, v176, v215\n"
  "v_sub_f32 v177, v177, v215\n"
  "v_sub_f32 v178, v178, v215\n"
  "v_sub_f32 v179, v179, v215\n"
  "v_sub_f32 v180, v180, v215\n"
  "v_sub_f32 v181, v181, v215\n"
  "v_sub_f32 v182, v182, v215\n"
  "v_sub_f32 v183, v183, v215\n"
  "v_sub_f32 v184, v184, v215\n"
  "v_sub_f32 v185, v185, v215\n"
  "v_sub_f32 v186, v186, v215\n"
  "v_sub_f32 v187, v187, v215\n"
  "v_sub_f32 v188, v188, v215\n"
  "v_sub_f32 v189, v189, v215\n"
  "v_sub_f32 v190, v190, v215\n"
  "v_sub_f32 v191, v191, v215\n"
  "v_sub_f32 v204, v204, v215\n"
  "v_sub_f32 v205, v205, v215\n"
  "v_sub_f32 v206, v206, v215\n"
  "v_sub_f32 v207, v207, v215\n"
  "s_cmp_eq_u32 s38, s39\n"
  "s_mov_b32 s38, 0x41800000\n"
  "s_cbranch_scc0 L_resc1%=\n"
  "s_nop 1\n"
  "s_setpc_b64 s[40:41]\n"
  "L_resc0%=:\n"
  "v_exp_f32 v214, -v214\n"
  "v_exp_f32 v215, -v215\n"
  "v_accvgpr_read_b32 v216, a0\n"
  "v_accvgpr_read_b32 v217, a1\n"
  "v_accvgpr_read_b32 v218, a2\n"
  "v_accvgpr_read_b32 v219, a3\n"
  "v_mul_f32 v216, v216, v214\n"
  "v_mul_f32 v217, v217, v214\n"
  "v_mul_f32 v218, v218, v214\n"
  "v_mul_f32 v219, v219, v214\n"
  "v_accvgpr_write_b32 a0, v216\n"
  "v_accvgpr_write_b32 a1, v217\n"
  "v_accvgpr_write_b32 a2, v218\n"
  "v_accvgpr_write_b32 a3, v219\n"
  "v_accvgpr_read_b32 v216, a4\n"
  "v_accvgpr_read_b32 v217, a5\n"
  "v_accvgpr_read_b32 v218, a6\n"
  "v_accvgpr_read_b32 v219, a7\n"
  "v_mul_f32 v216, v216, v215\n"
  "v_mul_f32 v217, v217, v215\n"
  "v_mul_f32 v218, v218, v215\n"
  "v_mul_f32 v219, v219, v215\n"
  "v_accvgpr_write_b32 a4, v216\n"
  "v_accvgpr_write_b32 a5, v217\n"
  "v_accvgpr_write_b32 a6, v218\n"
  "v_accvgpr_write_b32 a7, v219\n"
  "v_accvgpr_read_b32 v216, a16\n"
  "v_accvgpr_read_b32 v217, a17\n"
  "v_accvgpr_read_b32 v218, a18\n"
  "v_accvgpr_read_b32 v219, a19\n"
  "v_mul_f32 v216, v216, v214\n"
  "v_mul_f32 v217, v217, v214\n"
  "v_mul_f32 v218, v218, v214\n"
  "v_mul_f32 v219, v219, v214\n"
  "v_accvgpr_write_b32 a16, v216\n"
  "v_accvgpr_write_b32 a17, v217\n"
  "v_accvgpr_write_b32 a18, v218\n"
  "v_accvgpr_write_b32 a19, v219\n"
  "v_accvgpr_read_b32 v216, a20\n"
  "v_accvgpr_read_b32 v217, a21\n"
  "v_accvgpr_read_b32 v218, a22\n"
  "v_accvgpr_read_b32 v219, a23\n"
  "v_mul_f32 v216, v216, v215\n"
  "v_mul_f32 v217, v217, v215\n"
  "v_mul_f32 v218, v218, v215\n"
  "v_mul_f32 v219, v219, v215\n"
  "v_accvgpr_write_b32 a20, v216\n"
  "v_accvgpr_write_b32 a21, v217\n"
  "v_accvgpr_write_b32 a22, v218\n"
  "v_accvgpr_write_b32 a23, v219\n"
  "v_accvgpr_read_b32 v216, a32\n"
  "v_accvgpr_read_b32 v217, a33\n"
  "v_accvgpr_read_b32 v218, a34\n"
  "v_accvgpr_read_b32 v219, a35\n"
  "v_mul_f32 v216, v216, v214\n"
  "v_mul_f32 v217, v217, v214\n"
  "v_mul_f32 v218, v218, v214\n"
  "v_mul_f32 v219, v219, v214\n"
  "v_accvgpr_write_b32 a32, v216\n"
  "v_accvgpr_write_b32 a33, v217\n"
  "v_accvgpr_write_b32 a34, v218\n"
  "v_accvgpr_write_b32 a35, v219\n"
  "v_accvgpr_read_b32 v216, a36\n"
  "v_accvgpr_read_b32 v217, a37\n"
  "v_accvgpr_read_b32 v218, a38\n"
  "v_accvgpr_read_b32 v219, a39\n"
  "v_mul_f32 v216, v216, v215\n"
  "v_mul_f32 v217, v217, v215\n"
  "v_mul_f32 v218, v218, v215\n"
  "v_mul_f32 v219, v219, v215\n"
  "v_accvgpr_write_b32 a36, v216\n"
  "v_accvgpr_write_b32 a37, v217\n"
  "v_accvgpr_write_b32 a38, v218\n"
  "v_accvgpr_write_b32 a39, v219\n"
  "v_accvgpr_read_b32 v216, a48\n"
  "v_accvgpr_read_b32 v217, a49\n"
  "v_accvgpr_read_b32 v218, a50\n"
  "v_accvgpr_read_b32 v219, a51\n"
  "v_mul_f32 v216, v216, v214\n"
  "v_mul_f32 v217, v217, v214\n"
  "v_mul_f32 v218, v218, v214\n"
  "v_mul_f32 v219, v219, v214\n"
  "v_accvgpr_write_b32 a48, v216\n"
  "v_accvgpr_write_b32 a49, v217\n"
  "v_accvgpr_write_b32 a50, v218\n"
  "v_accvgpr_write_b32 a51, v219\n"
  "v_accvgpr_read_b32 v216, a52\n"
  "v_accvgpr_read_b32 v217, a53\n"
  "v_accvgpr_read_b32 v218, a54\n"
  "v_accvgpr_read_b32 v219, a55\n"
  "v_mul_f32 v216, v216, v215\n"
  "v_mul_f32 v217, v217, v215\n"
  "v_mul_f32 v218, v218, v215\n"
  "v_mul_f32 v219, v219, v215\n"
  "v_accvgpr_write_b32 a52, v216\n"
  "v_accvgpr_write_b32 a53, v217\n"
  "v_accvgpr_write_b32 a54, v218\n"
  "v_accvgpr_write_b32 a55, v219\n"
  "v_accvgpr_read_b32 v216, a64\n"
  "v_accvgpr_read_b32 v217, a65\n"
  "v_accvgpr_read_b32 v218, a66\n"
  "v_accvgpr_read_b32 v219, a67\n"
  "v_mul_f32 v216, v216, v214\n"
  "v_mul_f32 v217, v217, v214\n"
  "v_mul_f32 v218, v218, v214\n"
  "v_mul_f32 v219, v219, v214\n"
  "v_accvgpr_write_b32 a64, v216\n"
  "v_accvgpr_write_b32 a65, v217\n"
  "v_accvgpr_write_b32 a66, v218\n"
  "v_accvgpr_write_b32 a67, v219\n"
  "v_accvgpr_read_b32 v216, a68\n"
  "v_accvgpr_read_b32 v217, a69\n"
  "v_accvgpr_read_b32 v218, a70\n"
  "v_accvgpr_read_b32 v219, a71\n"
  "v_mul_f32 v216, v216, v215\n"
  "v_mul_f32 v217, v217, v215\n"
  "v_mul_f32 v218, v218, v215\n"
  "v_mul_f32 v219, v219, v215\n"
  "v_accvgpr_write_b32 a68, v216\n"
  "v_accvgpr_write_b32 a69, v217\n"
  "v_accvgpr_write_b32 a70, v218\n"
  "v_accvgpr_write_b32 a71, v219\n"
  "v_accvgpr_read_b32 v216, a80\n"
  "v_accvgpr_read_b32 v217, a81\n"
  "v_accvgpr_read_b32 v218, a82\n"
  "v_accvgpr_read_b32 v219, a83\n"
  "v_mul_f32 v216, v216, v214\n"
  "v_mul_f32 v217, v217, v214\n"
  "v_mul_f32 v218, v218, v214\n"
  "v_mul_f32 v219, v219, v214\n"
  "v_accvgpr_write_b32 a80, v216\n"
  "v_accvgpr_write_b32 a81, v217\n"
  "v_accvgpr_write_b32 a82, v218\n"
  "v_accvgpr_write_b32 a83, v219\n"
  "v_accvgpr_read_b32 v216, a84\n"
  "v_accvgpr_read_b32 v217, a85\n"
  "v_accvgpr_read_b32 v218, a86\n"
  "v_accvgpr_read_b32 v219, a87\n"
  "v_mul_f32 v216, v216, v215\n"
  "v_mul_f32 v217, v217, v215\n"
  "v_mul_f32 v218, v218, v215\n"
  "v_mul_f32 v219, v219, v215\n"
  "v_accvgpr_write_b32 a84, v216\n"
  "v_accvgpr_write_b32 a85, v217\n"
  "v_accvgpr_write_b32 a86, v218\n"
  "v_accvgpr_write_b32 a87, v219\n"
  "v_accvgpr_read_b32 v216, a96\n"
  "v_accvgpr_read_b32 v217, a97\n"
  "v_accvgpr_read_b32 v218, a98\n"
  "v_accvgpr_read_b32 v219, a99\n"
  "v_mul_f32 v216, v216, v214\n"
  "v_mul_f32 v217, v217, v214\n"
  "v_mul_f32 v218, v218, v214\n"
  "v_mul_f32 v219, v219, v214\n"
  "v_accvgpr_write_b32 a96, v216\n"
  "v_accvgpr_write_b32 a97, v217\n"
  "v_accvgpr_write_b32 a98, v218\n"
  "v_accvgpr_write_b32 a99, v219\n"
  "v_accvgpr_read_b32 v216, a100\n"
  "v_accvgpr_read_b32 v217, a101\n"
  "v_accvgpr_read_b32 v218, a102\n"
  "v_accvgpr_read_b32 v219, a103\n"
  "v_mul_f32 v216, v216, v215\n"
  "v_mul_f32 v217, v217, v215\n"
  "v_mul_f32 v218, v218, v215\n"
  "v_mul_f32 v219, v219, v215\n"
  "v_accvgpr_write_b32 a100, v216\n"
  "v_accvgpr_write_b32 a101, v217\n"
  "v_accvgpr_write_b32 a102, v218\n"
  "v_accvgpr_write_b32 a103, v219\n"
  "v_accvgpr_read_b32 v216, a112\n"
  "v_accvgpr_read_b32 v217, a113\n"
  "v_accvgpr_read_b32 v218, a114\n"
  "v_accvgpr_read_b32 v219, a115\n"
  "v_mul_f32 v216, v216, v214\n"
  "v_mul_f32 v217, v217, v214\n"
  "v_mul_f32 v218, v218, v214\n"
  "v_mul_f32 v219, v219, v214\n"
  "v_accvgpr_write_b32 a112, v216\n"
  "v_accvgpr_write_b32 a113, v217\n"
  "v_accvgpr_write_b32 a114, v218\n"
  "v_accvgpr_write_b32 a115, v219\n"
  "v_accvgpr_read_b32 v216, a116\n"
  "v_accvgpr_read_b32 v217, a117\n"
  "v_accvgpr_read_b32 v218, a118\n"
  "v_accvgpr_read_b32 v219, a119\n"
  "v_mul_f32 v216, v216, v215\n"
  "v_mul_f32 v217, v217, v215\n"
  "v_mul_f32 v218, v218, v215\n"
  "v_mul_f32 v219, v219, v215\n"
  "v_accvgpr_write_b32 a116, v216\n"
  "v_accvgpr_write_b32 a117, v217\n"
  "v_accvgpr_write_b32 a118, v218\n"
  "v_accvgpr_write_b32 a119, v219\n"
  "v_accvgpr_read_b32 v216, a128\n"
  "v_accvgpr_read_b32 v217, a129\n"
  "v_accvgpr_read_b32 v218, a130\n"
  "v_accvgpr_read_b32 v219, a131\n"
  "v_mul_f32 v216, v216, v214\n"
  "v_mul_f32 v217, v217, v214\n"
  "v_mul_f32 v218, v218, v214\n"
  "v_mul_f32 v219, v219, v214\n"
  "v_accvgpr_write_b32 a128, v216\n"
  "v_accvgpr_write_b32 a129, v217\n"
  "v_accvgpr_write_b32 a130, v218\n"
  "v_accvgpr_write_b32 a131, v219\n"
  "v_accvgpr_read_b32 v216, a132\n"
  "v_accvgpr_read_b32 v217, a133\n"
  "v_accvgpr_read_b32 v218, a134\n"
  "v_accvgpr_read_b32 v219, a135\n"
  "v_mul_f32 v216, v216, v215\n"
  "v_mul_f32 v217, v217, v215\n"
  "v_mul_f32 v218, v218, v215\n"
  "v_mul_f32 v219, v219, v215\n"
  "v_accvgpr_write_b32 a132, v216\n"
  "v_accvgpr_write_b32 a133, v217\n"
  "v_accvgpr_write_b32 a134, v218\n"
  "v_accvgpr_write_b32 a135, v219\n"
  "s_nop 1\n"
  "s_setpc_b64 s[40:41]\n"
  "L_resc1%=:\n"
  "v_exp_f32 v214, -v214\n"
  "v_exp_f32 v215, -v215\n"
  "v_accvgpr_read_b32 v216, a8\n"
  "v_accvgpr_read_b32 v217, a9\n"
  "v_accvgpr_read_b32 v218, a10\n"
  "v_accvgpr_read_b32 v219, a11\n"
  "v_mul_f32 v216, v216, v214\n"
  "v_mul_f32 v217, v217, v214\n"
  "v_mul_f32 v218, v218, v214\n"
  "v_mul_f32 v219, v219, v214\n"
  "v_accvgpr_write_b32 a8, v216\n"
  "v_accvgpr_write_b32 a9, v217\n"
  "v_accvgpr_write_b32 a10, v218\n"
  "v_accvgpr_write_b32 a11, v219\n"
  "v_accvgpr_read_b32 v216, a12\n"
  "v_accvgpr_read_b32 v217, a13\n"
  "v_accvgpr_read_b32 v218, a14\n"
  "v_accvgpr_read_b32 v219, a15\n"
  "v_mul_f32 v216, v216, v215\n"
  "v_mul_f32 v217, v217, v215\n"
  "v_mul_f32 v218, v218, v215\n"
  "v_mul_f32 v219, v219, v215\n"
  "v_accvgpr_write_b32 a12, v216\n"
  "v_accvgpr_write_b32 a13, v217\n"
  "v_accvgpr_write_b32 a14, v218\n"
  "v_accvgpr_write_b32 a15, v219\n"
  "v_accvgpr_read_b32 v216, a24\n"
  "v_accvgpr_read_b32 v217, a25\n"
  "v_accvgpr_read_b32 v218, a26\n"
  "v_accvgpr_read_b32 v219, a27\n"
  "v_mul_f32 v216, v216, v214\n"
  "v_mul_f32 v217, v217, v214\n"
  "v_mul_f32 v218, v218, v214\n"
  "v_mul_f32 v219, v219, v214\n"
  "v_accvgpr_write_b32 a24, v216\n"
  "v_accvgpr_write_b32 a25, v217\n"
  "v_accvgpr_write_b32 a26, v218\n"
  "v_accvgpr_write_b32 a27, v219\n"
  "v_accvgpr_read_b32 v216, a28\n"
  "v_accvgpr_read_b32 v217, a29\n"
  "v_accvgpr_read_b32 v218, a30\n"
  "v_accvgpr_read_b32 v219, a31\n"
  "v_mul_f32 v216, v216, v215\n"
  "v_mul_f32 v217, v217, v215\n"
  "v_mul_f32 v218, v218, v215\n"
  "v_mul_f32 v219, v219, v215\n"
  "v_accvgpr_write_b32 a28, v216\n"
  "v_accvgpr_write_b32 a29, v217\n"
  "v_accvgpr_write_b32 a30, v218\n"
  "v_accvgpr_write_b32 a31, v219\n"
  "v_accvgpr_read_b32 v216, a40\n"
  "v_accvgpr_read_b32 v217, a41\n"
  "v_accvgpr_read_b32 v218, a42\n"
  "v_accvgpr_read_b32 v219, a43\n"
  "v_mul_f32 v216, v216, v214\n"
  "v_mul_f32 v217, v217, v214\n"
  "v_mul_f32 v218, v218, v214\n"
  "v_mul_f32 v219, v219, v214\n"
  "v_accvgpr_write_b32 a40, v216\n"
  "v_accvgpr_write_b32 a41, v217\n"
  "v_accvgpr_write_b32 a42, v218\n"
  "v_accvgpr_write_b32 a43, v219\n"
  "v_accvgpr_read_b32 v216, a44\n"
  "v_accvgpr_read_b32 v217, a45\n"
  "v_accvgpr_read_b32 v218, a46\n"
  "v_accvgpr_read_b32 v219, a47\n"
  "v_mul_f32 v216, v216, v215\n"
  "v_mul_f32 v217, v217, v215\n"
  "v_mul_f32 v218, v218, v215\n"
  "v_mul_f32 v219, v219, v215\n"
  "v_accvgpr_write_b32 a44, v216\n"
  "v_accvgpr_write_b32 a45, v217\n"
  "v_accvgpr_write_b32 a46, v218\n"
  "v_accvgpr_write_b32 a47, v219\n"
  "v_accvgpr_read_b32 v216, a56\n"
  "v_accvgpr_read_b32 v217, a57\n"
  "v_accvgpr_read_b32 v218, a58\n"
  "v_accvgpr_read_b32 v219, a59\n"
  "v_mul_f32 v216, v216, v214\n"
  "v_mul_f32 v217, v217, v214\n"
  "v_mul_f32 v218, v218, v214\n"
  "v_mul_f32 v219, v219, v214\n"
  "v_accvgpr_write_b32 a56, v216\n"
  "v_accvgpr_write_b32 a57, v217\n"
  "v_accvgpr_write_b32 a58, v218\n"
  "v_accvgpr_write_b32 a59, v219\n"
  "v_accvgpr_read_b32 v216, a60\n"
  "v_accvgpr_read_b32 v217, a61\n"
  "v_accvgpr_read_b32 v218, a62\n"
  "v_accvgpr_read_b32 v219, a63\n"
  "v_mul_f32 v216, v216, v215\n"
  "v_mul_f32 v217, v217, v215\n"
  "v_mul_f32 v218, v218, v215\n"
  "v_mul_f32 v219, v219, v215\n"
  "v_accvgpr_write_b32 a60, v216\n"
  "v_accvgpr_write_b32 a61, v217\n"
  "v_accvgpr_write_b32 a62, v218\n"
  "v_accvgpr_write_b32 a63, v219\n"
  "v_accvgpr_read_b32 v216, a72\n"
  "v_accvgpr_read_b32 v217, a73\n"
  "v_accvgpr_read_b32 v218, a74\n"
  "v_accvgpr_read_b32 v219, a75\n"
  "v_mul_f32 v216, v216, v214\n"
  "v_mul_f32 v217, v217, v214\n"
  "v_mul_f32 v218, v218, v214\n"
  "v_mul_f32 v219, v219, v214\n"
  "v_accvgpr_write_b32 a72, v216\n"
  "v_accvgpr_write_b32 a73, v217\n"
  "v_accvgpr_write_b32 a74, v218\n"
  "v_accvgpr_write_b32 a75, v219\n"
  "v_accvgpr_read_b32 v216, a76\n"
  "v_accvgpr_read_b32 v217, a77\n"
  "v_accvgpr_read_b32 v218, a78\n"
  "v_accvgpr_read_b32 v219, a79\n"
  "v_mul_f32 v216, v216, v215\n"
  "v_mul_f32 v217, v217, v215\n"
  "v_mul_f32 v218, v218, v215\n"
  "v_mul_f32 v219, v219, v215\n"
  "v_accvgpr_write_b32 a76, v216\n"
  "v_accvgpr_write_b32 a77, v217\n"
  "v_accvgpr_write_b32 a78, v218\n"
  "v_accvgpr_write_b32 a79, v219\n"
  "v_accvgpr_read_b32 v216, a88\n"
  "v_accvgpr_read_b32 v217, a89\n"
  "v_accvgpr_read_b32 v218, a90\n"
  "v_accvgpr_read_b32 v219, a91\n"
  "v_mul_f32 v216, v216, v214\n"
  "v_mul_f32 v217, v217, v214\n"
  "v_mul_f32 v218, v218, v214\n"
  "v_mul_f32 v219, v219, v214\n"
  "v_accvgpr_write_b32 a88, v216\n"
  "v_accvgpr_write_b32 a89, v217\n"
  "v_accvgpr_write_b32 a90, v218\n"
  "v_accvgpr_write_b32 a91, v219\n"
  "v_accvgpr_read_b32 v216, a92\n"
  "v_accvgpr_read_b32 v217, a93\n"
  "v_accvgpr_read_b32 v218, a94\n"
  "v_accvgpr_read_b32 v219, a95\n"
  "v_mul_f32 v216, v216, v215\n"
  "v_mul_f32 v217, v217, v215\n"
  "v_mul_f32 v218, v218, v215\n"
  "v_mul_f32 v219, v219, v215\n"
  "v_accvgpr_write_b32 a92, v216\n"
  "v_accvgpr_write_b32 a93, v217\n"
  "v_accvgpr_write_b32 a94, v218\n"
  "v_accvgpr_write_b32 a95, v219\n"
  "v_accvgpr_read_b32 v216, a104\n"
  "v_accvgpr_read_b32 v217, a105\n"
  "v_accvgpr_read_b32 v218, a106\n"
  "v_accvgpr_read_b32 v219, a107\n"
  "v_mul_f32 v216, v216, v214\n"
  "v_mul_f32 v217, v217, v214\n"
  "v_mul_f32 v218, v218, v214\n"
  "v_mul_f32 v219, v219, v214\n"
  "v_accvgpr_write_b32 a104, v216\n"
  "v_accvgpr_write_b32 a105, v217\n"
  "v_accvgpr_write_b32 a106, v218\n"
  "v_accvgpr_write_b32 a107, v219\n"
  "v_accvgpr_read_b32 v216, a108\n"
  "v_accvgpr_read_b32 v217, a109\n"
  "v_accvgpr_read_b32 v218, a110\n"
  "v_accvgpr_read_b32 v219, a111\n"
  "v_mul_f32 v216, v216, v215\n"
  "v_mul_f32 v217, v217, v215\n"
  "v_mul_f32 v218, v218, v215\n"
  "v_mul_f32 v219, v219, v215\n"
  "v_accvgpr_write_b32 a108, v216\n"
  "v_accvgpr_write_b32 a109, v217\n"
  "v_accvgpr_write_b32 a110, v218\n"
  "v_accvgpr_write_b32 a111, v219\n"
  "v_accvgpr_read_b32 v216, a120\n"
  "v_accvgpr_read_b32 v217, a121\n"
  "v_accvgpr_read_b32 v218, a122\n"
  "v_accvgpr_read_b32 v219, a123\n"
  "v_mul_f32 v216, v216, v214\n"
  "v_mul_f32 v217, v217, v214\n"
  "v_mul_f32 v218, v218, v214\n"
  "v_mul_f32 v219, v219, v214\n"
  "v_accvgpr_write_b32 a120, v216\n"
  "v_accvgpr_write_b32 a121, v217\n"
  "v_accvgpr_write_b32 a122, v218\n"
  "v_accvgpr_write_b32 a123, v219\n"
  "v_accvgpr_read_b32 v216, a124\n"
  "v_accvgpr_read_b32 v217, a125\n"
  "v_accvgpr_read_b32 v218, a126\n"
  "v_accvgpr_read_b32 v219, a127\n"
  "v_mul_f32 v216, v216, v215\n"
  "v_mul_f32 v217, v217, v215\n"
  "v_mul_f32 v218, v218, v215\n"
  "v_mul_f32 v219, v219, v215\n"
  "v_accvgpr_write_b32 a124, v216\n"
  "v_accvgpr_write_b32 a125, v217\n"
  "v_accvgpr_write_b32 a126, v218\n"
  "v_accvgpr_write_b32 a127, v219\n"
  "v_accvgpr_read_b32 v216, a136\n"
  "v_accvgpr_read_b32 v217, a137\n"
  "v_accvgpr_read_b32 v218, a138\n"
  "v_accvgpr_read_b32 v219, a139\n"
  "v_mul_f32 v216, v216, v214\n"
  "v_mul_f32 v217, v217, v214\n"
  "v_mul_f32 v218, v218, v214\n"
  "v_mul_f32 v219, v219, v214\n"
  "v_accvgpr_write_b32 a136, v216\n"
  "v_accvgpr_write_b32 a137, v217\n"
  "v_accvgpr_write_b32 a138, v218\n"
  "v_accvgpr_write_b32 a139, v219\n"
  "v_accvgpr_read_b32 v216, a140\n"
  "v_accvgpr_read_b32 v217, a141\n"
  "v_accvgpr_read_b32 v218, a142\n"
  "v_accvgpr_read_b32 v219, a143\n"
  "v_mul_f32 v216, v216, v215\n"
  "v_mul_f32 v217, v217, v215\n"
  "v_mul_f32 v218, v218, v215\n"
  "v_mul_f32 v219, v219, v215\n"
  "v_accvgpr_write_b32 a140, v216\n"
  "v_accvgpr_write_b32 a141, v217\n"
  "v_accvgpr_write_b32 a142, v218\n"
  "v_accvgpr_write_b32 a143, v219\n"
  "s_nop 1\n"
  "s_setpc_b64 s[40:41]\n"
  "L_kev_m%=:\n"
  "s_cmp_lt_u32 s34, 2\n"
  "s_cbranch_scc1 L_kevr_m%=\n"
  "v_mov_b32 v235, 0x27f00\n"
  "ds_read_b32 v236, v235\n"
  "s_waitcnt lgkmcnt(0)\n"
  "v_readfirstlane_b32 s87, v236\n"
  "s_nop 3\n"
  "s_lshl_b32 s87, s87, 5\n"
  "s_add_u32 s87, s87, s45\n"
  "s_load_dwordx8 s[44:51], s[32:33], s87\n"
  "s_branch L_kevr_m%=\n"
  "L_kev_l%=:\n"
  "s_lshl_b32 s95, s95, 4\n"
  "s_or_b32 s95, s95, 15\n"
  "s_mov_b32 s99, 0x4000\n"
  "L_kc_l%=:\n"
  "v_cmp_ne_u32 vcc, s35, v231\n"
  "s_nop 4\n"
  "s_cbranch_vccnz L_kn_l%=\n"
  "s_mov_b32 s94, 64\n"
  "s_branch L_kevr_l%=\n"
  "L_kn_l%=:\n"
  "global_load_dword v231, v230, s[8:9] sc1\n"
  "s_or_b32 s87, vcc_lo, vcc_hi\n"
  "s_and_b32 s87, s87, s95\n"
  "s_cbranch_scc0 L_kevr_l%=\n"
  "s_waitcnt vmcnt(0)\n"
  "s_sleep 1\n"
  "s_sub_u32 s99, s99, 1\n"
  "s_cbranch_scc0 L_kc_l%=\n"
  "s_branch L_kevr_l%=\n"
  "L_u_fin%=:\n"
  "s_and_b32 s89, s40, 3\n"
  "s_lshr_b32 s87, s34, 1\n"
  "s_lshl_b32 s87, s87, 11\n"
  "s_and_b32 s90, s34, 1\n"
  "s_lshl_b32 s90, s90, 13\n"
  "v_and_b32 v128, 15, v255\n"
  "v_lshrrev_b32 v129, 4, v255\n"
  "v_lshlrev_b32 v130, 12, v129\n"
  "v_lshl_add_u32 v130, v128, 4, v130\n"
  "v_add_u32 v130, s87, v130\n"
  "v_bfe_u32 v131, v255, 2, 2\n"
  "v_and_b32 v132, 3, v255\n"
  "v_lshlrev_b32 v132, 3, v132\n"
  "v_lshl_add_u32 v132, v131, 7, v132\n"
  "v_lshl_add_u32 v132, v129, 9, v132\n"
  "v_bfe_u32 v133, v255, 3, 2\n"
  "v_lshl_add_u32 v132, v133, 5, v132\n"
  "v_add_u32 v132, s90, v132\n"
  "v_add_u32 v228, 0, v130\n"
  "v_add_u32 v230, 0x18000, v132\n"
  "v_xor_b32 v231, 32, v230\n"
  "v_xor_b32 v232, 64, v230\n"
  "v_xor_b32 v233, 0x60, v230\n"
  "v_add_u32 v234, 0x8000, v130\n"
  "v_add_u32 v236, 0x1c000, v132\n"
  "v_xor_b32 v237, 32, v236\n"
  "v_xor_b32 v238, 64, v236\n"
  "v_xor_b32 v239, 0x60, v236\n"
  "v_add_u32 v240, 0x10000, v130\n"
  "v_add_u32 v242, 0x20000, v132\n"
  "v_xor_b32 v243, 32, v242\n"
  "v_xor_b32 v244, 64, v242\n"
  "v_xor_b32 v245, 0x60, v242\n"
  "s_xor_b32 s87, s89, 0\n"
  "s_lshl_b32 s87, s87, 9\n"
  "v_add_u32 v200, s87, v228\n"
  "v_add_u32 v204, s87, v234\n"
  "v_add_u32 v208, s87, v240\n"
  "s_xor_b32 s87, s89, 1\n"
  "s_lshl_b32 s87, s87, 9\n"
  "v_add_u32 v201, s87, v228\n"
  "v_add_u32 v205, s87, v234\n"
  "v_add_u32 v209, s87, v240\n"
  "s_xor_b32 s87, s89, 2\n"
  "s_lshl_b32 s87, s87, 9\n"
  "v_add_u32 v202, s87, v228\n"
  "v_add_u32 v206, s87, v234\n"
  "v_add_u32 v210, s87, v240\n"
  "s_xor_b32 s87, s89, 3\n"
  "s_lshl_b32 s87, s87, 9\n"
  "v_add_u32 v203, s87, v228\n"
  "v_add_u32 v207, s87, v234\n"
  "v_add_u32 v211, s87, v240\n"
  "s_and_b32 s91, s34, 1\n"
  "s_lshl_b32 s91, s91, 16\n"
  "s_lshr_b32 s99, s34, 1\n"
  "s_lshl_b32 s99, s99, 7\n"
  "s_add_u32 s91, s91, s99\n"
  "s_bitcmp1_b32 s40, 2\n"
  "s_cselect_b64 s[92:93], s[26:27], s[20:21]\n"
  "s_add_u32 s90, s91, s41\n"
  "s_add_u32 s92, s92, s90\n"
  "s_addc_u32 s93, s93, 0\n"
  "s_bfe_u32 s85, s40, 0x60008\n"
  "s_sub_u32 s85, s85, 3\n"
  "s_bfe_u32 s88, s40, 0x50003\n"
  "s_sub_u32 s88, s88, 1\n"
  "s_lshl_b32 s94, 9, s88\n"
  "s_mul_i32 s87, s43, 0x30000\n"
  "s_lshl_b32 s90, s34, 12\n"
  "s_add_u32 s87, s87, s90\n"
  "s_add_u32 s87, s87, 0x4100000\n"
  "s_add_u32 s60, s8, s87\n"
  "s_addc_u32 s61, s9, 0\n"
  "s_mov_b32 s62, 0x30000\n"
  "s_mov_b32 s63, 0x20000\n"
  "v_lshlrev_b32 v192, 4, v255\n"
  "v_mov_b32 v193, s35\n"
  "s_lshl_b32 s87, s43, 11\n"
  "s_lshl_b32 s90, s34, 7\n"
  "s_add_u32 s87, s87, s90\n"
  "s_add_u32 s87, s87, 0x5a40000\n"
  "v_and_b32 v194, 3, v255\n"
  "v_lshl_add_u32 v194, v194, 9, s87\n"
  "s_lshl_b32 s90, s89, 9\n"
  "s_add_u32 s87, s87, s90\n"
  "v_mov_b32 v195, s87\n"
  "s_lshr_b32 s87, s34, 1\n"
  "s_lshl_b32 s87, s87, 18\n"
  "s_lshl_b32 s99, s89, 16\n"
  "s_add_u32 s87, s87, s99\n"
  "s_and_b32 s99, s34, 1\n"
  "s_lshl_b32 s99, s99, 7\n"
  "s_add_u32 s87, s87, s99\n"
  "s_add_u32 s87, s87, s39\n"
  "v_and_b32 v196, 15, v255\n"
  "v_lshl_add_u32 v196, v196, 11, s87\n"
  "v_and_b32 v197, 16, v255\n"
  "v_lshl_add_u32 v196, v197, 1, v196\n"
  "v_and_b32 v197, 32, v255\n"
  "v_lshrrev_b32 v197, 1, v197\n"
  "v_add_u32 v196, v196, v197\n"
  "v_add_u32 v197, 0x8000, v196\n"
  "s_cmp_eq_u32 s86, 0\n"
  "s_cbranch_scc1 L_f_ns%=\n"
  "s_waitcnt vmcnt(0)\n"
  "s_cmp_eq_u32 s86, 0\n"
  "s_cbranch_scc1 L_sg_f%=\n"
  "s_lshl2_add_u32 s86, s34, s86\n"
  "v_mov_b32 v220, s86\n"
  "v_mov_b32 v221, s35\n"
  "global_store_dword v220, v221, s[8:9] sc1\n"
  "s_mov_b32 s86, 0\n"
  "L_sg_f%=:\n"
  "L_f_ns%=:\n"
  "s_cmp_lg_u32 s101, 0\n"
  "s_cbranch_scc1 L_f_go%=\n"
  "s_bfe_u32 s87, s40, 0x5000e\n"
  "s_bfe_u32 s91, s40, 0x40013\n"
  "s_lshr_b32 s90, s43, 3\n"
  "s_lshl_b32 s90, s90, 5\n"
  "s_add_u32 s87, s87, s90\n"
  "s_bitcmp0_b32 s40, 2\n"
  "s_cselect_b32 s99, 0, 0x100\n"
  "s_add_u32 s87, s87, s99\n"
  "s_lshl_b32 s87, s87, 7\n"
  "s_add_u32 s87, s87, 0x5a18000\n"
  "v_lshrrev_b32 v218, 2, v255\n"
  "v_min_u32 v218, s91, v218\n"
  "v_lshl_add_u32 v218, v218, 7, s87\n"
  "v_and_b32 v219, 3, v255\n"
  "v_lshl_add_u32 v218, v219, 2, v218\n"
  "global_load_dword v219, v218, s[8:9] sc1\n"
  "s_waitcnt vmcnt(0) lgkmcnt(0)\n"
  "s_mov_b32 s99, 0x4000\n"
  "L_fc_f1%=:\n"
  "v_cmp_ne_u32 vcc, s35, v219\n"
  "s_nop 4\n"
  "s_cbranch_vccz L_fd_f1%=\n"
  "s_sleep 2\n"
  "global_load_dword v219, v218, s[8:9] sc1\n"
  "s_waitcnt vmcnt(0)\n"
  "s_sub_u32 s99, s99, 1\n"
  "s_cbranch_scc0 L_fc_f1%=\n"
  "L_fd_f1%=:\n"
  "s_mov_b64 s[44:45], s[36:37]\n"
  "s_mov_b64 s[46:47], s[38:39]\n"
  "s_mov_b64 s[48:49], s[40:41]\n"
  "s_mov_b64 s[50:51], s[42:43]\n"
  "s_getpc_b64 s[96:97]\n"
  "s_add_u32 s96, s96, 12\n"
  "s_addc_u32 s97, s97, 0\n"
  "s_branch L_sregsx%=\n"
  "s_getpc_b64 s[96:97]\n"
  "s_add_u32 s96, s96, 12\n"
  "s_addc_u32 s97, s97, 0\n"
  "s_branch L_sregsw%=\n"
  "s_getpc_b64 s[96:97]\n"
  "s_add_u32 s96, s96, 12\n"
  "s_addc_u32 s97, s97, 0\n"
  "s_branch L_issue%=\n"
  "s_mov_b32 s44, 0\n"
  "L_f_go%=:\n"
  "s_cmp_eq_u32 s101, 2\n"
  "s_cbranch_scc1 L_ew_f%=\n"
  "s_cmp_eq_u32 s101, 3\n"
  "s_cbranch_scc1 L_ewb_f%=\n"
  "s_waitcnt vmcnt(20)\n"
  "L_ewb_f%=:\n"
  "s_barrier\n"
  "L_ew_f%=:\n"
  "ds_read_b64_tr_b16 a[64:65], v230\n"
  "ds_read_b64_tr_b16 a[66:67], v230 offset:2048\n"
  "ds_read_b64_tr_b16 a[68:69], v230 offset:4096\n"
  "ds_read_b64_tr_b16 a[70:71], v230 offset:6144\n"
  "ds_read_b64_tr_b16 a[72:73], v231\n"
  "ds_read_b64_tr_b16 a[74:75], v231 offset:2048\n"
  "ds_read_b64_tr_b16 a[76:77], v231 offset:4096\n"
  "ds_read_b64_tr_b16 a[78:79], v231 offset:6144\n"
  "ds_read_b64_tr_b16 a[80:81], v232\n"
  "ds_read_b64_tr_b16 a[82:83], v232 offset:2048\n"
  "ds_read_b64_tr_b16 a[84:85], v232 offset:4096\n"
  "ds_read_b64_tr_b16 a[86:87], v232 offset:6144\n"
  "ds_read_b64_tr_b16 a[88:89], v233\n"
  "ds_read_b64_tr_b16 a[90:91], v233 offset:2048\n"
  "ds_read_b64_tr_b16 a[92:93], v233 offset:4096\n"
  "ds_read_b64_tr_b16 a[94:95], v233 offset:6144\n"
  "ds_read_b128 a[16:19], v201\n"
  "ds_read_b128 a[20:23], v201 offset:16384\n"
  "ds_read_b128 a[24:27], v201 offset:256\n"
  "ds_read_b128 a[28:31], v201 offset:16640\n"
  "ds_read_b128 a[32:35], v202\n"
  "ds_read_b128 a[36:39], v202 offset:16384\n"
  "ds_read_b128 a[40:43], v202 offset:256\n"
  "ds_read_b128 a[44:47], v202 offset:16640\n"
  "s_waitcnt lgkmcnt(0)\n"
  "L_f_fast%=:\n"
  "v_mfma_f32_16x16x32_bf16 v[32:35], a[64:67], a[16:19], 0\n"
  "ds_read_b128 a[48:51], v203\n"
  "s_add_u32 m0, s83, 0x11000\n"
  "v_mfma_f32_16x16x32_bf16 v[32:35], a[68:71], a[20:23], v[32:35]\n"
  "buffer_load_dwordx4 v252, s[52:55], s68 offen sc1 lds\n"
  "ds_read_b128 a[52:55], v203 offset:16384\n"
  "v_mfma_f32_16x16x32_bf16 v[36:39], a[76:79], a[20:23], 0\n"
  "ds_read_b128 a[56:59], v203 offset:256\n"
  "v_mfma_f32_16x16x32_bf16 v[36:39], a[72:75], a[16:19], v[36:39]\n"
  "ds_read_b128 a[60:63], v203 offset:16640\n"
  "v_mfma_f32_16x16x32_bf16 v[40:43], a[80:83], a[16:19], 0\n"
  "ds_read_b128 a[0:3], v200\n"
  "v_mfma_f32_16x16x32_bf16 v[40:43], a[84:87], a[20:23], v[40:43]\n"
  "ds_read_b128 a[4:7], v200 offset:16384\n"
  "v_mfma_f32_16x16x32_bf16 v[44:47], a[92:95], a[20:23], 0\n"
  "buffer_load_dwordx4 v253, s[52:55], s69 offen offset:1024 sc1 lds\n"
  "ds_read_b128 a[8:11], v200 offset:256\n"
  "v_mfma_f32_16x16x32_bf16 v[44:47], a[88:91], a[16:19], v[44:47]\n"
  "ds_read_b128 a[12:15], v200 offset:16640\n"
  "v_mfma_f32_16x16x32_bf16 v[60:63], a[88:91], a[24:27], 0\n"
  "v_mfma_f32_16x16x32_bf16 v[60:63], a[92:95], a[28:31], v[60:63]\n"
  "v_mfma_f32_16x16x32_bf16 v[56:59], a[84:87], a[28:31], 0\n"
  "v_mfma_f32_16x16x32_bf16 v[56:59], a[80:83], a[24:27], v[56:59]\n"
  "buffer_load_dwordx4 v252, s[52:55], s70 offen offset:2048 sc1 lds\n"
  "v_mfma_f32_16x16x32_bf16 v[52:55], a[72:75], a[24:27], 0\n"
  "v_mfma_f32_16x16x32_bf16 v[52:55], a[76:79], a[28:31], v[52:55]\n"
  "v_mfma_f32_16x16x32_bf16 v[48:51], a[68:71], a[28:31], 0\n"
  "v_mfma_f32_16x16x32_bf16 v[48:51], a[64:67], a[24:27], v[48:51]\n"
  "v_mfma_f32_16x16x32_bf16 v[64:67], a[64:67], a[32:35], 0\n"
  "buffer_load_dwordx4 v253, s[52:55], s71 offen offset:3072 sc1 lds\n"
  "v_mfma_f32_16x16x32_bf16 v[64:67], a[68:71], a[36:39], v[64:67]\n"
  "v_mfma_f32_16x16x32_bf16 v[68:71], a[76:79], a[36:39], 0\n"
  "s_add_u32 s52, s52, s81\n"
  "s_addc_u32 s53, s53, 0\n"
  "v_mfma_f32_16x16x32_bf16 v[68:71], a[72:75], a[32:35], v[68:71]\n"
  "s_add_u32 s56, s56, s80\n"
  "s_addc_u32 s57, s57, 0\n"
  "v_mfma_f32_16x16x32_bf16 v[72:75], a[80:83], a[32:35], 0\n"
  "s_sub_u32 s85, s85, 1\n"
  "s_cselect_b32 s54, 0, s54\n"
  "s_cselect_b32 s58, 0, s58\n"
  "v_mfma_f32_16x16x32_bf16 v[72:75], a[84:87], a[36:39], v[72:75]\n"
  "v_mfma_f32_16x16x32_bf16 v[76:79], a[92:95], a[36:39], 0\n"
  "s_cmp_eq_u32 s101, 3\n"
  "s_cbranch_scc1 L_pw_fin%=\n"
  "s_waitcnt vmcnt(12)\n"
  "L_pw_fin%=:\n"
  "s_waitcnt lgkmcnt(0)\n"
  "s_barrier\n"
  "v_mfma_f32_16x16x32_bf16 v[76:79], a[88:91], a[32:35], v[76:79]\n"
  "s_add_u32 m0, s84, 0\n"
  "v_mfma_f32_16x16x32_bf16 v[92:95], a[88:91], a[40:43], 0\n"
  "buffer_load_dwordx4 v250, s[56:59], s72 offen lds\n"
  "v_mfma_f32_16x16x32_bf16 v[92:95], a[92:95], a[44:47], v[92:95]\n"
  "ds_read_b64_tr_b16 a[96:97], v236\n"
  "v_mfma_f32_16x16x32_bf16 v[88:91], a[84:87], a[44:47], 0\n"
  "ds_read_b64_tr_b16 a[98:99], v236 offset:2048\n"
  "v_mfma_f32_16x16x32_bf16 v[88:91], a[80:83], a[40:43], v[88:91]\n"
  "ds_read_b64_tr_b16 a[100:101], v236 offset:4096\n"
  "v_mfma_f32_16x16x32_bf16 v[84:87], a[72:75], a[40:43], 0\n"
  "ds_read_b64_tr_b16 a[102:103], v236 offset:6144\n"
  "v_mfma_f32_16x16x32_bf16 v[84:87], a[76:79], a[44:47], v[84:87]\n"
  "buffer_load_dwordx4 v251, s[56:59], s73 offen offset:1024 lds\n"
  "v_mfma_f32_16x16x32_bf16 v[80:83], a[68:71], a[44:47], 0\n"
  "ds_read_b64_tr_b16 a[104:105], v237\n"
  "v_mfma_f32_16x16x32_bf16 v[80:83], a[64:67], a[40:43], v[80:83]\n"
  "ds_read_b64_tr_b16 a[106:107], v237 offset:2048\n"
  "v_mfma_f32_16x16x32_bf16 v[96:99], a[64:67], a[48:51], 0\n"
  "ds_read_b64_tr_b16 a[108:109], v237 offset:4096\n"
  "v_mfma_f32_16x16x32_bf16 v[96:99], a[68:71], a[52:55], v[96:99]\n"
  "ds_read_b64_tr_b16 a[110:111], v237 offset:6144\n"
  "v_mfma_f32_16x16x32_bf16 v[100:103], a[76:79], a[52:55], 0\n"
  "buffer_load_dwordx4 v250, s[56:59], s74 offen offset:2048 lds\n"
  "v_mfma_f32_16x16x32_bf16 v[100:103], a[72:75], a[48:51], v[100:103]\n"
  "ds_read_b64_tr_b16 a[112:113], v238\n"
  "v_mfma_f32_16x16x32_bf16 v[104:107], a[80:83], a[48:51], 0\n"
  "ds_read_b64_tr_b16 a[114:115], v238 offset:2048\n"
  "v_mfma_f32_16x16x32_bf16 v[104:107], a[84:87], a[52:55], v[104:107]\n"
  "ds_read_b64_tr_b16 a[116:117], v238 offset:4096\n"
  "v_mfma_f32_16x16x32_bf16 v[108:111], a[92:95], a[52:55], 0\n"
  "ds_read_b64_tr_b16 a[118:119], v238 offset:6144\n"
  "v_mfma_f32_16x16x32_bf16 v[108:111], a[88:91], a[48:51], v[108:111]\n"
  "buffer_load_dwordx4 v251, s[56:59], s75 offen offset:3072 lds\n"
  "v_mfma_f32_16x16x32_bf16 v[124:127], a[88:91], a[56:59], 0\n"
  "ds_read_b64_tr_b16 a[120:121], v239\n"
  "v_mfma_f32_16x16x32_bf16 v[124:127], a[92:95], a[60:63], v[124:127]\n"
  "ds_read_b64_tr_b16 a[122:123], v239 offset:2048\n"
  "v_mfma_f32_16x16x32_bf16 v[120:123], a[84:87], a[60:63], 0\n"
  "ds_read_b64_tr_b16 a[124:125], v239 offset:4096\n"
  "v_mfma_f32_16x16x32_bf16 v[120:123], a[80:83], a[56:59], v[120:123]\n"
  "ds_read_b64_tr_b16 a[126:127], v239 offset:6144\n"
  "s_add_u32 m0, s83, 0\n"
  "v_mfma_f32_16x16x32_bf16 v[116:119], a[72:75], a[56:59], 0\n"
  "buffer_load_dwordx4 v252, s[52:55], s64 offen sc1 lds\n"
  "v_mfma_f32_16x16x32_bf16 v[116:119], a[76:79], a[60:63], v[116:119]\n"
  "ds_read_b128 a[16:19], v205\n"
  "v_mfma_f32_16x16x32_bf16 v[112:115], a[68:71], a[60:63], 0\n"
  "ds_read_b128 a[20:23], v205 offset:16384\n"
  "v_mfma_f32_16x16x32_bf16 v[112:115], a[64:67], a[56:59], v[112:115]\n"
  "ds_read_b128 a[24:27], v205 offset:256\n"
  "v_mfma_f32_16x16x32_bf16 v[0:3], a[64:67], a[0:3], 0\n"
  "ds_read_b128 a[28:31], v205 offset:16640\n"
  "v_mfma_f32_16x16x32_bf16 v[0:3], a[68:71], a[4:7], v[0:3]\n"
  "buffer_load_dwordx4 v253, s[52:55], s65 offen offset:1024 sc1 lds\n"
  "v_mfma_f32_16x16x32_bf16 v[4:7], a[76:79], a[4:7], 0\n"
  "ds_read_b128 a[32:35], v206\n"
  "v_mfma_f32_16x16x32_bf16 v[4:7], a[72:75], a[0:3], v[4:7]\n"
  "ds_read_b128 a[36:39], v206 offset:16384\n"
  "v_mfma_f32_16x16x32_bf16 v[8:11], a[80:83], a[0:3], 0\n"
  "ds_read_b128 a[40:43], v206 offset:256\n"
  "v_mfma_f32_16x16x32_bf16 v[8:11], a[84:87], a[4:7], v[8:11]\n"
  "ds_read_b128 a[44:47], v206 offset:16640\n"
  "v_mfma_f32_16x16x32_bf16 v[12:15], a[92:95], a[4:7], 0\n"
  "buffer_load_dwordx4 v252, s[52:55], s66 offen offset:2048 sc1 lds\n"
  "v_mfma_f32_16x16x32_bf16 v[12:15], a[88:91], a[0:3], v[12:15]\n"
  "v_mfma_f32_16x16x32_bf16 v[28:31], a[88:91], a[8:11], 0\n"
  "v_mfma_f32_16x16x32_bf16 v[28:31], a[92:95], a[12:15], v[28:31]\n"
  "v_mfma_f32_16x16x32_bf16 v[24:27], a[84:87], a[12:15], 0\n"
  "v_mfma_f32_16x16x32_bf16 v[24:27], a[80:83], a[8:11], v[24:27]\n"
  "buffer_load_dwordx4 v253, s[52:55], s67 offen offset:3072 sc1 lds\n"
  "v_mfma_f32_16x16x32_bf16 v[20:23], a[72:75], a[8:11], 0\n"
  "v_mfma_f32_16x16x32_bf16 v[20:23], a[76:79], a[12:15], v[20:23]\n"
  "v_mfma_f32_16x16x32_bf16 v[16:19], a[68:71], a[12:15], 0\n"
  "v_mfma_f32_16x16x32_bf16 v[16:19], a[64:67], a[8:11], v[16:19]\n"
  "s_waitcnt lgkmcnt(0)\n"
  "s_branch L_f_mid%=\n"
  "L_f_loop%=:\n"
  "v_mfma_f32_16x16x32_bf16 v[32:35], a[64:67], a[16:19], v[32:35]\n"
  "ds_read_b128 a[48:51], v203\n"
  "s_add_u32 m0, s83, 0x11000\n"
  "v_mfma_f32_16x16x32_bf16 v[32:35], a[68:71], a[20:23], v[32:35]\n"
  "buffer_load_dwordx4 v252, s[52:55], s68 offen sc1 lds\n"
  "ds_read_b128 a[52:55], v203 offset:16384\n"
  "v_mfma_f32_16x16x32_bf16 v[36:39], a[76:79], a[20:23], v[36:39]\n"
  "ds_read_b128 a[56:59], v203 offset:256\n"
  "v_mfma_f32_16x16x32_bf16 v[36:39], a[72:75], a[16:19], v[36:39]\n"
  "ds_read_b128 a[60:63], v203 offset:16640\n"
  "v_mfma_f32_16x16x32_bf16 v[40:43], a[80:83], a[16:19], v[40:43]\n"
  "ds_read_b128 a[0:3], v200\n"
  "v_mfma_f32_16x16x32_bf16 v[40:43], a[84:87], a[20:23], v[40:43]\n"
  "ds_read_b128 a[4:7], v200 offset:16384\n"
  "v_mfma_f32_16x16x32_bf16 v[44:47], a[92:95], a[20:23], v[44:47]\n"
  "buffer_load_dwordx4 v253, s[52:55], s69 offen offset:1024 sc1 lds\n"
  "ds_read_b128 a[8:11], v200 offset:256\n"
  "v_mfma_f32_16x16x32_bf16 v[44:47], a[88:91], a[16:19], v[44:47]\n"
  "ds_read_b128 a[12:15], v200 offset:16640\n"
  "v_mfma_f32_16x16x32_bf16 v[60:63], a[88:91], a[24:27], v[60:63]\n"
  "v_mfma_f32_16x16x32_bf16 v[60:63], a[92:95], a[28:31], v[60:63]\n"
  "v_mfma_f32_16x16x32_bf16 v[56:59], a[84:87], a[28:31], v[56:59]\n"
  "v_mfma_f32_16x16x32_bf16 v[56:59], a[80:83], a[24:27], v[56:59]\n"
  "buffer_load_dwordx4 v252, s[52:55], s70 offen offset:2048 sc1 lds\n"
  "v_mfma_f32_16x16x32_bf16 v[52:55], a[72:75], a[24:27], v[52:55]\n"
  "v_mfma_f32_16x16x32_bf16 v[52:55], a[76:79], a[28:31], v[52:55]\n"
  "v_mfma_f32_16x16x32_bf16 v[48:51], a[68:71], a[28:31], v[48:51]\n"
  "v_mfma_f32_16x16x32_bf16 v[48:51], a[64:67], a[24:27], v[48:51]\n"
  "v_mfma_f32_16x16x32_bf16 v[64:67], a[64:67], a[32:35], v[64:67]\n"
  "buffer_load_dwordx4 v253, s[52:55], s71 offen offset:3072 sc1 lds\n"
  "v_mfma_f32_16x16x32_bf16 v[64:67], a[68:71], a[36:39], v[64:67]\n"
  "v_mfma_f32_16x16x32_bf16 v[68:71], a[76:79], a[36:39], v[68:71]\n"
  "s_add_u32 s52, s52, s81\n"
  "s_addc_u32 s53, s53, 0\n"
  "v_mfma_f32_16x16x32_bf16 v[68:71], a[72:75], a[32:35], v[68:71]\n"
  "s_add_u32 s56, s56, s80\n"
  "s_addc_u32 s57, s57, 0\n"
  "v_mfma_f32_16x16x32_bf16 v[72:75], a[80:83], a[32:35], v[72:75]\n"
  "s_sub_u32 s85, s85, 1\n"
  "s_cselect_b32 s54, 0, s54\n"
  "s_cselect_b32 s58, 0, s58\n"
  "v_mfma_f32_16x16x32_bf16 v[72:75], a[84:87], a[36:39], v[72:75]\n"
  "s_bitcmp1_b32 s94, s85\n"
  "v_mfma_f32_16x16x32_bf16 v[76:79], a[92:95], a[36:39], v[76:79]\n"
  "s_waitcnt vmcnt(12) lgkmcnt(0)\n"
  "s_barrier\n"
  "v_mfma_f32_16x16x32_bf16 v[76:79], a[88:91], a[32:35], v[76:79]\n"
  "s_cbranch_scc1 L_fev0%=\n"
  "L_fevr0%=:\n"
  "s_add_u32 m0, s84, 0\n"
  "v_mfma_f32_16x16x32_bf16 v[92:95], a[88:91], a[40:43], v[92:95]\n"
  "buffer_load_dwordx4 v250, s[56:59], s72 offen lds\n"
  "v_mfma_f32_16x16x32_bf16 v[92:95], a[92:95], a[44:47], v[92:95]\n"
  "ds_read_b64_tr_b16 a[96:97], v236\n"
  "v_mfma_f32_16x16x32_bf16 v[88:91], a[84:87], a[44:47], v[88:91]\n"
  "ds_read_b64_tr_b16 a[98:99], v236 offset:2048\n"
  "v_mfma_f32_16x16x32_bf16 v[88:91], a[80:83], a[40:43], v[88:91]\n"
  "ds_read_b64_tr_b16 a[100:101], v236 offset:4096\n"
  "v_mfma_f32_16x16x32_bf16 v[84:87], a[72:75], a[40:43], v[84:87]\n"
  "ds_read_b64_tr_b16 a[102:103], v236 offset:6144\n"
  "v_mfma_f32_16x16x32_bf16 v[84:87], a[76:79], a[44:47], v[84:87]\n"
  "buffer_load_dwordx4 v251, s[56:59], s73 offen offset:1024 lds\n"
  "v_mfma_f32_16x16x32_bf16 v[80:83], a[68:71], a[44:47], v[80:83]\n"
  "ds_read_b64_tr_b16 a[104:105], v237\n"
  "v_mfma_f32_16x16x32_bf16 v[80:83], a[64:67], a[40:43], v[80:83]\n"
  "ds_read_b64_tr_b16 a[106:107], v237 offset:2048\n"
  "v_mfma_f32_16x16x32_bf16 v[96:99], a[64:67], a[48:51], v[96:99]\n"
  "ds_read_b64_tr_b16 a[108:109], v237 offset:4096\n"
  "v_mfma_f32_16x16x32_bf16 v[96:99], a[68:71], a[52:55], v[96:99]\n"
  "ds_read_b64_tr_b16 a[110:111], v237 offset:6144\n"
  "v_mfma_f32_16x16x32_bf16 v[100:103], a[76:79], a[52:55], v[100:103]\n"
  "buffer_load_dwordx4 v250, s[56:59], s74 offen offset:2048 lds\n"
  "v_mfma_f32_16x16x32_bf16 v[100:103], a[72:75], a[48:51], v[100:103]\n"
  "ds_read_b64_tr_b16 a[112:113], v238\n"
  "v_mfma_f32_16x16x32_bf16 v[104:107], a[80:83], a[48:51], v[104:107]\n"
  "ds_read_b64_tr_b16 a[114:115], v238 offset:2048\n"
  "v_mfma_f32_16x16x32_bf16 v[104:107], a[84:87], a[52:55], v[104:107]\n"
  "ds_read_b64_tr_b16 a[116:117], v238 offset:4096\n"
  "v_mfma_f32_16x16x32_bf16 v[108:111], a[92:95], a[52:55], v[108:111]\n"
  "ds_read_b64_tr_b16 a[118:119], v238 offset:6144\n"
  "v_mfma_f32_16x16x32_bf16 v[108:111], a[88:91], a[48:51], v[108:111]\n"
  "buffer_load_dwordx4 v251, s[56:59], s75 offen offset:3072 lds\n"
  "v_mfma_f32_16x16x32_bf16 v[124:127], a[88:91], a[56:59], v[124:127]\n"
  "ds_read_b64_tr_b16 a[120:121], v239\n"
  "v_mfma_f32_16x16x32_bf16 v[124:127], a[92:95], a[60:63], v[124:127]\n"
  "ds_read_b64_tr_b16 a[122:123], v239 offset:2048\n"
  "v_mfma_f32_16x16x32_bf16 v[120:123], a[84:87], a[60:63], v[120:123]\n"
  "ds_read_b64_tr_b16 a[124:125], v239 offset:4096\n"
  "v_mfma_f32_16x16x32_bf16 v[120:123], a[80:83], a[56:59], v[120:123]\n"
  "ds_read_b64_tr_b16 a[126:127], v239 offset:6144\n"
  "s_add_u32 m0, s83, 0\n"
  "v_mfma_f32_16x16x32_bf16 v[116:119], a[72:75], a[56:59], v[116:119]\n"
  "buffer_load_dwordx4 v252, s[52:55], s64 offen sc1 lds\n"
  "v_mfma_f32_16x16x32_bf16 v[116:119], a[76:79], a[60:63], v[116:119]\n"
  "ds_read_b128 a[16:19], v205\n"
  "v_mfma_f32_16x16x32_bf16 v[112:115], a[68:71], a[60:63], v[112:115]\n"
  "ds_read_b128 a[20:23], v205 offset:16384\n"
  "v_mfma_f32_16x16x32_bf16 v[112:115], a[64:67], a[56:59], v[112:115]\n"
  "ds_read_b128 a[24:27], v205 offset:256\n"
  "v_mfma_f32_16x16x32_bf16 v[0:3], a[64:67], a[0:3], v[0:3]\n"
  "ds_read_b128 a[28:31], v205 offset:16640\n"
  "v_mfma_f32_16x16x32_bf16 v[0:3], a[68:71], a[4:7], v[0:3]\n"
  "buffer_load_dwordx4 v253, s[52:55], s65 offen offset:1024 sc1 lds\n"
  "v_mfma_f32_16x16x32_bf16 v[4:7], a[76:79], a[4:7], v[4:7]\n"
  "ds_read_b128 a[32:35], v206\n"
  "v_mfma_f32_16x16x32_bf16 v[4:7], a[72:75], a[0:3], v[4:7]\n"
  "ds_read_b128 a[36:39], v206 offset:16384\n"
  "v_mfma_f32_16x16x32_bf16 v[8:11], a[80:83], a[0:3], v[8:11]\n"
  "ds_read_b128 a[40:43], v206 offset:256\n"
  "v_mfma_f32_16x16x32_bf16 v[8:11], a[84:87], a[4:7], v[8:11]\n"
  "ds_read_b128 a[44:47], v206 offset:16640\n"
  "v_mfma_f32_16x16x32_bf16 v[12:15], a[92:95], a[4:7], v[12:15]\n"
  "buffer_load_dwordx4 v252, s[52:55], s66 offen offset:2048 sc1 lds\n"
  "v_mfma_f32_16x16x32_bf16 v[12:15], a[88:91], a[0:3], v[12:15]\n"
  "v_mfma_f32_16x16x32_bf16 v[28:31], a[88:91], a[8:11], v[28:31]\n"
  "v_mfma_f32_16x16x32_bf16 v[28:31], a[92:95], a[12:15], v[28:31]\n"
  "v_mfma_f32_16x16x32_bf16 v[24:27], a[84:87], a[12:15], v[24:27]\n"
  "v_mfma_f32_16x16x32_bf16 v[24:27], a[80:83], a[8:11], v[24:27]\n"
  "buffer_load_dwordx4 v253, s[52:55], s67 offen offset:3072 sc1 lds\n"
  "v_mfma_f32_16x16x32_bf16 v[20:23], a[72:75], a[8:11], v[20:23]\n"
  "v_mfma_f32_16x16x32_bf16 v[20:23], a[76:79], a[12:15], v[20:23]\n"
  "v_mfma_f32_16x16x32_bf16 v[16:19], a[68:71], a[12:15], v[16:19]\n"
  "v_mfma_f32_16x16x32_bf16 v[16:19], a[64:67], a[8:11], v[16:19]\n"
  "s_waitcnt lgkmcnt(0)\n"
  "L_f_mid%=:\n"
  "v_mfma_f32_16x16x32_bf16 v[32:35], a[96:99], a[16:19], v[32:35]\n"
  "ds_read_b128 a[48:51], v207\n"
  "s_add_u32 m0, s83, 0x1000\n"
  "v_mfma_f32_16x16x32_bf16 v[32:35], a[100:103], a[20:23], v[32:35]\n"
  "buffer_load_dwordx4 v252, s[52:55], s68 offen sc1 lds\n"
  "ds_read_b128 a[52:55], v207 offset:16384\n"
  "v_mfma_f32_16x16x32_bf16 v[36:39], a[108:111], a[20:23], v[36:39]\n"
  "ds_read_b128 a[56:59], v207 offset:256\n"
  "v_mfma_f32_16x16x32_bf16 v[36:39], a[104:107], a[16:19], v[36:39]\n"
  "ds_read_b128 a[60:63], v207 offset:16640\n"
  "v_mfma_f32_16x16x32_bf16 v[40:43], a[112:115], a[16:19], v[40:43]\n"
  "ds_read_b128 a[0:3], v204\n"
  "v_mfma_f32_16x16x32_bf16 v[40:43], a[116:119], a[20:23], v[40:43]\n"
  "ds_read_b128 a[4:7], v204 offset:16384\n"
  "v_mfma_f32_16x16x32_bf16 v[44:47], a[124:127], a[20:23], v[44:47]\n"
  "buffer_load_dwordx4 v253, s[52:55], s69 offen offset:1024 sc1 lds\n"
  "ds_read_b128 a[8:11], v204 offset:256\n"
  "v_mfma_f32_16x16x32_bf16 v[44:47], a[120:123], a[16:19], v[44:47]\n"
  "ds_read_b128 a[12:15], v204 offset:16640\n"
  "v_mfma_f32_16x16x32_bf16 v[60:63], a[120:123], a[24:27], v[60:63]\n"
  "v_mfma_f32_16x16x32_bf16 v[60:63], a[124:127], a[28:31], v[60:63]\n"
  "v_mfma_f32_16x16x32_bf16 v[56:59], a[116:119], a[28:31], v[56:59]\n"
  "v_mfma_f32_16x16x32_bf16 v[56:59], a[112:115], a[24:27], v[56:59]\n"
  "buffer_load_dwordx4 v252, s[52:55], s70 offen offset:2048 sc1 lds\n"
  "v_mfma_f32_16x16x32_bf16 v[52:55], a[104:107], a[24:27], v[52:55]\n"
  "v_mfma_f32_16x16x32_bf16 v[52:55], a[108:111], a[28:31], v[52:55]\n"
  "v_mfma_f32_16x16x32_bf16 v[48:51], a[100:103], a[28:31], v[48:51]\n"
  "v_mfma_f32_16x16x32_bf16 v[48:51], a[96:99], a[24:27], v[48:51]\n"
  "v_mfma_f32_16x16x32_bf16 v[64:67], a[96:99], a[32:35], v[64:67]\n"
  "buffer_load_dwordx4 v253, s[52:55], s71 offen offset:3072 sc1 lds\n"
  "v_mfma_f32_16x16x32_bf16 v[64:67], a[100:103], a[36:39], v[64:67]\n"
  "v_mfma_f32_16x16x32_bf16 v[68:71], a[108:111], a[36:39], v[68:71]\n"
  "s_add_u32 s52, s52, s81\n"
  "s_addc_u32 s53, s53, 0\n"
  "v_mfma_f32_16x16x32_bf16 v[68:71], a[104:107], a[32:35], v[68:71]\n"
  "s_add_u32 s56, s56, s80\n"
  "s_addc_u32 s57, s57, 0\n"
  "v_mfma_f32_16x16x32_bf16 v[72:75], a[112:115], a[32:35], v[72:75]\n"
  "s_sub_u32 s85, s85, 1\n"
  "s_cselect_b32 s54, 0, s54\n"
  "s_cselect_b32 s58, 0, s58\n"
  "v_mfma_f32_16x16x32_bf16 v[72:75], a[116:119], a[36:39], v[72:75]\n"
  "s_bitcmp1_b32 s94, s85\n"
  "v_mfma_f32_16x16x32_bf16 v[76:79], a[124:127], a[36:39], v[76:79]\n"
  "s_waitcnt vmcnt(12) lgkmcnt(0)\n"
  "s_barrier\n"
  "v_mfma_f32_16x16x32_bf16 v[76:79], a[120:123], a[32:35], v[76:79]\n"
  "s_cbranch_scc1 L_fev1%=\n"
  "L_fevr1%=:\n"
  "s_add_u32 m0, s84, 0x4000\n"
  "v_mfma_f32_16x16x32_bf16 v[92:95], a[120:123], a[40:43], v[92:95]\n"
  "buffer_load_dwordx4 v250, s[56:59], s72 offen lds\n"
  "v_mfma_f32_16x16x32_bf16 v[92:95], a[124:127], a[44:47], v[92:95]\n"
  "ds_read_b64_tr_b16 a[128:129], v242\n"
  "v_mfma_f32_16x16x32_bf16 v[88:91], a[116:119], a[44:47], v[88:91]\n"
  "ds_read_b64_tr_b16 a[130:131], v242 offset:2048\n"
  "v_mfma_f32_16x16x32_bf16 v[88:91], a[112:115], a[40:43], v[88:91]\n"
  "ds_read_b64_tr_b16 a[132:133], v242 offset:4096\n"
  "v_mfma_f32_16x16x32_bf16 v[84:87], a[104:107], a[40:43], v[84:87]\n"
  "ds_read_b64_tr_b16 a[134:135], v242 offset:6144\n"
  "v_mfma_f32_16x16x32_bf16 v[84:87], a[108:111], a[44:47], v[84:87]\n"
  "buffer_load_dwordx4 v251, s[56:59], s73 offen offset:1024 lds\n"
  "v_mfma_f32_16x16x32_bf16 v[80:83], a[100:103], a[44:47], v[80:83]\n"
  "ds_read_b64_tr_b16 a[136:137], v243\n"
  "v_mfma_f32_16x16x32_bf16 v[80:83], a[96:99], a[40:43], v[80:83]\n"
  "ds_read_b64_tr_b16 a[138:139], v243 offset:2048\n"
  "v_mfma_f32_16x16x32_bf16 v[96:99], a[96:99], a[48:51], v[96:99]\n"
  "ds_read_b64_tr_b16 a[140:141], v243 offset:4096\n"
  "v_mfma_f32_16x16x32_bf16 v[96:99], a[100:103], a[52:55], v[96:99]\n"
  "ds_read_b64_tr_b16 a[142:143], v243 offset:6144\n"
  "v_mfma_f32_16x16x32_bf16 v[100:103], a[108:111], a[52:55], v[100:103]\n"
  "buffer_load_dwordx4 v250, s[56:59], s74 offen offset:2048 lds\n"
  "v_mfma_f32_16x16x32_bf16 v[100:103], a[104:107], a[48:51], v[100:103]\n"
  "ds_read_b64_tr_b16 a[144:145], v244\n"
  "v_mfma_f32_16x16x32_bf16 v[104:107], a[112:115], a[48:51], v[104:107]\n"
  "ds_read_b64_tr_b16 a[146:147], v244 offset:2048\n"
  "v_mfma_f32_16x16x32_bf16 v[104:107], a[116:119], a[52:55], v[104:107]\n"
  "ds_read_b64_tr_b16 a[148:149], v244 offset:4096\n"
  "v_mfma_f32_16x16x32_bf16 v[108:111], a[124:127], a[52:55], v[108:111]\n"
  "ds_read_b64_tr_b16 a[150:151], v244 offset:6144\n"
  "v_mfma_f32_16x16x32_bf16 v[108:111], a[120:123], a[48:51], v[108:111]\n"
  "buffer_load_dwordx4 v251, s[56:59], s75 offen offset:3072 lds\n"
  "v_mfma_f32_16x16x32_bf16 v[124:127], a[120:123], a[56:59], v[124:127]\n"
  "ds_read_b64_tr_b16 a[152:153], v245\n"
  "v_mfma_f32_16x16x32_bf16 v[124:127], a[124:127], a[60:63], v[124:127]\n"
  "ds_read_b64_tr_b16 a[154:155], v245 offset:2048\n"
  "v_mfma_f32_16x16x32_bf16 v[120:123], a[116:119], a[60:63], v[120:123]\n"
  "ds_read_b64_tr_b16 a[156:157], v245 offset:4096\n"
  "v_mfma_f32_16x16x32_bf16 v[120:123], a[112:115], a[56:59], v[120:123]\n"
  "ds_read_b64_tr_b16 a[158:159], v245 offset:6144\n"
  "s_add_u32 m0, s83, 0x8000\n"
  "v_mfma_f32_16x16x32_bf16 v[116:119], a[104:107], a[56:59], v[116:119]\n"
  "buffer_load_dwordx4 v252, s[52:55], s64 offen sc1 lds\n"
  "v_mfma_f32_16x16x32_bf16 v[116:119], a[108:111], a[60:63], v[116:119]\n"
  "ds_read_b128 a[16:19], v209\n"
  "v_mfma_f32_16x16x32_bf16 v[112:115], a[100:103], a[60:63], v[112:115]\n"
  "ds_read_b128 a[20:23], v209 offset:16384\n"
  "v_mfma_f32_16x16x32_bf16 v[112:115], a[96:99], a[56:59], v[112:115]\n"
  "ds_read_b128 a[24:27], v209 offset:256\n"
  "v_mfma_f32_16x16x32_bf16 v[0:3], a[96:99], a[0:3], v[0:3]\n"
  "ds_read_b128 a[28:31], v209 offset:16640\n"
  "v_mfma_f32_16x16x32_bf16 v[0:3], a[100:103], a[4:7], v[0:3]\n"
  "buffer_load_dwordx4 v253, s[52:55], s65 offen offset:1024 sc1 lds\n"
  "v_mfma_f32_16x16x32_bf16 v[4:7], a[108:111], a[4:7], v[4:7]\n"
  "ds_read_b128 a[32:35], v210\n"
  "v_mfma_f32_16x16x32_bf16 v[4:7], a[104:107], a[0:3], v[4:7]\n"
  "ds_read_b128 a[36:39], v210 offset:16384\n"
  "v_mfma_f32_16x16x32_bf16 v[8:11], a[112:115], a[0:3], v[8:11]\n"
  "ds_read_b128 a[40:43], v210 offset:256\n"
  "v_mfma_f32_16x16x32_bf16 v[8:11], a[116:119], a[4:7], v[8:11]\n"
  "ds_read_b128 a[44:47], v210 offset:16640\n"
  "v_mfma_f32_16x16x32_bf16 v[12:15], a[124:127], a[4:7], v[12:15]\n"
  "buffer_load_dwordx4 v252, s[52:55], s66 offen offset:2048 sc1 lds\n"
  "v_mfma_f32_16x16x32_bf16 v[12:15], a[120:123], a[0:3], v[12:15]\n"
  "v_mfma_f32_16x16x32_bf16 v[28:31], a[120:123], a[8:11], v[28:31]\n"
  "v_mfma_f32_16x16x32_bf16 v[28:31], a[124:127], a[12:15], v[28:31]\n"
  "v_mfma_f32_16x16x32_bf16 v[24:27], a[116:119], a[12:15], v[24:27]\n"
  "v_mfma_f32_16x16x32_bf16 v[24:27], a[112:115], a[8:11], v[24:27]\n"
  "buffer_load_dwordx4 v253, s[52:55], s67 offen offset:3072 sc1 lds\n"
  "v_mfma_f32_16x16x32_bf16 v[20:23], a[104:107], a[8:11], v[20:23]\n"
  "v_mfma_f32_16x16x32_bf16 v[20:23], a[108:111], a[12:15], v[20:23]\n"
  "v_mfma_f32_16x16x32_bf16 v[16:19], a[100:103], a[12:15], v[16:19]\n"
  "v_mfma_f32_16x16x32_bf16 v[16:19], a[96:99], a[8:11], v[16:19]\n"
  "s_waitcnt lgkmcnt(0)\n"
  "v_mfma_f32_16x16x32_bf16 v[32:35], a[128:131], a[16:19], v[32:35]\n"
  "ds_read_b128 a[48:51], v211\n"
  "s_add_u32 m0, s83, 0x9000\n"
  "v_mfma_f32_16x16x32_bf16 v[32:35], a[132:135], a[20:23], v[32:35]\n"
  "buffer_load_dwordx4 v252, s[52:55], s68 offen sc1 lds\n"
  "ds_read_b128 a[52:55], v211 offset:16384\n"
  "v_mfma_f32_16x16x32_bf16 v[36:39], a[140:143], a[20:23], v[36:39]\n"
  "ds_read_b128 a[56:59], v211 offset:256\n"
  "v_mfma_f32_16x16x32_bf16 v[36:39], a[136:139], a[16:19], v[36:39]\n"
  "ds_read_b128 a[60:63], v211 offset:16640\n"
  "v_mfma_f32_16x16x32_bf16 v[40:43], a[144:147], a[16:19], v[40:43]\n"
  "ds_read_b128 a[0:3], v208\n"
  "v_mfma_f32_16x16x32_bf16 v[40:43], a[148:151], a[20:23], v[40:43]\n"
  "ds_read_b128 a[4:7], v208 offset:16384\n"
  "v_mfma_f32_16x16x32_bf16 v[44:47], a[156:159], a[20:23], v[44:47]\n"
  "buffer_load_dwordx4 v253, s[52:55], s69 offen offset:1024 sc1 lds\n"
  "ds_read_b128 a[8:11], v208 offset:256\n"
  "v_mfma_f32_16x16x32_bf16 v[44:47], a[152:155], a[16:19], v[44:47]\n"
  "ds_read_b128 a[12:15], v208 offset:16640\n"
  "v_mfma_f32_16x16x32_bf16 v[60:63], a[152:155], a[24:27], v[60:63]\n"
  "v_mfma_f32_16x16x32_bf16 v[60:63], a[156:159], a[28:31], v[60:63]\n"
  "v_mfma_f32_16x16x32_bf16 v[56:59], a[148:151], a[28:31], v[56:59]\n"
  "v_mfma_f32_16x16x32_bf16 v[56:59], a[144:147], a[24:27], v[56:59]\n"
  "buffer_load_dwordx4 v252, s[52:55], s70 offen offset:2048 sc1 lds\n"
  "v_mfma_f32_16x16x32_bf16 v[52:55], a[136:139], a[24:27], v[52:55]\n"
  "v_mfma_f32_16x16x32_bf16 v[52:55], a[140:143], a[28:31], v[52:55]\n"
  "v_mfma_f32_16x16x32_bf16 v[48:51], a[132:135], a[28:31], v[48:51]\n"
  "v_mfma_f32_16x16x32_bf16 v[48:51], a[128:131], a[24:27], v[48:51]\n"
  "v_mfma_f32_16x16x32_bf16 v[64:67], a[128:131], a[32:35], v[64:67]\n"
  "buffer_load_dwordx4 v253, s[52:55], s71 offen offset:3072 sc1 lds\n"
  "v_mfma_f32_16x16x32_bf16 v[64:67], a[132:135], a[36:39], v[64:67]\n"
  "v_mfma_f32_16x16x32_bf16 v[68:71], a[140:143], a[36:39], v[68:71]\n"
  "s_add_u32 s52, s52, s81\n"
  "s_addc_u32 s53, s53, 0\n"
  "v_mfma_f32_16x16x32_bf16 v[68:71], a[136:139], a[32:35], v[68:71]\n"
  "s_add_u32 s56, s56, s80\n"
  "s_addc_u32 s57, s57, 0\n"
  "v_mfma_f32_16x16x32_bf16 v[72:75], a[144:147], a[32:35], v[72:75]\n"
  "s_sub_u32 s85, s85, 1\n"
  "s_cselect_b32 s54, 0, s54\n"
  "s_cselect_b32 s58, 0, s58\n"
  "v_mfma_f32_16x16x32_bf16 v[72:75], a[148:151], a[36:39], v[72:75]\n"
  "s_bitcmp1_b32 s94, s85\n"
  "v_mfma_f32_16x16x32_bf16 v[76:79], a[156:159], a[36:39], v[76:79]\n"
  "s_waitcnt vmcnt(12) lgkmcnt(0)\n"
  "s_barrier\n"
  "v_mfma_f32_16x16x32_bf16 v[76:79], a[152:155], a[32:35], v[76:79]\n"
  "s_cbranch_scc1 L_fev2%=\n"
  "L_fevr2%=:\n"
  "s_add_u32 m0, s84, 0x8000\n"
  "v_mfma_f32_16x16x32_bf16 v[92:95], a[152:155], a[40:43], v[92:95]\n"
  "buffer_load_dwordx4 v250, s[56:59], s72 offen lds\n"
  "v_mfma_f32_16x16x32_bf16 v[92:95], a[156:159], a[44:47], v[92:95]\n"
  "ds_read_b64_tr_b16 a[64:65], v230\n"
  "v_mfma_f32_16x16x32_bf16 v[88:91], a[148:151], a[44:47], v[88:91]\n"
  "ds_read_b64_tr_b16 a[66:67], v230 offset:2048\n"
  "v_mfma_f32_16x16x32_bf16 v[88:91], a[144:147], a[40:43], v[88:91]\n"
  "ds_read_b64_tr_b16 a[68:69], v230 offset:4096\n"
  "v_mfma_f32_16x16x32_bf16 v[84:87], a[136:139], a[40:43], v[84:87]\n"
  "ds_read_b64_tr_b16 a[70:71], v230 offset:6144\n"
  "v_mfma_f32_16x16x32_bf16 v[84:87], a[140:143], a[44:47], v[84:87]\n"
  "buffer_load_dwordx4 v251, s[56:59], s73 offen offset:1024 lds\n"
  "v_mfma_f32_16x16x32_bf16 v[80:83], a[132:135], a[44:47], v[80:83]\n"
  "ds_read_b64_tr_b16 a[72:73], v231\n"
  "v_mfma_f32_16x16x32_bf16 v[80:83], a[128:131], a[40:43], v[80:83]\n"
  "ds_read_b64_tr_b16 a[74:75], v231 offset:2048\n"
  "v_mfma_f32_16x16x32_bf16 v[96:99], a[128:131], a[48:51], v[96:99]\n"
  "ds_read_b64_tr_b16 a[76:77], v231 offset:4096\n"
  "v_mfma_f32_16x16x32_bf16 v[96:99], a[132:135], a[52:55], v[96:99]\n"
  "ds_read_b64_tr_b16 a[78:79], v231 offset:6144\n"
  "v_mfma_f32_16x16x32_bf16 v[100:103], a[140:143], a[52:55], v[100:103]\n"
  "buffer_load_dwordx4 v250, s[56:59], s74 offen offset:2048 lds\n"
  "v_mfma_f32_16x16x32_bf16 v[100:103], a[136:139], a[48:51], v[100:103]\n"
  "ds_read_b64_tr_b16 a[80:81], v232\n"
  "v_mfma_f32_16x16x32_bf16 v[104:107], a[144:147], a[48:51], v[104:107]\n"
  "ds_read_b64_tr_b16 a[82:83], v232 offset:2048\n"
  "v_mfma_f32_16x16x32_bf16 v[104:107], a[148:151], a[52:55], v[104:107]\n"
  "ds_read_b64_tr_b16 a[84:85], v232 offset:4096\n"
  "v_mfma_f32_16x16x32_bf16 v[108:111], a[156:159], a[52:55], v[108:111]\n"
  "ds_read_b64_tr_b16 a[86:87], v232 offset:6144\n"
  "v_mfma_f32_16x16x32_bf16 v[108:111], a[152:155], a[48:51], v[108:111]\n"
  "buffer_load_dwordx4 v251, s[56:59], s75 offen offset:3072 lds\n"
  "v_mfma_f32_16x16x32_bf16 v[124:127], a[152:155], a[56:59], v[124:127]\n"
  "ds_read_b64_tr_b16 a[88:89], v233\n"
  "v_mfma_f32_16x16x32_bf16 v[124:127], a[156:159], a[60:63], v[124:127]\n"
  "ds_read_b64_tr_b16 a[90:91], v233 offset:2048\n"
  "v_mfma_f32_16x16x32_bf16 v[120:123], a[148:151], a[60:63], v[120:123]\n"
  "ds_read_b64_tr_b16 a[92:93], v233 offset:4096\n"
  "v_mfma_f32_16x16x32_bf16 v[120:123], a[144:147], a[56:59], v[120:123]\n"
  "ds_read_b64_tr_b16 a[94:95], v233 offset:6144\n"
  "s_add_u32 m0, s83, 0x10000\n"
  "v_mfma_f32_16x16x32_bf16 v[116:119], a[136:139], a[56:59], v[116:119]\n"
  "buffer_load_dwordx4 v252, s[52:55], s64 offen sc1 lds\n"
  "v_mfma_f32_16x16x32_bf16 v[116:119], a[140:143], a[60:63], v[116:119]\n"
  "ds_read_b128 a[16:19], v201\n"
  "v_mfma_f32_16x16x32_bf16 v[112:115], a[132:135], a[60:63], v[112:115]\n"
  "ds_read_b128 a[20:23], v201 offset:16384\n"
  "v_mfma_f32_16x16x32_bf16 v[112:115], a[128:131], a[56:59], v[112:115]\n"
  "ds_read_b128 a[24:27], v201 offset:256\n"
  "v_mfma_f32_16x16x32_bf16 v[0:3], a[128:131], a[0:3], v[0:3]\n"
  "ds_read_b128 a[28:31], v201 offset:16640\n"
  "v_mfma_f32_16x16x32_bf16 v[0:3], a[132:135], a[4:7], v[0:3]\n"
  "buffer_load_dwordx4 v253, s[52:55], s65 offen offset:1024 sc1 lds\n"
  "v_mfma_f32_16x16x32_bf16 v[4:7], a[140:143], a[4:7], v[4:7]\n"
  "ds_read_b128 a[32:35], v202\n"
  "v_mfma_f32_16x16x32_bf16 v[4:7], a[136:139], a[0:3], v[4:7]\n"
  "ds_read_b128 a[36:39], v202 offset:16384\n"
  "v_mfma_f32_16x16x32_bf16 v[8:11], a[144:147], a[0:3], v[8:11]\n"
  "ds_read_b128 a[40:43], v202 offset:256\n"
  "v_mfma_f32_16x16x32_bf16 v[8:11], a[148:151], a[4:7], v[8:11]\n"
  "ds_read_b128 a[44:47], v202 offset:16640\n"
  "v_mfma_f32_16x16x32_bf16 v[12:15], a[156:159], a[4:7], v[12:15]\n"
  "buffer_load_dwordx4 v252, s[52:55], s66 offen offset:2048 sc1 lds\n"
  "v_mfma_f32_16x16x32_bf16 v[12:15], a[152:155], a[0:3], v[12:15]\n"
  "v_mfma_f32_16x16x32_bf16 v[28:31], a[152:155], a[8:11], v[28:31]\n"
  "v_mfma_f32_16x16x32_bf16 v[28:31], a[156:159], a[12:15], v[28:31]\n"
  "v_mfma_f32_16x16x32_bf16 v[24:27], a[148:151], a[12:15], v[24:27]\n"
  "v_mfma_f32_16x16x32_bf16 v[24:27], a[144:147], a[8:11], v[24:27]\n"
  "buffer_load_dwordx4 v253, s[52:55], s67 offen offset:3072 sc1 lds\n"
  "v_mfma_f32_16x16x32_bf16 v[20:23], a[136:139], a[8:11], v[20:23]\n"
  "v_mfma_f32_16x16x32_bf16 v[20:23], a[140:143], a[12:15], v[20:23]\n"
  "v_mfma_f32_16x16x32_bf16 v[16:19], a[132:135], a[12:15], v[16:19]\n"
  "v_mfma_f32_16x16x32_bf16 v[16:19], a[128:131], a[8:11], v[16:19]\n"
  "s_waitcnt lgkmcnt(0)\n"
  "s_branch L_f_loop%=\n"
  "L_f_last1%=:\n"
  "v_mfma_f32_16x16x32_bf16 v[92:95], a[120:123], a[40:43], v[92:95]\n"
  "s_xor_b32 s90, s89, 1\n"
  "s_mul_i32 s90, s90, 0xc000\n"
  "v_mfma_f32_16x16x32_bf16 v[92:95], a[124:127], a[44:47], v[92:95]\n"
  "s_add_u32 s90, s90, 0\n"
  "s_xor_b32 s91, s89, 2\n"
  "v_mfma_f32_16x16x32_bf16 v[88:91], a[116:119], a[44:47], v[88:91]\n"
  "s_mul_i32 s91, s91, 0xc000\n"
  "s_add_u32 s91, s91, 0x4000\n"
  "v_mfma_f32_16x16x32_bf16 v[88:91], a[112:115], a[40:43], v[88:91]\n"
  "s_xor_b32 s99, s89, 3\n"
  "s_mul_i32 s99, s99, 0xc000\n"
  "v_mfma_f32_16x16x32_bf16 v[84:87], a[104:107], a[40:43], v[84:87]\n"
  "s_add_u32 s99, s99, 0x8000\n"
  "v_mfma_f32_16x16x32_bf16 v[84:87], a[108:111], a[44:47], v[84:87]\n"
  "v_cvt_pk_bf16_f32 v128, v32, v33\n"
  "v_cvt_pk_bf16_f32 v129, v34, v35\n"
  "v_mfma_f32_16x16x32_bf16 v[80:83], a[100:103], a[44:47], v[80:83]\n"
  "v_cvt_pk_bf16_f32 v130, v36, v37\n"
  "v_cvt_pk_bf16_f32 v131, v38, v39\n"
  "v_mfma_f32_16x16x32_bf16 v[80:83], a[96:99], a[40:43], v[80:83]\n"
  "buffer_store_dwordx4 v[128:131], v192, s[60:63], s90 offen sc1\n"
  "v_cvt_pk_bf16_f32 v132, v40, v41\n"
  "v_mfma_f32_16x16x32_bf16 v[96:99], a[96:99], a[48:51], v[96:99]\n"
  "v_cvt_pk_bf16_f32 v133, v42, v43\n"
  "v_cvt_pk_bf16_f32 v134, v44, v45\n"
  "v_mfma_f32_16x16x32_bf16 v[96:99], a[100:103], a[52:55], v[96:99]\n"
  "v_cvt_pk_bf16_f32 v135, v46, v47\n"
  "v_cvt_pk_bf16_f32 v136, v48, v49\n"
  "v_mfma_f32_16x16x32_bf16 v[100:103], a[108:111], a[52:55], v[100:103]\n"
  "buffer_store_dwordx4 v[132:135], v192, s[60:63], s90 offen offset:1024 sc1\n"
  "v_cvt_pk_bf16_f32 v137, v50, v51\n"
  "v_mfma_f32_16x16x32_bf16 v[100:103], a[104:107], a[48:51], v[100:103]\n"
  "v_cvt_pk_bf16_f32 v138, v52, v53\n"
  "v_cvt_pk_bf16_f32 v139, v54, v55\n"
  "v_mfma_f32_16x16x32_bf16 v[104:107], a[112:115], a[48:51], v[104:107]\n"
  "buffer_store_dwordx4 v[136:139], v192, s[60:63], s90 offen offset:2048 sc1\n"
  "v_cvt_pk_bf16_f32 v140, v56, v57\n"
  "v_mfma_f32_16x16x32_bf16 v[104:107], a[116:119], a[52:55], v[104:107]\n"
  "v_cvt_pk_bf16_f32 v141, v58, v59\n"
  "v_cvt_pk_bf16_f32 v142, v60, v61\n"
  "v_mfma_f32_16x16x32_bf16 v[108:111], a[124:127], a[52:55], v[108:111]\n"
  "v_cvt_pk_bf16_f32 v143, v62, v63\n"
  "v_cvt_pk_bf16_f32 v144, v64, v65\n"
  "v_mfma_f32_16x16x32_bf16 v[108:111], a[120:123], a[48:51], v[108:111]\n"
  "buffer_store_dwordx4 v[140:143], v192, s[60:63], s90 offen offset:3072 sc1\n"
  "v_cvt_pk_bf16_f32 v145, v66, v67\n"
  "v_mfma_f32_16x16x32_bf16 v[124:127], a[120:123], a[56:59], v[124:127]\n"
  "v_cvt_pk_bf16_f32 v146, v68, v69\n"
  "v_cvt_pk_bf16_f32 v147, v70, v71\n"
  "v_mfma_f32_16x16x32_bf16 v[124:127], a[124:127], a[60:63], v[124:127]\n"
  "buffer_store_dwordx4 v[144:147], v192, s[60:63], s91 offen sc1\n"
  "v_cvt_pk_bf16_f32 v148, v72, v73\n"
  "v_mfma_f32_16x16x32_bf16 v[120:123], a[116:119], a[60:63], v[120:123]\n"
  "v_cvt_pk_bf16_f32 v149, v74, v75\n"
  "v_cvt_pk_bf16_f32 v150, v76, v77\n"
  "v_mfma_f32_16x16x32_bf16 v[120:123], a[112:115], a[56:59], v[120:123]\n"
  "v_cvt_pk_bf16_f32 v151, v78, v79\n"
  "v_cvt_pk_bf16_f32 v156, v88, v89\n"
  "v_mfma_f32_16x16x32_bf16 v[116:119], a[104:107], a[56:59], v[116:119]\n"
  "buffer_store_dwordx4 v[148:151], v192, s[60:63], s91 offen offset:1024 sc1\n"
  "v_cvt_pk_bf16_f32 v157, v90, v91\n"
  "v_mfma_f32_16x16x32_bf16 v[116:119], a[108:111], a[60:63], v[116:119]\n"
  "v_cvt_pk_bf16_f32 v158, v92, v93\n"
  "v_cvt_pk_bf16_f32 v159, v94, v95\n"
  "v_mfma_f32_16x16x32_bf16 v[112:115], a[100:103], a[60:63], v[112:115]\n"
  "buffer_store_dwordx4 v[156:159], v192, s[60:63], s91 offen offset:3072 sc1\n"
  "v_cvt_pk_bf16_f32 v152, v80, v81\n"
  "v_mfma_f32_16x16x32_bf16 v[112:115], a[96:99], a[56:59], v[112:115]\n"
  "v_cvt_pk_bf16_f32 v153, v82, v83\n"
  "v_cvt_pk_bf16_f32 v154, v84, v85\n"
  "v_mfma_f32_16x16x32_bf16 v[0:3], a[96:99], a[0:3], v[0:3]\n"
  "v_cvt_pk_bf16_f32 v155, v86, v87\n"
  "v_cvt_pk_bf16_f32 v160, v96, v97\n"
  "v_mfma_f32_16x16x32_bf16 v[0:3], a[100:103], a[4:7], v[0:3]\n"
  "buffer_store_dwordx4 v[152:155], v192, s[60:63], s91 offen offset:2048 sc1\n"
  "v_cvt_pk_bf16_f32 v161, v98, v99\n"
  "v_mfma_f32_16x16x32_bf16 v[4:7], a[108:111], a[4:7], v[4:7]\n"
  "v_cvt_pk_bf16_f32 v162, v100, v101\n"
  "v_cvt_pk_bf16_f32 v163, v102, v103\n"
  "v_mfma_f32_16x16x32_bf16 v[4:7], a[104:107], a[0:3], v[4:7]\n"
  "buffer_store_dwordx4 v[160:163], v192, s[60:63], s99 offen sc1\n"
  "v_cvt_pk_bf16_f32 v164, v104, v105\n"
  "v_mfma_f32_16x16x32_bf16 v[8:11], a[112:115], a[0:3], v[8:11]\n"
  "v_cvt_pk_bf16_f32 v165, v106, v107\n"
  "v_cvt_pk_bf16_f32 v166, v108, v109\n"
  "v_mfma_f32_16x16x32_bf16 v[8:11], a[116:119], a[4:7], v[8:11]\n"
  "v_cvt_pk_bf16_f32 v167, v110, v111\n"
  "v_cvt_pk_bf16_f32 v172, v120, v121\n"
  "v_mfma_f32_16x16x32_bf16 v[12:15], a[124:127], a[4:7], v[12:15]\n"
  "buffer_store_dwordx4 v[164:167], v192, s[60:63], s99 offen offset:1024 sc1\n"
  "v_cvt_pk_bf16_f32 v173, v122, v123\n"
  "v_mfma_f32_16x16x32_bf16 v[12:15], a[120:123], a[0:3], v[12:15]\n"
  "v_cvt_pk_bf16_f32 v174, v124, v125\n"
  "v_cvt_pk_bf16_f32 v175, v126, v127\n"
  "v_mfma_f32_16x16x32_bf16 v[28:31], a[120:123], a[8:11], v[28:31]\n"
  "buffer_store_dwordx4 v[172:175], v192, s[60:63], s99 offen offset:3072 sc1\n"
  "v_cvt_pk_bf16_f32 v168, v112, v113\n"
  "v_mfma_f32_16x16x32_bf16 v[28:31], a[124:127], a[12:15], v[28:31]\n"
  "v_cvt_pk_bf16_f32 v169, v114, v115\n"
  "v_cvt_pk_bf16_f32 v170, v116, v117\n"
  "v_mfma_f32_16x16x32_bf16 v[24:27], a[116:119], a[12:15], v[24:27]\n"
  "v_cvt_pk_bf16_f32 v171, v118, v119\n"
  "v_mfma_f32_16x16x32_bf16 v[24:27], a[112:115], a[8:11], v[24:27]\n"
  "buffer_store_dwordx4 v[168:171], v192, s[60:63], s99 offen offset:2048 sc1\n"
  "v_mfma_f32_16x16x32_bf16 v[20:23], a[104:107], a[8:11], v[20:23]\n"
  "v_mfma_f32_16x16x32_bf16 v[20:23], a[108:111], a[12:15], v[20:23]\n"
  "v_mfma_f32_16x16x32_bf16 v[16:19], a[100:103], a[12:15], v[16:19]\n"
  "v_mfma_f32_16x16x32_bf16 v[16:19], a[96:99], a[8:11], v[16:19]\n"
  "s_branch L_f_x%=\n"
  "L_f_last2%=:\n"
  "v_mfma_f32_16x16x32_bf16 v[92:95], a[152:155], a[40:43], v[92:95]\n"
  "s_xor_b32 s90, s89, 1\n"
  "s_mul_i32 s90, s90, 0xc000\n"
  "v_mfma_f32_16x16x32_bf16 v[92:95], a[156:159], a[44:47], v[92:95]\n"
  "s_add_u32 s90, s90, 0\n"
  "s_xor_b32 s91, s89, 2\n"
  "v_mfma_f32_16x16x32_bf16 v[88:91], a[148:151], a[44:47], v[88:91]\n"
  "s_mul_i32 s91, s91, 0xc000\n"
  "s_add_u32 s91, s91, 0x4000\n"
  "v_mfma_f32_16x16x32_bf16 v[88:91], a[144:147], a[40:43], v[88:91]\n"
  "s_xor_b32 s99, s89, 3\n"
  "s_mul_i32 s99, s99, 0xc000\n"
  "v_mfma_f32_16x16x32_bf16 v[84:87], a[136:139], a[40:43], v[84:87]\n"
  "s_add_u32 s99, s99, 0x8000\n"
  "v_mfma_f32_16x16x32_bf16 v[84:87], a[140:143], a[44:47], v[84:87]\n"
  "v_cvt_pk_bf16_f32 v128, v32, v33\n"
  "v_cvt_pk_bf16_f32 v129, v34, v35\n"
  "v_mfma_f32_16x16x32_bf16 v[80:83], a[132:135], a[44:47], v[80:83]\n"
  "v_cvt_pk_bf16_f32 v130, v36, v37\n"
  "v_cvt_pk_bf16_f32 v131, v38, v39\n"
  "v_mfma_f32_16x16x32_bf16 v[80:83], a[128:131], a[40:43], v[80:83]\n"
  "buffer_store_dwordx4 v[128:131], v192, s[60:63], s90 offen sc1\n"
  "v_cvt_pk_bf16_f32 v132, v40, v41\n"
  "v_mfma_f32_16x16x32_bf16 v[96:99], a[128:131], a[48:51], v[96:99]\n"
  "v_cvt_pk_bf16_f32 v133, v42, v43\n"
  "v_cvt_pk_bf16_f32 v134, v44, v45\n"
  "v_mfma_f32_16x16x32_bf16 v[96:99], a[132:135], a[52:55], v[96:99]\n"
  "v_cvt_pk_bf16_f32 v135, v46, v47\n"
  "v_cvt_pk_bf16_f32 v136, v48, v49\n"
  "v_mfma_f32_16x16x32_bf16 v[100:103], a[140:143], a[52:55], v[100:103]\n"
  "buffer_store_dwordx4 v[132:135], v192, s[60:63], s90 offen offset:1024 sc1\n"
  "v_cvt_pk_bf16_f32 v137, v50, v51\n"
  "v_mfma_f32_16x16x32_bf16 v[100:103], a[136:139], a[48:51], v[100:103]\n"
  "v_cvt_pk_bf16_f32 v138, v52, v53\n"
  "v_cvt_pk_bf16_f32 v139, v54, v55\n"
  "v_mfma_f32_16x16x32_bf16 v[104:107], a[144:147], a[48:51], v[104:107]\n"
  "buffer_store_dwordx4 v[136:139], v192, s[60:63], s90 offen offset:2048 sc1\n"
  "v_cvt_pk_bf16_f32 v140, v56, v57\n"
  "v_mfma_f32_16x16x32_bf16 v[104:107], a[148:151], a[52:55], v[104:107]\n"
  "v_cvt_pk_bf16_f32 v141, v58, v59\n"
  "v_cvt_pk_bf16_f32 v142, v60, v61\n"
  "v_mfma_f32_16x16x32_bf16 v[108:111], a[156:159], a[52:55], v[108:111]\n"
  "v_cvt_pk_bf16_f32 v143, v62, v63\n"
  "v_cvt_pk_bf16_f32 v144, v64, v65\n"
  "v_mfma_f32_16x16x32_bf16 v[108:111], a[152:155], a[48:51], v[108:111]\n"
  "buffer_store_dwordx4 v[140:143], v192, s[60:63], s90 offen offset:3072 sc1\n"
  "v_cvt_pk_bf16_f32 v145, v66, v67\n"
  "v_mfma_f32_16x16x32_bf16 v[124:127], a[152:155], a[56:59], v[124:127]\n"
  "v_cvt_pk_bf16_f32 v146, v68, v69\n"
  "v_cvt_pk_bf16_f32 v147, v70, v71\n"
  "v_mfma_f32_16x16x32_bf16 v[124:127], a[156:159], a[60:63], v[124:127]\n"
  "buffer_store_dwordx4 v[144:147], v192, s[60:63], s91 offen sc1\n"
  "v_cvt_pk_bf16_f32 v148, v72, v73\n"
  "v_mfma_f32_16x16x32_bf16 v[120:123], a[148:151], a[60:63], v[120:123]\n"
  "v_cvt_pk_bf16_f32 v149, v74, v75\n"
  "v_cvt_pk_bf16_f32 v150, v76, v77\n"
  "v_mfma_f32_16x16x32_bf16 v[120:123], a[144:147], a[56:59], v[120:123]\n"
  "v_cvt_pk_bf16_f32 v151, v78, v79\n"
  "v_cvt_pk_bf16_f32 v156, v88, v89\n"
  "v_mfma_f32_16x16x32_bf16 v[116:119], a[136:139], a[56:59], v[116:119]\n"
  "buffer_store_dwordx4 v[148:151], v192, s[60:63], s91 offen offset:1024 sc1\n"
  "v_cvt_pk_bf16_f32 v157, v90, v91\n"
  "v_mfma_f32_16x16x32_bf16 v[116:119], a[140:143], a[60:63], v[116:119]\n"
  "v_cvt_pk_bf16_f32 v158, v92, v93\n"
  "v_cvt_pk_bf16_f32 v159, v94, v95\n"
  "v_mfma_f32_16x16x32_bf16 v[112:115], a[132:135], a[60:63], v[112:115]\n"
  "buffer_store_dwordx4 v[156:159], v192, s[60:63], s91 offen offset:3072 sc1\n"
  "v_cvt_pk_bf16_f32 v152, v80, v81\n"
  "v_mfma_f32_16x16x32_bf16 v[112:115], a[128:131], a[56:59], v[112:115]\n"
  "v_cvt_pk_bf16_f32 v153, v82, v83\n"
  "v_cvt_pk_bf16_f32 v154, v84, v85\n"
  "v_mfma_f32_16x16x32_bf16 v[0:3], a[128:131], a[0:3], v[0:3]\n"
  "v_cvt_pk_bf16_f32 v155, v86, v87\n"
  "v_cvt_pk_bf16_f32 v160, v96, v97\n"
  "v_mfma_f32_16x16x32_bf16 v[0:3], a[132:135], a[4:7], v[0:3]\n"
  "buffer_store_dwordx4 v[152:155], v192, s[60:63], s91 offen offset:2048 sc1\n"
  "v_cvt_pk_bf16_f32 v161, v98, v99\n"
  "v_mfma_f32_16x16x32_bf16 v[4:7], a[140:143], a[4:7], v[4:7]\n"
  "v_cvt_pk_bf16_f32 v162, v100, v101\n"
  "v_cvt_pk_bf16_f32 v163, v102, v103\n"
  "v_mfma_f32_16x16x32_bf16 v[4:7], a[136:139], a[0:3], v[4:7]\n"
  "buffer_store_dwordx4 v[160:163], v192, s[60:63], s99 offen sc1\n"
  "v_cvt_pk_bf16_f32 v164, v104, v105\n"
  "v_mfma_f32_16x16x32_bf16 v[8:11], a[144:147], a[0:3], v[8:11]\n"
  "v_cvt_pk_bf16_f32 v165, v106, v107\n"
  "v_cvt_pk_bf16_f32 v166, v108, v109\n"
  "v_mfma_f32_16x16x32_bf16 v[8:11], a[148:151], a[4:7], v[8:11]\n"
  "v_cvt_pk_bf16_f32 v167, v110, v111\n"
  "v_cvt_pk_bf16_f32 v172, v120, v121\n"
  "v_mfma_f32_16x16x32_bf16 v[12:15], a[156:159], a[4:7], v[12:15]\n"
  "buffer_store_dwordx4 v[164:167], v192, s[60:63], s99 offen offset:1024 sc1\n"
  "v_cvt_pk_bf16_f32 v173, v122, v123\n"
  "v_mfma_f32_16x16x32_bf16 v[12:15], a[152:155], a[0:3], v[12:15]\n"
  "v_cvt_pk_bf16_f32 v174, v124, v125\n"
  "v_cvt_pk_bf16_f32 v175, v126, v127\n"
  "v_mfma_f32_16x16x32_bf16 v[28:31], a[152:155], a[8:11], v[28:31]\n"
  "buffer_store_dwordx4 v[172:175], v192, s[60:63], s99 offen offset:3072 sc1\n"
  "v_cvt_pk_bf16_f32 v168, v112, v113\n"
  "v_mfma_f32_16x16x32_bf16 v[28:31], a[156:159], a[12:15], v[28:31]\n"
  "v_cvt_pk_bf16_f32 v169, v114, v115\n"
  "v_cvt_pk_bf16_f32 v170, v116, v117\n"
  "v_mfma_f32_16x16x32_bf16 v[24:27], a[148:151], a[12:15], v[24:27]\n"
  "v_cvt_pk_bf16_f32 v171, v118, v119\n"
  "v_mfma_f32_16x16x32_bf16 v[24:27], a[144:147], a[8:11], v[24:27]\n"
  "buffer_store_dwordx4 v[168:171], v192, s[60:63], s99 offen offset:2048 sc1\n"
  "v_mfma_f32_16x16x32_bf16 v[20:23], a[136:139], a[8:11], v[20:23]\n"
  "v_mfma_f32_16x16x32_bf16 v[20:23], a[140:143], a[12:15], v[20:23]\n"
  "v_mfma_f32_16x16x32_bf16 v[16:19], a[132:135], a[12:15], v[16:19]\n"
  "v_mfma_f32_16x16x32_bf16 v[16:19], a[128:131], a[8:11], v[16:19]\n"
  "s_branch L_f_x%=\n"
  "L_f_last0%=:\n"
  "v_mfma_f32_16x16x32_bf16 v[92:95], a[88:91], a[40:43], v[92:95]\n"
  "s_xor_b32 s90, s89, 1\n"
  "s_mul_i32 s90, s90, 0xc000\n"
  "v_mfma_f32_16x16x32_bf16 v[92:95], a[92:95], a[44:47], v[92:95]\n"
  "s_add_u32 s90, s90, 0\n"
  "s_xor_b32 s91, s89, 2\n"
  "v_mfma_f32_16x16x32_bf16 v[88:91], a[84:87], a[44:47], v[88:91]\n"
  "s_mul_i32 s91, s91, 0xc000\n"
  "s_add_u32 s91, s91, 0x4000\n"
  "v_mfma_f32_16x16x32_bf16 v[88:91], a[80:83], a[40:43], v[88:91]\n"
  "s_xor_b32 s99, s89, 3\n"
  "s_mul_i32 s99, s99, 0xc000\n"
  "v_mfma_f32_16x16x32_bf16 v[84:87], a[72:75], a[40:43], v[84:87]\n"
  "s_add_u32 s99, s99, 0x8000\n"
  "v_mfma_f32_16x16x32_bf16 v[84:87], a[76:79], a[44:47], v[84:87]\n"
  "v_cvt_pk_bf16_f32 v128, v32, v33\n"
  "v_cvt_pk_bf16_f32 v129, v34, v35\n"
  "v_mfma_f32_16x16x32_bf16 v[80:83], a[68:71], a[44:47], v[80:83]\n"
  "v_cvt_pk_bf16_f32 v130, v36, v37\n"
  "v_cvt_pk_bf16_f32 v131, v38, v39\n"
  "v_mfma_f32_16x16x32_bf16 v[80:83], a[64:67], a[40:43], v[80:83]\n"
  "buffer_store_dwordx4 v[128:131], v192, s[60:63], s90 offen sc1\n"
  "v_cvt_pk_bf16_f32 v132, v40, v41\n"
  "v_mfma_f32_16x16x32_bf16 v[96:99], a[64:67], a[48:51], v[96:99]\n"
  "v_cvt_pk_bf16_f32 v133, v42, v43\n"
  "v_cvt_pk_bf16_f32 v134, v44, v45\n"
  "v_mfma_f32_16x16x32_bf16 v[96:99], a[68:71], a[52:55], v[96:99]\n"
  "v_cvt_pk_bf16_f32 v135, v46, v47\n"
  "v_cvt_pk_bf16_f32 v136, v48, v49\n"
  "v_mfma_f32_16x16x32_bf16 v[100:103], a[76:79], a[52:55], v[100:103]\n"
  "buffer_store_dwordx4 v[132:135], v192, s[60:63], s90 offen offset:1024 sc1\n"
  "v_cvt_pk_bf16_f32 v137, v50, v51\n"
  "v_mfma_f32_16x16x32_bf16 v[100:103], a[72:75], a[48:51], v[100:103]\n"
  "v_cvt_pk_bf16_f32 v138, v52, v53\n"
  "v_cvt_pk_bf16_f32 v139, v54, v55\n"
  "v_mfma_f32_16x16x32_bf16 v[104:107], a[80:83], a[48:51], v[104:107]\n"
  "buffer_store_dwordx4 v[136:139], v192, s[60:63], s90 offen offset:2048 sc1\n"
  "v_cvt_pk_bf16_f32 v140, v56, v57\n"
  "v_mfma_f32_16x16x32_bf16 v[104:107], a[84:87], a[52:55], v[104:107]\n"
  "v_cvt_pk_bf16_f32 v141, v58, v59\n"
  "v_cvt_pk_bf16_f32 v142, v60, v61\n"
  "v_mfma_f32_16x16x32_bf16 v[108:111], a[92:95], a[52:55], v[108:111]\n"
  "v_cvt_pk_bf16_f32 v143, v62, v63\n"
  "v_cvt_pk_bf16_f32 v144, v64, v65\n"
  "v_mfma_f32_16x16x32_bf16 v[108:111], a[88:91], a[48:51], v[108:111]\n"
  "buffer_store_dwordx4 v[140:143], v192, s[60:63], s90 offen offset:3072 sc1\n"
  "v_cvt_pk_bf16_f32 v145, v66, v67\n"
  "v_mfma_f32_16x16x32_bf16 v[124:127], a[88:91], a[56:59], v[124:127]\n"
  "v_cvt_pk_bf16_f32 v146, v68, v69\n"
  "v_cvt_pk_bf16_f32 v147, v70, v71\n"
  "v_mfma_f32_16x16x32_bf16 v[124:127], a[92:95], a[60:63], v[124:127]\n"
  "buffer_store_dwordx4 v[144:147], v192, s[60:63], s91 offen sc1\n"
  "v_cvt_pk_bf16_f32 v148, v72, v73\n"
  "v_mfma_f32_16x16x32_bf16 v[120:123], a[84:87], a[60:63], v[120:123]\n"
  "v_cvt_pk_bf16_f32 v149, v74, v75\n"
  "v_cvt_pk_bf16_f32 v150, v76, v77\n"
  "v_mfma_f32_16x16x32_bf16 v[120:123], a[80:83], a[56:59], v[120:123]\n"
  "v_cvt_pk_bf16_f32 v151, v78, v79\n"
  "v_cvt_pk_bf16_f32 v156, v88, v89\n"
  "v_mfma_f32_16x16x32_bf16 v[116:119], a[72:75], a[56:59], v[116:119]\n"
  "buffer_store_dwordx4 v[148:151], v192, s[60:63], s91 offen offset:1024 sc1\n"
  "v_cvt_pk_bf16_f32 v157, v90, v91\n"
  "v_mfma_f32_16x16x32_bf16 v[116:119], a[76:79], a[60:63], v[116:119]\n"
  "v_cvt_pk_bf16_f32 v158, v92, v93\n"
  "v_cvt_pk_bf16_f32 v159, v94, v95\n"
  "v_mfma_f32_16x16x32_bf16 v[112:115], a[68:71], a[60:63], v[112:115]\n"
  "buffer_store_dwordx4 v[156:159], v192, s[60:63], s91 offen offset:3072 sc1\n"
  "v_cvt_pk_bf16_f32 v152, v80, v81\n"
  "v_mfma_f32_16x16x32_bf16 v[112:115], a[64:67], a[56:59], v[112:115]\n"
  "v_cvt_pk_bf16_f32 v153, v82, v83\n"
  "v_cvt_pk_bf16_f32 v154, v84, v85\n"
  "v_mfma_f32_16x16x32_bf16 v[0:3], a[64:67], a[0:3], v[0:3]\n"
  "v_cvt_pk_bf16_f32 v155, v86, v87\n"
  "v_cvt_pk_bf16_f32 v160, v96, v97\n"
  "v_mfma_f32_16x16x32_bf16 v[0:3], a[68:71], a[4:7], v[0:3]\n"
  "buffer_store_dwordx4 v[152:155], v192, s[60:63], s91 offen offset:2048 sc1\n"
  "v_cvt_pk_bf16_f32 v161, v98, v99\n"
  "v_mfma_f32_16x16x32_bf16 v[4:7], a[76:79], a[4:7], v[4:7]\n"
  "v_cvt_pk_bf16_f32 v162, v100, v101\n"
  "v_cvt_pk_bf16_f32 v163, v102, v103\n"
  "v_mfma_f32_16x16x32_bf16 v[4:7], a[72:75], a[0:3], v[4:7]\n"
  "buffer_store_dwordx4 v[160:163], v192, s[60:63], s99 offen sc1\n"
  "v_cvt_pk_bf16_f32 v164, v104, v105\n"
  "v_mfma_f32_16x16x32_bf16 v[8:11], a[80:83], a[0:3], v[8:11]\n"
  "v_cvt_pk_bf16_f32 v165, v106, v107\n"
  "v_cvt_pk_bf16_f32 v166, v108, v109\n"
  "v_mfma_f32_16x16x32_bf16 v[8:11], a[84:87], a[4:7], v[8:11]\n"
  "v_cvt_pk_bf16_f32 v167, v110, v111\n"
  "v_cvt_pk_bf16_f32 v172, v120, v121\n"
  "v_mfma_f32_16x16x32_bf16 v[12:15], a[92:95], a[4:7], v[12:15]\n"
  "buffer_store_dwordx4 v[164:167], v192, s[60:63], s99 offen offset:1024 sc1\n"
  "v_cvt_pk_bf16_f32 v173, v122, v123\n"
  "v_mfma_f32_16x16x32_bf16 v[12:15], a[88:91], a[0:3], v[12:15]\n"
  "v_cvt_pk_bf16_f32 v174, v124, v125\n"
  "v_cvt_pk_bf16_f32 v175, v126, v127\n"
  "v_mfma_f32_16x16x32_bf16 v[28:31], a[88:91], a[8:11], v[28:31]\n"
  "buffer_store_dwordx4 v[172:175], v192, s[60:63], s99 offen offset:3072 sc1\n"
  "v_cvt_pk_bf16_f32 v168, v112, v113\n"
  "v_mfma_f32_16x16x32_bf16 v[28:31], a[92:95], a[12:15], v[28:31]\n"
  "v_cvt_pk_bf16_f32 v169, v114, v115\n"
  "v_cvt_pk_bf16_f32 v170, v116, v117\n"
  "v_mfma_f32_16x16x32_bf16 v[24:27], a[84:87], a[12:15], v[24:27]\n"
  "v_cvt_pk_bf16_f32 v171, v118, v119\n"
  "v_mfma_f32_16x16x32_bf16 v[24:27], a[80:83], a[8:11], v[24:27]\n"
  "buffer_store_dwordx4 v[168:171], v192, s[60:63], s99 offen offset:2048 sc1\n"
  "v_mfma_f32_16x16x32_bf16 v[20:23], a[72:75], a[8:11], v[20:23]\n"
  "v_mfma_f32_16x16x32_bf16 v[20:23], a[76:79], a[12:15], v[20:23]\n"
  "v_mfma_f32_16x16x32_bf16 v[16:19], a[68:71], a[12:15], v[16:19]\n"
  "v_mfma_f32_16x16x32_bf16 v[16:19], a[64:67], a[8:11], v[16:19]\n"
  "L_f_x%=:\n"
  "s_waitcnt vmcnt(0)\n"
  "global_store_dword v195, v193, s[8:9] sc1\n"
  "global_load_dword v219, v194, s[8:9] sc1\n"
  "s_waitcnt vmcnt(0)\n"
  "s_mov_b32 s99, 0x4000\n"
  "L_fc_fx%=:\n"
  "v_cmp_ne_u32 vcc, s35, v219\n"
  "s_nop 4\n"
  "s_cbranch_vccz L_fd_fx%=\n"
  "s_sleep 2\n"
  "global_load_dword v219, v194, s[8:9] sc1\n"
  "s_waitcnt vmcnt(0)\n"
  "s_sub_u32 s99, s99, 1\n"
  "s_cbranch_scc0 L_fc_fx%=\n"
  "L_fd_fx%=:\n"
  "s_mul_i32 s90, s89, 0xc000\n"
  "buffer_load_dwordx4 v[32:35], v192, s[60:63], s90 offen sc1\n"
  "buffer_load_dwordx4 v[36:39], v192, s[60:63], s90 offen offset:1024 sc1\n"
  "buffer_load_dwordx4 v[40:43], v192, s[60:63], s90 offen offset:2048 sc1\n"
  "buffer_load_dwordx4 v[44:47], v192, s[60:63], s90 offen offset:3072 sc1\n"
  "s_add_u32 s90, s90, 0x4000\n"
  "buffer_load_dwordx4 v[48:51], v192, s[60:63], s90 offen sc1\n"
  "buffer_load_dwordx4 v[52:55], v192, s[60:63], s90 offen offset:1024 sc1\n"
  "buffer_load_dwordx4 v[56:59], v192, s[60:63], s90 offen offset:2048 sc1\n"
  "buffer_load_dwordx4 v[60:63], v192, s[60:63], s90 offen offset:3072 sc1\n"
  "s_add_u32 s90, s90, 0x4000\n"
  "buffer_load_dwordx4 v[64:67], v192, s[60:63], s90 offen sc1\n"
  "buffer_load_dwordx4 v[68:71], v192, s[60:63], s90 offen offset:1024 sc1\n"
  "buffer_load_dwordx4 v[72:75], v192, s[60:63], s90 offen offset:2048 sc1\n"
  "buffer_load_dwordx4 v[76:79], v192, s[60:63], s90 offen offset:3072 sc1\n"
  "s_mov_b32 s99, 0xffff0000\n"
  "s_waitcnt vmcnt(8)\n"
  "v_lshlrev_b32 v96, 16, v32\n"
  "v_and_b32 v97, s99, v32\n"
  "v_lshlrev_b32 v98, 16, v33\n"
  "v_and_b32 v99, s99, v33\n"
  "v_lshlrev_b32 v100, 16, v34\n"
  "v_and_b32 v101, s99, v34\n"
  "v_lshlrev_b32 v102, 16, v35\n"
  "v_and_b32 v103, s99, v35\n"
  "v_lshlrev_b32 v104, 16, v36\n"
  "v_and_b32 v105, s99, v36\n"
  "v_lshlrev_b32 v106, 16, v37\n"
  "v_and_b32 v107, s99, v37\n"
  "v_lshlrev_b32 v108, 16, v38\n"
  "v_and_b32 v109, s99, v38\n"
  "v_lshlrev_b32 v110, 16, v39\n"
  "v_and_b32 v111, s99, v39\n"
  "v_lshlrev_b32 v112, 16, v40\n"
  "v_and_b32 v113, s99, v40\n"
  "v_lshlrev_b32 v114, 16, v41\n"
  "v_and_b32 v115, s99, v41\n"
  "v_lshlrev_b32 v116, 16, v42\n"
  "v_and_b32 v117, s99, v42\n"
  "v_lshlrev_b32 v118, 16, v43\n"
  "v_and_b32 v119, s99, v43\n"
  "v_lshlrev_b32 v120, 16, v44\n"
  "v_and_b32 v121, s99, v44\n"
  "v_lshlrev_b32 v122, 16, v45\n"
  "v_and_b32 v123, s99, v45\n"
  "v_lshlrev_b32 v124, 16, v46\n"
  "v_and_b32 v125, s99, v46\n"
  "v_lshlrev_b32 v126, 16, v47\n"
  "v_and_b32 v127, s99, v47\n"
  "v_pk_add_f32 v[0:1], v[0:1], v[96:97]\n"
  "v_pk_add_f32 v[2:3], v[2:3], v[98:99]\n"
  "v_pk_add_f32 v[4:5], v[4:5], v[100:101]\n"
  "v_pk_add_f32 v[6:7], v[6:7], v[102:103]\n"
  "v_pk_add_f32 v[8:9], v[8:9], v[104:105]\n"
  "v_pk_add_f32 v[10:11], v[10:11], v[106:107]\n"
  "v_pk_add_f32 v[12:13], v[12:13], v[108:109]\n"
  "v_pk_add_f32 v[14:15], v[14:15], v[110:111]\n"
  "v_pk_add_f32 v[16:17], v[16:17], v[112:113]\n"
  "v_pk_add_f32 v[18:19], v[18:19], v[114:115]\n"
  "v_pk_add_f32 v[20:21], v[20:21], v[116:117]\n"
  "v_pk_add_f32 v[22:23], v[22:23], v[118:119]\n"
  "v_pk_add_f32 v[24:25], v[24:25], v[120:121]\n"
  "v_pk_add_f32 v[26:27], v[26:27], v[122:123]\n"
  "v_pk_add_f32 v[28:29], v[28:29], v[124:125]\n"
  "v_pk_add_f32 v[30:31], v[30:31], v[126:127]\n"
  "s_waitcnt vmcnt(4)\n"
  "v_lshlrev_b32 v96, 16, v48\n"
  "v_and_b32 v97, s99, v48\n"
  "v_lshlrev_b32 v98, 16, v49\n"
  "v_and_b32 v99, s99, v49\n"
  "v_lshlrev_b32 v100, 16, v50\n"
  "v_and_b32 v101, s99, v50\n"
  "v_lshlrev_b32 v102, 16, v51\n"
  "v_and_b32 v103, s99, v51\n"
  "v_lshlrev_b32 v104, 16, v52\n"
  "v_and_b32 v105, s99, v52\n"
  "v_lshlrev_b32 v106, 16, v53\n"
  "v_and_b32 v107, s99, v53\n"
  "v_lshlrev_b32 v108, 16, v54\n"
  "v_and_b32 v109, s99, v54\n"
  "v_lshlrev_b32 v110, 16, v55\n"
  "v_and_b32 v111, s99, v55\n"
  "v_lshlrev_b32 v112, 16, v56\n"
  "v_and_b32 v113, s99, v56\n"
  "v_lshlrev_b32 v114, 16, v57\n"
  "v_and_b32 v115, s99, v57\n"
  "v_lshlrev_b32 v116, 16, v58\n"
  "v_and_b32 v117, s99, v58\n"
  "v_lshlrev_b32 v118, 16, v59\n"
  "v_and_b32 v119, s99, v59\n"
  "v_lshlrev_b32 v120, 16, v60\n"
  "v_and_b32 v121, s99, v60\n"
  "v_lshlrev_b32 v122, 16, v61\n"
  "v_and_b32 v123, s99, v61\n"
  "v_lshlrev_b32 v124, 16, v62\n"
  "v_and_b32 v125, s99, v62\n"
  "v_lshlrev_b32 v126, 16, v63\n"
  "v_and_b32 v127, s99, v63\n"
  "v_pk_add_f32 v[0:1], v[0:1], v[96:97]\n"
  "v_pk_add_f32 v[2:3], v[2:3], v[98:99]\n"
  "v_pk_add_f32 v[4:5], v[4:5], v[100:101]\n"
  "v_pk_add_f32 v[6:7], v[6:7], v[102:103]\n"
  "v_pk_add_f32 v[8:9], v[8:9], v[104:105]\n"
  "v_pk_add_f32 v[10:11], v[10:11], v[106:107]\n"
  "v_pk_add_f32 v[12:13], v[12:13], v[108:109]\n"
  "v_pk_add_f32 v[14:15], v[14:15], v[110:111]\n"
  "v_pk_add_f32 v[16:17], v[16:17], v[112:113]\n"
  "v_pk_add_f32 v[18:19], v[18:19], v[114:115]\n"
  "v_pk_add_f32 v[20:21], v[20:21], v[116:117]\n"
  "v_pk_add_f32 v[22:23], v[22:23], v[118:119]\n"
  "v_pk_add_f32 v[24:25], v[24:25], v[120:121]\n"
  "v_pk_add_f32 v[26:27], v[26:27], v[122:123]\n"
  "v_pk_add_f32 v[28:29], v[28:29], v[124:125]\n"
  "v_pk_add_f32 v[30:31], v[30:31], v[126:127]\n"
  "s_waitcnt vmcnt(0)\n"
  "v_lshlrev_b32 v96, 16, v64\n"
  "v_and_b32 v97, s99, v64\n"
  "v_lshlrev_b32 v98, 16, v65\n"
  "v_and_b32 v99, s99, v65\n"
  "v_lshlrev_b32 v100, 16, v66\n"
  "v_and_b32 v101, s99, v66\n"
  "v_lshlrev_b32 v102, 16, v67\n"
  "v_and_b32 v103, s99, v67\n"
  "v_lshlrev_b32 v104, 16, v68\n"
  "v_and_b32 v105, s99, v68\n"
  "v_lshlrev_b32 v106, 16, v69\n"
  "v_and_b32 v107, s99, v69\n"
  "v_lshlrev_b32 v108, 16, v70\n"
  "v_and_b32 v109, s99, v70\n"
  "v_lshlrev_b32 v110, 16, v71\n"
  "v_and_b32 v111, s99, v71\n"
  "v_lshlrev_b32 v112, 16, v72\n"
  "v_and_b32 v113, s99, v72\n"
  "v_lshlrev_b32 v114, 16, v73\n"
  "v_and_b32 v115, s99, v73\n"
  "v_lshlrev_b32 v116, 16, v74\n"
  "v_and_b32 v117, s99, v74\n"
  "v_lshlrev_b32 v118, 16, v75\n"
  "v_and_b32 v119, s99, v75\n"
  "v_lshlrev_b32 v120, 16, v76\n"
  "v_and_b32 v121, s99, v76\n"
  "v_lshlrev_b32 v122, 16, v77\n"
  "v_and_b32 v123, s99, v77\n"
  "v_lshlrev_b32 v124, 16, v78\n"
  "v_and_b32 v125, s99, v78\n"
  "v_lshlrev_b32 v126, 16, v79\n"
  "v_and_b32 v127, s99, v79\n"
  "v_pk_add_f32 v[0:1], v[0:1], v[96:97]\n"
  "v_pk_add_f32 v[2:3], v[2:3], v[98:99]\n"
  "v_pk_add_f32 v[4:5], v[4:5], v[100:101]\n"
  "v_pk_add_f32 v[6:7], v[6:7], v[102:103]\n"
  "v_pk_add_f32 v[8:9], v[8:9], v[104:105]\n"
  "v_pk_add_f32 v[10:11], v[10:11], v[106:107]\n"
  "v_pk_add_f32 v[12:13], v[12:13], v[108:109]\n"
  "v_pk_add_f32 v[14:15], v[14:15], v[110:111]\n"
  "v_pk_add_f32 v[16:17], v[16:17], v[112:113]\n"
  "v_pk_add_f32 v[18:19], v[18:19], v[114:115]\n"
  "v_pk_add_f32 v[20:21], v[20:21], v[116:117]\n"
  "v_pk_add_f32 v[22:23], v[22:23], v[118:119]\n"
  "v_pk_add_f32 v[24:25], v[24:25], v[120:121]\n"
  "v_pk_add_f32 v[26:27], v[26:27], v[122:123]\n"
  "v_pk_add_f32 v[28:29], v[28:29], v[124:125]\n"
  "v_pk_add_f32 v[30:31], v[30:31], v[126:127]\n"
  "v_cvt_pk_bf16_f32 v176, v0, v1\n"
  "v_cvt_pk_bf16_f32 v177, v2, v3\n"
  "v_cvt_pk_bf16_f32 v178, v4, v5\n"
  "v_cvt_pk_bf16_f32 v179, v6, v7\n"
  "s_nop 1\n"
  "v_permlane16_swap_b32 v176, v178\n"
  "v_permlane16_swap_b32 v177, v179\n"
  "global_store_dwordx4 v196, v[176:179], s[30:31] offset:0\n"
  "v_cvt_pk_bf16_f32 v180, v8, v9\n"
  "v_cvt_pk_bf16_f32 v181, v10, v11\n"
  "v_cvt_pk_bf16_f32 v182, v12, v13\n"
  "v_cvt_pk_bf16_f32 v183, v14, v15\n"
  "s_nop 1\n"
  "v_permlane16_swap_b32 v180, v182\n"
  "v_permlane16_swap_b32 v181, v183\n"
  "global_store_dwordx4 v196, v[180:183], s[30:31] offset:64\n"
  "v_cvt_pk_bf16_f32 v184, v16, v17\n"
  "v_cvt_pk_bf16_f32 v185, v18, v19\n"
  "v_cvt_pk_bf16_f32 v186, v20, v21\n"
  "v_cvt_pk_bf16_f32 v187, v22, v23\n"
  "s_nop 1\n"
  "v_permlane16_swap_b32 v184, v186\n"
  "v_permlane16_swap_b32 v185, v187\n"
  "global_store_dwordx4 v197, v[184:187], s[30:31] offset:0\n"
  "v_cvt_pk_bf16_f32 v188, v24, v25\n"
  "v_cvt_pk_bf16_f32 v189, v26, v27\n"
  "v_cvt_pk_bf16_f32 v190, v28, v29\n"
  "v_cvt_pk_bf16_f32 v191, v30, v31\n"
  "s_nop 1\n"
  "v_permlane16_swap_b32 v188, v190\n"
  "v_permlane16_swap_b32 v189, v191\n"
  "global_store_dwordx4 v197, v[188:191], s[30:31] offset:64\n"
  "L_f_done%=:\n"
  "s_branch L_next%=\n"
  "L_fev0%=:\n"
  "s_mov_b32 s100, 0\n"
  "s_cmp_eq_u32 s85, -3\n"
  "s_cbranch_scc1 L_f_last0%=\n"
  "s_branch L_fev%=\n"
  "L_fev1%=:\n"
  "s_mov_b32 s100, 1\n"
  "s_cmp_eq_u32 s85, -3\n"
  "s_cbranch_scc1 L_f_last1%=\n"
  "s_branch L_fev%=\n"
  "L_fev2%=:\n"
  "s_mov_b32 s100, 2\n"
  "s_cmp_eq_u32 s85, -3\n"
  "s_cbranch_scc1 L_f_last2%=\n"
  "L_fev%=:\n"
  "s_cmp_eq_u32 s85, s88\n"
  "s_cbranch_scc1 L_fsw%=\n"
  "s_bfe_u32 s87, s40, 0x50017\n"
  "s_bfe_u32 s91, s40, 0x4001c\n"
  "s_lshr_b32 s90, s43, 3\n"
  "s_lshl_b32 s90, s90, 5\n"
  "s_add_u32 s87, s87, s90\n"
  "s_bitcmp1_b32 s40, 2\n"
  "s_cselect_b32 s99, 0, 0x100\n"
  "s_add_u32 s87, s87, s99\n"
  "s_lshl_b32 s87, s87, 7\n"
  "s_add_u32 s87, s87, 0x5a18000\n"
  "v_lshrrev_b32 v218, 2, v255\n"
  "v_min_u32 v218, s91, v218\n"
  "v_lshl_add_u32 v218, v218, 7, s87\n"
  "v_and_b32 v219, 3, v255\n"
  "v_lshl_add_u32 v218, v219, 2, v218\n"
  "global_load_dword v219, v218, s[8:9] sc1\n"
  "s_branch L_fevr%=\n"
  "; (covered by loop waits)\n"
  "L_fsw%=:\n"
  "s_mov_b32 s99, 0x4000\n"
  "L_fc_f2%=:\n"
  "v_cmp_ne_u32 vcc, s35, v219\n"
  "s_nop 4\n"
  "s_cbranch_vccz L_fd_f2%=\n"
  "s_sleep 2\n"
  "global_load_dword v219, v218, s[8:9] sc1\n"
  "s_waitcnt vmcnt(0)\n"
  "s_sub_u32 s99, s99, 1\n"
  "s_cbranch_scc0 L_fc_f2%=\n"
  "L_fd_f2%=:\n"
  "s_lshl_b32 s87, s34, 13\n"
  "s_add_u32 s90, s87, s42\n"
  "s_add_u32 s52, s8, s90\n"
  "s_addc_u32 s53, s9, 0\n"
  "s_mov_b64 s[56:57], s[92:93]\n"
  "s_mov_b32 s94, 0x20000000\n"
  "L_fevr%=:\n"
  "s_cmp_eq_u32 s100, 0\n"
  "s_cbranch_scc1 L_fevr0%=\n"
  "s_cmp_eq_u32 s100, 1\n"
  "s_cbranch_scc1 L_fevr1%=\n"
  "s_branch L_fevr2%=\n"
  "L_end%=:\n"
  "s_waitcnt vmcnt(0) lgkmcnt(0)\n"
  :
  : "s"(__builtin_amdgcn_kernarg_segment_ptr()), "s"(tab), "s"(w), "v"(lane), "s"(blockIdx.x)
  : "v0", "v1", "v2", "v3", "v4", "v5", "v6", "v7", "v8", "v9", "v10", "v11", "v12", "v13", "v14", "v15", "v16", "v17", "v18", "v19", "v20", "v21", "v22", "v23", "v24", "v25", "v26", "v27", "v28", "v29", "v30", "v31", "v32", "v33", "v34", "v35", "v36", "v37", "v38", "v39", "v40", "v41", "v42", "v43", "v44", "v45", "v46", "v47", "v48", "v49", "v50", "v51", "v52", "v53", "v54", "v55", "v56", "v57", "v58", "v59", "v60", "v61", "v62", "v63", "v64", "v65", "v66", "v67", "v68", "v69", "v70", "v71", "v72", "v73", "v74", "v75", "v76", "v77", "v78", "v79", "v80", "v81", "v82", "v83", "v84", "v85", "v86", "v87", "v88", "v89", "v90", "v91", "v92", "v93", "v94", "v95", "v96", "v97", "v98", "v99", "v100", "v101", "v102", "v103", "v104", "v105", "v106", "v107", "v108", "v109", "v110", "v111", "v112", "v113", "v114", "v115", "v116", "v117", "v118", "v119", "v120", "v121", "v122", "v123", "v124", "v125", "v126", "v127", "v128", "v129", "v130", "v131", "v132", "v133", "v134", "v135", "v136", "v137", "v138", "v139", "v140", "v141", "v142", "v143", "v144", "v145", "v146", "v147", "v148", "v149", "v150", "v151", "v152", "v153", "v154", "v155", "v156", "v157", "v158", "v159", "v160", "v161", "v162", "v163", "v164", "v165", "v166", "v167", "v168", "v169", "v170", "v171", "v172", "v173", "v174", "v175", "v176", "v177", "v178", "v179", "v180", "v181", "v182", "v183", "v184", "v185", "v186", "v187", "v188", "v189", "v190", "v191", "v192", "v193", "v194", "v195", "v196", "v197", "v198", "v199", "v200", "v201", "v202", "v203", "v204", "v205", "v206", "v207", "v208", "v209", "v210", "v211", "v212", "v213", "v214", "v215", "v216", "v217", "v218", "v219", "v220", "v221", "v222", "v223", "v224", "v225", "v226", "v227", "v228", "v229", "v230", "v231", "v232", "v233", "v234", "v235", "v236", "v237", "v238", "v239", "v240", "v241", "v242", "v243", "v244", "v245", "v246", "v247", "v248", "v249", "v250", "v251", "v252", "v253", "v255", "a0", "a1", "a2", "a3", "a4", "a5", "a6", "a7", "a8", "a9", "a10", "a11", "a12", "a13", "a14", "a15", "a16", "a17", "a18", "a19", "a20", "a21", "a22", "a23", "a24", "a25", "a26", "a27", "a28", "a29", "a30", "a31", "a32", "a33", "a34", "a35", "a36", "a37", "a38", "a39", "a40", "a41", "a42", "a43", "a44", "a45", "a46", "a47", "a48", "a49", "a50", "a51", "a52", "a53", "a54", "a55", "a56", "a57", "a58", "a59", "a60", "a61", "a62", "a63", "a64", "a65", "a66", "a67", "a68", "a69", "a70", "a71", "a72", "a73", "a74", "a75", "a76", "a77", "a78", "a79", "a80", "a81", "a82", "a83", "a84", "a85", "a86", "a87", "a88", "a89", "a90", "a91", "a92", "a93", "a94", "a95", "a96", "a97", "a98", "a99", "a100", "a101", "a102", "a103", "a104", "a105", "a106", "a107", "a108", "a109", "a110", "a111", "a112", "a113", "a114", "a115", "a116", "a117", "a118", "a119", "a120", "a121", "a122", "a123", "a124", "a125", "a126", "a127", "a128", "a129", "a130", "a131", "a132", "a133", "a134", "a135", "a136", "a137", "a138", "a139", "a140", "a141", "a142", "a143", "a144", "a145", "a146", "a147", "a148", "a149", "a150", "a151", "a152", "a153", "a154", "a155", "a156", "a157", "a158", "a159", "a160", "a161", "a162", "a163", "a164", "a165", "a166", "a167", "a168", "a169", "a170", "a171", "a172", "a173", "a174", "a175", "a176", "a177", "a178", "a179", "a180", "a181", "a182", "a183", "a184", "a185", "a186", "a187", "a188", "a189", "a190", "a191", "a192", "a193", "a194", "a195", "a196", "a197", "a198", "a199", "a200", "a201", "a202", "a203", "a204", "a205", "a206", "a207", "a208", "a209", "a210", "a211", "a212", "a213", "a214", "a215", "a216", "a217", "a218", "a219", "a220", "a221", "a222", "a223", "a224", "a225", "a226", "a227", "a228", "a229", "a230", "a231", "a232", "a233", "a234", "a235", "a236", "a237", "a238", "a239", "a240", "a241", "a242", "a243", "a244", "a245", "a246", "a247", "a248", "a249", "a250", "a251", "a252", "a253", "a254", "a255", "s8", "s9", "s10", "s11", "s12", "s13", "s14", "s15", "s16", "s17", "s18", "s19", "s20", "s21", "s22", "s23", "s24", "s25", "s26", "s27", "s28", "s29", "s30", "s31", "s32", "s33", "s34", "s35", "s36", "s37", "s38", "s39", "s40", "s41", "s42", "s43", "s44", "s45", "s46", "s47", "s48", "s49", "s50", "s51", "s52", "s53", "s54", "s55", "s56", "s57", "s58", "s59", "s60", "s61", "s62", "s63", "s64", "s65", "s66", "s67", "s68", "s69", "s70", "s71", "s72", "s73", "s74", "s75", "s76", "s77", "s78", "s79", "s80", "s81", "s82", "s83", "s84", "s85", "s86", "s87", "s88", "s89", "s90", "s91", "s92", "s93", "s94", "s95", "s96", "s97", "s98", "s99", "s100", "s101", "memory", "scc", "vcc");
}
extern "C" void kernel_launch(void* const* d_in, const int* in_sizes, int n_in, void* d_out, int out_size, void* d_ws, size_t ws_size, hipStream_t stream) {
  MArgs a; a.ws = (char*)d_ws + 0; a.x = d_in[0]; a.ckv = d_in[1]; a.wq = d_in[2]; a.wuk = d_in[3]; a.wuv = d_in[4]; a.wo = d_in[5]; a.wg = d_in[7]; a.wu = d_in[8]; a.wd = d_in[9]; a.gw = d_in[10]; a.out = d_out;
  mega<<<256, 256, 0, stream>>>(a);
}
```
